```python
import torch

def _softmax(x, axis=-1):
    return torch.softmax(x, dim=axis)

def _gelu(x):
    return torch.nn.functional.gelu(x, approximate='tanh')

def _silu(x):
    return torch.nn.functional.silu(x)

def _sigmoid(x):
    return torch.sigmoid(x)

def _layernorm(x, axis=-1, eps=1e-05):
    m = x.mean(dim=axis, keepdim=True)
    v = x.var(dim=axis, keepdim=True, unbiased=False)
    return (x - m) / torch.sqrt(v + eps)

def _scatter_add(tgt, idx, src, axis=0):
    return tgt.clone().index_add_(axis, idx, src)

def _segment_sum(src, seg, n):
    out = torch.zeros((n,) + src.shape[1:], dtype=src.dtype)
    return out.index_add_(0, seg, src)

def _pad(x, pw):
    flat = tuple((v for pair in reversed(pw) for v in pair))
    return torch.nn.functional.pad(x, flat)
kernel_name = 'interleave_layers__ssm_conv_h3__memory_aug_recurrent_mem_tr__prompt'

def setup_inputs():

    _w = lambda a: (lambda t: t.to(torch.bfloat16) if t.is_floating_point() else t)(torch.as_tensor(a))

    def _A_setup():
        B, S, D = (1, 2048, 576)
        ks = 4
        return {'q': _w((lambda *s: torch.randn(*s, dtype=torch.bfloat16) * 0.05)(B, S, D)), 'k': _w((lambda *s: torch.randn(*s, dtype=torch.bfloat16) * 0.2)(B, S, D)), 'v': _w((lambda *s: torch.randn(*s, dtype=torch.bfloat16) * 0.2)(B, S, D)), 'shift_k': _w((lambda *s: torch.randn(*s, dtype=torch.bfloat16) * 0.2)(D, ks) * 0.5), 'ssm_k': _w((lambda *s: torch.randn(*s, dtype=torch.bfloat16) * 0.4)(D, S) * (1.0 / S ** 0.5)), 'ssm_D': _w((lambda *s: torch.randn(*s, dtype=torch.bfloat16) * 0.2)(D))}
    _A_in = _A_setup()

    def _B_setup():
        B, S, Dm = (1, 2048, 576)
        H, Dh = (32, 128)
        M, seg = (8, 32)
        seg = min(seg, S)
        S = S // seg * seg
        return {'x': _w((lambda *s: torch.randn(*s, dtype=torch.bfloat16) * 0.05)(B, S, Dm)), 'mem0': _w((lambda *s: torch.randn(*s, dtype=torch.bfloat16) * 0.2)(B, M, Dm)), 'Wq': _w((lambda *s: torch.randn(*s, dtype=torch.bfloat16) * 1.6)(Dm, H, Dh) * Dm ** (-0.5)), 'Wk': _w((lambda *s: torch.randn(*s, dtype=torch.bfloat16) * 1.6)(Dm, H, Dh) * Dm ** (-0.5)), 'Wv': _w((lambda *s: torch.randn(*s, dtype=torch.bfloat16) * 0.2)(Dm, H, Dh) * Dm ** (-0.5)), 'Wo': _w((lambda *s: torch.randn(*s, dtype=torch.bfloat16) * 0.2)(H, Dh, Dm) * (H * Dh) ** (-0.5)), 'seg_len': seg}
    _B_in = _B_setup()
    return {'A_q': _A_in['q'], 'A_k': _A_in['k'], 'A_v': _A_in['v'], 'A_shift_k': _A_in['shift_k'], 'A_ssm_k': _A_in['ssm_k'], 'A_ssm_D': _A_in['ssm_D'], 'B_x': _B_in['x'], 'B_mem0': _B_in['mem0'], 'B_Wq': _B_in['Wq'], 'B_Wk': _B_in['Wk'], 'B_Wv': _B_in['Wv'], 'B_Wo': _B_in['Wo'], 'B_seg_len': _B_in['seg_len']}

def reference(A_q, A_k, A_v, A_shift_k, A_ssm_k, A_ssm_D, B_x, B_mem0, B_Wq, B_Wk, B_Wv, B_Wo, B_seg_len):

    def _A_ref(q, k, v, shift_k, ssm_k, ssm_D):
        B, S, D = q.shape
        ks = shift_k.shape[-1]
        k_pad = _pad(k, ((0, 0), (ks - 1, 0), (0, 0)))
        k_shift = torch.zeros_like(k)
        for t in range(ks):
            k_shift = k_shift + k_pad[:, t:t + S, :] * shift_k[:, ks - 1 - t][None, None, :]
        kv = k_shift * v
        L = 2 * S
        kv_pad = _pad(kv, ((0, 0), (0, S), (0, 0)))
        kf_pad = _pad(ssm_k, ((0, 0), (0, S)))
        kv_f = (lambda x, axes=(-2, -1): torch.fft.rfft2(x.float(), dim=axes))(kv_pad[:, None, :, :], axes=(1, 2))
        kf_f = (lambda x, axes=(-2, -1): torch.fft.rfft2(x.float(), dim=axes))(kf_pad[:, None, :], axes=(1, 2))
        kf_f = (lambda x, *d: x.permute(*d))(kf_f, 1, 2, 0)
        y_f = kv_f * kf_f[None, :, :, :]
        y = (lambda x, s, axes=(-2, -1): torch.fft.irfft2(x, s=s, dim=axes).to(torch.bfloat16))(y_f, (1, L), axes=(1, 2))[:, 0, :S, :]
        y = y + kv * ssm_D[None, None, :]
        return q * y

    def _B_ref(x, mem0, Wq, Wk, Wv, Wo, seg_len):
        B, S, Dm = x.shape
        H, Dh = (Wq.shape[1], Wq.shape[2])
        M = mem0.shape[1]
        scale = Dh ** (-0.5)
        n_seg = S // seg_len
        L = M + seg_len
        idx = torch.arange(L)
        mask = (idx[:, None] < M) | (idx[:, None] >= idx[None, :])
        mem = mem0
        outs = []
        for s in range(n_seg):
            xs = x[:, s * seg_len:(s + 1) * seg_len, :].clone()
            h = torch.concatenate([mem, xs], axis=1)
            q = torch.einsum('btd,dhe->bhte', h, Wq)
            k = torch.einsum('btd,dhe->bhte', h, Wk)
            v = torch.einsum('btd,dhe->bhte', h, Wv)
            scores = torch.einsum('bhqe,bhke->bhqk', q, k) * scale
            scores = torch.where(mask[None, None], scores, -1000000000.0)
            p = _softmax(scores, axis=-1)
            o = torch.einsum('bhqk,bhke->bhqe', p, v)
            o = torch.einsum('bhte,hed->btd', o, Wo)
            mem = o[:, :M, :].clone()
            outs.append(o[:, M:, :])
        return torch.concatenate(outs, axis=1)
    x = A_q
    x = x + _A_ref(_layernorm(x, axis=-1), A_k, A_v, A_shift_k, A_ssm_k, A_ssm_D)
    x = x + _B_ref(_layernorm(x, axis=-1), B_mem0, B_Wq, B_Wk, B_Wv, B_Wo, B_seg_len)
    x = x + _A_ref(_layernorm(x, axis=-1), A_k, A_v, A_shift_k, A_ssm_k, A_ssm_D)
    x = x + _B_ref(_layernorm(x, axis=-1), B_mem0, B_Wq, B_Wk, B_Wv, B_Wo, B_seg_len)
    return x
```

```cpp
#include <hip/hip_runtime.h>
#include <hip/hip_bf16.h>
#include <stdint.h>

typedef unsigned short u16;
using bf16x8 = __attribute__((ext_vector_type(8))) short;
using s16x4 = __attribute__((ext_vector_type(4))) short;
using f32x4 = __attribute__((ext_vector_type(4))) float;
using f32x2 = __attribute__((ext_vector_type(2))) float;
using f32x16 = __attribute__((ext_vector_type(16))) float;
using bfx2 = __attribute__((ext_vector_type(2))) __bf16;
typedef unsigned v4u __attribute__((ext_vector_type(4)));
typedef unsigned v2u __attribute__((ext_vector_type(2)));

#define S_LEN 2048
#define DM 576
#define NH 32
#define DH 128
#define HD (NH * DH)
#define SEG 32
#define NSEG 64
#define RGB 9216u
#define SEGB (4u * RGB)

__device__ __forceinline__ unsigned pk2(float a, float b) { f32x2 v = {a, b}; bfx2 r = __builtin_convertvector(v, bfx2); return __builtin_bit_cast(unsigned, r); }
__device__ __forceinline__ float lo2f(unsigned u) { return __uint_as_float(u << 16); }
__device__ __forceinline__ float hi2f(unsigned u) { return __uint_as_float(u & 0xFFFF0000u); }
__device__ __forceinline__ unsigned nosent(unsigned u) { return u < 0xFFFFFFFEu ? u : 0xFFFFFFFEu; }
__device__ __forceinline__ bf16x8 asb8(v4u v) { return __builtin_bit_cast(bf16x8, v); }
#define MFMA32(a, b, c) __builtin_amdgcn_mfma_f32_16x16x32_bf16(asb8(a), asb8(b), c, 0, 0, 0)

typedef __amdgpu_buffer_rsrc_t rsrc_t;
__device__ __forceinline__ rsrc_t mkr(const void* p, unsigned bytes) { return __builtin_amdgcn_make_buffer_rsrc((void*)p, 0, bytes, 0x00020000); }
__device__ __forceinline__ void st16(rsrc_t r, unsigned vo, unsigned so, v4u v) {
  asm volatile("" : "+v"(v));
  __builtin_amdgcn_raw_buffer_store_b128(v, r, vo, so, 16);
  asm volatile("s_nop 1" :: "v"(v));
}
__device__ __forceinline__ void st4(rsrc_t r, unsigned vo, unsigned so, unsigned v) { __builtin_amdgcn_raw_buffer_store_b32(v, r, vo, so, 16); }
__device__ __forceinline__ v4u ld16(rsrc_t r, unsigned vo, unsigned so) { return __builtin_amdgcn_raw_buffer_load_b128(r, vo, so, 16); }
__device__ __forceinline__ unsigned ld4(rsrc_t r, unsigned vo, unsigned so) { return __builtin_amdgcn_raw_buffer_load_b32(r, vo, so, 16); }

#define WIN_BYTES (2u << 20)
#define CH 16384u
#define NREP 4
#define MEM_CH 0
#define PM_CH 32
#define PT_CH 64
#define WIN_USED_CH 88
#define CTL_CH 96
#define CAL_CH 127
#define CT_TICK 0
#define CT_TMO 256
#define CT_FLIP 352
#define CT_YC 384
#define CT_TOTAL 640
#define YS 4352u
#define SENT 0xFFFFFFFFu
#define SPIN_CAP 400000u

struct MP {
  const u16* WP; const u16* BP; const u16* x0; const u16* kvT; const u16* ssm_k; const u16* ssm_D; const u16* mem0;
  u16* out; unsigned* win; unsigned* ct; u16* Y; u16* H; u16* OT;
};

#define BAR() do { asm volatile("s_waitcnt lgkmcnt(0)\n\ts_barrier" ::: "memory"); } while (0)
#define VM0() __builtin_amdgcn_s_waitcnt(0x0F70)
#define PIN() __builtin_amdgcn_sched_barrier(0)
__device__ __forceinline__ unsigned umax3(unsigned a, unsigned b, unsigned c) { return __builtin_elementwise_max(__builtin_elementwise_max(a, b), c); }
__device__ __forceinline__ unsigned mx4(unsigned m, v4u v) { return umax3(umax3(m, v.x, v.y), v.z, v.w); }
__device__ __forceinline__ unsigned phys(unsigned o) { return (o >> 13) * CH + (o & 8191u); }
#define AOWN() asm volatile("" ::: "a0", "a1", "a2", "a3", "a4", "a5", "a6", "a7", "a8", "a9", "a10", "a11", "a12", "a13", "a14", "a15", "a16", "a17", "a18", "a19", "a20", "a21", "a22", "a23", "a24", "a25", "a26", "a27", "a28", "a29", "a30", "a31", "a32", "a33", "a34", "a35", "a36", "a37", "a38", "a39", "a40", "a41", "a42", "a43", "a44", "a45", "a46", "a47", "a48", "a49", "a50", "a51", "a52", "a53", "a54", "a55", "a56", "a57", "a58", "a59", "a60", "a61", "a62", "a63", "a64", "a65", "a66", "a67", "a68", "a69", "a70", "a71", "a72", "a73", "a74", "a75", "a76", "a77", "a78", "a79", "a80", "a81", "a82", "a83", "a84", "a85", "a86", "a87", "a88", "a89", "a90", "a91", "a92", "a93", "a94", "a95", "a96", "a97", "a98", "a99", "a100", "a101", "a102", "a103", "a104", "a105", "a106", "a107", "a108", "a109", "a110", "a111", "a112", "a113", "a114", "a115", "a116", "a117", "a118", "a119", "a120", "a121", "a122", "a123", "a124", "a125", "a126", "a127", "a128", "a129", "a130", "a131", "a132", "a133", "a134", "a135", "a136", "a137", "a138", "a139", "a140", "a141", "a142", "a143", "a144", "a145", "a146", "a147", "a148", "a149", "a150", "a151", "a152", "a153", "a154", "a155", "a156", "a157", "a158", "a159", "a160", "a161", "a162", "a163", "a164", "a165", "a166", "a167", "a168", "a169", "a170", "a171", "a172", "a173", "a174", "a175", "a176", "a177", "a178", "a179", "a180", "a181", "a182", "a183", "a184", "a185", "a186", "a187", "a188", "a189", "a190", "a191", "a192", "a193", "a194", "a195", "a196", "a197", "a198", "a199", "a200", "a201", "a202", "a203", "a204", "a205", "a206", "a207", "a208", "a209", "a210", "a211", "a212", "a213", "a214", "a215", "a216", "a217", "a218", "a219", "a220", "a221", "a222", "a223", "a224", "a225", "a226", "a227", "a228", "a229", "a230", "a231", "a232", "a233", "a234", "a235", "a236", "a237", "a238", "a239", "a240", "a241", "a242", "a243", "a244", "a245", "a246", "a247", "a248", "a249", "a250", "a251", "a252", "a253", "a254", "a255")
template <int N> struct IC { static constexpr int v = N; };
template <int B, int E, typename F> __device__ __forceinline__ void sfor(F&& f) { if constexpr (B < E) { f(IC<B>{}); sfor<B + 1, E>(f); } }

__device__ __forceinline__ bool spin_fail(unsigned& spins, rsrc_t F) {
  if (__builtin_expect((++spins & 1023u) != 0u, 1)) return false;
  if (spins > SPIN_CAP) st4(F, CT_TMO * 4u, 0, 1u);
  VM0();
  return (__builtin_amdgcn_readfirstlane(ld4(F, CT_TMO * 4u, 0)) != 0) | (spins > SPIN_CAP);
}

#define RED8(name, ins) __device__ __forceinline__ float name(float a) { float d, t; asm volatile("s_nop 1\n\t" ins "_dpp %0, %2, %2 row_ror:8 row_mask:0xf bank_mask:0xf\n\t" \
    "v_mov_b32 %1, %0\n\ts_nop 1\n\tv_permlane16_swap_b32 %0, %1\n\t" ins " %0, %0, %1\n\tv_mov_b32 %1, %0\n\ts_nop 1\n\tv_permlane32_swap_b32 %0, %1\n\t" ins " %0, %0, %1" \
    : "=&v"(d), "=&v"(t) : "v"(a)); return d; }
RED8(i_red8add, "v_add_f32")

__device__ __forceinline__ void wait_time(const rsrc_t X, unsigned o, int c) {
  unsigned v = 0;
#pragma nounroll
  for (int i = 0; i < 2000; ++i) {
    asm volatile("" ::: "memory");
    v = __builtin_amdgcn_readfirstlane(ld4(X, o, 0));
    if (v) break;
    __builtin_amdgcn_s_sleep(12);
  }
  v += (unsigned)c;
#pragma nounroll
  while ((unsigned)__builtin_amdgcn_s_memrealtime() - v > 0xFFFFF000u) __builtin_amdgcn_s_sleep(1);
}
__device__ __forceinline__ v4u mkd(const void* p, unsigned bytes) { return (v4u){(unsigned)(uintptr_t)p, (unsigned)((uintptr_t)p >> 32) & 0xFFFFu, bytes, 0x00020000u}; }
typedef __attribute__((address_space(3))) char* lds_ptr;
#define DROP 0x40000000u

#define L_QI 36864
#define L_KT 102400
#define LV_MEM 36864
#define LV_RM 49152
#define LV_RT 53248
#define L_ROLE 137728
#define L_TOTAL 137792
__device__ __forceinline__ v2u trd(const char* a) { return __builtin_bit_cast(v2u, __builtin_amdgcn_ds_read_tr16_b64_v4i16((__attribute__((address_space(3))) s16x4*)a)); }
template <int A0>
__device__ __forceinline__ void ld_w(const char* sb, unsigned vo) {
  asm volatile("global_load_dwordx4 a[%c2:%c3], %0, %1\n\tglobal_load_dwordx4 a[%c4:%c5], %0, %1 offset:1024" :: "v"(vo), "s"(sb), "i"(A0), "i"(A0 + 3), "i"(A0 + 4), "i"(A0 + 7) : "memory");
}
template <int S0, int S1>
__device__ __forceinline__ void load_w(const u16* base) {
  unsigned vo = (threadIdx.x >> 6) * (4 * 18 * 1024) + (threadIdx.x & 63) * 16;
  asm volatile("" : "+v"(vo));
  AOWN();
  sfor<S0, S1>([&](auto S) {
    const char* Ws = (const char*)base + 2048 * S.v;
    sfor<0, 4>([&](auto K) { ld_w<(S.v * 4 + K.v) * 8>(Ws + K.v * 18 * 1024, vo); });
  });
}

#define QK_ASM \
  "s_nop 4\n" \
  "s_cmp_eq_u32 %[grp], 0\n" \
  "s_cbranch_scc1 .Lqk_nw\n" \
  "v_add_u32 v229, 16384, %[wo]\n" \
  "global_load_dwordx4 v[32:35], v229, %[wp] offset:0\n" \
  "global_load_dwordx4 v[36:39], v229, %[wp] offset:1024\n" \
  "v_add_u32 v229, 18432, v229\n" \
  "global_load_dwordx4 v[40:43], v229, %[wp] offset:0\n" \
  "global_load_dwordx4 v[44:47], v229, %[wp] offset:1024\n" \
  "v_add_u32 v229, 18432, v229\n" \
  "global_load_dwordx4 v[48:51], v229, %[wp] offset:0\n" \
  "global_load_dwordx4 v[52:55], v229, %[wp] offset:1024\n" \
  "v_add_u32 v229, 18432, v229\n" \
  "global_load_dwordx4 v[56:59], v229, %[wp] offset:0\n" \
  "global_load_dwordx4 v[60:63], v229, %[wp] offset:1024\n" \
  ".Lqk_nw:\n" \
  "s_mov_b32 s60, -2\n" \
  "s_mov_b32 s70, 0x3db504f3\n" \
  "s_mov_b32 s71, 0x3e0293ee\n" \
  "v_add_u32 v228, 0x1000, %[mo]\n" \
  "v_mov_b32 v230, 0x7fffffff\n" \
  "v_mov_b32 v232, 0xff800000\n" \
  "s_mov_b32 s78, 0\n" \
  "s_mov_b32 s79, 0\n" \
  "s_mov_b32 s80, 0\n" \
  "s_mov_b32 s65, 2.0\n" \
  "s_mov_b32 s81, -1\n" \
  "s_mov_b32 s87, 2.0\n" \
  "s_mov_b32 s93, 0\n" \
  "s_mov_b32 s94, 0\n" \
  "s_lshl_b32 s96, s60, 4\n" \
  "s_add_i32 s96, s96, %[bas]\n" \
  "v_cmp_lt_i32_e64 vcc, %[lim], 0\n" \
  "s_nop 1\n" \
  "v_cndmask_b32 v212, 0, v232, vcc\n" \
  "v_cmp_lt_i32_e64 vcc, %[lim], 1\n" \
  "s_nop 1\n" \
  "v_cndmask_b32 v213, 0, v232, vcc\n" \
  "v_cmp_lt_i32_e64 vcc, %[lim], 2\n" \
  "s_nop 1\n" \
  "v_cndmask_b32 v214, 0, v232, vcc\n" \
  "v_cmp_lt_i32_e64 vcc, %[lim], 3\n" \
  "s_nop 1\n" \
  "v_cndmask_b32 v215, 0, v232, vcc\n" \
  "v_cmp_lt_i32_e64 vcc, %[lim], 16\n" \
  "s_nop 1\n" \
  "v_cndmask_b32 v216, 0, v232, vcc\n" \
  "v_cmp_lt_i32_e64 vcc, %[lim], 17\n" \
  "s_nop 1\n" \
  "v_cndmask_b32 v217, 0, v232, vcc\n" \
  "v_cmp_lt_i32_e64 vcc, %[lim], 18\n" \
  "s_nop 1\n" \
  "v_cndmask_b32 v218, 0, v232, vcc\n" \
  "v_cmp_lt_i32_e64 vcc, %[lim], 19\n" \
  "s_nop 1\n" \
  "v_cndmask_b32 v219, 0, v232, vcc\n" \
  "s_mov_b32 s90, 0\n" \
  "s_waitcnt vmcnt(0)\n" \
  ".Lqk_top:\n" \
  "v_mfma_f32_16x16x32_bf16 v[116:119], v[164:167], a[32:35], v[116:119]\n" \
  "ds_read_b128 v[148:151], %[hA] offset:2048\n" \
  "buffer_store_dwordx2 v[232:233], %[po3], %[X], s65 offen sc1\n" \
  "v_mfma_f32_16x16x32_bf16 v[120:123], v[164:167], a[40:43], v[120:123]\n" \
  "ds_read_b128 v[152:155], %[hA] offset:20480\n" \
  "v_mfma_f32_16x16x32_bf16 v[124:127], a[48:51], v[164:167], v[124:127]\n" \
  "buffer_load_dword v251, %[cpo], %[X], s94 offen sc1\n" \
  "s_xor_b32 s76, s96, 0x2000\n" \
  "v_mfma_f32_16x16x32_bf16 v[128:131], a[56:59], v[164:167], v[128:131]\n" \
  "buffer_load_dword v255, %[cpa], %[X], s76 offen sc1\n" \
  "v_mfma_f32_16x16x32_bf16 v[132:135], v[168:171], a[32:35], v[132:135]\n" \
  "s_add_i32 s67, s60, 2\n" \
  "s_min_i32 s67, s67, 63\n" \
  "s_mul_i32 s61, s67, 36864\n" \
  "v_mfma_f32_16x16x32_bf16 v[136:139], v[168:171], a[40:43], v[136:139]\n" \
  "s_lshl_b32 s67, s60, 16\n" \
  "s_and_b32 s67, s67, 0x10000\n" \
  "s_add_i32 s62, %[hl0], s67\n" \
  "v_mfma_f32_16x16x32_bf16 v[140:143], a[48:51], v[168:171], v[140:143]\n" \
  "s_add_i32 m0, s62, 0\n" \
  "v_mfma_f32_16x16x32_bf16 v[144:147], a[56:59], v[168:171], v[144:147]\n" \
  "s_add_i32 s68, s60, 1\n" \
  "s_lshl_b32 s73, s68, 17\n" \
  "s_and_b32 s73, s73, 0x60000\n" \
  "v_mfma_f32_16x16x32_bf16 v[116:119], v[172:175], a[36:39], v[116:119]\n" \
  "ds_read_b128 v[156:159], %[hA] offset:2560\n" \
  "buffer_load_dword v250, off, %[X], s96 offset:16 sc1\n" \
  "v_mfma_f32_16x16x32_bf16 v[120:123], v[172:175], a[44:47], v[120:123]\n" \
  "ds_read_b128 v[160:163], %[hA] offset:20992\n" \
  "v_mov_b64 v[100:101], 0\n" \
  "v_mfma_f32_16x16x32_bf16 v[124:127], a[52:55], v[172:175], v[124:127]\n" \
  "buffer_load_dwordx4 %[hvo], %[HS], s61 offen offset:0 sc1 lds\n" \
  "v_mov_b64 v[102:103], 0\n" \
  "v_mfma_f32_16x16x32_bf16 v[128:131], a[60:63], v[172:175], v[128:131]\n" \
  "s_add_i32 s63, s87, s73\n" \
  "s_add_i32 s64, s63, 0x4000\n" \
  "s_lshl_b32 s73, s68, 18\n" \
  "v_mfma_f32_16x16x32_bf16 v[132:135], v[176:179], a[36:39], v[132:135]\n" \
  "s_and_b32 s73, s73, 0x40000\n" \
  "s_add_i32 s65, %[pm0], s73\n" \
  "s_lshl_b32 s73, s68, 30\n" \
  "v_mfma_f32_16x16x32_bf16 v[136:139], v[176:179], a[44:47], v[136:139]\n" \
  "s_and_b32 s66, s73, 0x80000000\n" \
  "s_cmp_lt_u32 s68, 64\n" \
  "s_cselect_b32 s65, s65, 2.0\n" \
  "v_mfma_f32_16x16x32_bf16 v[140:143], a[52:55], v[176:179], v[140:143]\n" \
  "buffer_load_dwordx4 %[hvo], %[HS], s61 offen offset:1024 sc1 lds\n" \
  "v_mov_b64 v[104:105], 0\n" \
  "v_mfma_f32_16x16x32_bf16 v[144:147], a[60:63], v[176:179], v[144:147]\n" \
  "v_mov_b64 v[106:107], 0\n" \
  "s_waitcnt lgkmcnt(2)\n" \
  "v_mfma_f32_16x16x32_bf16 v[116:119], v[148:151], a[64:67], v[116:119]\n" \
  "ds_read_b128 v[164:167], %[hA] offset:3072\n" \
  "v_mov_b64 v[108:109], 0\n" \
  "v_mfma_f32_16x16x32_bf16 v[120:123], v[148:151], a[72:75], v[120:123]\n" \
  "ds_read_b128 v[168:171], %[hA] offset:21504\n" \
  "v_mov_b64 v[110:111], 0\n" \
  "v_mfma_f32_16x16x32_bf16 v[124:127], a[80:83], v[148:151], v[124:127]\n" \
  "buffer_load_dwordx4 %[hvo], %[HS], s61 offen offset:2048 sc1 lds\n" \
  "v_mov_b64 v[112:113], 0\n" \
  "v_mfma_f32_16x16x32_bf16 v[128:131], a[88:91], v[148:151], v[128:131]\n" \
  "s_ashr_i32 s67, s66, 31\n" \
  "v_not_b32 v232, s67\n" \
  "v_mov_b32 v233, s90\n" \
  "v_mfma_f32_16x16x32_bf16 v[132:135], v[152:155], a[64:67], v[132:135]\n" \
  "v_mov_b64 v[114:115], 0\n" \
  "v_mfma_f32_16x16x32_bf16 v[136:139], v[152:155], a[72:75], v[136:139]\n" \
  "v_mfma_f32_16x16x32_bf16 v[140:143], a[80:83], v[152:155], v[140:143]\n" \
  "buffer_load_dwordx4 %[hvo], %[HS], s61 offen offset:3072 sc1 lds\n" \
  "v_mfma_f32_16x16x32_bf16 v[144:147], a[88:91], v[152:155], v[144:147]\n" \
  "s_add_i32 s61, s61, 0x1000\n" \
  "s_add_i32 m0, s62, 4096\n" \
  "s_waitcnt lgkmcnt(2)\n" \
  "v_mfma_f32_16x16x32_bf16 v[116:119], v[156:159], a[68:71], v[116:119]\n" \
  "ds_read_b128 v[172:175], %[hA] offset:3584\n" \
  "v_mfma_f32_16x16x32_bf16 v[120:123], v[156:159], a[76:79], v[120:123]\n" \
  "ds_read_b128 v[176:179], %[hA] offset:22016\n" \
  "v_mfma_f32_16x16x32_bf16 v[124:127], a[84:87], v[156:159], v[124:127]\n" \
  "buffer_load_dwordx4 %[hvo], %[HS], s61 offen offset:0 sc1 lds\n" \
  "v_mfma_f32_16x16x32_bf16 v[128:131], a[92:95], v[156:159], v[128:131]\n" \
  "v_mfma_f32_16x16x32_bf16 v[132:135], v[160:163], a[68:71], v[132:135]\n" \
  "v_mfma_f32_16x16x32_bf16 v[136:139], v[160:163], a[76:79], v[136:139]\n" \
  "v_mfma_f32_16x16x32_bf16 v[140:143], a[84:87], v[160:163], v[140:143]\n" \
  "buffer_load_dwordx4 %[hvo], %[HS], s61 offen offset:1024 sc1 lds\n" \
  "v_mfma_f32_16x16x32_bf16 v[144:147], a[92:95], v[160:163], v[144:147]\n" \
  "s_waitcnt lgkmcnt(2)\n" \
  "v_mfma_f32_16x16x32_bf16 v[116:119], v[164:167], a[96:99], v[116:119]\n" \
  "ds_read_b128 v[148:151], %[hA] offset:4096\n" \
  "v_mfma_f32_16x16x32_bf16 v[120:123], v[164:167], a[104:107], v[120:123]\n" \
  "ds_read_b128 v[152:155], %[hA] offset:22528\n" \
  "v_mfma_f32_16x16x32_bf16 v[124:127], a[112:115], v[164:167], v[124:127]\n" \
  "buffer_load_dwordx4 %[hvo], %[HS], s61 offen offset:2048 sc1 lds\n" \
  "v_mfma_f32_16x16x32_bf16 v[128:131], a[120:123], v[164:167], v[128:131]\n" \
  "v_mfma_f32_16x16x32_bf16 v[132:135], v[168:171], a[96:99], v[132:135]\n" \
  "v_mfma_f32_16x16x32_bf16 v[136:139], v[168:171], a[104:107], v[136:139]\n" \
  "v_mfma_f32_16x16x32_bf16 v[140:143], a[112:115], v[168:171], v[140:143]\n" \
  "buffer_load_dwordx4 %[hvo], %[HS], s61 offen offset:3072 sc1 lds\n" \
  "v_mfma_f32_16x16x32_bf16 v[144:147], a[120:123], v[168:171], v[144:147]\n" \
  "s_add_i32 s61, s61, 0x1000\n" \
  "s_add_i32 m0, s62, 8192\n" \
  "s_waitcnt lgkmcnt(2)\n" \
  "v_mfma_f32_16x16x32_bf16 v[116:119], v[172:175], a[100:103], v[116:119]\n" \
  "ds_read_b128 v[156:159], %[hA] offset:4608\n" \
  "v_mfma_f32_16x16x32_bf16 v[120:123], v[172:175], a[108:111], v[120:123]\n" \
  "ds_read_b128 v[160:163], %[hA] offset:23040\n" \
  "v_mfma_f32_16x16x32_bf16 v[124:127], a[116:119], v[172:175], v[124:127]\n" \
  "buffer_load_dwordx4 %[hvo], %[HS], s61 offen offset:0 sc1 lds\n" \
  "v_mfma_f32_16x16x32_bf16 v[128:131], a[124:127], v[172:175], v[128:131]\n" \
  "v_mfma_f32_16x16x32_bf16 v[132:135], v[176:179], a[100:103], v[132:135]\n" \
  "v_mfma_f32_16x16x32_bf16 v[136:139], v[176:179], a[108:111], v[136:139]\n" \
  "buffer_store_dword v232, %[pcl], %[X], s65 offen sc1\n" \
  "v_mfma_f32_16x16x32_bf16 v[140:143], a[116:119], v[176:179], v[140:143]\n" \
  "v_mfma_f32_16x16x32_bf16 v[144:147], a[124:127], v[176:179], v[144:147]\n" \
  "buffer_atomic_umax v233, %[rpo], %[X], s93 offen sc1\n" \
  "s_waitcnt lgkmcnt(2)\n" \
  "v_mfma_f32_16x16x32_bf16 v[116:119], v[148:151], a[128:131], v[116:119]\n" \
  "ds_read_b128 v[164:167], %[hA] offset:5120\n" \
  "v_mfma_f32_16x16x32_bf16 v[120:123], v[148:151], a[136:139], v[120:123]\n" \
  "ds_read_b128 v[168:171], %[hA] offset:23552\n" \
  "v_mfma_f32_16x16x32_bf16 v[124:127], a[144:147], v[148:151], v[124:127]\n" \
  "v_mfma_f32_16x16x32_bf16 v[128:131], a[152:155], v[148:151], v[128:131]\n" \
  "v_mfma_f32_16x16x32_bf16 v[132:135], v[152:155], a[128:131], v[132:135]\n" \
  "v_mfma_f32_16x16x32_bf16 v[136:139], v[152:155], a[136:139], v[136:139]\n" \
  "v_mfma_f32_16x16x32_bf16 v[140:143], a[144:147], v[152:155], v[140:143]\n" \
  "v_mfma_f32_16x16x32_bf16 v[144:147], a[152:155], v[152:155], v[144:147]\n" \
  "s_waitcnt lgkmcnt(2)\n" \
  "v_mfma_f32_16x16x32_bf16 v[116:119], v[156:159], a[132:135], v[116:119]\n" \
  "ds_read_b128 v[172:175], %[hA] offset:5632\n" \
  "v_mfma_f32_16x16x32_bf16 v[120:123], v[156:159], a[140:143], v[120:123]\n" \
  "ds_read_b128 v[176:179], %[hA] offset:24064\n" \
  "v_mfma_f32_16x16x32_bf16 v[124:127], a[148:151], v[156:159], v[124:127]\n" \
  "v_mfma_f32_16x16x32_bf16 v[128:131], a[156:159], v[156:159], v[128:131]\n" \
  "v_mfma_f32_16x16x32_bf16 v[132:135], v[160:163], a[132:135], v[132:135]\n" \
  "s_waitcnt vmcnt(11)\n" \
  "v_readfirstlane_b32 s72, v250\n" \
  "v_mfma_f32_16x16x32_bf16 v[136:139], v[160:163], a[140:143], v[136:139]\n" \
  "v_max_u32_dpp v251, v251, v251 quad_perm:[1,0,3,2] row_mask:0xf bank_mask:0xf\n" \
  "v_mfma_f32_16x16x32_bf16 v[140:143], a[148:151], v[160:163], v[140:143]\n" \
  "v_mfma_f32_16x16x32_bf16 v[144:147], a[156:159], v[160:163], v[144:147]\n" \
  "v_max_u32_dpp v251, v251, v251 quad_perm:[2,3,0,1] row_mask:0xf bank_mask:0xf\n" \
  "s_waitcnt lgkmcnt(2)\n" \
  "v_mfma_f32_16x16x32_bf16 v[116:119], v[164:167], a[160:163], v[116:119]\n" \
  "ds_read_b128 v[148:151], %[hA] offset:6144\n" \
  "v_mfma_f32_16x16x32_bf16 v[120:123], v[164:167], a[168:171], v[120:123]\n" \
  "ds_read_b128 v[152:155], %[hA] offset:24576\n" \
  "v_max_u32_dpp v251, v251, v251 row_half_mirror row_mask:0xf bank_mask:0xf\n" \
  "v_mfma_f32_16x16x32_bf16 v[124:127], a[176:179], v[164:167], v[124:127]\n" \
  "v_mfma_f32_16x16x32_bf16 v[128:131], a[184:187], v[164:167], v[128:131]\n" \
  "v_readfirstlane_b32 s92, v251\n" \
  "v_readfirstlane_b32 s73, v255\n" \
  "v_mfma_f32_16x16x32_bf16 v[132:135], v[168:171], a[160:163], v[132:135]\n" \
  "s_sub_i32 s94, s95, %[dn]\n" \
  "s_max_i32 s92, s92, s94\n" \
  "s_add_i32 s94, s95, %[up]\n" \
  "v_mfma_f32_16x16x32_bf16 v[136:139], v[168:171], a[168:171], v[136:139]\n" \
  "s_min_i32 s92, s92, s94\n" \
  "s_cmp_lt_i32 s60, 2\n" \
  "s_cselect_b32 s95, s95, s92\n" \
  "v_mfma_f32_16x16x32_bf16 v[140:143], a[176:179], v[168:171], v[140:143]\n" \
  "s_mul_i32 s76, s95, 209\n" \
  "s_lshr_b32 s76, s76, 8\n" \
  "s_add_i32 s76, s76, %[c2]\n" \
  "v_mfma_f32_16x16x32_bf16 v[144:147], a[184:187], v[168:171], v[144:147]\n" \
  "s_waitcnt lgkmcnt(2)\n" \
  "v_mfma_f32_16x16x32_bf16 v[116:119], v[172:175], a[164:167], v[116:119]\n" \
  "ds_read_b128 v[156:159], %[hA] offset:6656\n" \
  "v_mfma_f32_16x16x32_bf16 v[120:123], v[172:175], a[172:175], v[120:123]\n" \
  "ds_read_b128 v[160:163], %[hA] offset:25088\n" \
  "v_mfma_f32_16x16x32_bf16 v[124:127], a[180:183], v[172:175], v[124:127]\n" \
  "s_add_i32 s92, s95, %[mrg]\n" \
  "s_max_i32 s92, s92, s76\n" \
  "s_add_i32 s94, s72, s92\n" \
  "v_mfma_f32_16x16x32_bf16 v[128:131], a[188:191], v[172:175], v[128:131]\n" \
  "s_sub_i32 s76, s92, s76\n" \
  "s_lshr_b32 s76, s76, 1\n" \
  "s_add_i32 s76, s76, %[c4]\n" \
  "v_mfma_f32_16x16x32_bf16 v[132:135], v[176:179], a[164:167], v[132:135]\n" \
  "s_mul_i32 s92, s95, 135\n" \
  "s_lshr_b32 s92, s92, 8\n" \
  "s_add_i32 s76, s76, s92\n" \
  "v_mfma_f32_16x16x32_bf16 v[136:139], v[176:179], a[172:175], v[136:139]\n" \
  "s_add_i32 s92, s73, %[lag]\n" \
  "s_sub_i32 s92, s92, s94\n" \
  "s_max_i32 s92, s92, 0\n" \
  "v_mfma_f32_16x16x32_bf16 v[140:143], a[180:183], v[176:179], v[140:143]\n" \
  "s_cmp_lt_i32 s60, 59\n" \
  "s_cselect_b32 s77, %[gg], 0\n" \
  "v_mfma_f32_16x16x32_bf16 v[144:147], a[188:191], v[176:179], v[144:147]\n" \
  "s_waitcnt lgkmcnt(2)\n" \
  "v_mfma_f32_16x16x32_bf16 v[116:119], v[148:151], a[192:195], v[116:119]\n" \
  "ds_read_b128 v[164:167], %[hA] offset:7168\n" \
  "v_mfma_f32_16x16x32_bf16 v[120:123], v[148:151], a[200:203], v[120:123]\n" \
  "ds_read_b128 v[168:171], %[hA] offset:25600\n" \
  "v_mfma_f32_16x16x32_bf16 v[124:127], a[208:211], v[148:151], v[124:127]\n" \
  "s_cmp_lg_u32 s73, 0\n" \
  "s_cselect_b32 s92, s92, s77\n" \
  "s_add_i32 s86, s94, s92\n" \
  "v_mfma_f32_16x16x32_bf16 v[128:131], a[216:219], v[148:151], v[128:131]\n" \
  "s_add_i32 s76, s76, s86\n" \
  "v_mov_b32 v252, s86\n" \
  "v_mov_b32 v253, s76\n" \
  "v_mfma_f32_16x16x32_bf16 v[132:135], v[152:155], a[192:195], v[132:135]\n" \
  "s_cmp_lt_i32 s60, 0\n" \
  "s_cselect_b32 s73, 2.0, s96\n" \
  "v_mfma_f32_16x16x32_bf16 v[136:139], v[152:155], a[200:203], v[136:139]\n" \
  "v_mfma_f32_16x16x32_bf16 v[140:143], a[208:211], v[152:155], v[140:143]\n" \
  "v_mfma_f32_16x16x32_bf16 v[144:147], a[216:219], v[152:155], v[144:147]\n" \
  "s_waitcnt lgkmcnt(2)\n" \
  "v_mfma_f32_16x16x32_bf16 v[116:119], v[156:159], a[196:199], v[116:119]\n" \
  "ds_read_b128 v[172:175], %[hA] offset:7680\n" \
  "v_mfma_f32_16x16x32_bf16 v[120:123], v[156:159], a[204:207], v[120:123]\n" \
  "ds_read_b128 v[176:179], %[hA] offset:26112\n" \
  "v_mfma_f32_16x16x32_bf16 v[124:127], a[212:215], v[156:159], v[124:127]\n" \
  "ds_read_b128 v[148:151], %[hA] offset:8192\n" \
  "v_mfma_f32_16x16x32_bf16 v[128:131], a[220:223], v[156:159], v[128:131]\n" \
  "ds_read_b128 v[152:155], %[hA] offset:26624\n" \
  "v_mfma_f32_16x16x32_bf16 v[132:135], v[160:163], a[196:199], v[132:135]\n" \
  "v_mfma_f32_16x16x32_bf16 v[136:139], v[160:163], a[204:207], v[136:139]\n" \
  "v_mfma_f32_16x16x32_bf16 v[140:143], a[212:215], v[160:163], v[140:143]\n" \
  "v_mfma_f32_16x16x32_bf16 v[144:147], a[220:223], v[160:163], v[144:147]\n" \
  "s_waitcnt lgkmcnt(4)\n" \
  "v_mfma_f32_16x16x32_bf16 v[124:127], a[240:243], v[164:167], v[124:127]\n" \
  "ds_read_b128 v[156:159], %[hA] offset:8704\n" \
  "s_memrealtime s[74:75]\n" \
  "v_mfma_f32_16x16x32_bf16 v[128:131], a[248:251], v[164:167], v[128:131]\n" \
  "ds_read_b128 v[160:163], %[hA] offset:27136\n" \
  "v_mfma_f32_16x16x32_bf16 v[140:143], a[240:243], v[168:171], v[140:143]\n" \
  "v_mfma_f32_16x16x32_bf16 v[144:147], a[248:251], v[168:171], v[144:147]\n" \
  "s_waitcnt lgkmcnt(4)\n" \
  "v_mfma_f32_16x16x32_bf16 v[124:127], a[244:247], v[172:175], v[124:127]\n" \
  "s_waitcnt lgkmcnt(0)\n" \
  "s_sub_i32 s91, s74, s72\n" \
  "s_cmp_gt_u32 s91, 14\n" \
  "v_mfma_f32_16x16x32_bf16 v[128:131], a[252:255], v[172:175], v[128:131]\n" \
  "s_cbranch_scc1 .Lqk_late\n" \
  ".Lqk_al_ok:\n" \
  "v_mfma_f32_16x16x32_bf16 v[140:143], a[244:247], v[176:179], v[140:143]\n" \
  "buffer_load_dwordx4 v[64:67], %[mo], %[X], s63 offen offset:0 sc1\n" \
  "v_mfma_f32_16x16x32_bf16 v[144:147], a[252:255], v[176:179], v[144:147]\n" \
  "v_mfma_f32_16x16x32_bf16 v[124:127], v[48:51], v[148:151], v[124:127]\n" \
  "v_mfma_f32_16x16x32_bf16 v[128:131], v[56:59], v[148:151], v[128:131]\n" \
  "buffer_load_dwordx4 v[68:71], %[mo], %[X], s63 offen offset:1024 sc1\n" \
  "v_mfma_f32_16x16x32_bf16 v[140:143], v[48:51], v[152:155], v[140:143]\n" \
  "v_mfma_f32_16x16x32_bf16 v[144:147], v[56:59], v[152:155], v[144:147]\n" \
  "v_mfma_f32_16x16x32_bf16 v[124:127], v[52:55], v[156:159], v[124:127]\n" \
  "buffer_load_dwordx4 v[72:75], %[mo], %[X], s63 offen offset:2048 sc1\n" \
  "v_mfma_f32_16x16x32_bf16 v[128:131], v[60:63], v[156:159], v[128:131]\n" \
  "v_mfma_f32_16x16x32_bf16 v[140:143], v[52:55], v[160:163], v[140:143]\n" \
  "v_mfma_f32_16x16x32_bf16 v[144:147], v[60:63], v[160:163], v[144:147]\n" \
  "buffer_load_dwordx4 v[76:79], %[mo], %[X], s63 offen offset:3072 sc1\n" \
  "v_mfma_f32_16x16x32_bf16 v[116:119], v[164:167], a[224:227], v[116:119]\n" \
  "v_mfma_f32_16x16x32_bf16 v[120:123], v[164:167], a[232:235], v[120:123]\n" \
  "v_mfma_f32_16x16x32_bf16 v[132:135], v[168:171], a[224:227], v[132:135]\n" \
  "buffer_load_dwordx4 v[80:83], v228, %[X], s63 offen offset:0 sc1\n" \
  "v_cvt_pk_bf16_f32 v220, v124, v125\n" \
  "v_mfma_f32_16x16x32_bf16 v[136:139], v[168:171], a[232:235], v[136:139]\n" \
  "v_cvt_pk_bf16_f32 v221, v126, v127\n" \
  "v_cvt_pk_bf16_f32 v222, v128, v129\n" \
  "v_mfma_f32_16x16x32_bf16 v[116:119], v[172:175], a[228:231], v[116:119]\n" \
  "v_cvt_pk_bf16_f32 v223, v130, v131\n" \
  "v_add_f32 v231, v124, v140\n" \
  "v_mfma_f32_16x16x32_bf16 v[120:123], v[172:175], a[236:239], v[120:123]\n" \
  "buffer_load_dwordx4 v[84:87], v228, %[X], s63 offen offset:1024 sc1\n" \
  "v_cvt_pk_bf16_f32 v224, v140, v141\n" \
  "v_mfma_f32_16x16x32_bf16 v[132:135], v[176:179], a[228:231], v[132:135]\n" \
  "ds_write_b128 %[ktW], v[220:223] offset:0\n" \
  "v_mfma_f32_16x16x32_bf16 v[136:139], v[176:179], a[236:239], v[136:139]\n" \
  "v_cvt_pk_bf16_f32 v225, v142, v143\n" \
  "v_cvt_pk_bf16_f32 v226, v144, v145\n" \
  "v_mfma_f32_16x16x32_bf16 v[116:119], v[148:151], v[32:35], v[116:119]\n" \
  "buffer_load_dwordx4 v[88:91], v228, %[X], s63 offen offset:2048 sc1\n" \
  "v_cvt_pk_bf16_f32 v227, v146, v147\n" \
  "v_mfma_f32_16x16x32_bf16 v[120:123], v[148:151], v[40:43], v[120:123]\n" \
  "ds_write_b128 %[ktW], v[224:227] offset:4096\n" \
  "v_mfma_f32_16x16x32_bf16 v[132:135], v[152:155], v[32:35], v[132:135]\n" \
  "v_xor_b32 %[hA], 0x10000, %[hA]\n" \
  "v_mfma_f32_16x16x32_bf16 v[136:139], v[152:155], v[40:43], v[136:139]\n" \
  "buffer_load_dwordx4 v[92:95], v228, %[X], s63 offen offset:3072 sc1\n" \
  "v_mfma_f32_16x16x32_bf16 v[116:119], v[156:159], v[36:39], v[116:119]\n" \
  "v_mfma_f32_16x16x32_bf16 v[120:123], v[156:159], v[44:47], v[120:123]\n" \
  "s_waitcnt vmcnt(10)\n" \
  "s_waitcnt lgkmcnt(0)\n" \
  "s_barrier\n" \
  "v_mfma_f32_16x16x32_bf16 v[132:135], v[160:163], v[36:39], v[132:135]\n" \
  "buffer_load_dwordx4 v[96:99], %[mo], %[X], s64 offen offset:0 sc1\n" \
  "ds_read_b128 v[180:183], %[ktR] offset:0\n" \
  "v_mfma_f32_16x16x32_bf16 v[136:139], v[160:163], v[44:47], v[136:139]\n" \
  "ds_read_b128 v[184:187], %[ktR] offset:1024\n" \
  ".Lqk_m:\n" \
  "s_waitcnt vmcnt(8)\n" \
  "v_smfmac_f32_16x16x64_bf16 v[100:103], v[64:67], a[0:7], %[idx]\n" \
  "ds_read_b128 v[188:191], %[ktR] offset:2048\n" \
  "v_smfmac_f32_16x16x64_bf16 v[104:107], v[64:67], a[8:15], %[idx]\n" \
  "ds_read_b128 v[192:195], %[ktR] offset:3072\n" \
  "v_smfmac_f32_16x16x64_bf16 v[108:111], v[64:67], a[16:23], %[idx]\n" \
  "ds_read_b128 v[196:199], %[ktR] offset:4096\n" \
  "v_cvt_pk_bf16_f32 v220, v116, v117\n" \
  "v_smfmac_f32_16x16x64_bf16 v[112:115], v[64:67], a[24:31], %[idx]\n" \
  "ds_read_b128 v[200:203], %[ktR] offset:5120\n" \
  "s_waitcnt vmcnt(7)\n" \
  "v_smfmac_f32_16x16x64_bf16 v[100:103], v[68:71], a[32:39], %[idx]\n" \
  "ds_read_b128 v[204:207], %[ktR] offset:6144\n" \
  "v_cvt_pk_bf16_f32 v221, v118, v119\n" \
  "v_smfmac_f32_16x16x64_bf16 v[104:107], v[68:71], a[40:47], %[idx]\n" \
  "ds_read_b128 v[208:211], %[ktR] offset:7168\n" \
  "v_cvt_pk_bf16_f32 v222, v120, v121\n" \
  "v_smfmac_f32_16x16x64_bf16 v[108:111], v[68:71], a[48:55], %[idx]\n" \
  "ds_read_b128 v[164:167], %[hA] offset:1024\n" \
  "v_cvt_pk_bf16_f32 v223, v122, v123\n" \
  "v_smfmac_f32_16x16x64_bf16 v[112:115], v[68:71], a[56:63], %[idx]\n" \
  "ds_read_b128 v[168:171], %[hA] offset:19456\n" \
  "s_waitcnt vmcnt(6)\n" \
  "v_smfmac_f32_16x16x64_bf16 v[100:103], v[72:75], a[64:71], %[idx]\n" \
  "ds_read_b128 v[172:175], %[hA] offset:1536\n" \
  "v_cvt_pk_bf16_f32 v224, v132, v133\n" \
  "v_smfmac_f32_16x16x64_bf16 v[104:107], v[72:75], a[72:79], %[idx]\n" \
  "ds_read_b128 v[176:179], %[hA] offset:19968\n" \
  "v_cvt_pk_bf16_f32 v225, v134, v135\n" \
  "v_smfmac_f32_16x16x64_bf16 v[108:111], v[72:75], a[80:87], %[idx]\n" \
  "ds_read_b128 v[148:151], %[hA] offset:0\n" \
  "v_cvt_pk_bf16_f32 v226, v136, v137\n" \
  "v_smfmac_f32_16x16x64_bf16 v[112:115], v[72:75], a[88:95], %[idx]\n" \
  "ds_read_b128 v[152:155], %[hA] offset:18432\n" \
  "s_waitcnt vmcnt(5)\n" \
  "v_smfmac_f32_16x16x64_bf16 v[100:103], v[76:79], a[96:103], %[idx]\n" \
  "ds_read_b128 v[156:159], %[hA] offset:512\n" \
  "v_cvt_pk_bf16_f32 v227, v138, v139\n" \
  "v_smfmac_f32_16x16x64_bf16 v[104:107], v[76:79], a[104:111], %[idx]\n" \
  "ds_write_b64 %[qtW], v[220:221] offset:0\n" \
  "v_smfmac_f32_16x16x64_bf16 v[108:111], v[76:79], a[112:119], %[idx]\n" \
  "ds_write_b64 %[qtW], v[222:223] offset:2816\n" \
  "v_smfmac_f32_16x16x64_bf16 v[112:115], v[76:79], a[120:127], %[idx]\n" \
  "ds_read_b128 v[160:163], %[hA] offset:18944\n" \
  "s_waitcnt vmcnt(4)\n" \
  "v_smfmac_f32_16x16x64_bf16 v[100:103], v[80:83], a[128:135], %[idx]\n" \
  "ds_write_b64 %[qtW], v[224:225] offset:64\n" \
  "v_smfmac_f32_16x16x64_bf16 v[104:107], v[80:83], a[136:143], %[idx]\n" \
  "ds_write_b64 %[qtW], v[226:227] offset:2880\n" \
  "v_smfmac_f32_16x16x64_bf16 v[108:111], v[80:83], a[144:151], %[idx]\n" \
  "s_add_i32 s60, s60, 1\n" \
  "s_add_i32 s96, s96, 16\n" \
  "s_mov_b32 s94, s93\n" \
  "v_smfmac_f32_16x16x64_bf16 v[112:115], v[80:83], a[152:159], %[idx]\n" \
  "s_waitcnt vmcnt(3)\n" \
  "v_smfmac_f32_16x16x64_bf16 v[100:103], v[84:87], a[160:167], %[idx]\n" \
  "s_sub_i32 s90, s74, s89\n" \
  "s_max_i32 s92, s91, -2\n" \
  "s_add_i32 s89, s72, s92\n" \
  "v_smfmac_f32_16x16x64_bf16 v[104:107], v[84:87], a[168:175], %[idx]\n" \
  "s_ashr_i32 s92, s92, 1\n" \
  "s_add_i32 s90, s90, s92\n" \
  "v_smfmac_f32_16x16x64_bf16 v[108:111], v[84:87], a[176:183], %[idx]\n" \
  "s_mul_i32 s93, s60, 130\n" \
  "s_and_b32 s93, s93, 0xfc\n" \
  "v_smfmac_f32_16x16x64_bf16 v[112:115], v[84:87], a[184:191], %[idx]\n" \
  "s_waitcnt vmcnt(2)\n" \
  "v_smfmac_f32_16x16x64_bf16 v[100:103], v[88:91], a[192:199], %[idx]\n" \
  "v_smfmac_f32_16x16x64_bf16 v[104:107], v[88:91], a[200:207], %[idx]\n" \
  "v_smfmac_f32_16x16x64_bf16 v[108:111], v[88:91], a[208:215], %[idx]\n" \
  "v_smfmac_f32_16x16x64_bf16 v[112:115], v[88:91], a[216:223], %[idx]\n" \
  "s_waitcnt vmcnt(1)\n" \
  "v_smfmac_f32_16x16x64_bf16 v[100:103], v[92:95], a[224:231], %[idx]\n" \
  "v_smfmac_f32_16x16x64_bf16 v[104:107], v[92:95], a[232:239], %[idx]\n" \
  "v_smfmac_f32_16x16x64_bf16 v[108:111], v[92:95], a[240:247], %[idx]\n" \
  "v_smfmac_f32_16x16x64_bf16 v[112:115], v[92:95], a[248:255], %[idx]\n" \
  "s_waitcnt vmcnt(0)\n" \
  "v_smfmac_f32_16x16x64_bf16 v[100:103], v[96:99], v[32:39], %[idx]\n" \
  "v_smfmac_f32_16x16x64_bf16 v[104:107], v[96:99], v[40:47], %[idx]\n" \
  "s_nop 1\n" \
  "v_smfmac_f32_16x16x64_bf16 v[108:111], v[96:99], v[48:55], %[idx]\n" \
  "s_nop 1\n" \
  "v_smfmac_f32_16x16x64_bf16 v[112:115], v[96:99], v[56:63], %[idx]\n" \
  "s_waitcnt lgkmcnt(2)\n" \
  "s_nop 0\n" \
  "v_pk_add_f32 v[232:233], v[100:101], v[102:103]\n" \
  "v_pk_add_f32 v[234:235], v[104:105], v[106:107]\n" \
  "v_cvt_pk_bf16_f32 v236, v232, v233\n" \
  "v_cvt_pk_bf16_f32 v237, v234, v235\n" \
  "ds_write2st64_b32 %[qW], v236, v237 offset1:11\n" \
  "v_pk_add_f32 v[232:233], v[108:109], v[110:111]\n" \
  "v_pk_add_f32 v[234:235], v[112:113], v[114:115]\n" \
  "v_cvt_pk_bf16_f32 v236, v232, v233\n" \
  "v_cvt_pk_bf16_f32 v237, v234, v235\n" \
  "ds_write2st64_b32 %[kW], v236, v237 offset1:11\n" \
  "v_fma_f32 v232, v232, v233, v231\n" \
  "v_cmp_u_f32 vcc, v232, v232\n" \
  "v_mfma_f32_16x16x32_bf16 v[116:119], v[148:151], a[0:3], 0\n" \
  "v_mfma_f32_16x16x32_bf16 v[120:123], v[148:151], a[8:11], 0\n" \
  "s_cbranch_vccnz .Lqk_sm\n" \
  ".Lqk_sm_back:\n" \
  "s_waitcnt lgkmcnt(0)\n" \
  "s_barrier\n" \
  "ds_read_b64_tr_b16 v[64:65], %[qR] offset:0\n" \
  "ds_read_b64_tr_b16 v[66:67], %[qR] offset:2816\n" \
  "v_mfma_f32_16x16x32_bf16 v[124:127], a[16:19], v[148:151], 0\n" \
  "ds_read_b64_tr_b16 v[68:69], %[qR] offset:5632\n" \
  "ds_read_b64_tr_b16 v[70:71], %[qR] offset:8448\n" \
  "v_mfma_f32_16x16x32_bf16 v[128:131], a[24:27], v[148:151], 0\n" \
  "ds_read_b64_tr_b16 v[72:73], %[qR] offset:11264\n" \
  "ds_read_b64_tr_b16 v[74:75], %[qR] offset:14080\n" \
  "v_mfma_f32_16x16x32_bf16 v[132:135], v[152:155], a[0:3], 0\n" \
  "ds_read_b64_tr_b16 v[76:77], %[qR] offset:16896\n" \
  "ds_read_b64_tr_b16 v[78:79], %[qR] offset:19712\n" \
  "s_waitcnt lgkmcnt(6)\n" \
  "v_mfma_f32_16x16x32_bf16 v[100:103], v[180:183], v[64:67], v[212:215]\n" \
  "ds_read_b64_tr_b16 v[80:81], %[kR] offset:0\n" \
  "ds_read_b64_tr_b16 v[82:83], %[kR] offset:2816\n" \
  "s_waitcnt lgkmcnt(6)\n" \
  "v_mfma_f32_16x16x32_bf16 v[100:103], v[184:187], v[68:71], v[100:103]\n" \
  "ds_read_b64_tr_b16 v[84:85], %[kR] offset:5632\n" \
  "ds_read_b64_tr_b16 v[86:87], %[kR] offset:8448\n" \
  "s_waitcnt lgkmcnt(6)\n" \
  "v_mfma_f32_16x16x32_bf16 v[100:103], v[188:191], v[72:75], v[100:103]\n" \
  "ds_read_b64_tr_b16 v[88:89], %[kR] offset:11264\n" \
  "ds_read_b64_tr_b16 v[90:91], %[kR] offset:14080\n" \
  "s_waitcnt lgkmcnt(6)\n" \
  "v_mfma_f32_16x16x32_bf16 v[100:103], v[192:195], v[76:79], v[100:103]\n" \
  "ds_read_b64_tr_b16 v[92:93], %[kR] offset:16896\n" \
  "ds_read_b64_tr_b16 v[94:95], %[kR] offset:19712\n" \
  "v_mfma_f32_16x16x32_bf16 v[104:107], v[196:199], v[64:67], v[216:219]\n" \
  "buffer_store_dwordx2 v[252:253], %[cpw], %[X], s73 offen offset:32 sc1\n" \
  "v_mfma_f32_16x16x32_bf16 v[104:107], v[200:203], v[68:71], v[104:107]\n" \
  "v_mfma_f32_16x16x32_bf16 v[104:107], v[204:207], v[72:75], v[104:107]\n" \
  "v_mfma_f32_16x16x32_bf16 v[104:107], v[208:211], v[76:79], v[104:107]\n" \
  "s_waitcnt lgkmcnt(6)\n" \
  "v_mfma_f32_16x16x32_bf16 v[108:111], v[80:83], v[64:67], 0\n" \
  "s_waitcnt lgkmcnt(4)\n" \
  "v_mfma_f32_16x16x32_bf16 v[108:111], v[84:87], v[68:71], v[108:111]\n" \
  "v_max3_f32 v248, v100, v101, v102\n" \
  "s_waitcnt lgkmcnt(2)\n" \
  "v_mfma_f32_16x16x32_bf16 v[108:111], v[88:91], v[72:75], v[108:111]\n" \
  "s_waitcnt lgkmcnt(0)\n" \
  "v_mfma_f32_16x16x32_bf16 v[108:111], v[92:95], v[76:79], v[108:111]\n" \
  "v_max3_f32 v249, v103, v104, v105\n" \
  "v_max3_f32 v248, v248, v106, v107\n" \
  "v_mfma_f32_16x16x32_bf16 v[136:139], v[152:155], a[8:11], 0\n" \
  "s_nop 4\n" \
  "v_max3_f32 v249, v249, v108, v109\n" \
  "v_max_f32 v232, v248, v249\n" \
  "v_max_f32 v233, v248, v249\n" \
  "v_mfma_f32_16x16x32_bf16 v[140:143], a[16:19], v[152:155], 0\n" \
  "s_nop 0\n" \
  "v_permlane16_swap_b32 v232, v233\n" \
  "v_max_f32 v248, v232, v233\n" \
  "v_max_f32 v249, v232, v233\n" \
  "v_mfma_f32_16x16x32_bf16 v[144:147], a[24:27], v[152:155], 0\n" \
  "s_nop 0\n" \
  "v_permlane32_swap_b32 v248, v249\n" \
  "v_max_f32 v248, v248, v249\n" \
  "v_mul_f32_e64 v248, -s71, v248\n" \
  "v_pk_fma_f32 v[100:101], v[100:101], s[70:71], v[248:249] op_sel:[0,1,0] op_sel_hi:[1,1,0]\n" \
  "v_pk_fma_f32 v[102:103], v[102:103], s[70:71], v[248:249] op_sel:[0,1,0] op_sel_hi:[1,1,0]\n" \
  "v_pk_fma_f32 v[104:105], v[104:105], s[70:71], v[248:249] op_sel:[0,1,0] op_sel_hi:[1,1,0]\n" \
  "v_pk_fma_f32 v[106:107], v[106:107], s[70:71], v[248:249] op_sel:[0,1,0] op_sel_hi:[1,1,0]\n" \
  "v_pk_fma_f32 v[108:109], v[108:109], s[70:71], v[248:249] op_sel:[0,1,0] op_sel_hi:[1,1,0]\n" \
  "v_exp_f32 v100, v100\n" \
  "v_exp_f32 v101, v101\n" \
  "v_exp_f32 v102, v102\n" \
  "v_exp_f32 v103, v103\n" \
  "v_exp_f32 v104, v104\n" \
  "v_exp_f32 v105, v105\n" \
  "v_exp_f32 v106, v106\n" \
  "v_exp_f32 v107, v107\n" \
  "v_exp_f32 v108, v108\n" \
  "v_exp_f32 v109, v109\n" \
  "v_pk_add_f32 v[248:249], v[100:101], v[102:103]\n" \
  "v_pk_add_f32 v[232:233], v[104:105], v[106:107]\n" \
  "v_pk_add_f32 v[248:249], v[248:249], v[108:109]\n" \
  "v_pk_add_f32 v[248:249], v[248:249], v[232:233]\n" \
  "v_add_f32 v232, v248, v249\n" \
  "v_add_f32 v233, v248, v249\n" \
  "v_mfma_f32_16x16x32_bf16 v[116:119], v[156:159], a[4:7], v[116:119]\n" \
  "s_nop 0\n" \
  "v_permlane16_swap_b32 v232, v233\n" \
  "v_add_f32 v248, v232, v233\n" \
  "v_add_f32 v249, v232, v233\n" \
  "v_mfma_f32_16x16x32_bf16 v[120:123], v[156:159], a[12:15], v[120:123]\n" \
  "s_nop 0\n" \
  "v_permlane32_swap_b32 v248, v249\n" \
  "v_add_f32 v248, v248, v249\n" \
  "v_rcp_f32 v248, v248\n" \
  "s_cmp_lg_u32 s60, s81\n" \
  "v_bfi_b32 v248, v230, v248, s66\n" \
  "v_pk_mul_f32 v[100:101], v[248:249], v[100:101] op_sel_hi:[0,1]\n" \
  "v_pk_mul_f32 v[102:103], v[248:249], v[102:103] op_sel_hi:[0,1]\n" \
  "v_pk_mul_f32 v[104:105], v[248:249], v[104:105] op_sel_hi:[0,1]\n" \
  "v_pk_mul_f32 v[106:107], v[248:249], v[106:107] op_sel_hi:[0,1]\n" \
  "v_pk_mul_f32 v[108:109], v[248:249], v[108:109] op_sel_hi:[0,1]\n" \
  "v_cvt_pk_bf16_f32 v232, v100, v101\n" \
  "v_cvt_pk_bf16_f32 v233, v102, v103\n" \
  "v_cvt_pk_bf16_f32 v234, v104, v105\n" \
  "v_cvt_pk_bf16_f32 v235, v106, v107\n" \
  "v_cvt_pk_bf16_f32 v236, v108, v109\n" \
  "buffer_store_dwordx4 v[232:235], %[po1], %[X], s65 offen sc1\n" \
  "v_mfma_f32_16x16x32_bf16 v[124:127], a[20:23], v[156:159], v[124:127]\n" \
  "v_mfma_f32_16x16x32_bf16 v[128:131], a[28:31], v[156:159], v[128:131]\n" \
  "buffer_store_dword v236, %[po2], %[X], s65 offen sc1\n" \
  "v_mfma_f32_16x16x32_bf16 v[132:135], v[160:163], a[4:7], v[132:135]\n" \
  "v_mfma_f32_16x16x32_bf16 v[136:139], v[160:163], a[12:15], v[136:139]\n" \
  "v_mfma_f32_16x16x32_bf16 v[140:143], a[20:23], v[160:163], v[140:143]\n" \
  "v_mfma_f32_16x16x32_bf16 v[144:147], a[28:31], v[160:163], v[144:147]\n" \
  "s_cbranch_scc1 .Lqk_top\n" \
  "s_cmp_eq_u32 s60, -1\n" \
  "s_cbranch_scc1 .Lqk_entry\n" \
  "buffer_store_dwordx2 v[232:233], %[po3], %[X], s65 offen sc1\n" \
  "s_branch .Lqk_end\n" \
  ".Lqk_sm:\n" \
  "s_cmp_lt_i32 s60, 0\n" \
  "s_cbranch_scc1 .Lqk_sm_back\n" \
  "v_cmp_u_f32 vcc, v231, v231\n" \
  "s_cbranch_vccnz .Lqk_sh\n" \
  "s_mov_b32 s69, 0\n" \
  ".Lqk_smw:\n" \
  "buffer_load_dwordx4 v[64:67], %[mo], %[X], s63 offen offset:0 sc1\n" \
  "buffer_load_dwordx4 v[68:71], %[mo], %[X], s63 offen offset:1024 sc1\n" \
  "buffer_load_dwordx4 v[72:75], %[mo], %[X], s63 offen offset:2048 sc1\n" \
  "buffer_load_dwordx4 v[76:79], %[mo], %[X], s63 offen offset:3072 sc1\n" \
  "buffer_load_dwordx4 v[80:83], v228, %[X], s63 offen offset:0 sc1\n" \
  "buffer_load_dwordx4 v[84:87], v228, %[X], s63 offen offset:1024 sc1\n" \
  "buffer_load_dwordx4 v[88:91], v228, %[X], s63 offen offset:2048 sc1\n" \
  "buffer_load_dwordx4 v[92:95], v228, %[X], s63 offen offset:3072 sc1\n" \
  "buffer_load_dwordx4 v[96:99], %[mo], %[X], s64 offen offset:0 sc1\n" \
  "s_waitcnt vmcnt(0)\n" \
  "v_max3_u32 v229, v64, v65, v66\n" \
  "v_max3_u32 v229, v229, v67, v68\n" \
  "v_max3_u32 v229, v229, v69, v70\n" \
  "v_max3_u32 v229, v229, v71, v72\n" \
  "v_max3_u32 v229, v229, v73, v74\n" \
  "v_max3_u32 v229, v229, v75, v76\n" \
  "v_max3_u32 v229, v229, v77, v78\n" \
  "v_max3_u32 v229, v229, v79, v80\n" \
  "v_max3_u32 v229, v229, v81, v82\n" \
  "v_max3_u32 v229, v229, v83, v84\n" \
  "v_max3_u32 v229, v229, v85, v86\n" \
  "v_max3_u32 v229, v229, v87, v88\n" \
  "v_max3_u32 v229, v229, v89, v90\n" \
  "v_max3_u32 v229, v229, v91, v92\n" \
  "v_max3_u32 v229, v229, v93, v94\n" \
  "v_max3_u32 v229, v229, v95, v96\n" \
  "v_max3_u32 v229, v229, v97, v98\n" \
  "v_max_u32 v229, v229, v99\n" \
  "v_cmp_eq_u32 vcc, -1, v229\n" \
  "s_cbranch_vccz .Lqk_smw_ok\n" \
  "s_add_i32 s69, s69, 1\n" \
  "s_and_b32 s67, s69, 1023\n" \
  "s_cbranch_scc1 .Lqk_smw\n" \
  "buffer_load_dword v229, off, %[F], 0 offset:1024 sc1\n" \
  "s_waitcnt vmcnt(0)\n" \
  "v_readfirstlane_b32 s67, v229\n" \
  "s_cmp_lg_u32 s67, 0\n" \
  "s_cbranch_scc1 .Lqk_smw_ok\n" \
  "s_cmp_lt_u32 s69, 400000\n" \
  "s_cbranch_scc1 .Lqk_smw\n" \
  "v_mov_b32 v229, 1\n" \
  "buffer_store_dword v229, off, %[F], 0 offset:1024 sc1\n" \
  ".Lqk_smw_ok:\n" \
  "s_memrealtime s[76:77]\n" \
  "s_waitcnt lgkmcnt(0)\n" \
  "s_add_i32 s76, s76, s95\n" \
  "s_add_i32 s76, s76, %[sh1]\n" \
  "s_sub_i32 s76, s76, s86\n" \
  "s_max_i32 s76, s76, 0\n" \
  "s_add_i32 s86, s86, s76\n" \
  "v_add_u32 v252, s76, v252\n" \
  "v_add_u32 v253, s76, v253\n" \
  "v_mov_b64 v[100:101], 0\n" \
  "v_mov_b64 v[102:103], 0\n" \
  "v_mov_b64 v[104:105], 0\n" \
  "v_mov_b64 v[106:107], 0\n" \
  "v_mov_b64 v[108:109], 0\n" \
  "v_mov_b64 v[110:111], 0\n" \
  "v_mov_b64 v[112:113], 0\n" \
  "v_mov_b64 v[114:115], 0\n" \
  "s_nop 1\n" \
  "v_smfmac_f32_16x16x64_bf16 v[100:103], v[64:67], a[0:7], %[idx]\n" \
  "v_smfmac_f32_16x16x64_bf16 v[104:107], v[64:67], a[8:15], %[idx]\n" \
  "v_smfmac_f32_16x16x64_bf16 v[108:111], v[64:67], a[16:23], %[idx]\n" \
  "v_smfmac_f32_16x16x64_bf16 v[112:115], v[64:67], a[24:31], %[idx]\n" \
  "v_smfmac_f32_16x16x64_bf16 v[100:103], v[68:71], a[32:39], %[idx]\n" \
  "v_smfmac_f32_16x16x64_bf16 v[104:107], v[68:71], a[40:47], %[idx]\n" \
  "v_smfmac_f32_16x16x64_bf16 v[108:111], v[68:71], a[48:55], %[idx]\n" \
  "v_smfmac_f32_16x16x64_bf16 v[112:115], v[68:71], a[56:63], %[idx]\n" \
  "v_smfmac_f32_16x16x64_bf16 v[100:103], v[72:75], a[64:71], %[idx]\n" \
  "v_smfmac_f32_16x16x64_bf16 v[104:107], v[72:75], a[72:79], %[idx]\n" \
  "v_smfmac_f32_16x16x64_bf16 v[108:111], v[72:75], a[80:87], %[idx]\n" \
  "v_smfmac_f32_16x16x64_bf16 v[112:115], v[72:75], a[88:95], %[idx]\n" \
  "v_smfmac_f32_16x16x64_bf16 v[100:103], v[76:79], a[96:103], %[idx]\n" \
  "v_smfmac_f32_16x16x64_bf16 v[104:107], v[76:79], a[104:111], %[idx]\n" \
  "v_smfmac_f32_16x16x64_bf16 v[108:111], v[76:79], a[112:119], %[idx]\n" \
  "v_smfmac_f32_16x16x64_bf16 v[112:115], v[76:79], a[120:127], %[idx]\n" \
  "v_smfmac_f32_16x16x64_bf16 v[100:103], v[80:83], a[128:135], %[idx]\n" \
  "v_smfmac_f32_16x16x64_bf16 v[104:107], v[80:83], a[136:143], %[idx]\n" \
  "v_smfmac_f32_16x16x64_bf16 v[108:111], v[80:83], a[144:151], %[idx]\n" \
  "v_smfmac_f32_16x16x64_bf16 v[112:115], v[80:83], a[152:159], %[idx]\n" \
  "v_smfmac_f32_16x16x64_bf16 v[100:103], v[84:87], a[160:167], %[idx]\n" \
  "v_smfmac_f32_16x16x64_bf16 v[104:107], v[84:87], a[168:175], %[idx]\n" \
  "v_smfmac_f32_16x16x64_bf16 v[108:111], v[84:87], a[176:183], %[idx]\n" \
  "v_smfmac_f32_16x16x64_bf16 v[112:115], v[84:87], a[184:191], %[idx]\n" \
  "v_smfmac_f32_16x16x64_bf16 v[100:103], v[88:91], a[192:199], %[idx]\n" \
  "v_smfmac_f32_16x16x64_bf16 v[104:107], v[88:91], a[200:207], %[idx]\n" \
  "v_smfmac_f32_16x16x64_bf16 v[108:111], v[88:91], a[208:215], %[idx]\n" \
  "v_smfmac_f32_16x16x64_bf16 v[112:115], v[88:91], a[216:223], %[idx]\n" \
  "v_smfmac_f32_16x16x64_bf16 v[100:103], v[92:95], a[224:231], %[idx]\n" \
  "v_smfmac_f32_16x16x64_bf16 v[104:107], v[92:95], a[232:239], %[idx]\n" \
  "v_smfmac_f32_16x16x64_bf16 v[108:111], v[92:95], a[240:247], %[idx]\n" \
  "v_smfmac_f32_16x16x64_bf16 v[112:115], v[92:95], a[248:255], %[idx]\n" \
  "v_smfmac_f32_16x16x64_bf16 v[100:103], v[96:99], v[32:39], %[idx]\n" \
  "v_smfmac_f32_16x16x64_bf16 v[104:107], v[96:99], v[40:47], %[idx]\n" \
  "v_smfmac_f32_16x16x64_bf16 v[108:111], v[96:99], v[48:55], %[idx]\n" \
  "v_smfmac_f32_16x16x64_bf16 v[112:115], v[96:99], v[56:63], %[idx]\n" \
  "s_nop 7\n" \
  "v_pk_add_f32 v[232:233], v[100:101], v[102:103]\n" \
  "v_pk_add_f32 v[234:235], v[104:105], v[106:107]\n" \
  "v_cvt_pk_bf16_f32 v236, v232, v233\n" \
  "v_cvt_pk_bf16_f32 v237, v234, v235\n" \
  "ds_write2st64_b32 %[qW], v236, v237 offset1:11\n" \
  "v_pk_add_f32 v[232:233], v[108:109], v[110:111]\n" \
  "v_pk_add_f32 v[234:235], v[112:113], v[114:115]\n" \
  "v_cvt_pk_bf16_f32 v236, v232, v233\n" \
  "v_cvt_pk_bf16_f32 v237, v234, v235\n" \
  "ds_write2st64_b32 %[kW], v236, v237 offset1:11\n" \
  "s_branch .Lqk_sm_back\n" \
  ".Lqk_late:\n" \
  "s_cmp_gt_u32 s91, -3000\n" \
  "s_cbranch_scc0 .Lqk_late2\n" \
  ".Lqk_wt:\n" \
  "s_memrealtime s[76:77]\n" \
  "s_waitcnt lgkmcnt(0)\n" \
  "s_sub_i32 s76, s76, s72\n" \
  "s_cmp_lt_i32 s76, 0\n" \
  "s_cbranch_scc1 .Lqk_wt\n" \
  "s_branch .Lqk_al_ok\n" \
  ".Lqk_late2:\n" \
  "s_cmp_lt_i32 s60, -1\n" \
  "s_cbranch_scc1 .Lqk_al_ok\n" \
  "s_cmp_lg_u32 s72, 0\n" \
  "s_cbranch_scc1 .Lqk_late3\n" \
  "s_mov_b32 s69, 0\n" \
  ".Lqk_tw:\n" \
  "buffer_load_dword v229, off, %[X], s96 offset:16 sc1\n" \
  "s_waitcnt vmcnt(0)\n" \
  "v_readfirstlane_b32 s72, v229\n" \
  "s_cmp_lg_u32 s72, 0\n" \
  "s_cbranch_scc1 .Lqk_tw_ok\n" \
  "s_sleep 1\n" \
  "s_add_i32 s69, s69, 1\n" \
  "s_cmp_lt_u32 s69, 200\n" \
  "s_cbranch_scc1 .Lqk_tw\n" \
  ".Lqk_tw_ok:\n" \
  "s_sub_i32 s91, s74, s72\n" \
  "s_cmp_gt_u32 s91, -3000\n" \
  "s_cbranch_scc1 .Lqk_wt\n" \
  ".Lqk_late3:\n" \
  "s_memrealtime s[76:77]\n" \
  "s_waitcnt lgkmcnt(0)\n" \
  "s_add_i32 s76, s76, s95\n" \
  "s_add_i32 s76, s76, 90\n" \
  "s_sub_i32 s76, s76, s86\n" \
  "s_max_i32 s76, s76, 0\n" \
  "s_add_i32 s86, s86, s76\n" \
  "v_add_u32 v252, s76, v252\n" \
  "v_add_u32 v253, s76, v253\n" \
  "s_branch .Lqk_al_ok\n" \
  ".Lqk_sh:\n" \
  "s_mul_i32 s75, s60, 36864\n" \
  "s_add_i32 s76, s75, 0x1000\n" \
  "s_add_i32 s77, s75, 0x2000\n" \
  "s_add_i32 s60, s60, -1\n" \
  "s_add_i32 s96, s96, -16\n" \
  "s_mov_b32 s93, s94\n" \
  "v_xor_b32 %[hA], 0x10000, %[hA]\n" \
  "s_waitcnt vmcnt(0) lgkmcnt(0)\n" \
  "s_barrier\n" \
  "s_mov_b32 s69, 0\n" \
  ".Lqk_shw:\n" \
  "buffer_load_dwordx4 v[64:67], %[hvo], %[HS], s75 offen offset:0 sc1\n" \
  "buffer_load_dwordx4 v[68:71], %[hvo], %[HS], s75 offen offset:1024 sc1\n" \
  "buffer_load_dwordx4 v[72:75], %[hvo], %[HS], s75 offen offset:2048 sc1\n" \
  "buffer_load_dwordx4 v[76:79], %[hvo], %[HS], s75 offen offset:3072 sc1\n" \
  "buffer_load_dwordx4 v[80:83], %[hvo], %[HS], s76 offen offset:0 sc1\n" \
  "buffer_load_dwordx4 v[84:87], %[hvo], %[HS], s76 offen offset:1024 sc1\n" \
  "buffer_load_dwordx4 v[88:91], %[hvo], %[HS], s76 offen offset:2048 sc1\n" \
  "buffer_load_dwordx4 v[92:95], %[hvo], %[HS], s76 offen offset:3072 sc1\n" \
  "buffer_load_dwordx4 v[96:99], %[hvo], %[HS], s77 offen offset:0 sc1\n" \
  "s_waitcnt vmcnt(0)\n" \
  "v_max3_u32 v229, v64, v65, v66\n" \
  "v_max3_u32 v229, v229, v67, v68\n" \
  "v_max3_u32 v229, v229, v69, v70\n" \
  "v_max3_u32 v229, v229, v71, v72\n" \
  "v_max3_u32 v229, v229, v73, v74\n" \
  "v_max3_u32 v229, v229, v75, v76\n" \
  "v_max3_u32 v229, v229, v77, v78\n" \
  "v_max3_u32 v229, v229, v79, v80\n" \
  "v_max3_u32 v229, v229, v81, v82\n" \
  "v_max3_u32 v229, v229, v83, v84\n" \
  "v_max3_u32 v229, v229, v85, v86\n" \
  "v_max3_u32 v229, v229, v87, v88\n" \
  "v_max3_u32 v229, v229, v89, v90\n" \
  "v_max3_u32 v229, v229, v91, v92\n" \
  "v_max3_u32 v229, v229, v93, v94\n" \
  "v_max3_u32 v229, v229, v95, v96\n" \
  "v_max3_u32 v229, v229, v97, v98\n" \
  "v_max_u32 v229, v229, v99\n" \
  "v_cmp_eq_u32 vcc, -1, v229\n" \
  "s_cbranch_vccz .Lqk_shw_ok\n" \
  "s_sleep 4\n" \
  "s_add_i32 s69, s69, 1\n" \
  "s_and_b32 s67, s69, 1023\n" \
  "s_cbranch_scc1 .Lqk_shw\n" \
  "buffer_load_dword v229, off, %[F], 0 offset:1024 sc1\n" \
  "s_waitcnt vmcnt(0)\n" \
  "v_readfirstlane_b32 s67, v229\n" \
  "s_cmp_lg_u32 s67, 0\n" \
  "s_cbranch_scc1 .Lqk_shw_ok\n" \
  "s_cmp_lt_u32 s69, 400000\n" \
  "s_cbranch_scc1 .Lqk_shw\n" \
  "v_mov_b32 v229, 1\n" \
  "buffer_store_dword v229, off, %[F], 0 offset:1024 sc1\n" \
  ".Lqk_shw_ok:\n" \
  "s_xor_b32 s67, s62, 0x10000\n" \
  "v_add_u32 v229, s67, %[mo]\n" \
  "ds_write_b128 v229, v[64:67] offset:0\n" \
  "ds_write_b128 v229, v[68:71] offset:1024\n" \
  "ds_write_b128 v229, v[72:75] offset:2048\n" \
  "ds_write_b128 v229, v[76:79] offset:3072\n" \
  "ds_write_b128 v229, v[80:83] offset:4096\n" \
  "ds_write_b128 v229, v[84:87] offset:5120\n" \
  "ds_write_b128 v229, v[88:91] offset:6144\n" \
  "ds_write_b128 v229, v[92:95] offset:7168\n" \
  "ds_write_b128 v229, v[96:99] offset:8192\n" \
  "s_waitcnt lgkmcnt(0)\n" \
  "s_barrier\n" \
  "ds_read_b128 v[148:151], %[hA] offset:0\n" \
  "ds_read_b128 v[152:155], %[hA] offset:18432\n" \
  "ds_read_b128 v[156:159], %[hA] offset:512\n" \
  "ds_read_b128 v[160:163], %[hA] offset:18944\n" \
  "ds_read_b128 v[164:167], %[hA] offset:1024\n" \
  "ds_read_b128 v[168:171], %[hA] offset:19456\n" \
  "ds_read_b128 v[172:175], %[hA] offset:1536\n" \
  "ds_read_b128 v[176:179], %[hA] offset:19968\n" \
  "s_waitcnt lgkmcnt(0)\n" \
  "v_mfma_f32_16x16x32_bf16 v[116:119], v[148:151], a[0:3], 0\n" \
  "v_mfma_f32_16x16x32_bf16 v[120:123], v[148:151], a[8:11], 0\n" \
  "v_mfma_f32_16x16x32_bf16 v[124:127], a[16:19], v[148:151], 0\n" \
  "v_mfma_f32_16x16x32_bf16 v[128:131], a[24:27], v[148:151], 0\n" \
  "v_mfma_f32_16x16x32_bf16 v[132:135], v[152:155], a[0:3], 0\n" \
  "v_mfma_f32_16x16x32_bf16 v[136:139], v[152:155], a[8:11], 0\n" \
  "v_mfma_f32_16x16x32_bf16 v[140:143], a[16:19], v[152:155], 0\n" \
  "v_mfma_f32_16x16x32_bf16 v[144:147], a[24:27], v[152:155], 0\n" \
  "v_mfma_f32_16x16x32_bf16 v[116:119], v[156:159], a[4:7], v[116:119]\n" \
  "v_mfma_f32_16x16x32_bf16 v[120:123], v[156:159], a[12:15], v[120:123]\n" \
  "v_mfma_f32_16x16x32_bf16 v[124:127], a[20:23], v[156:159], v[124:127]\n" \
  "v_mfma_f32_16x16x32_bf16 v[128:131], a[28:31], v[156:159], v[128:131]\n" \
  "v_mfma_f32_16x16x32_bf16 v[132:135], v[160:163], a[4:7], v[132:135]\n" \
  "v_mfma_f32_16x16x32_bf16 v[136:139], v[160:163], a[12:15], v[136:139]\n" \
  "v_mfma_f32_16x16x32_bf16 v[140:143], a[20:23], v[160:163], v[140:143]\n" \
  "v_mfma_f32_16x16x32_bf16 v[144:147], a[28:31], v[160:163], v[144:147]\n" \
  "v_mfma_f32_16x16x32_bf16 v[116:119], v[164:167], a[32:35], v[116:119]\n" \
  "ds_read_b128 v[148:151], %[hA] offset:2048\n" \
  "v_mfma_f32_16x16x32_bf16 v[120:123], v[164:167], a[40:43], v[120:123]\n" \
  "ds_read_b128 v[152:155], %[hA] offset:20480\n" \
  "v_mfma_f32_16x16x32_bf16 v[124:127], a[48:51], v[164:167], v[124:127]\n" \
  "v_mfma_f32_16x16x32_bf16 v[128:131], a[56:59], v[164:167], v[128:131]\n" \
  "v_mfma_f32_16x16x32_bf16 v[132:135], v[168:171], a[32:35], v[132:135]\n" \
  "v_mfma_f32_16x16x32_bf16 v[136:139], v[168:171], a[40:43], v[136:139]\n" \
  "v_mfma_f32_16x16x32_bf16 v[140:143], a[48:51], v[168:171], v[140:143]\n" \
  "v_mfma_f32_16x16x32_bf16 v[144:147], a[56:59], v[168:171], v[144:147]\n" \
  "v_mfma_f32_16x16x32_bf16 v[116:119], v[172:175], a[36:39], v[116:119]\n" \
  "ds_read_b128 v[156:159], %[hA] offset:2560\n" \
  "v_mfma_f32_16x16x32_bf16 v[120:123], v[172:175], a[44:47], v[120:123]\n" \
  "ds_read_b128 v[160:163], %[hA] offset:20992\n" \
  "v_mfma_f32_16x16x32_bf16 v[124:127], a[52:55], v[172:175], v[124:127]\n" \
  "v_mfma_f32_16x16x32_bf16 v[128:131], a[60:63], v[172:175], v[128:131]\n" \
  "v_mfma_f32_16x16x32_bf16 v[132:135], v[176:179], a[36:39], v[132:135]\n" \
  "v_mfma_f32_16x16x32_bf16 v[136:139], v[176:179], a[44:47], v[136:139]\n" \
  "v_mfma_f32_16x16x32_bf16 v[140:143], a[52:55], v[176:179], v[140:143]\n" \
  "v_mfma_f32_16x16x32_bf16 v[144:147], a[60:63], v[176:179], v[144:147]\n" \
  "s_waitcnt lgkmcnt(2)\n" \
  "v_mfma_f32_16x16x32_bf16 v[116:119], v[148:151], a[64:67], v[116:119]\n" \
  "ds_read_b128 v[164:167], %[hA] offset:3072\n" \
  "v_mfma_f32_16x16x32_bf16 v[120:123], v[148:151], a[72:75], v[120:123]\n" \
  "ds_read_b128 v[168:171], %[hA] offset:21504\n" \
  "v_mfma_f32_16x16x32_bf16 v[124:127], a[80:83], v[148:151], v[124:127]\n" \
  "v_mfma_f32_16x16x32_bf16 v[128:131], a[88:91], v[148:151], v[128:131]\n" \
  "v_mfma_f32_16x16x32_bf16 v[132:135], v[152:155], a[64:67], v[132:135]\n" \
  "v_mfma_f32_16x16x32_bf16 v[136:139], v[152:155], a[72:75], v[136:139]\n" \
  "v_mfma_f32_16x16x32_bf16 v[140:143], a[80:83], v[152:155], v[140:143]\n" \
  "v_mfma_f32_16x16x32_bf16 v[144:147], a[88:91], v[152:155], v[144:147]\n" \
  "s_waitcnt lgkmcnt(2)\n" \
  "v_mfma_f32_16x16x32_bf16 v[116:119], v[156:159], a[68:71], v[116:119]\n" \
  "ds_read_b128 v[172:175], %[hA] offset:3584\n" \
  "v_mfma_f32_16x16x32_bf16 v[120:123], v[156:159], a[76:79], v[120:123]\n" \
  "ds_read_b128 v[176:179], %[hA] offset:22016\n" \
  "v_mfma_f32_16x16x32_bf16 v[124:127], a[84:87], v[156:159], v[124:127]\n" \
  "v_mfma_f32_16x16x32_bf16 v[128:131], a[92:95], v[156:159], v[128:131]\n" \
  "v_mfma_f32_16x16x32_bf16 v[132:135], v[160:163], a[68:71], v[132:135]\n" \
  "v_mfma_f32_16x16x32_bf16 v[136:139], v[160:163], a[76:79], v[136:139]\n" \
  "v_mfma_f32_16x16x32_bf16 v[140:143], a[84:87], v[160:163], v[140:143]\n" \
  "v_mfma_f32_16x16x32_bf16 v[144:147], a[92:95], v[160:163], v[144:147]\n" \
  "s_waitcnt lgkmcnt(2)\n" \
  "v_mfma_f32_16x16x32_bf16 v[116:119], v[164:167], a[96:99], v[116:119]\n" \
  "ds_read_b128 v[148:151], %[hA] offset:4096\n" \
  "v_mfma_f32_16x16x32_bf16 v[120:123], v[164:167], a[104:107], v[120:123]\n" \
  "ds_read_b128 v[152:155], %[hA] offset:22528\n" \
  "v_mfma_f32_16x16x32_bf16 v[124:127], a[112:115], v[164:167], v[124:127]\n" \
  "v_mfma_f32_16x16x32_bf16 v[128:131], a[120:123], v[164:167], v[128:131]\n" \
  "v_mfma_f32_16x16x32_bf16 v[132:135], v[168:171], a[96:99], v[132:135]\n" \
  "v_mfma_f32_16x16x32_bf16 v[136:139], v[168:171], a[104:107], v[136:139]\n" \
  "v_mfma_f32_16x16x32_bf16 v[140:143], a[112:115], v[168:171], v[140:143]\n" \
  "v_mfma_f32_16x16x32_bf16 v[144:147], a[120:123], v[168:171], v[144:147]\n" \
  "s_waitcnt lgkmcnt(2)\n" \
  "v_mfma_f32_16x16x32_bf16 v[116:119], v[172:175], a[100:103], v[116:119]\n" \
  "ds_read_b128 v[156:159], %[hA] offset:4608\n" \
  "v_mfma_f32_16x16x32_bf16 v[120:123], v[172:175], a[108:111], v[120:123]\n" \
  "ds_read_b128 v[160:163], %[hA] offset:23040\n" \
  "v_mfma_f32_16x16x32_bf16 v[124:127], a[116:119], v[172:175], v[124:127]\n" \
  "v_mfma_f32_16x16x32_bf16 v[128:131], a[124:127], v[172:175], v[128:131]\n" \
  "v_mfma_f32_16x16x32_bf16 v[132:135], v[176:179], a[100:103], v[132:135]\n" \
  "v_mfma_f32_16x16x32_bf16 v[136:139], v[176:179], a[108:111], v[136:139]\n" \
  "v_mfma_f32_16x16x32_bf16 v[140:143], a[116:119], v[176:179], v[140:143]\n" \
  "v_mfma_f32_16x16x32_bf16 v[144:147], a[124:127], v[176:179], v[144:147]\n" \
  "s_waitcnt lgkmcnt(2)\n" \
  "v_mfma_f32_16x16x32_bf16 v[116:119], v[148:151], a[128:131], v[116:119]\n" \
  "ds_read_b128 v[164:167], %[hA] offset:5120\n" \
  "v_mfma_f32_16x16x32_bf16 v[120:123], v[148:151], a[136:139], v[120:123]\n" \
  "ds_read_b128 v[168:171], %[hA] offset:23552\n" \
  "v_mfma_f32_16x16x32_bf16 v[124:127], a[144:147], v[148:151], v[124:127]\n" \
  "v_mfma_f32_16x16x32_bf16 v[128:131], a[152:155], v[148:151], v[128:131]\n" \
  "v_mfma_f32_16x16x32_bf16 v[132:135], v[152:155], a[128:131], v[132:135]\n" \
  "v_mfma_f32_16x16x32_bf16 v[136:139], v[152:155], a[136:139], v[136:139]\n" \
  "v_mfma_f32_16x16x32_bf16 v[140:143], a[144:147], v[152:155], v[140:143]\n" \
  "v_mfma_f32_16x16x32_bf16 v[144:147], a[152:155], v[152:155], v[144:147]\n" \
  "s_waitcnt lgkmcnt(2)\n" \
  "v_mfma_f32_16x16x32_bf16 v[116:119], v[156:159], a[132:135], v[116:119]\n" \
  "ds_read_b128 v[172:175], %[hA] offset:5632\n" \
  "v_mfma_f32_16x16x32_bf16 v[120:123], v[156:159], a[140:143], v[120:123]\n" \
  "ds_read_b128 v[176:179], %[hA] offset:24064\n" \
  "v_mfma_f32_16x16x32_bf16 v[124:127], a[148:151], v[156:159], v[124:127]\n" \
  "v_mfma_f32_16x16x32_bf16 v[128:131], a[156:159], v[156:159], v[128:131]\n" \
  "v_mfma_f32_16x16x32_bf16 v[132:135], v[160:163], a[132:135], v[132:135]\n" \
  "v_mfma_f32_16x16x32_bf16 v[136:139], v[160:163], a[140:143], v[136:139]\n" \
  "v_mfma_f32_16x16x32_bf16 v[140:143], a[148:151], v[160:163], v[140:143]\n" \
  "v_mfma_f32_16x16x32_bf16 v[144:147], a[156:159], v[160:163], v[144:147]\n" \
  "s_waitcnt lgkmcnt(2)\n" \
  "v_mfma_f32_16x16x32_bf16 v[116:119], v[164:167], a[160:163], v[116:119]\n" \
  "ds_read_b128 v[148:151], %[hA] offset:6144\n" \
  "v_mfma_f32_16x16x32_bf16 v[120:123], v[164:167], a[168:171], v[120:123]\n" \
  "ds_read_b128 v[152:155], %[hA] offset:24576\n" \
  "v_mfma_f32_16x16x32_bf16 v[124:127], a[176:179], v[164:167], v[124:127]\n" \
  "v_mfma_f32_16x16x32_bf16 v[128:131], a[184:187], v[164:167], v[128:131]\n" \
  "v_mfma_f32_16x16x32_bf16 v[132:135], v[168:171], a[160:163], v[132:135]\n" \
  "v_mfma_f32_16x16x32_bf16 v[136:139], v[168:171], a[168:171], v[136:139]\n" \
  "v_mfma_f32_16x16x32_bf16 v[140:143], a[176:179], v[168:171], v[140:143]\n" \
  "v_mfma_f32_16x16x32_bf16 v[144:147], a[184:187], v[168:171], v[144:147]\n" \
  "s_waitcnt lgkmcnt(2)\n" \
  "v_mfma_f32_16x16x32_bf16 v[116:119], v[172:175], a[164:167], v[116:119]\n" \
  "ds_read_b128 v[156:159], %[hA] offset:6656\n" \
  "v_mfma_f32_16x16x32_bf16 v[120:123], v[172:175], a[172:175], v[120:123]\n" \
  "ds_read_b128 v[160:163], %[hA] offset:25088\n" \
  "v_mfma_f32_16x16x32_bf16 v[124:127], a[180:183], v[172:175], v[124:127]\n" \
  "v_mfma_f32_16x16x32_bf16 v[128:131], a[188:191], v[172:175], v[128:131]\n" \
  "v_mfma_f32_16x16x32_bf16 v[132:135], v[176:179], a[164:167], v[132:135]\n" \
  "v_mfma_f32_16x16x32_bf16 v[136:139], v[176:179], a[172:175], v[136:139]\n" \
  "v_mfma_f32_16x16x32_bf16 v[140:143], a[180:183], v[176:179], v[140:143]\n" \
  "v_mfma_f32_16x16x32_bf16 v[144:147], a[188:191], v[176:179], v[144:147]\n" \
  "s_waitcnt lgkmcnt(2)\n" \
  "v_mfma_f32_16x16x32_bf16 v[116:119], v[148:151], a[192:195], v[116:119]\n" \
  "ds_read_b128 v[164:167], %[hA] offset:7168\n" \
  "v_mfma_f32_16x16x32_bf16 v[120:123], v[148:151], a[200:203], v[120:123]\n" \
  "ds_read_b128 v[168:171], %[hA] offset:25600\n" \
  "v_mfma_f32_16x16x32_bf16 v[124:127], a[208:211], v[148:151], v[124:127]\n" \
  "v_mfma_f32_16x16x32_bf16 v[128:131], a[216:219], v[148:151], v[128:131]\n" \
  "v_mfma_f32_16x16x32_bf16 v[132:135], v[152:155], a[192:195], v[132:135]\n" \
  "v_mfma_f32_16x16x32_bf16 v[136:139], v[152:155], a[200:203], v[136:139]\n" \
  "v_mfma_f32_16x16x32_bf16 v[140:143], a[208:211], v[152:155], v[140:143]\n" \
  "v_mfma_f32_16x16x32_bf16 v[144:147], a[216:219], v[152:155], v[144:147]\n" \
  "s_waitcnt lgkmcnt(2)\n" \
  "v_mfma_f32_16x16x32_bf16 v[116:119], v[156:159], a[196:199], v[116:119]\n" \
  "ds_read_b128 v[172:175], %[hA] offset:7680\n" \
  "v_mfma_f32_16x16x32_bf16 v[120:123], v[156:159], a[204:207], v[120:123]\n" \
  "ds_read_b128 v[176:179], %[hA] offset:26112\n" \
  "v_mfma_f32_16x16x32_bf16 v[124:127], a[212:215], v[156:159], v[124:127]\n" \
  "ds_read_b128 v[148:151], %[hA] offset:8192\n" \
  "v_mfma_f32_16x16x32_bf16 v[128:131], a[220:223], v[156:159], v[128:131]\n" \
  "ds_read_b128 v[152:155], %[hA] offset:26624\n" \
  "v_mfma_f32_16x16x32_bf16 v[132:135], v[160:163], a[196:199], v[132:135]\n" \
  "v_mfma_f32_16x16x32_bf16 v[136:139], v[160:163], a[204:207], v[136:139]\n" \
  "v_mfma_f32_16x16x32_bf16 v[140:143], a[212:215], v[160:163], v[140:143]\n" \
  "v_mfma_f32_16x16x32_bf16 v[144:147], a[220:223], v[160:163], v[144:147]\n" \
  "s_waitcnt lgkmcnt(4)\n" \
  "v_mfma_f32_16x16x32_bf16 v[124:127], a[240:243], v[164:167], v[124:127]\n" \
  "ds_read_b128 v[156:159], %[hA] offset:8704\n" \
  "v_mfma_f32_16x16x32_bf16 v[128:131], a[248:251], v[164:167], v[128:131]\n" \
  "ds_read_b128 v[160:163], %[hA] offset:27136\n" \
  "v_mfma_f32_16x16x32_bf16 v[140:143], a[240:243], v[168:171], v[140:143]\n" \
  "v_mfma_f32_16x16x32_bf16 v[144:147], a[248:251], v[168:171], v[144:147]\n" \
  "s_waitcnt lgkmcnt(4)\n" \
  "v_mfma_f32_16x16x32_bf16 v[124:127], a[244:247], v[172:175], v[124:127]\n" \
  "v_mfma_f32_16x16x32_bf16 v[128:131], a[252:255], v[172:175], v[128:131]\n" \
  "v_mfma_f32_16x16x32_bf16 v[140:143], a[244:247], v[176:179], v[140:143]\n" \
  "v_mfma_f32_16x16x32_bf16 v[144:147], a[252:255], v[176:179], v[144:147]\n" \
  "s_waitcnt lgkmcnt(2)\n" \
  "v_mfma_f32_16x16x32_bf16 v[124:127], v[48:51], v[148:151], v[124:127]\n" \
  "v_mfma_f32_16x16x32_bf16 v[128:131], v[56:59], v[148:151], v[128:131]\n" \
  "v_mfma_f32_16x16x32_bf16 v[140:143], v[48:51], v[152:155], v[140:143]\n" \
  "v_mfma_f32_16x16x32_bf16 v[144:147], v[56:59], v[152:155], v[144:147]\n" \
  "s_waitcnt lgkmcnt(0)\n" \
  "v_mfma_f32_16x16x32_bf16 v[124:127], v[52:55], v[156:159], v[124:127]\n" \
  "v_mfma_f32_16x16x32_bf16 v[128:131], v[60:63], v[156:159], v[128:131]\n" \
  "v_mfma_f32_16x16x32_bf16 v[140:143], v[52:55], v[160:163], v[140:143]\n" \
  "v_mfma_f32_16x16x32_bf16 v[144:147], v[60:63], v[160:163], v[144:147]\n" \
  "v_mfma_f32_16x16x32_bf16 v[116:119], v[164:167], a[224:227], v[116:119]\n" \
  "v_mfma_f32_16x16x32_bf16 v[120:123], v[164:167], a[232:235], v[120:123]\n" \
  "v_mfma_f32_16x16x32_bf16 v[132:135], v[168:171], a[224:227], v[132:135]\n" \
  "v_mfma_f32_16x16x32_bf16 v[136:139], v[168:171], a[232:235], v[136:139]\n" \
  "v_mfma_f32_16x16x32_bf16 v[116:119], v[172:175], a[228:231], v[116:119]\n" \
  "v_mfma_f32_16x16x32_bf16 v[120:123], v[172:175], a[236:239], v[120:123]\n" \
  "v_mfma_f32_16x16x32_bf16 v[132:135], v[176:179], a[228:231], v[132:135]\n" \
  "v_mfma_f32_16x16x32_bf16 v[136:139], v[176:179], a[236:239], v[136:139]\n" \
  "v_mfma_f32_16x16x32_bf16 v[116:119], v[148:151], v[32:35], v[116:119]\n" \
  "v_mfma_f32_16x16x32_bf16 v[120:123], v[148:151], v[40:43], v[120:123]\n" \
  "v_mfma_f32_16x16x32_bf16 v[132:135], v[152:155], v[32:35], v[132:135]\n" \
  "v_mfma_f32_16x16x32_bf16 v[136:139], v[152:155], v[40:43], v[136:139]\n" \
  "v_mfma_f32_16x16x32_bf16 v[116:119], v[156:159], v[36:39], v[116:119]\n" \
  "v_mfma_f32_16x16x32_bf16 v[120:123], v[156:159], v[44:47], v[120:123]\n" \
  "v_mfma_f32_16x16x32_bf16 v[132:135], v[160:163], v[36:39], v[132:135]\n" \
  "v_mfma_f32_16x16x32_bf16 v[136:139], v[160:163], v[44:47], v[136:139]\n" \
  "s_memrealtime s[76:77]\n" \
  "s_waitcnt lgkmcnt(0)\n" \
  "s_add_i32 s76, s76, s95\n" \
  "s_add_i32 s76, s76, 90\n" \
  "s_sub_i32 s76, s76, s86\n" \
  "s_max_i32 s76, s76, 0\n" \
  "s_add_i32 s86, s86, s76\n" \
  "v_add_u32 v252, s76, v252\n" \
  "v_add_u32 v253, s76, v253\n" \
  "buffer_load_dwordx4 v[64:67], %[mo], %[X], s63 offen offset:0 sc1\n" \
  "buffer_load_dwordx4 v[68:71], %[mo], %[X], s63 offen offset:1024 sc1\n" \
  "buffer_load_dwordx4 v[72:75], %[mo], %[X], s63 offen offset:2048 sc1\n" \
  "buffer_load_dwordx4 v[76:79], %[mo], %[X], s63 offen offset:3072 sc1\n" \
  "buffer_load_dwordx4 v[80:83], v228, %[X], s63 offen offset:0 sc1\n" \
  "buffer_load_dwordx4 v[84:87], v228, %[X], s63 offen offset:1024 sc1\n" \
  "buffer_load_dwordx4 v[88:91], v228, %[X], s63 offen offset:2048 sc1\n" \
  "buffer_load_dwordx4 v[92:95], v228, %[X], s63 offen offset:3072 sc1\n" \
  "buffer_load_dwordx4 v[96:99], %[mo], %[X], s64 offen offset:0 sc1\n" \
  "v_mov_b64 v[100:101], 0\n" \
  "v_mov_b64 v[102:103], 0\n" \
  "v_mov_b64 v[104:105], 0\n" \
  "v_mov_b64 v[106:107], 0\n" \
  "v_mov_b64 v[108:109], 0\n" \
  "v_mov_b64 v[110:111], 0\n" \
  "v_mov_b64 v[112:113], 0\n" \
  "v_mov_b64 v[114:115], 0\n" \
  "v_cvt_pk_bf16_f32 v220, v124, v125\n" \
  "v_cvt_pk_bf16_f32 v221, v126, v127\n" \
  "v_cvt_pk_bf16_f32 v222, v128, v129\n" \
  "v_cvt_pk_bf16_f32 v223, v130, v131\n" \
  "v_cvt_pk_bf16_f32 v224, v140, v141\n" \
  "v_cvt_pk_bf16_f32 v225, v142, v143\n" \
  "v_cvt_pk_bf16_f32 v226, v144, v145\n" \
  "v_cvt_pk_bf16_f32 v227, v146, v147\n" \
  "ds_write_b128 %[ktW], v[220:223] offset:0\n" \
  "ds_write_b128 %[ktW], v[224:227] offset:4096\n" \
  "v_xor_b32 %[hA], 0x10000, %[hA]\n" \
  "v_mov_b32 v231, 0\n" \
  "s_waitcnt lgkmcnt(0)\n" \
  "s_barrier\n" \
  "ds_read_b128 v[180:183], %[ktR] offset:0\n" \
  "ds_read_b128 v[184:187], %[ktR] offset:1024\n" \
  "ds_read_b128 v[188:191], %[ktR] offset:2048\n" \
  "ds_read_b128 v[192:195], %[ktR] offset:3072\n" \
  "ds_read_b128 v[196:199], %[ktR] offset:4096\n" \
  "ds_read_b128 v[200:203], %[ktR] offset:5120\n" \
  "ds_read_b128 v[204:207], %[ktR] offset:6144\n" \
  "ds_read_b128 v[208:211], %[ktR] offset:7168\n" \
  "s_waitcnt lgkmcnt(0)\n" \
  "s_branch .Lqk_m\n" \
  ".Lqk_entry:\n" \
  "s_mov_b32 s81, 63\n" \
  "s_mov_b32 s87, %[mr0]\n" \
  "s_cmp_lg_u32 %[grp], 0\n" \
  "s_cbranch_scc1 .Lqk_now\n" \
  "v_mov_b32 v229, %[wo]\n" \
  "global_load_dwordx4 a[0:3], v229, %[wp] offset:0\n" \
  "global_load_dwordx4 a[4:7], v229, %[wp] offset:1024\n" \
  "global_load_dwordx4 a[32:35], v229, %[wp] offset:2048\n" \
  "global_load_dwordx4 a[36:39], v229, %[wp] offset:3072\n" \
  "v_add_u32 v229, 4096, v229\n" \
  "global_load_dwordx4 a[64:67], v229, %[wp] offset:0\n" \
  "global_load_dwordx4 a[68:71], v229, %[wp] offset:1024\n" \
  "global_load_dwordx4 a[96:99], v229, %[wp] offset:2048\n" \
  "global_load_dwordx4 a[100:103], v229, %[wp] offset:3072\n" \
  "v_add_u32 v229, 4096, v229\n" \
  "global_load_dwordx4 a[128:131], v229, %[wp] offset:0\n" \
  "global_load_dwordx4 a[132:135], v229, %[wp] offset:1024\n" \
  "global_load_dwordx4 a[160:163], v229, %[wp] offset:2048\n" \
  "global_load_dwordx4 a[164:167], v229, %[wp] offset:3072\n" \
  "v_add_u32 v229, 4096, v229\n" \
  "global_load_dwordx4 a[192:195], v229, %[wp] offset:0\n" \
  "global_load_dwordx4 a[196:199], v229, %[wp] offset:1024\n" \
  "global_load_dwordx4 a[224:227], v229, %[wp] offset:2048\n" \
  "global_load_dwordx4 a[228:231], v229, %[wp] offset:3072\n" \
  "v_add_u32 v229, 4096, v229\n" \
  "global_load_dwordx4 v[32:35], v229, %[wp] offset:0\n" \
  "global_load_dwordx4 v[36:39], v229, %[wp] offset:1024\n" \
  "v_add_u32 v229, 2048, v229\n" \
  "global_load_dwordx4 a[8:11], v229, %[wp] offset:0\n" \
  "global_load_dwordx4 a[12:15], v229, %[wp] offset:1024\n" \
  "global_load_dwordx4 a[40:43], v229, %[wp] offset:2048\n" \
  "global_load_dwordx4 a[44:47], v229, %[wp] offset:3072\n" \
  "v_add_u32 v229, 4096, v229\n" \
  "global_load_dwordx4 a[72:75], v229, %[wp] offset:0\n" \
  "global_load_dwordx4 a[76:79], v229, %[wp] offset:1024\n" \
  "global_load_dwordx4 a[104:107], v229, %[wp] offset:2048\n" \
  "global_load_dwordx4 a[108:111], v229, %[wp] offset:3072\n" \
  "v_add_u32 v229, 4096, v229\n" \
  "global_load_dwordx4 a[136:139], v229, %[wp] offset:0\n" \
  "global_load_dwordx4 a[140:143], v229, %[wp] offset:1024\n" \
  "global_load_dwordx4 a[168:171], v229, %[wp] offset:2048\n" \
  "global_load_dwordx4 a[172:175], v229, %[wp] offset:3072\n" \
  "v_add_u32 v229, 4096, v229\n" \
  "global_load_dwordx4 a[200:203], v229, %[wp] offset:0\n" \
  "global_load_dwordx4 a[204:207], v229, %[wp] offset:1024\n" \
  "global_load_dwordx4 a[232:235], v229, %[wp] offset:2048\n" \
  "global_load_dwordx4 a[236:239], v229, %[wp] offset:3072\n" \
  "v_add_u32 v229, 4096, v229\n" \
  "global_load_dwordx4 v[40:43], v229, %[wp] offset:0\n" \
  "global_load_dwordx4 v[44:47], v229, %[wp] offset:1024\n" \
  "v_add_u32 v229, 2048, v229\n" \
  "global_load_dwordx4 a[16:19], v229, %[wp] offset:0\n" \
  "global_load_dwordx4 a[20:23], v229, %[wp] offset:1024\n" \
  "global_load_dwordx4 a[48:51], v229, %[wp] offset:2048\n" \
  "global_load_dwordx4 a[52:55], v229, %[wp] offset:3072\n" \
  "v_add_u32 v229, 4096, v229\n" \
  "global_load_dwordx4 a[80:83], v229, %[wp] offset:0\n" \
  "global_load_dwordx4 a[84:87], v229, %[wp] offset:1024\n" \
  "global_load_dwordx4 a[112:115], v229, %[wp] offset:2048\n" \
  "global_load_dwordx4 a[116:119], v229, %[wp] offset:3072\n" \
  "v_add_u32 v229, 4096, v229\n" \
  "global_load_dwordx4 a[144:147], v229, %[wp] offset:0\n" \
  "global_load_dwordx4 a[148:151], v229, %[wp] offset:1024\n" \
  "global_load_dwordx4 a[176:179], v229, %[wp] offset:2048\n" \
  "global_load_dwordx4 a[180:183], v229, %[wp] offset:3072\n" \
  "v_add_u32 v229, 4096, v229\n" \
  "global_load_dwordx4 a[208:211], v229, %[wp] offset:0\n" \
  "global_load_dwordx4 a[212:215], v229, %[wp] offset:1024\n" \
  "global_load_dwordx4 a[240:243], v229, %[wp] offset:2048\n" \
  "global_load_dwordx4 a[244:247], v229, %[wp] offset:3072\n" \
  "v_add_u32 v229, 4096, v229\n" \
  "global_load_dwordx4 v[48:51], v229, %[wp] offset:0\n" \
  "global_load_dwordx4 v[52:55], v229, %[wp] offset:1024\n" \
  "v_add_u32 v229, 2048, v229\n" \
  "global_load_dwordx4 a[24:27], v229, %[wp] offset:0\n" \
  "global_load_dwordx4 a[28:31], v229, %[wp] offset:1024\n" \
  "global_load_dwordx4 a[56:59], v229, %[wp] offset:2048\n" \
  "global_load_dwordx4 a[60:63], v229, %[wp] offset:3072\n" \
  "v_add_u32 v229, 4096, v229\n" \
  "global_load_dwordx4 a[88:91], v229, %[wp] offset:0\n" \
  "global_load_dwordx4 a[92:95], v229, %[wp] offset:1024\n" \
  "global_load_dwordx4 a[120:123], v229, %[wp] offset:2048\n" \
  "global_load_dwordx4 a[124:127], v229, %[wp] offset:3072\n" \
  "v_add_u32 v229, 4096, v229\n" \
  "global_load_dwordx4 a[152:155], v229, %[wp] offset:0\n" \
  "global_load_dwordx4 a[156:159], v229, %[wp] offset:1024\n" \
  "global_load_dwordx4 a[184:187], v229, %[wp] offset:2048\n" \
  "global_load_dwordx4 a[188:191], v229, %[wp] offset:3072\n" \
  "v_add_u32 v229, 4096, v229\n" \
  "global_load_dwordx4 a[216:219], v229, %[wp] offset:0\n" \
  "global_load_dwordx4 a[220:223], v229, %[wp] offset:1024\n" \
  "global_load_dwordx4 a[248:251], v229, %[wp] offset:2048\n" \
  "global_load_dwordx4 a[252:255], v229, %[wp] offset:3072\n" \
  "v_add_u32 v229, 4096, v229\n" \
  "global_load_dwordx4 v[56:59], v229, %[wp] offset:0\n" \
  "global_load_dwordx4 v[60:63], v229, %[wp] offset:1024\n" \
  "v_add_u32 v229, 2048, v229\n" \
  ".Lqk_now:\n" \
  "s_cmp_eq_u32 %[grp], 0\n" \
  "s_cbranch_scc1 .Lqk_go\n" \
  "s_mov_b32 s69, 0\n" \
  "s_xor_b32 s75, %[bas], 0x2000\n" \
  ".Lqk_g1:\n" \
  "buffer_load_dword v229, off, %[X], s75 offset:48 sc1\n" \
  "s_waitcnt vmcnt(0)\n" \
  "v_readfirstlane_b32 s74, v229\n" \
  "s_cmp_lg_u32 s74, 0\n" \
  "s_cbranch_scc1 .Lqk_g2\n" \
  "s_sleep 8\n" \
  "s_add_i32 s69, s69, 1\n" \
  "s_cmp_lt_u32 s69, 400\n" \
  "s_cbranch_scc1 .Lqk_g1\n" \
  ".Lqk_g2:\n" \
  "s_add_i32 s74, s74, %[g1t]\n" \
  ".Lqk_g3:\n" \
  "s_memrealtime s[76:77]\n" \
  "s_waitcnt lgkmcnt(0)\n" \
  "s_sub_i32 s76, s76, s74\n" \
  "s_cmp_gt_u32 s76, -3000\n" \
  "s_cbranch_scc1 .Lqk_g3\n" \
  ".Lqk_go:\n" \
  "s_waitcnt vmcnt(0) lgkmcnt(0)\n" \
  "s_barrier\n" \
  "s_mov_b32 s75, 0\n" \
  "s_movk_i32 s76, 0x1000\n" \
  "s_movk_i32 s77, 0x2000\n" \
  "v_mov_b32 v236, 1\n" \
  "buffer_atomic_add v236, %[rdy], %[X], %[bas] offen offset:1536 sc0 sc1\n" \
  "s_mov_b32 s69, 0\n" \
  ".Lqk_enw:\n" \
  "buffer_load_dwordx4 v[64:67], %[hvo], %[HS], s75 offen offset:0 sc1\n" \
  "buffer_load_dwordx4 v[68:71], %[hvo], %[HS], s75 offen offset:1024 sc1\n" \
  "buffer_load_dwordx4 v[72:75], %[hvo], %[HS], s75 offen offset:2048 sc1\n" \
  "buffer_load_dwordx4 v[76:79], %[hvo], %[HS], s75 offen offset:3072 sc1\n" \
  "buffer_load_dwordx4 v[80:83], %[hvo], %[HS], s76 offen offset:0 sc1\n" \
  "buffer_load_dwordx4 v[84:87], %[hvo], %[HS], s76 offen offset:1024 sc1\n" \
  "buffer_load_dwordx4 v[88:91], %[hvo], %[HS], s76 offen offset:2048 sc1\n" \
  "buffer_load_dwordx4 v[92:95], %[hvo], %[HS], s76 offen offset:3072 sc1\n" \
  "buffer_load_dwordx4 v[96:99], %[hvo], %[HS], s77 offen offset:0 sc1\n" \
  "s_waitcnt vmcnt(0)\n" \
  "v_max3_u32 v229, v64, v65, v66\n" \
  "v_max3_u32 v229, v229, v67, v68\n" \
  "v_max3_u32 v229, v229, v69, v70\n" \
  "v_max3_u32 v229, v229, v71, v72\n" \
  "v_max3_u32 v229, v229, v73, v74\n" \
  "v_max3_u32 v229, v229, v75, v76\n" \
  "v_max3_u32 v229, v229, v77, v78\n" \
  "v_max3_u32 v229, v229, v79, v80\n" \
  "v_max3_u32 v229, v229, v81, v82\n" \
  "v_max3_u32 v229, v229, v83, v84\n" \
  "v_max3_u32 v229, v229, v85, v86\n" \
  "v_max3_u32 v229, v229, v87, v88\n" \
  "v_max3_u32 v229, v229, v89, v90\n" \
  "v_max3_u32 v229, v229, v91, v92\n" \
  "v_max3_u32 v229, v229, v93, v94\n" \
  "v_max3_u32 v229, v229, v95, v96\n" \
  "v_max3_u32 v229, v229, v97, v98\n" \
  "v_max_u32 v229, v229, v99\n" \
  "v_cmp_eq_u32 vcc, -1, v229\n" \
  "s_cbranch_vccz .Lqk_enw_ok\n" \
  "s_sleep 4\n" \
  "s_add_i32 s69, s69, 1\n" \
  "s_and_b32 s67, s69, 1023\n" \
  "s_cbranch_scc1 .Lqk_enw\n" \
  "buffer_load_dword v229, off, %[F], 0 offset:1024 sc1\n" \
  "s_waitcnt vmcnt(0)\n" \
  "v_readfirstlane_b32 s67, v229\n" \
  "s_cmp_lg_u32 s67, 0\n" \
  "s_cbranch_scc1 .Lqk_enw_ok\n" \
  "s_cmp_lt_u32 s69, 400000\n" \
  "s_cbranch_scc1 .Lqk_enw\n" \
  "v_mov_b32 v229, 1\n" \
  "buffer_store_dword v229, off, %[F], 0 offset:1024 sc1\n" \
  ".Lqk_enw_ok:\n" \
  "ds_write_b128 %[hvo], v[64:67] offset:0\n" \
  "ds_write_b128 %[hvo], v[68:71] offset:1024\n" \
  "ds_write_b128 %[hvo], v[72:75] offset:2048\n" \
  "ds_write_b128 %[hvo], v[76:79] offset:3072\n" \
  "ds_write_b128 %[hvo], v[80:83] offset:4096\n" \
  "ds_write_b128 %[hvo], v[84:87] offset:5120\n" \
  "ds_write_b128 %[hvo], v[88:91] offset:6144\n" \
  "ds_write_b128 %[hvo], v[92:95] offset:7168\n" \
  "ds_write_b128 %[hvo], v[96:99] offset:8192\n" \
  "v_readfirstlane_b32 s92, v236\n" \
  "s_cmp_lg_u32 s92, 103\n" \
  "s_cbranch_scc1 .Lqk_nrdy\n" \
  "s_memrealtime s[74:75]\n" \
  "s_waitcnt lgkmcnt(0)\n" \
  "s_add_i32 s74, s74, %[x0]\n" \
  "s_mul_i32 s75, %[tau], 162\n" \
  "s_lshr_b32 s75, s75, 8\n" \
  "s_add_i32 s75, s75, s74\n" \
  "v_mov_b32 v236, s74\n" \
  "v_mov_b32 v237, s75\n" \
  "buffer_store_dwordx2 v[236:237], %[rdy], %[X], %[bas] offen offset:0 sc1\n" \
  "s_add_i32 s74, s74, %[tau]\n" \
  "s_add_i32 s75, s75, %[tau]\n" \
  "v_mov_b32 v236, s74\n" \
  "v_mov_b32 v237, s75\n" \
  "buffer_store_dwordx2 v[236:237], %[rdy], %[X], %[bas] offen offset:16 sc1\n" \
  ".Lqk_nrdy:\n" \
  "s_mov_b32 s95, %[tau]\n" \
  "s_mov_b32 s90, 0\n" \
  "s_mov_b32 s78, 0\n" \
  "s_waitcnt lgkmcnt(0)\n" \
  "s_barrier\n" \
  "ds_read_b128 v[148:151], %[hA] offset:0\n" \
  "ds_read_b128 v[152:155], %[hA] offset:18432\n" \
  "ds_read_b128 v[156:159], %[hA] offset:512\n" \
  "ds_read_b128 v[160:163], %[hA] offset:18944\n" \
  "ds_read_b128 v[164:167], %[hA] offset:1024\n" \
  "ds_read_b128 v[168:171], %[hA] offset:19456\n" \
  "ds_read_b128 v[172:175], %[hA] offset:1536\n" \
  "ds_read_b128 v[176:179], %[hA] offset:19968\n" \
  "s_waitcnt lgkmcnt(0)\n" \
  "v_mfma_f32_16x16x32_bf16 v[116:119], v[148:151], a[0:3], 0\n" \
  "v_mfma_f32_16x16x32_bf16 v[120:123], v[148:151], a[8:11], 0\n" \
  "v_mfma_f32_16x16x32_bf16 v[124:127], a[16:19], v[148:151], 0\n" \
  "v_mfma_f32_16x16x32_bf16 v[128:131], a[24:27], v[148:151], 0\n" \
  "v_mfma_f32_16x16x32_bf16 v[132:135], v[152:155], a[0:3], 0\n" \
  "v_mfma_f32_16x16x32_bf16 v[136:139], v[152:155], a[8:11], 0\n" \
  "v_mfma_f32_16x16x32_bf16 v[140:143], a[16:19], v[152:155], 0\n" \
  "v_mfma_f32_16x16x32_bf16 v[144:147], a[24:27], v[152:155], 0\n" \
  "v_mfma_f32_16x16x32_bf16 v[116:119], v[156:159], a[4:7], v[116:119]\n" \
  "v_mfma_f32_16x16x32_bf16 v[120:123], v[156:159], a[12:15], v[120:123]\n" \
  "v_mfma_f32_16x16x32_bf16 v[124:127], a[20:23], v[156:159], v[124:127]\n" \
  "v_mfma_f32_16x16x32_bf16 v[128:131], a[28:31], v[156:159], v[128:131]\n" \
  "v_mfma_f32_16x16x32_bf16 v[132:135], v[160:163], a[4:7], v[132:135]\n" \
  "v_mfma_f32_16x16x32_bf16 v[136:139], v[160:163], a[12:15], v[136:139]\n" \
  "v_mfma_f32_16x16x32_bf16 v[140:143], a[20:23], v[160:163], v[140:143]\n" \
  "v_mfma_f32_16x16x32_bf16 v[144:147], a[28:31], v[160:163], v[144:147]\n" \
  "s_waitcnt lgkmcnt(0)\n" \
  "s_branch .Lqk_top\n" \
  ".Lqk_end:\n"
#define QK_CLOB "v32", "v33", "v34", "v35", "v36", "v37", "v38", "v39", "v40", "v41", "v42", "v43", "v44", "v45", "v46", "v47", "v48", "v49", "v50", "v51", "v52", "v53", "v54", "v55", "v56", "v57", "v58", "v59", "v60", "v61", "v62", "v63", "v64", "v65", "v66", "v67", "v68", "v69", "v70", "v71", "v72", "v73", "v74", "v75", "v76", "v77", "v78", "v79", "v80", "v81", "v82", "v83", "v84", "v85", "v86", "v87", "v88", "v89", "v90", "v91", "v92", "v93", "v94", "v95", "v96", "v97", "v98", "v99", "v100", "v101", "v102", "v103", "v104", "v105", "v106", "v107", "v108", "v109", "v110", "v111", "v112", "v113", "v114", "v115", "v116", "v117", "v118", "v119", "v120", "v121", "v122", "v123", "v124", "v125", "v126", "v127", "v128", "v129", "v130", "v131", "v132", "v133", "v134", "v135", "v136", "v137", "v138", "v139", "v140", "v141", "v142", "v143", "v144", "v145", "v146", "v147", "v148", "v149", "v150", "v151", "v152", "v153", "v154", "v155", "v156", "v157", "v158", "v159", "v160", "v161", "v162", "v163", "v164", "v165", "v166", "v167", "v168", "v169", "v170", "v171", "v172", "v173", "v174", "v175", "v176", "v177", "v178", "v179", "v180", "v181", "v182", "v183", "v184", "v185", "v186", "v187", "v188", "v189", "v190", "v191", "v192", "v193", "v194", "v195", "v196", "v197", "v198", "v199", "v200", "v201", "v202", "v203", "v204", "v205", "v206", "v207", "v208", "v209", "v210", "v211", "v212", "v213", "v214", "v215", "v216", "v217", "v218", "v219", "v220", "v221", "v222", "v223", "v224", "v225", "v226", "v227", "v228", "v229", "v230", "v231", "v232", "v233", "v234", "v235", "v236", "v237", "v238", "v239", "v240", "v241", "v242", "v243", "v244", "v245", "v246", "v247", "v248", "v249", "v250", "v251", "v252", "v253", "v254", "v255", "s60", "s61", "s62", "s63", "s64", "s65", "s66", "s67", "s68", "s69", "s70", "s71", "s72", "s73", "s74", "s75", "s76", "s77", "s78", "s79", "s80", "s81", "s82", "s83", "s84", "s85", "s86", "s87", "s88", "s89", "s90", "s91", "s92", "s93", "s94", "s95", "s96", "a0", "a1", "a2", "a3", "a4", "a5", "a6", "a7", "a8", "a9", "a10", "a11", "a12", "a13", "a14", "a15", "a16", "a17", "a18", "a19", "a20", "a21", "a22", "a23", "a24", "a25", "a26", "a27", "a28", "a29", "a30", "a31", "a32", "a33", "a34", "a35", "a36", "a37", "a38", "a39", "a40", "a41", "a42", "a43", "a44", "a45", "a46", "a47", "a48", "a49", "a50", "a51", "a52", "a53", "a54", "a55", "a56", "a57", "a58", "a59", "a60", "a61", "a62", "a63", "a64", "a65", "a66", "a67", "a68", "a69", "a70", "a71", "a72", "a73", "a74", "a75", "a76", "a77", "a78", "a79", "a80", "a81", "a82", "a83", "a84", "a85", "a86", "a87", "a88", "a89", "a90", "a91", "a92", "a93", "a94", "a95", "a96", "a97", "a98", "a99", "a100", "a101", "a102", "a103", "a104", "a105", "a106", "a107", "a108", "a109", "a110", "a111", "a112", "a113", "a114", "a115", "a116", "a117", "a118", "a119", "a120", "a121", "a122", "a123", "a124", "a125", "a126", "a127", "a128", "a129", "a130", "a131", "a132", "a133", "a134", "a135", "a136", "a137", "a138", "a139", "a140", "a141", "a142", "a143", "a144", "a145", "a146", "a147", "a148", "a149", "a150", "a151", "a152", "a153", "a154", "a155", "a156", "a157", "a158", "a159", "a160", "a161", "a162", "a163", "a164", "a165", "a166", "a167", "a168", "a169", "a170", "a171", "a172", "a173", "a174", "a175", "a176", "a177", "a178", "a179", "a180", "a181", "a182", "a183", "a184", "a185", "a186", "a187", "a188", "a189", "a190", "a191", "a192", "a193", "a194", "a195", "a196", "a197", "a198", "a199", "a200", "a201", "a202", "a203", "a204", "a205", "a206", "a207", "a208", "a209", "a210", "a211", "a212", "a213", "a214", "a215", "a216", "a217", "a218", "a219", "a220", "a221", "a222", "a223", "a224", "a225", "a226", "a227", "a228", "a229", "a230", "a231", "a232", "a233", "a234", "a235", "a236", "a237", "a238", "a239", "a240", "a241", "a242", "a243", "a244", "a245", "a246", "a247", "a248", "a249", "a250", "a251", "a252", "a253", "a254", "a255", "vcc", "scc", "m0", "memory"
#define V_ASM \
  "s_nop 4\n" \
  "v_add_u32 v251, 16384, %[wo]\n" \
  "global_load_dwordx4 v[24:27], v251, %[wp] offset:0\n" \
  "global_load_dwordx4 v[28:31], v251, %[wp] offset:1024\n" \
  "v_add_u32 v251, 18432, v251\n" \
  "global_load_dwordx4 v[32:35], v251, %[wp] offset:0\n" \
  "global_load_dwordx4 v[36:39], v251, %[wp] offset:1024\n" \
  "v_add_u32 v251, 18432, v251\n" \
  "global_load_dwordx4 v[40:43], v251, %[wp] offset:0\n" \
  "global_load_dwordx4 v[44:47], v251, %[wp] offset:1024\n" \
  "v_add_u32 v251, 18432, v251\n" \
  "global_load_dwordx4 v[48:51], v251, %[wp] offset:0\n" \
  "global_load_dwordx4 v[52:55], v251, %[wp] offset:1024\n" \
  "s_sub_i32 s56, -1, %[grp]\n" \
  "v_add_u32 v250, 0x1000, %[la]\n" \
  "s_mov_b32 s72, -1\n" \
  "s_mov_b32 s73, 0\n" \
  "s_mov_b32 s78, 0\n" \
  "s_mov_b32 s79, 0\n" \
  "s_mov_b32 s80, 0\n" \
  "s_mov_b32 s66, 0\n" \
  "s_mov_b32 s81, -1\n" \
  "s_mov_b32 s87, 2.0\n" \
  "s_add_i32 s69, s56, 2\n" \
  "s_min_i32 s68, s69, 63\n" \
  "s_mul_i32 s57, s68, 36864\n" \
  "s_lshl_b32 s68, s69, 16\n" \
  "s_and_b32 s68, s68, 0x10000\n" \
  "s_add_i32 s58, %[hl0], s68\n" \
  "s_add_i32 s68, s56, -1\n" \
  "s_mul_i32 s63, s68, 36864\n" \
  "s_add_i32 s63, s63, %[hl0]\n" \
  "s_cmp_gt_i32 s56, 0\n" \
  "s_cselect_b32 s63, s63, 2.0\n" \
  "s_waitcnt vmcnt(0)\n" \
  "s_cmp_eq_u32 s56, -1\n" \
  "s_cbranch_scc1 .Lv_entry\n" \
  ".Lv_top:\n" \
  "v_mfma_f32_16x16x32_bf16 v[64:67], v[204:207], a[20:23], v[64:67]\n" \
  "ds_read_b128 v[104:107], %[hA] offset:1024\n" \
  "s_add_i32 m0, s58, 0\n" \
  "v_mfma_f32_16x16x32_bf16 v[68:71], v[204:207], a[28:31], v[68:71]\n" \
  "ds_read_b128 v[108:111], %[hA] offset:19456\n" \
  "v_mfma_f32_16x16x32_bf16 v[72:75], v[208:211], a[4:7], v[72:75]\n" \
  "ds_read_b128 v[88:91], %[hA] offset:1536\n" \
  "v_mfma_f32_16x16x32_bf16 v[76:79], v[208:211], a[12:15], v[76:79]\n" \
  "ds_read_b128 v[92:95], %[hA] offset:19968\n" \
  "v_mfma_f32_16x16x32_bf16 v[80:83], v[208:211], a[20:23], v[80:83]\n" \
  "ds_read2st64_b64 v[128:131], %[rtA] offset0:0 offset1:4\n" \
  "buffer_load_dwordx4 %[hvo], %[HS], s57 offen offset:0 sc1 lds\n" \
  "v_mfma_f32_16x16x32_bf16 v[84:87], v[208:211], a[28:31], v[84:87]\n" \
  "ds_read2st64_b64 v[132:135], %[rtA] offset0:8 offset1:12\n" \
  "s_waitcnt lgkmcnt(4)\n" \
  "v_mfma_f32_16x16x32_bf16 v[56:59], v[104:107], a[32:35], v[56:59]\n" \
  "ds_read_b128 v[96:99], %[hA] offset:2048\n" \
  "v_mfma_f32_16x16x32_bf16 v[60:63], v[104:107], a[40:43], v[60:63]\n" \
  "ds_read_b128 v[100:103], %[hA] offset:20480\n" \
  "v_mfma_f32_16x16x32_bf16 v[64:67], v[104:107], a[48:51], v[64:67]\n" \
  "buffer_load_dwordx4 %[hvo], %[HS], s57 offen offset:1024 sc1 lds\n" \
  "v_mfma_f32_16x16x32_bf16 v[68:71], v[104:107], a[56:59], v[68:71]\n" \
  "s_add_i32 s56, s56, 1\n" \
  "s_lshl_b32 s94, s56, 4\n" \
  "s_add_i32 s94, s94, %[bas]\n" \
  "v_mfma_f32_16x16x32_bf16 v[72:75], v[108:111], a[32:35], v[72:75]\n" \
  "s_waitcnt lgkmcnt(2)\n" \
  "v_add_f32 v128, v128, v130\n" \
  "v_add_f32 v129, v129, v131\n" \
  "v_mfma_f32_16x16x32_bf16 v[76:79], v[108:111], a[40:43], v[76:79]\n" \
  "v_add_f32 v132, v132, v134\n" \
  "v_add_f32 v133, v133, v135\n" \
  "v_mfma_f32_16x16x32_bf16 v[80:83], v[108:111], a[48:51], v[80:83]\n" \
  "v_add_f32 v128, v128, v132\n" \
  "v_add_f32 v129, v129, v133\n" \
  "v_mfma_f32_16x16x32_bf16 v[84:87], v[108:111], a[56:59], v[84:87]\n" \
  "buffer_load_dwordx4 %[hvo], %[HS], s57 offen offset:2048 sc1 lds\n" \
  "v_mfma_f32_16x16x32_bf16 v[56:59], v[88:91], a[36:39], v[56:59]\n" \
  "ds_read_b128 v[104:107], %[hA] offset:2560\n" \
  "v_add_f32_dpp v128, v128, v128 quad_perm:[1,0,3,2] row_mask:0xf bank_mask:0xf\n" \
  "v_mfma_f32_16x16x32_bf16 v[60:63], v[88:91], a[44:47], v[60:63]\n" \
  "ds_read_b128 v[108:111], %[hA] offset:20992\n" \
  "v_add_f32_dpp v129, v129, v129 quad_perm:[1,0,3,2] row_mask:0xf bank_mask:0xf\n" \
  "v_mfma_f32_16x16x32_bf16 v[64:67], v[88:91], a[52:55], v[64:67]\n" \
  "v_cvt_pk_bf16_f32 v128, v128, v129\n" \
  "v_xor_b32 v128, s62, v128\n" \
  "v_mfma_f32_16x16x32_bf16 v[68:71], v[88:91], a[60:63], v[68:71]\n" \
  "buffer_load_dwordx4 %[hvo], %[HS], s57 offen offset:3072 sc1 lds\n" \
  "v_min_u32 v128, -2, v128\n" \
  "v_mfma_f32_16x16x32_bf16 v[72:75], v[92:95], a[36:39], v[72:75]\n" \
  "buffer_store_dword v128, %[lo], %[OS], s63 offen sc1\n" \
  "v_mov_b32 v251, -1\n" \
  "v_mfma_f32_16x16x32_bf16 v[76:79], v[92:95], a[44:47], v[76:79]\n" \
  "s_add_i32 s57, s57, 0x1000\n" \
  "s_add_i32 m0, s58, 4096\n" \
  "v_mov_b64 v[228:229], v[212:213]\n" \
  "v_mfma_f32_16x16x32_bf16 v[80:83], v[92:95], a[52:55], v[80:83]\n" \
  "s_lshl_b32 s68, s56, 17\n" \
  "s_and_b32 s68, s68, 0x60000\n" \
  "s_add_i32 s59, s87, s68\n" \
  "v_mfma_f32_16x16x32_bf16 v[84:87], v[92:95], a[60:63], v[84:87]\n" \
  "s_xor_b32 s68, s68, 0x40000\n" \
  "s_add_i32 s64, %[mw0], s68\n" \
  "s_waitcnt lgkmcnt(2)\n" \
  "v_mfma_f32_16x16x32_bf16 v[56:59], v[96:99], a[64:67], v[56:59]\n" \
  "ds_read_b128 v[88:91], %[hA] offset:3072\n" \
  "buffer_load_dwordx4 %[hvo], %[HS], s57 offen offset:0 sc1 lds\n" \
  "v_mfma_f32_16x16x32_bf16 v[60:63], v[96:99], a[72:75], v[60:63]\n" \
  "ds_read_b128 v[92:95], %[hA] offset:21504\n" \
  "v_mov_b64 v[230:231], v[216:217]\n" \
  "v_mfma_f32_16x16x32_bf16 v[64:67], v[96:99], a[80:83], v[64:67]\n" \
  "s_cmp_gt_i32 s56, 1\n" \
  "s_cselect_b32 s64, s64, 2.0\n" \
  "s_add_i32 s69, s56, 1\n" \
  "v_mfma_f32_16x16x32_bf16 v[68:71], v[96:99], a[88:91], v[68:71]\n" \
  "s_lshl_b32 s68, s69, 17\n" \
  "s_and_b32 s68, s68, 0x60000\n" \
  "s_add_i32 s67, %[mw0], s68\n" \
  "v_mfma_f32_16x16x32_bf16 v[72:75], v[100:103], a[64:67], v[72:75]\n" \
  "buffer_load_dwordx4 %[hvo], %[HS], s57 offen offset:1024 sc1 lds\n" \
  "v_mov_b64 v[232:233], v[220:221]\n" \
  "v_mfma_f32_16x16x32_bf16 v[76:79], v[100:103], a[72:75], v[76:79]\n" \
  "s_cmp_lt_u32 s56, 63\n" \
  "s_cselect_b32 s67, s67, 2.0\n" \
  "s_lshl_b32 s69, s56, 18\n" \
  "v_mfma_f32_16x16x32_bf16 v[80:83], v[100:103], a[80:83], v[80:83]\n" \
  "s_and_b32 s69, s69, 0x40000\n" \
  "s_add_i32 s65, %[pm0], s69\n" \
  "s_xor_b32 s69, s69, 0x40000\n" \
  "v_mfma_f32_16x16x32_bf16 v[84:87], v[100:103], a[88:91], v[84:87]\n" \
  "v_mov_b64 v[234:235], v[224:225]\n" \
  "s_waitcnt lgkmcnt(2)\n" \
  "v_mfma_f32_16x16x32_bf16 v[56:59], v[104:107], a[68:71], v[56:59]\n" \
  "ds_read_b128 v[96:99], %[hA] offset:3584\n" \
  "v_mov_b64 v[236:237], v[244:245]\n" \
  "v_mfma_f32_16x16x32_bf16 v[60:63], v[104:107], a[76:79], v[60:63]\n" \
  "ds_read_b128 v[100:103], %[hA] offset:22016\n" \
  "buffer_load_dwordx4 %[hvo], %[HS], s57 offen offset:2048 sc1 lds\n" \
  "v_mfma_f32_16x16x32_bf16 v[64:67], v[104:107], a[84:87], v[64:67]\n" \
  "s_add_i32 s60, %[pt0], s69\n" \
  "s_cmp_gt_i32 s56, 0\n" \
  "s_cselect_b32 s60, s60, 2.0\n" \
  "v_mfma_f32_16x16x32_bf16 v[68:71], v[104:107], a[92:95], v[68:71]\n" \
  "s_add_i32 s61, s60, 0x4000\n" \
  "v_mov_b64 v[238:239], v[246:247]\n" \
  "v_mov_b64 v[112:113], 0\n" \
  "v_mfma_f32_16x16x32_bf16 v[72:75], v[108:111], a[68:71], v[72:75]\n" \
  "s_mov_b32 s62, s66\n" \
  "s_bfe_i32 s68, s56, 0x10001\n" \
  "s_and_b32 s66, s68, 0x80008000\n" \
  "v_mfma_f32_16x16x32_bf16 v[76:79], v[108:111], a[76:79], v[76:79]\n" \
  "buffer_load_dwordx4 %[hvo], %[HS], s57 offen offset:3072 sc1 lds\n" \
  "v_mov_b64 v[114:115], 0\n" \
  "v_mfma_f32_16x16x32_bf16 v[80:83], v[108:111], a[84:87], v[80:83]\n" \
  "s_add_i32 s57, s57, 0x1000\n" \
  "s_add_i32 m0, s58, 8192\n" \
  "v_mov_b64 v[116:117], 0\n" \
  "v_mfma_f32_16x16x32_bf16 v[84:87], v[108:111], a[92:95], v[84:87]\n" \
  "v_mov_b64 v[118:119], 0\n" \
  "s_waitcnt lgkmcnt(2)\n" \
  "v_mfma_f32_16x16x32_bf16 v[56:59], v[88:91], a[96:99], v[56:59]\n" \
  "ds_read_b128 v[104:107], %[hA] offset:4096\n" \
  "v_mov_b64 v[120:121], 0\n" \
  "v_mfma_f32_16x16x32_bf16 v[60:63], v[88:91], a[104:107], v[60:63]\n" \
  "ds_read_b128 v[108:111], %[hA] offset:22528\n" \
  "v_mov_b64 v[122:123], 0\n" \
  "v_mfma_f32_16x16x32_bf16 v[64:67], v[88:91], a[112:115], v[64:67]\n" \
  "buffer_load_dwordx4 %[hvo], %[HS], s57 offen offset:0 sc1 lds\n" \
  "v_mov_b64 v[124:125], 0\n" \
  "v_mfma_f32_16x16x32_bf16 v[68:71], v[88:91], a[120:123], v[68:71]\n" \
  "s_mov_b32 m0, %[ml0]\n" \
  "v_mov_b64 v[126:127], 0\n" \
  "v_mfma_f32_16x16x32_bf16 v[72:75], v[92:95], a[96:99], v[72:75]\n" \
  "v_mfma_f32_16x16x32_bf16 v[76:79], v[92:95], a[104:107], v[76:79]\n" \
  "v_mfma_f32_16x16x32_bf16 v[80:83], v[92:95], a[112:115], v[80:83]\n" \
  "buffer_load_dwordx4 %[c0], %[X], s59 offen sc1 lds\n" \
  "v_mfma_f32_16x16x32_bf16 v[84:87], v[92:95], a[120:123], v[84:87]\n" \
  "s_add_i32 s59, s59, 0x1000\n" \
  "s_add_i32 m0, %[ml0], 0x1000\n" \
  "s_waitcnt lgkmcnt(2)\n" \
  "v_mfma_f32_16x16x32_bf16 v[56:59], v[96:99], a[100:103], v[56:59]\n" \
  "ds_read_b128 v[88:91], %[hA] offset:4608\n" \
  "v_mfma_f32_16x16x32_bf16 v[60:63], v[96:99], a[108:111], v[60:63]\n" \
  "ds_read_b128 v[92:95], %[hA] offset:23040\n" \
  "v_mfma_f32_16x16x32_bf16 v[64:67], v[96:99], a[116:119], v[64:67]\n" \
  "v_mfma_f32_16x16x32_bf16 v[68:71], v[96:99], a[124:127], v[68:71]\n" \
  "buffer_load_dwordx4 %[c0], %[X], s59 offen sc1 lds\n" \
  "v_mfma_f32_16x16x32_bf16 v[72:75], v[100:103], a[100:103], v[72:75]\n" \
  "s_add_i32 s59, s59, 0x3000\n" \
  "s_add_i32 m0, %[ml0], 0x2000\n" \
  "v_mfma_f32_16x16x32_bf16 v[76:79], v[100:103], a[108:111], v[76:79]\n" \
  "v_mfma_f32_16x16x32_bf16 v[80:83], v[100:103], a[116:119], v[80:83]\n" \
  "v_mfma_f32_16x16x32_bf16 v[84:87], v[100:103], a[124:127], v[84:87]\n" \
  "buffer_load_dwordx4 %[c2], %[X], s59 offen sc1 lds\n" \
  "s_waitcnt lgkmcnt(2)\n" \
  "v_mfma_f32_16x16x32_bf16 v[56:59], v[104:107], a[128:131], v[56:59]\n" \
  "ds_read_b128 v[96:99], %[hA] offset:5120\n" \
  "buffer_load_dword v248, off, %[X], s94 offset:4 sc1\n" \
  "v_mfma_f32_16x16x32_bf16 v[60:63], v[104:107], a[136:139], v[60:63]\n" \
  "ds_read_b128 v[100:103], %[hA] offset:23552\n" \
  "v_mfma_f32_16x16x32_bf16 v[64:67], v[104:107], a[144:147], v[64:67]\n" \
  "s_add_i32 s69, s56, 2\n" \
  "s_min_i32 s68, s69, 63\n" \
  "s_mul_i32 s57, s68, 36864\n" \
  "v_mfma_f32_16x16x32_bf16 v[68:71], v[104:107], a[152:155], v[68:71]\n" \
  "s_lshl_b32 s68, s69, 16\n" \
  "s_and_b32 s68, s68, 0x10000\n" \
  "s_add_i32 s58, %[hl0], s68\n" \
  "v_mfma_f32_16x16x32_bf16 v[72:75], v[108:111], a[128:131], v[72:75]\n" \
  "buffer_load_dwordx4 v[148:151], %[la], %[X], s60 offen offset:0 sc1\n" \
  "v_mfma_f32_16x16x32_bf16 v[76:79], v[108:111], a[136:139], v[76:79]\n" \
  "s_add_i32 s68, s56, -1\n" \
  "s_mul_i32 s63, s68, 36864\n" \
  "s_add_i32 s63, s63, %[hl0]\n" \
  "v_mfma_f32_16x16x32_bf16 v[80:83], v[108:111], a[144:147], v[80:83]\n" \
  "s_cmp_gt_i32 s56, 0\n" \
  "s_cselect_b32 s63, s63, 2.0\n" \
  "v_mfma_f32_16x16x32_bf16 v[84:87], v[108:111], a[152:155], v[84:87]\n" \
  "s_waitcnt lgkmcnt(2)\n" \
  "v_mfma_f32_16x16x32_bf16 v[56:59], v[88:91], a[132:135], v[56:59]\n" \
  "ds_read_b128 v[104:107], %[hA] offset:5632\n" \
  "buffer_load_dwordx4 v[152:155], %[la], %[X], s60 offen offset:1024 sc1\n" \
  "v_mfma_f32_16x16x32_bf16 v[60:63], v[88:91], a[140:143], v[60:63]\n" \
  "ds_read_b128 v[108:111], %[hA] offset:24064\n" \
  "v_mfma_f32_16x16x32_bf16 v[64:67], v[88:91], a[148:151], v[64:67]\n" \
  "v_mfma_f32_16x16x32_bf16 v[68:71], v[88:91], a[156:159], v[68:71]\n" \
  "v_mfma_f32_16x16x32_bf16 v[72:75], v[92:95], a[132:135], v[72:75]\n" \
  "v_mfma_f32_16x16x32_bf16 v[76:79], v[92:95], a[140:143], v[76:79]\n" \
  "buffer_load_dwordx4 v[156:159], %[la], %[X], s60 offen offset:2048 sc1\n" \
  "v_mfma_f32_16x16x32_bf16 v[80:83], v[92:95], a[148:151], v[80:83]\n" \
  "v_mfma_f32_16x16x32_bf16 v[84:87], v[92:95], a[156:159], v[84:87]\n" \
  "s_waitcnt lgkmcnt(2)\n" \
  "v_mfma_f32_16x16x32_bf16 v[56:59], v[96:99], a[160:163], v[56:59]\n" \
  "ds_read_b128 v[88:91], %[hA] offset:6144\n" \
  "v_mfma_f32_16x16x32_bf16 v[60:63], v[96:99], a[168:171], v[60:63]\n" \
  "ds_read_b128 v[92:95], %[hA] offset:24576\n" \
  "buffer_load_dwordx4 v[160:163], %[la], %[X], s60 offen offset:3072 sc1\n" \
  "v_mfma_f32_16x16x32_bf16 v[64:67], v[96:99], a[176:179], v[64:67]\n" \
  "v_mfma_f32_16x16x32_bf16 v[68:71], v[96:99], a[184:187], v[68:71]\n" \
  "v_mfma_f32_16x16x32_bf16 v[72:75], v[100:103], a[160:163], v[72:75]\n" \
  "v_mfma_f32_16x16x32_bf16 v[76:79], v[100:103], a[168:171], v[76:79]\n" \
  "v_mfma_f32_16x16x32_bf16 v[80:83], v[100:103], a[176:179], v[80:83]\n" \
  "buffer_load_dwordx4 v[164:167], v250, %[X], s60 offen offset:0 sc1\n" \
  "v_mfma_f32_16x16x32_bf16 v[84:87], v[100:103], a[184:187], v[84:87]\n" \
  "s_waitcnt lgkmcnt(2)\n" \
  "v_mfma_f32_16x16x32_bf16 v[56:59], v[104:107], a[164:167], v[56:59]\n" \
  "ds_read_b128 v[96:99], %[hA] offset:6656\n" \
  "v_mfma_f32_16x16x32_bf16 v[60:63], v[104:107], a[172:175], v[60:63]\n" \
  "ds_read_b128 v[100:103], %[hA] offset:25088\n" \
  "v_mfma_f32_16x16x32_bf16 v[64:67], v[104:107], a[180:183], v[64:67]\n" \
  "buffer_load_dwordx4 v[168:171], v250, %[X], s60 offen offset:1024 sc1\n" \
  "v_mfma_f32_16x16x32_bf16 v[68:71], v[104:107], a[188:191], v[68:71]\n" \
  "v_mfma_f32_16x16x32_bf16 v[72:75], v[108:111], a[164:167], v[72:75]\n" \
  "v_mfma_f32_16x16x32_bf16 v[76:79], v[108:111], a[172:175], v[76:79]\n" \
  "v_mfma_f32_16x16x32_bf16 v[80:83], v[108:111], a[180:183], v[80:83]\n" \
  "v_mfma_f32_16x16x32_bf16 v[84:87], v[108:111], a[188:191], v[84:87]\n" \
  "buffer_load_dwordx4 v[172:175], v250, %[X], s60 offen offset:2048 sc1\n" \
  "s_waitcnt lgkmcnt(2)\n" \
  "v_mfma_f32_16x16x32_bf16 v[56:59], v[88:91], a[192:195], v[56:59]\n" \
  "ds_read_b128 v[104:107], %[hA] offset:7168\n" \
  "v_mfma_f32_16x16x32_bf16 v[60:63], v[88:91], a[200:203], v[60:63]\n" \
  "ds_read_b128 v[108:111], %[hA] offset:25600\n" \
  "v_mfma_f32_16x16x32_bf16 v[64:67], v[88:91], a[208:211], v[64:67]\n" \
  "v_mfma_f32_16x16x32_bf16 v[68:71], v[88:91], a[216:219], v[68:71]\n" \
  "buffer_load_dwordx4 v[176:179], v250, %[X], s60 offen offset:3072 sc1\n" \
  "v_mfma_f32_16x16x32_bf16 v[72:75], v[92:95], a[192:195], v[72:75]\n" \
  "v_mfma_f32_16x16x32_bf16 v[76:79], v[92:95], a[200:203], v[76:79]\n" \
  "v_mfma_f32_16x16x32_bf16 v[80:83], v[92:95], a[208:211], v[80:83]\n" \
  "s_waitcnt vmcnt(9)\n" \
  "s_barrier\n" \
  "v_mfma_f32_16x16x32_bf16 v[84:87], v[92:95], a[216:219], v[84:87]\n" \
  "ds_read_b128 v[128:131], %[mA] offset:0\n" \
  "s_waitcnt lgkmcnt(3)\n" \
  "v_mfma_f32_16x16x32_bf16 v[56:59], v[96:99], a[196:199], v[56:59]\n" \
  "ds_read_b128 v[88:91], %[hA] offset:7680\n" \
  "buffer_load_dwordx4 v[180:183], %[la], %[X], s61 offen offset:0 sc1\n" \
  "ds_read_b128 v[132:135], %[mA] offset:1024\n" \
  "v_mfma_f32_16x16x32_bf16 v[60:63], v[96:99], a[204:207], v[60:63]\n" \
  "ds_read_b128 v[92:95], %[hA] offset:26112\n" \
  "v_mfma_f32_16x16x32_bf16 v[64:67], v[96:99], a[212:215], v[64:67]\n" \
  "ds_read_b128 v[136:139], %[mA] offset:2048\n" \
  "v_mfma_f32_16x16x32_bf16 v[68:71], v[96:99], a[220:223], v[68:71]\n" \
  "ds_read_b128 v[140:143], %[mA] offset:3072\n" \
  "v_mfma_f32_16x16x32_bf16 v[72:75], v[100:103], a[196:199], v[72:75]\n" \
  "buffer_load_dwordx4 v[184:187], %[la], %[X], s61 offen offset:1024 sc1\n" \
  "s_waitcnt lgkmcnt(5)\n" \
  "v_smfmac_f32_16x16x64_bf16 v[112:115], v[128:131], a[0:7], %[idx]\n" \
  "ds_read_b128 v[144:147], %[mA] offset:4096\n" \
  "v_smfmac_f32_16x16x64_bf16 v[116:119], v[128:131], a[8:15], %[idx]\n" \
  "s_waitcnt vmcnt(10)\n" \
  "v_readfirstlane_b32 s71, v248\n" \
  "v_smfmac_f32_16x16x64_bf16 v[120:123], v[128:131], a[16:23], %[idx]\n" \
  "s_waitcnt vmcnt(9)\n" \
  "v_bitop3_b32 v240, v148, v149, s62 bitop3:0x7e\n" \
  "v_smfmac_f32_16x16x64_bf16 v[124:127], v[128:131], a[24:31], %[idx]\n" \
  "s_waitcnt lgkmcnt(4)\n" \
  "v_smfmac_f32_16x16x64_bf16 v[112:115], v[132:135], a[32:39], %[idx]\n" \
  "ds_read_b128 v[128:131], %[mA] offset:5120\n" \
  "buffer_load_dwordx4 v[188:191], %[la], %[X], s61 offen offset:2048 sc1\n" \
  "v_smfmac_f32_16x16x64_bf16 v[116:119], v[132:135], a[40:47], %[idx]\n" \
  "v_bitop3_b32 v241, v150, v151, s62 bitop3:0x7e\n" \
  "v_smfmac_f32_16x16x64_bf16 v[120:123], v[132:135], a[48:55], %[idx]\n" \
  "s_waitcnt vmcnt(9)\n" \
  "v_bitop3_b32 v242, v152, v153, s62 bitop3:0x7e\n" \
  "v_smfmac_f32_16x16x64_bf16 v[124:127], v[132:135], a[56:63], %[idx]\n" \
  "s_waitcnt lgkmcnt(3)\n" \
  "v_smfmac_f32_16x16x64_bf16 v[112:115], v[136:139], a[64:71], %[idx]\n" \
  "ds_read_b128 v[132:135], %[mA] offset:6144\n" \
  "buffer_load_dwordx4 v[192:195], %[la], %[X], s61 offen offset:3072 sc1\n" \
  "v_smfmac_f32_16x16x64_bf16 v[116:119], v[136:139], a[72:79], %[idx]\n" \
  "v_or3_b32 v240, v240, v241, v242\n" \
  "v_smfmac_f32_16x16x64_bf16 v[120:123], v[136:139], a[80:87], %[idx]\n" \
  "v_bitop3_b32 v241, v154, v155, s62 bitop3:0x7e\n" \
  "v_smfmac_f32_16x16x64_bf16 v[124:127], v[136:139], a[88:95], %[idx]\n" \
  "s_waitcnt lgkmcnt(3)\n" \
  "v_smfmac_f32_16x16x64_bf16 v[112:115], v[140:143], a[96:103], %[idx]\n" \
  "ds_read_b128 v[136:139], %[mA] offset:7168\n" \
  "v_smfmac_f32_16x16x64_bf16 v[116:119], v[140:143], a[104:111], %[idx]\n" \
  "buffer_load_dwordx4 v[196:199], v250, %[X], s61 offen offset:0 sc1\n" \
  "v_smfmac_f32_16x16x64_bf16 v[120:123], v[140:143], a[112:119], %[idx]\n" \
  "s_waitcnt vmcnt(10)\n" \
  "v_bitop3_b32 v242, v156, v157, s62 bitop3:0x7e\n" \
  "v_smfmac_f32_16x16x64_bf16 v[124:127], v[140:143], a[120:127], %[idx]\n" \
  "s_waitcnt lgkmcnt(3)\n" \
  "v_smfmac_f32_16x16x64_bf16 v[112:115], v[144:147], a[128:135], %[idx]\n" \
  "ds_read_b128 v[140:143], %[mA8] offset:0\n" \
  "v_or3_b32 v240, v240, v241, v242\n" \
  "v_smfmac_f32_16x16x64_bf16 v[116:119], v[144:147], a[136:143], %[idx]\n" \
  "buffer_load_dwordx4 v[200:203], v250, %[X], s61 offen offset:1024 sc1\n" \
  "v_bitop3_b32 v241, v158, v159, s62 bitop3:0x7e\n" \
  "v_smfmac_f32_16x16x64_bf16 v[120:123], v[144:147], a[144:151], %[idx]\n" \
  "s_waitcnt vmcnt(10)\n" \
  "v_bitop3_b32 v242, v160, v161, s62 bitop3:0x7e\n" \
  "v_smfmac_f32_16x16x64_bf16 v[124:127], v[144:147], a[152:159], %[idx]\n" \
  "s_waitcnt lgkmcnt(3)\n" \
  "v_smfmac_f32_16x16x64_bf16 v[112:115], v[128:131], a[160:167], %[idx]\n" \
  "v_or3_b32 v240, v240, v241, v242\n" \
  "v_smfmac_f32_16x16x64_bf16 v[116:119], v[128:131], a[168:175], %[idx]\n" \
  "v_bitop3_b32 v241, v162, v163, s62 bitop3:0x7e\n" \
  "v_smfmac_f32_16x16x64_bf16 v[120:123], v[128:131], a[176:183], %[idx]\n" \
  "buffer_load_dwordx4 v[204:207], v250, %[X], s61 offen offset:2048 sc1\n" \
  "v_smfmac_f32_16x16x64_bf16 v[124:127], v[128:131], a[184:191], %[idx]\n" \
  "s_waitcnt lgkmcnt(2)\n" \
  "v_smfmac_f32_16x16x64_bf16 v[112:115], v[132:135], a[192:199], %[idx]\n" \
  "s_waitcnt vmcnt(9)\n" \
  "v_bitop3_b32 v242, v164, v168, s62 bitop3:0x7e\n" \
  "v_smfmac_f32_16x16x64_bf16 v[116:119], v[132:135], a[200:207], %[idx]\n" \
  "v_or3_b32 v240, v240, v241, v242\n" \
  "v_smfmac_f32_16x16x64_bf16 v[120:123], v[132:135], a[208:215], %[idx]\n" \
  "buffer_load_dwordx4 v[208:211], v250, %[X], s61 offen offset:3072 sc1\n" \
  "v_smfmac_f32_16x16x64_bf16 v[124:127], v[132:135], a[216:223], %[idx]\n" \
  "s_waitcnt lgkmcnt(1)\n" \
  "v_smfmac_f32_16x16x64_bf16 v[112:115], v[136:139], a[224:231], %[idx]\n" \
  "s_waitcnt vmcnt(8)\n" \
  "v_bitop3_b32 v241, v172, v176, s62 bitop3:0x7e\n" \
  "v_smfmac_f32_16x16x64_bf16 v[116:119], v[136:139], a[232:239], %[idx]\n" \
  "v_smfmac_f32_16x16x64_bf16 v[120:123], v[136:139], a[240:247], %[idx]\n" \
  "v_smfmac_f32_16x16x64_bf16 v[124:127], v[136:139], a[248:255], %[idx]\n" \
  "s_waitcnt lgkmcnt(0)\n" \
  "v_smfmac_f32_16x16x64_bf16 v[112:115], v[140:143], v[24:31], %[idx]\n" \
  "v_smfmac_f32_16x16x64_bf16 v[116:119], v[140:143], v[32:39], %[idx]\n" \
  "v_smfmac_f32_16x16x64_bf16 v[120:123], v[140:143], v[40:47], %[idx]\n" \
  "s_nop 1\n" \
  "v_smfmac_f32_16x16x64_bf16 v[124:127], v[140:143], v[48:55], %[idx]\n" \
  "s_waitcnt vmcnt(6)\n" \
  "v_bitop3_b32 v242, v180, v184, s62 bitop3:0x7e\n" \
  "v_mfma_f32_16x16x32_bf16 v[76:79], v[100:103], a[204:207], v[76:79]\n" \
  "v_add_f32 v112, v112, v114\n" \
  "v_add_f32 v113, v113, v115\n" \
  "v_mfma_f32_16x16x32_bf16 v[80:83], v[100:103], a[212:215], v[80:83]\n" \
  "v_cmp_u_f32 vcc, v112, v113\n" \
  "v_add_f32 v116, v116, v118\n" \
  "v_mfma_f32_16x16x32_bf16 v[84:87], v[100:103], a[220:223], v[84:87]\n" \
  "v_add_f32 v117, v117, v119\n" \
  "v_mfma_f32_16x16x32_bf16 v[56:59], v[104:107], a[224:227], v[56:59]\n" \
  "ds_read_b128 v[96:99], %[hA] offset:8192\n" \
  "v_add_f32 v120, v120, v122\n" \
  "v_mfma_f32_16x16x32_bf16 v[60:63], v[104:107], a[232:235], v[60:63]\n" \
  "ds_read_b128 v[100:103], %[hA] offset:26624\n" \
  "v_add_f32 v121, v121, v123\n" \
  "v_mfma_f32_16x16x32_bf16 v[64:67], v[104:107], a[240:243], v[64:67]\n" \
  "s_cbranch_vccnz .Lv_sm\n" \
  ".Lv_sm_back:\n" \
  "v_mfma_f32_16x16x32_bf16 v[68:71], v[104:107], a[248:251], v[68:71]\n" \
  "v_add_f32 v124, v124, v126\n" \
  "v_add_f32 v125, v125, v127\n" \
  "v_mfma_f32_16x16x32_bf16 v[72:75], v[108:111], a[224:227], v[72:75]\n" \
  "v_cvt_pk_bf16_f32 v244, v112, v113\n" \
  "v_cvt_pk_bf16_f32 v246, v120, v121\n" \
  "v_mfma_f32_16x16x32_bf16 v[76:79], v[108:111], a[232:235], v[76:79]\n" \
  "v_cvt_pk_bf16_f32 v245, v116, v117\n" \
  "v_cvt_pk_bf16_f32 v247, v124, v125\n" \
  "v_mfma_f32_16x16x32_bf16 v[80:83], v[108:111], a[240:243], v[80:83]\n" \
  "v_permlane32_swap_b32 v244, v246\n" \
  "v_mfma_f32_16x16x32_bf16 v[84:87], v[108:111], a[248:251], v[84:87]\n" \
  "v_or3_b32 v240, v240, v241, v242\n" \
  "s_waitcnt vmcnt(5)\n" \
  "v_mfma_f32_16x16x32_bf16 v[112:115], v[236:239], v[148:151], 0\n" \
  "s_memrealtime s[74:75]\n" \
  "v_permlane32_swap_b32 v245, v247\n" \
  "v_mfma_f32_16x16x32_bf16 v[116:119], v[236:239], v[152:155], 0\n" \
  "v_mfma_f32_16x16x32_bf16 v[120:123], v[236:239], v[156:159], 0\n" \
  "v_permlane16_swap_b32 v244, v245\n" \
  "v_mfma_f32_16x16x32_bf16 v[124:127], v[236:239], v[160:163], 0\n" \
  "v_permlane16_swap_b32 v246, v247\n" \
  "v_mfma_f32_16x16x32_bf16 v[120:123], v[212:215], v[164:167], v[120:123]\n" \
  "s_waitcnt lgkmcnt(0)\n" \
  "s_sub_i32 s91, s74, s71\n" \
  "s_cmp_gt_u32 s91, 14\n" \
  "v_mfma_f32_16x16x32_bf16 v[124:127], v[212:215], v[168:171], v[124:127]\n" \
  "s_cbranch_scc1 .Lv_late\n" \
  ".Lv_al_ok:\n" \
  "v_mfma_f32_16x16x32_bf16 v[120:123], v[216:219], v[172:175], v[120:123]\n" \
  "buffer_load_dwordx4 v[128:131], %[la], %[X], s65 offen offset:0 sc1\n" \
  "s_sub_i32 s90, s74, s89\n" \
  "v_mfma_f32_16x16x32_bf16 v[124:127], v[216:219], v[176:179], v[124:127]\n" \
  "s_max_i32 s92, s91, -2\n" \
  "s_add_i32 s89, s71, s92\n" \
  "s_ashr_i32 s92, s92, 1\n" \
  "v_mfma_f32_16x16x32_bf16 v[120:123], v[220:223], v[180:183], v[120:123]\n" \
  "s_add_i32 s90, s90, s92\n" \
  "s_mul_i32 s93, s56, 130\n" \
  "s_and_b32 s93, s93, 0xfc\n" \
  "v_mfma_f32_16x16x32_bf16 v[124:127], v[220:223], v[184:187], v[124:127]\n" \
  "buffer_load_dwordx4 v[132:135], %[la], %[X], s65 offen offset:1024 sc1\n" \
  "v_mov_b32 v249, s90\n" \
  "v_mfma_f32_16x16x32_bf16 v[120:123], v[224:227], v[188:191], v[120:123]\n" \
  "s_waitcnt vmcnt(6)\n" \
  "v_mfma_f32_16x16x32_bf16 v[124:127], v[224:227], v[192:195], v[124:127]\n" \
  "v_bitop3_b32 v241, v188, v192, s62 bitop3:0x7e\n" \
  "s_waitcnt vmcnt(5)\n" \
  "v_mfma_f32_16x16x32_bf16 v[112:115], v[228:231], v[196:199], v[112:115]\n" \
  "buffer_load_dwordx4 v[136:139], %[la], %[X], s65 offen offset:2048 sc1\n" \
  "s_waitcnt vmcnt(5)\n" \
  "v_mfma_f32_16x16x32_bf16 v[116:119], v[228:231], v[200:203], v[116:119]\n" \
  "v_bitop3_b32 v242, v196, v198, s62 bitop3:0x7e\n" \
  "s_waitcnt vmcnt(4)\n" \
  "v_mfma_f32_16x16x32_bf16 v[112:115], v[232:235], v[204:207], v[112:115]\n" \
  "v_or3_b32 v240, v240, v241, v242\n" \
  "s_waitcnt vmcnt(3)\n" \
  "v_mfma_f32_16x16x32_bf16 v[116:119], v[232:235], v[208:211], v[116:119]\n" \
  "buffer_load_dwordx4 v[140:143], %[la], %[X], s65 offen offset:3072 sc1\n" \
  "v_mfma_f32_16x16x32_bf16 v[56:59], v[88:91], a[228:231], v[56:59]\n" \
  "ds_read_b128 v[104:107], %[hA] offset:8704\n" \
  "v_bitop3_b32 v241, v200, v202, s62 bitop3:0x7e\n" \
  "v_mfma_f32_16x16x32_bf16 v[72:75], v[92:95], a[228:231], v[72:75]\n" \
  "ds_read_b128 v[108:111], %[hA] offset:27136\n" \
  "v_mfma_f32_16x16x32_bf16 v[56:59], v[96:99], v[24:27], v[56:59]\n" \
  "buffer_load_dwordx4 v[144:147], v250, %[X], s65 offen offset:0 sc1\n" \
  "v_bitop3_b32 v242, v204, v206, s62 bitop3:0x7e\n" \
  "v_mfma_f32_16x16x32_bf16 v[72:75], v[100:103], v[24:27], v[72:75]\n" \
  "v_or3_b32 v240, v240, v241, v242\n" \
  "v_bitop3_b32 v240, v240, v208, s62 bitop3:0xf6\n" \
  "v_mfma_f32_16x16x32_bf16 v[60:63], v[88:91], a[236:239], v[60:63]\n" \
  "v_bitop3_b32 v240, v240, v210, s62 bitop3:0xf6\n" \
  "v_cmp_gt_i32 vcc, 0, v240\n" \
  "v_mfma_f32_16x16x32_bf16 v[76:79], v[92:95], a[236:239], v[76:79]\n" \
  "v_cndmask_b32 v120, v124, v120, s[72:73]\n" \
  "v_cndmask_b32 v121, v125, v121, s[72:73]\n" \
  "v_mfma_f32_16x16x32_bf16 v[60:63], v[96:99], v[32:35], v[60:63]\n" \
  "v_cndmask_b32 v122, v126, v122, s[72:73]\n" \
  "v_cndmask_b32 v123, v127, v123, s[72:73]\n" \
  "v_mfma_f32_16x16x32_bf16 v[76:79], v[100:103], v[32:35], v[76:79]\n" \
  "v_xor_b32 %[hA], 0x10000, %[hA]\n" \
  "s_waitcnt lgkmcnt(0)\n" \
  "v_mfma_f32_16x16x32_bf16 v[56:59], v[104:107], v[28:31], v[56:59]\n" \
  "s_cbranch_vccnz .Lv_sp\n" \
  ".Lv_sp_back:\n" \
  "v_mfma_f32_16x16x32_bf16 v[72:75], v[108:111], v[28:31], v[72:75]\n" \
  "ds_write_b128 %[rtW], v[120:123] offset:1024\n" \
  "v_mfma_f32_16x16x32_bf16 v[60:63], v[104:107], v[36:39], v[60:63]\n" \
  "v_cndmask_b32 v112, v116, v112, s[72:73]\n" \
  "v_cndmask_b32 v113, v117, v113, s[72:73]\n" \
  "v_mfma_f32_16x16x32_bf16 v[76:79], v[108:111], v[36:39], v[76:79]\n" \
  "v_cndmask_b32 v114, v118, v114, s[72:73]\n" \
  "v_cndmask_b32 v115, v119, v115, s[72:73]\n" \
  "v_mfma_f32_16x16x32_bf16 v[64:67], v[88:91], a[244:247], v[64:67]\n" \
  "buffer_atomic_umax v249, %[rpo], %[X], s93 offen sc1\n" \
  "buffer_store_dword v251, %[lm], %[X], s64 offen sc1\n" \
  "ds_read_b128 v[196:199], %[hA] offset:0\n" \
  "v_mfma_f32_16x16x32_bf16 v[80:83], v[92:95], a[244:247], v[80:83]\n" \
  "ds_write_b128 %[rtW], v[112:115] offset:0\n" \
  "v_mfma_f32_16x16x32_bf16 v[64:67], v[96:99], v[40:43], v[64:67]\n" \
  "ds_read_b128 v[200:203], %[hA] offset:18432\n" \
  "v_cvt_pk_bf16_f32 v212, v56, v57\n" \
  "v_mfma_f32_16x16x32_bf16 v[80:83], v[100:103], v[40:43], v[80:83]\n" \
  "ds_read_b128 v[204:207], %[hA] offset:512\n" \
  "v_cvt_pk_bf16_f32 v213, v58, v59\n" \
  "v_mfma_f32_16x16x32_bf16 v[64:67], v[104:107], v[44:47], v[64:67]\n" \
  "ds_read_b128 v[208:211], %[hA] offset:18944\n" \
  "v_cvt_pk_bf16_f32 v214, v72, v73\n" \
  "v_mfma_f32_16x16x32_bf16 v[80:83], v[108:111], v[44:47], v[80:83]\n" \
  "v_cvt_pk_bf16_f32 v215, v74, v75\n" \
  "v_cvt_pk_bf16_f32 v216, v60, v61\n" \
  "v_mfma_f32_16x16x32_bf16 v[68:71], v[88:91], a[252:255], v[68:71]\n" \
  "v_cvt_pk_bf16_f32 v217, v62, v63\n" \
  "v_cvt_pk_bf16_f32 v218, v76, v77\n" \
  "v_mfma_f32_16x16x32_bf16 v[84:87], v[92:95], a[252:255], v[84:87]\n" \
  "v_cvt_pk_bf16_f32 v219, v78, v79\n" \
  "v_mfma_f32_16x16x32_bf16 v[68:71], v[96:99], v[48:51], v[68:71]\n" \
  "v_mfma_f32_16x16x32_bf16 v[84:87], v[100:103], v[48:51], v[84:87]\n" \
  "v_cvt_pk_bf16_f32 v220, v64, v65\n" \
  "v_cvt_pk_bf16_f32 v221, v66, v67\n" \
  "v_mfma_f32_16x16x32_bf16 v[68:71], v[104:107], v[52:55], v[68:71]\n" \
  "v_cvt_pk_bf16_f32 v222, v80, v81\n" \
  "v_cvt_pk_bf16_f32 v223, v82, v83\n" \
  "v_mfma_f32_16x16x32_bf16 v[84:87], v[108:111], v[52:55], v[84:87]\n" \
  "s_waitcnt vmcnt(6)\n" \
  "v_mfma_f32_16x16x32_bf16 v[240:243], v[212:215], v[128:131], 0\n" \
  "s_waitcnt lgkmcnt(0)\n" \
  "s_nop 0\n" \
  "s_waitcnt vmcnt(5)\n" \
  "v_mfma_f32_16x16x32_bf16 v[240:243], v[216:219], v[132:135], v[240:243]\n" \
  "v_bitop3_b32 v251, v128, v132, s66 bitop3:0x7e\n" \
  "s_waitcnt vmcnt(4)\n" \
  "v_mfma_f32_16x16x32_bf16 v[240:243], v[220:223], v[136:139], v[240:243]\n" \
  "v_bitop3_b32 v251, v251, v136, s66 bitop3:0xf6\n" \
  "v_cvt_pk_bf16_f32 v224, v68, v69\n" \
  "v_cvt_pk_bf16_f32 v225, v70, v71\n" \
  "v_cvt_pk_bf16_f32 v226, v84, v85\n" \
  "v_cvt_pk_bf16_f32 v227, v86, v87\n" \
  "s_waitcnt vmcnt(3)\n" \
  "v_bitop3_b32 v251, v251, v140, s66 bitop3:0xf6\n" \
  "s_nop 0\n" \
  "v_mfma_f32_16x16x32_bf16 v[240:243], v[224:227], v[140:143], v[240:243]\n" \
  "s_waitcnt vmcnt(2)\n" \
  "v_mfma_f32_16x16x32_bf16 v[240:243], v[244:247], v[144:147], v[240:243]\n" \
  "v_bitop3_b32 v252, v144, v145, s66 bitop3:0x7e\n" \
  "v_bitop3_b32 v253, v146, v147, s66 bitop3:0x7e\n" \
  "v_or3_b32 v251, v251, v252, v253\n" \
  "v_ashrrev_i32 v252, 31, v251\n" \
  "v_mfma_f32_16x16x32_bf16 v[56:59], v[196:199], a[0:3], 0\n" \
  "s_nop 2\n" \
  "ds_write_b128 %[rmW], v[240:243]\n" \
  "v_cmp_u_f32 vcc, v240, v252\n" \
  "v_mfma_f32_16x16x32_bf16 v[60:63], v[196:199], a[8:11], 0\n" \
  "v_mfma_f32_16x16x32_bf16 v[64:67], v[196:199], a[16:19], 0\n" \
  "s_cbranch_vccnz .Lv_sq\n" \
  ".Lv_sq_back:\n" \
  "s_waitcnt lgkmcnt(0)\n" \
  "s_barrier\n" \
  "ds_read2st64_b64 v[136:139], %[rmA] offset0:0 offset1:2\n" \
  "ds_read2st64_b64 v[140:143], %[rmA] offset0:4 offset1:6\n" \
  "v_mfma_f32_16x16x32_bf16 v[68:71], v[196:199], a[24:27], 0\n" \
  "v_mfma_f32_16x16x32_bf16 v[72:75], v[200:203], a[0:3], 0\n" \
  "v_mfma_f32_16x16x32_bf16 v[76:79], v[200:203], a[8:11], 0\n" \
  "v_mfma_f32_16x16x32_bf16 v[80:83], v[200:203], a[16:19], 0\n" \
  "v_mfma_f32_16x16x32_bf16 v[84:87], v[200:203], a[24:27], 0\n" \
  "s_cmp_lg_u32 s56, s81\n" \
  "s_waitcnt lgkmcnt(0)\n" \
  "v_pk_add_f32 v[136:137], v[136:137], v[138:139]\n" \
  "v_pk_add_f32 v[140:141], v[140:141], v[142:143]\n" \
  "v_pk_add_f32 v[136:137], v[136:137], v[140:141]\n" \
  "v_mfma_f32_16x16x32_bf16 v[56:59], v[204:207], a[4:7], v[56:59]\n" \
  "s_nop 0\n" \
  "v_add_f32_dpp v136, v136, v136 quad_perm:[1,0,3,2] row_mask:0xf bank_mask:0xf\n" \
  "v_add_f32_dpp v137, v137, v137 quad_perm:[1,0,3,2] row_mask:0xf bank_mask:0xf\n" \
  "v_cvt_pk_bf16_f32 v136, v136, v137\n" \
  "v_xor_b32 v136, s66, v136\n" \
  "v_min_u32 v136, -2, v136\n" \
  "buffer_store_dword v136, %[lm], %[X], s67 offen sc1\n" \
  "v_mfma_f32_16x16x32_bf16 v[60:63], v[204:207], a[12:15], v[60:63]\n" \
  "s_cbranch_scc1 .Lv_top\n" \
  "s_cmp_eq_u32 s56, -1\n" \
  "s_cbranch_scc1 .Lv_entry\n" \
  "ds_read2st64_b64 v[128:131], %[rtA] offset0:0 offset1:4\n" \
  "ds_read2st64_b64 v[132:135], %[rtA] offset0:8 offset1:12\n" \
  "s_waitcnt lgkmcnt(0)\n" \
  "v_pk_add_f32 v[128:129], v[128:129], v[130:131]\n" \
  "v_pk_add_f32 v[132:133], v[132:133], v[134:135]\n" \
  "v_pk_add_f32 v[128:129], v[128:129], v[132:133]\n" \
  "s_nop 1\n" \
  "v_add_f32_dpp v128, v128, v128 quad_perm:[1,0,3,2] row_mask:0xf bank_mask:0xf\n" \
  "v_add_f32_dpp v129, v129, v129 quad_perm:[1,0,3,2] row_mask:0xf bank_mask:0xf\n" \
  "v_cvt_pk_bf16_f32 v128, v128, v129\n" \
  "v_xor_b32 v128, s62, v128\n" \
  "v_min_u32 v128, -2, v128\n" \
  "buffer_store_dword v128, %[lo], %[OS], s63 offen sc1\n" \
  "v_mov_b64 v[228:229], v[212:213]\n" \
  "v_mov_b64 v[230:231], v[216:217]\n" \
  "v_mov_b64 v[232:233], v[220:221]\n" \
  "v_mov_b64 v[234:235], v[224:225]\n" \
  "v_mov_b64 v[236:237], v[244:245]\n" \
  "v_mov_b64 v[238:239], v[246:247]\n" \
  "s_mov_b32 s62, s66\n" \
  "s_add_i32 s60, %[pt0], 0x40000\n" \
  "s_add_i32 s61, s60, 0x4000\n" \
  "s_add_i32 s63, %[hl0], 2322432\n" \
  "s_mov_b32 s70, 0\n" \
  ".Lv_fin:\n" \
  "buffer_load_dwordx4 v[148:151], %[la], %[X], s60 offen offset:0 sc1\n" \
  "buffer_load_dwordx4 v[152:155], %[la], %[X], s60 offen offset:1024 sc1\n" \
  "buffer_load_dwordx4 v[156:159], %[la], %[X], s60 offen offset:2048 sc1\n" \
  "buffer_load_dwordx4 v[160:163], %[la], %[X], s60 offen offset:3072 sc1\n" \
  "buffer_load_dwordx4 v[164:167], v250, %[X], s60 offen offset:0 sc1\n" \
  "buffer_load_dwordx4 v[168:171], v250, %[X], s60 offen offset:1024 sc1\n" \
  "buffer_load_dwordx4 v[172:175], v250, %[X], s60 offen offset:2048 sc1\n" \
  "buffer_load_dwordx4 v[176:179], v250, %[X], s60 offen offset:3072 sc1\n" \
  "buffer_load_dwordx4 v[180:183], %[la], %[X], s61 offen offset:0 sc1\n" \
  "buffer_load_dwordx4 v[184:187], %[la], %[X], s61 offen offset:1024 sc1\n" \
  "buffer_load_dwordx4 v[188:191], %[la], %[X], s61 offen offset:2048 sc1\n" \
  "buffer_load_dwordx4 v[192:195], %[la], %[X], s61 offen offset:3072 sc1\n" \
  "buffer_load_dwordx4 v[196:199], v250, %[X], s61 offen offset:0 sc1\n" \
  "buffer_load_dwordx4 v[200:203], v250, %[X], s61 offen offset:1024 sc1\n" \
  "buffer_load_dwordx4 v[204:207], v250, %[X], s61 offen offset:2048 sc1\n" \
  "buffer_load_dwordx4 v[208:211], v250, %[X], s61 offen offset:3072 sc1\n" \
  "s_waitcnt vmcnt(0)\n" \
  "v_bitop3_b32 v240, v148, v149, s62 bitop3:0x7e\n" \
  "v_bitop3_b32 v241, v150, v151, s62 bitop3:0x7e\n" \
  "v_bitop3_b32 v242, v152, v153, s62 bitop3:0x7e\n" \
  "v_or3_b32 v240, v240, v241, v242\n" \
  "v_bitop3_b32 v241, v154, v155, s62 bitop3:0x7e\n" \
  "v_bitop3_b32 v242, v156, v157, s62 bitop3:0x7e\n" \
  "v_or3_b32 v240, v240, v241, v242\n" \
  "v_bitop3_b32 v241, v158, v159, s62 bitop3:0x7e\n" \
  "v_bitop3_b32 v242, v160, v161, s62 bitop3:0x7e\n" \
  "v_or3_b32 v240, v240, v241, v242\n" \
  "v_bitop3_b32 v241, v162, v163, s62 bitop3:0x7e\n" \
  "v_bitop3_b32 v242, v164, v168, s62 bitop3:0x7e\n" \
  "v_or3_b32 v240, v240, v241, v242\n" \
  "v_bitop3_b32 v241, v172, v176, s62 bitop3:0x7e\n" \
  "v_bitop3_b32 v242, v180, v184, s62 bitop3:0x7e\n" \
  "v_or3_b32 v240, v240, v241, v242\n" \
  "v_bitop3_b32 v241, v188, v192, s62 bitop3:0x7e\n" \
  "v_bitop3_b32 v242, v196, v198, s62 bitop3:0x7e\n" \
  "v_or3_b32 v240, v240, v241, v242\n" \
  "v_bitop3_b32 v241, v200, v202, s62 bitop3:0x7e\n" \
  "v_bitop3_b32 v242, v204, v206, s62 bitop3:0x7e\n" \
  "v_or3_b32 v240, v240, v241, v242\n" \
  "v_bitop3_b32 v240, v240, v208, s62 bitop3:0xf6\n" \
  "v_bitop3_b32 v240, v240, v210, s62 bitop3:0xf6\n" \
  "v_cmp_gt_i32 vcc, 0, v240\n" \
  "s_cbranch_vccz .Lv_fin_ok\n" \
  "s_add_i32 s70, s70, 1\n" \
  "s_and_b32 s68, s70, 1023\n" \
  "s_cbranch_scc1 .Lv_fin\n" \
  "buffer_load_dword v253, off, %[F], 0 offset:1024 sc1\n" \
  "s_waitcnt vmcnt(0)\n" \
  "v_readfirstlane_b32 s68, v253\n" \
  "s_cmp_lg_u32 s68, 0\n" \
  "s_cbranch_scc1 .Lv_fin_ok\n" \
  "s_cmp_lt_u32 s70, 400000\n" \
  "s_cbranch_scc1 .Lv_fin\n" \
  "v_mov_b32 v253, 1\n" \
  "buffer_store_dword v253, off, %[F], 0 offset:1024 sc1\n" \
  ".Lv_fin_ok:\n" \
  "v_mfma_f32_16x16x32_bf16 v[112:115], v[236:239], v[148:151], 0\n" \
  "v_mfma_f32_16x16x32_bf16 v[116:119], v[236:239], v[152:155], 0\n" \
  "v_mfma_f32_16x16x32_bf16 v[120:123], v[236:239], v[156:159], 0\n" \
  "v_mfma_f32_16x16x32_bf16 v[124:127], v[236:239], v[160:163], 0\n" \
  "v_mfma_f32_16x16x32_bf16 v[120:123], v[212:215], v[164:167], v[120:123]\n" \
  "v_mfma_f32_16x16x32_bf16 v[124:127], v[212:215], v[168:171], v[124:127]\n" \
  "v_mfma_f32_16x16x32_bf16 v[120:123], v[216:219], v[172:175], v[120:123]\n" \
  "v_mfma_f32_16x16x32_bf16 v[124:127], v[216:219], v[176:179], v[124:127]\n" \
  "v_mfma_f32_16x16x32_bf16 v[120:123], v[220:223], v[180:183], v[120:123]\n" \
  "v_mfma_f32_16x16x32_bf16 v[124:127], v[220:223], v[184:187], v[124:127]\n" \
  "v_mfma_f32_16x16x32_bf16 v[120:123], v[224:227], v[188:191], v[120:123]\n" \
  "v_mfma_f32_16x16x32_bf16 v[124:127], v[224:227], v[192:195], v[124:127]\n" \
  "v_mfma_f32_16x16x32_bf16 v[112:115], v[228:231], v[196:199], v[112:115]\n" \
  "v_mfma_f32_16x16x32_bf16 v[116:119], v[228:231], v[200:203], v[116:119]\n" \
  "v_mfma_f32_16x16x32_bf16 v[112:115], v[232:235], v[204:207], v[112:115]\n" \
  "v_mfma_f32_16x16x32_bf16 v[116:119], v[232:235], v[208:211], v[116:119]\n" \
  "s_nop 7\n" \
  "v_cndmask_b32 v120, v124, v120, s[72:73]\n" \
  "v_cndmask_b32 v121, v125, v121, s[72:73]\n" \
  "v_cndmask_b32 v122, v126, v122, s[72:73]\n" \
  "v_cndmask_b32 v123, v127, v123, s[72:73]\n" \
  "v_cndmask_b32 v112, v116, v112, s[72:73]\n" \
  "v_cndmask_b32 v113, v117, v113, s[72:73]\n" \
  "v_cndmask_b32 v114, v118, v114, s[72:73]\n" \
  "v_cndmask_b32 v115, v119, v115, s[72:73]\n" \
  "s_barrier\n" \
  "ds_write_b128 %[rtW], v[120:123] offset:1024\n" \
  "ds_write_b128 %[rtW], v[112:115] offset:0\n" \
  "s_waitcnt lgkmcnt(0)\n" \
  "s_barrier\n" \
  "ds_read2st64_b64 v[128:131], %[rtA] offset0:0 offset1:4\n" \
  "ds_read2st64_b64 v[132:135], %[rtA] offset0:8 offset1:12\n" \
  "s_waitcnt lgkmcnt(0)\n" \
  "v_pk_add_f32 v[128:129], v[128:129], v[130:131]\n" \
  "v_pk_add_f32 v[132:133], v[132:133], v[134:135]\n" \
  "v_pk_add_f32 v[128:129], v[128:129], v[132:133]\n" \
  "s_nop 1\n" \
  "v_add_f32_dpp v128, v128, v128 quad_perm:[1,0,3,2] row_mask:0xf bank_mask:0xf\n" \
  "v_add_f32_dpp v129, v129, v129 quad_perm:[1,0,3,2] row_mask:0xf bank_mask:0xf\n" \
  "v_cvt_pk_bf16_f32 v128, v128, v129\n" \
  "v_xor_b32 v128, s62, v128\n" \
  "v_min_u32 v128, -2, v128\n" \
  "buffer_store_dword v128, %[lo], %[OS], s63 offen sc1\n" \
  "s_branch .Lv_end\n" \
  ".Lv_sp:\n" \
  "s_cmp_lt_i32 s56, 1\n" \
  "s_cbranch_scc1 .Lv_sp_back\n" \
  "s_nop 7\n" \
  "s_mov_b32 s70, 0\n" \
  ".Lv_spw:\n" \
  "buffer_load_dwordx4 v[148:151], %[la], %[X], s60 offen offset:0 sc1\n" \
  "buffer_load_dwordx4 v[152:155], %[la], %[X], s60 offen offset:1024 sc1\n" \
  "buffer_load_dwordx4 v[156:159], %[la], %[X], s60 offen offset:2048 sc1\n" \
  "buffer_load_dwordx4 v[160:163], %[la], %[X], s60 offen offset:3072 sc1\n" \
  "buffer_load_dwordx4 v[164:167], v250, %[X], s60 offen offset:0 sc1\n" \
  "buffer_load_dwordx4 v[168:171], v250, %[X], s60 offen offset:1024 sc1\n" \
  "buffer_load_dwordx4 v[172:175], v250, %[X], s60 offen offset:2048 sc1\n" \
  "buffer_load_dwordx4 v[176:179], v250, %[X], s60 offen offset:3072 sc1\n" \
  "buffer_load_dwordx4 v[180:183], %[la], %[X], s61 offen offset:0 sc1\n" \
  "buffer_load_dwordx4 v[184:187], %[la], %[X], s61 offen offset:1024 sc1\n" \
  "buffer_load_dwordx4 v[188:191], %[la], %[X], s61 offen offset:2048 sc1\n" \
  "buffer_load_dwordx4 v[192:195], %[la], %[X], s61 offen offset:3072 sc1\n" \
  "buffer_load_dwordx4 v[196:199], v250, %[X], s61 offen offset:0 sc1\n" \
  "buffer_load_dwordx4 v[200:203], v250, %[X], s61 offen offset:1024 sc1\n" \
  "buffer_load_dwordx4 v[204:207], v250, %[X], s61 offen offset:2048 sc1\n" \
  "buffer_load_dwordx4 v[208:211], v250, %[X], s61 offen offset:3072 sc1\n" \
  "s_waitcnt vmcnt(0)\n" \
  "v_bitop3_b32 v240, v148, v149, s62 bitop3:0x7e\n" \
  "v_bitop3_b32 v241, v150, v151, s62 bitop3:0x7e\n" \
  "v_bitop3_b32 v242, v152, v153, s62 bitop3:0x7e\n" \
  "v_or3_b32 v240, v240, v241, v242\n" \
  "v_bitop3_b32 v241, v154, v155, s62 bitop3:0x7e\n" \
  "v_bitop3_b32 v242, v156, v157, s62 bitop3:0x7e\n" \
  "v_or3_b32 v240, v240, v241, v242\n" \
  "v_bitop3_b32 v241, v158, v159, s62 bitop3:0x7e\n" \
  "v_bitop3_b32 v242, v160, v161, s62 bitop3:0x7e\n" \
  "v_or3_b32 v240, v240, v241, v242\n" \
  "v_bitop3_b32 v241, v162, v163, s62 bitop3:0x7e\n" \
  "v_bitop3_b32 v242, v164, v168, s62 bitop3:0x7e\n" \
  "v_or3_b32 v240, v240, v241, v242\n" \
  "v_bitop3_b32 v241, v172, v176, s62 bitop3:0x7e\n" \
  "v_bitop3_b32 v242, v180, v184, s62 bitop3:0x7e\n" \
  "v_or3_b32 v240, v240, v241, v242\n" \
  "v_bitop3_b32 v241, v188, v192, s62 bitop3:0x7e\n" \
  "v_bitop3_b32 v242, v196, v198, s62 bitop3:0x7e\n" \
  "v_or3_b32 v240, v240, v241, v242\n" \
  "v_bitop3_b32 v241, v200, v202, s62 bitop3:0x7e\n" \
  "v_bitop3_b32 v242, v204, v206, s62 bitop3:0x7e\n" \
  "v_or3_b32 v240, v240, v241, v242\n" \
  "v_bitop3_b32 v240, v240, v208, s62 bitop3:0xf6\n" \
  "v_bitop3_b32 v240, v240, v210, s62 bitop3:0xf6\n" \
  "v_cmp_gt_i32 vcc, 0, v240\n" \
  "s_cbranch_vccz .Lv_spw_ok\n" \
  "s_add_i32 s70, s70, 1\n" \
  "s_and_b32 s68, s70, 1023\n" \
  "s_cbranch_scc1 .Lv_spw\n" \
  "buffer_load_dword v253, off, %[F], 0 offset:1024 sc1\n" \
  "s_waitcnt vmcnt(0)\n" \
  "v_readfirstlane_b32 s68, v253\n" \
  "s_cmp_lg_u32 s68, 0\n" \
  "s_cbranch_scc1 .Lv_spw_ok\n" \
  "s_cmp_lt_u32 s70, 400000\n" \
  "s_cbranch_scc1 .Lv_spw\n" \
  "v_mov_b32 v253, 1\n" \
  "buffer_store_dword v253, off, %[F], 0 offset:1024 sc1\n" \
  ".Lv_spw_ok:\n" \
  "v_mfma_f32_16x16x32_bf16 v[112:115], v[236:239], v[148:151], 0\n" \
  "v_mfma_f32_16x16x32_bf16 v[116:119], v[236:239], v[152:155], 0\n" \
  "v_mfma_f32_16x16x32_bf16 v[120:123], v[236:239], v[156:159], 0\n" \
  "v_mfma_f32_16x16x32_bf16 v[124:127], v[236:239], v[160:163], 0\n" \
  "v_mfma_f32_16x16x32_bf16 v[120:123], v[212:215], v[164:167], v[120:123]\n" \
  "v_mfma_f32_16x16x32_bf16 v[124:127], v[212:215], v[168:171], v[124:127]\n" \
  "v_mfma_f32_16x16x32_bf16 v[120:123], v[216:219], v[172:175], v[120:123]\n" \
  "v_mfma_f32_16x16x32_bf16 v[124:127], v[216:219], v[176:179], v[124:127]\n" \
  "v_mfma_f32_16x16x32_bf16 v[120:123], v[220:223], v[180:183], v[120:123]\n" \
  "v_mfma_f32_16x16x32_bf16 v[124:127], v[220:223], v[184:187], v[124:127]\n" \
  "v_mfma_f32_16x16x32_bf16 v[120:123], v[224:227], v[188:191], v[120:123]\n" \
  "v_mfma_f32_16x16x32_bf16 v[124:127], v[224:227], v[192:195], v[124:127]\n" \
  "v_mfma_f32_16x16x32_bf16 v[112:115], v[228:231], v[196:199], v[112:115]\n" \
  "v_mfma_f32_16x16x32_bf16 v[116:119], v[228:231], v[200:203], v[116:119]\n" \
  "v_mfma_f32_16x16x32_bf16 v[112:115], v[232:235], v[204:207], v[112:115]\n" \
  "v_mfma_f32_16x16x32_bf16 v[116:119], v[232:235], v[208:211], v[116:119]\n" \
  "s_nop 7\n" \
  "v_cndmask_b32 v120, v124, v120, s[72:73]\n" \
  "v_cndmask_b32 v121, v125, v121, s[72:73]\n" \
  "v_cndmask_b32 v122, v126, v122, s[72:73]\n" \
  "v_cndmask_b32 v123, v127, v123, s[72:73]\n" \
  "s_branch .Lv_sp_back\n" \
  ".Lv_sq:\n" \
  "s_cmp_lt_i32 s56, 0\n" \
  "s_cbranch_scc1 .Lv_sq_back\n" \
  "v_cmp_gt_i32 vcc, 0, v251\n" \
  "s_cbranch_vccz .Lv_sv\n" \
  "s_mov_b32 s70, 0\n" \
  ".Lv_sqw:\n" \
  "buffer_load_dwordx4 v[128:131], %[la], %[X], s65 offen offset:0 sc1\n" \
  "buffer_load_dwordx4 v[132:135], %[la], %[X], s65 offen offset:1024 sc1\n" \
  "buffer_load_dwordx4 v[136:139], %[la], %[X], s65 offen offset:2048 sc1\n" \
  "buffer_load_dwordx4 v[140:143], %[la], %[X], s65 offen offset:3072 sc1\n" \
  "buffer_load_dwordx4 v[144:147], v250, %[X], s65 offen offset:0 sc1\n" \
  "s_waitcnt vmcnt(4)\n" \
  "v_mfma_f32_16x16x32_bf16 v[240:243], v[212:215], v[128:131], 0\n" \
  "s_waitcnt vmcnt(3)\n" \
  "v_mfma_f32_16x16x32_bf16 v[240:243], v[216:219], v[132:135], v[240:243]\n" \
  "v_bitop3_b32 v251, v128, v132, s66 bitop3:0x7e\n" \
  "s_waitcnt vmcnt(2)\n" \
  "v_mfma_f32_16x16x32_bf16 v[240:243], v[220:223], v[136:139], v[240:243]\n" \
  "v_bitop3_b32 v251, v251, v136, s66 bitop3:0xf6\n" \
  "s_waitcnt vmcnt(1)\n" \
  "v_mfma_f32_16x16x32_bf16 v[240:243], v[224:227], v[140:143], v[240:243]\n" \
  "v_bitop3_b32 v251, v251, v140, s66 bitop3:0xf6\n" \
  "s_waitcnt vmcnt(0)\n" \
  "v_mfma_f32_16x16x32_bf16 v[240:243], v[244:247], v[144:147], v[240:243]\n" \
  "v_bitop3_b32 v252, v144, v145, s66 bitop3:0x7e\n" \
  "v_bitop3_b32 v253, v146, v147, s66 bitop3:0x7e\n" \
  "v_or3_b32 v251, v251, v252, v253\n" \
  "v_cmp_gt_i32 vcc, 0, v251\n" \
  "s_cbranch_vccz .Lv_sqw_ok\n" \
  "s_add_i32 s70, s70, 1\n" \
  "s_and_b32 s68, s70, 1023\n" \
  "s_cbranch_scc1 .Lv_sqw\n" \
  "buffer_load_dword v253, off, %[F], 0 offset:1024 sc1\n" \
  "s_waitcnt vmcnt(0)\n" \
  "v_readfirstlane_b32 s68, v253\n" \
  "s_cmp_lg_u32 s68, 0\n" \
  "s_cbranch_scc1 .Lv_sqw_ok\n" \
  "s_cmp_lt_u32 s70, 400000\n" \
  "s_cbranch_scc1 .Lv_sqw\n" \
  "v_mov_b32 v253, 1\n" \
  "buffer_store_dword v253, off, %[F], 0 offset:1024 sc1\n" \
  ".Lv_sqw_ok:\n" \
  "s_nop 2\n" \
  "ds_write_b128 %[rmW], v[240:243]\n" \
  "v_cmp_u_f32 vcc, v240, v240\n" \
  "s_waitcnt lgkmcnt(0)\n" \
  "s_cbranch_vccz .Lv_sq_back\n" \
  ".Lv_sv:\n" \
  "v_and_b32 v251, 0xffff, %[hA]\n" \
  "s_mul_i32 s76, s56, 36864\n" \
  "s_add_i32 s77, s76, 18432\n" \
  "s_mov_b32 s70, 0\n" \
  ".Lv_shw0:\n" \
  "buffer_load_dwordx4 v[148:151], v251, %[HS], s76 offen offset:0 sc1\n" \
  "buffer_load_dwordx4 v[152:155], v251, %[HS], s77 offen offset:0 sc1\n" \
  "buffer_load_dwordx4 v[156:159], v251, %[HS], s76 offen offset:512 sc1\n" \
  "buffer_load_dwordx4 v[160:163], v251, %[HS], s77 offen offset:512 sc1\n" \
  "buffer_load_dwordx4 v[164:167], v251, %[HS], s76 offen offset:1024 sc1\n" \
  "buffer_load_dwordx4 v[168:171], v251, %[HS], s77 offen offset:1024 sc1\n" \
  "buffer_load_dwordx4 v[172:175], v251, %[HS], s76 offen offset:1536 sc1\n" \
  "buffer_load_dwordx4 v[176:179], v251, %[HS], s77 offen offset:1536 sc1\n" \
  "buffer_load_dwordx4 v[180:183], v251, %[HS], s76 offen offset:2048 sc1\n" \
  "buffer_load_dwordx4 v[184:187], v251, %[HS], s77 offen offset:2048 sc1\n" \
  "buffer_load_dwordx4 v[188:191], v251, %[HS], s76 offen offset:2560 sc1\n" \
  "buffer_load_dwordx4 v[192:195], v251, %[HS], s77 offen offset:2560 sc1\n" \
  "s_waitcnt vmcnt(0)\n" \
  "v_max3_u32 v253, v148, v149, v150\n" \
  "v_max3_u32 v253, v253, v151, v152\n" \
  "v_max3_u32 v253, v253, v153, v154\n" \
  "v_max3_u32 v253, v253, v155, v156\n" \
  "v_max3_u32 v253, v253, v157, v158\n" \
  "v_max3_u32 v253, v253, v159, v160\n" \
  "v_max3_u32 v253, v253, v161, v162\n" \
  "v_max3_u32 v253, v253, v163, v164\n" \
  "v_max3_u32 v253, v253, v165, v166\n" \
  "v_max3_u32 v253, v253, v167, v168\n" \
  "v_max3_u32 v253, v253, v169, v170\n" \
  "v_max3_u32 v253, v253, v171, v172\n" \
  "v_max3_u32 v253, v253, v173, v174\n" \
  "v_max3_u32 v253, v253, v175, v176\n" \
  "v_max3_u32 v253, v253, v177, v178\n" \
  "v_max3_u32 v253, v253, v179, v180\n" \
  "v_max3_u32 v253, v253, v181, v182\n" \
  "v_max3_u32 v253, v253, v183, v184\n" \
  "v_max3_u32 v253, v253, v185, v186\n" \
  "v_max3_u32 v253, v253, v187, v188\n" \
  "v_max3_u32 v253, v253, v189, v190\n" \
  "v_max3_u32 v253, v253, v191, v192\n" \
  "v_max3_u32 v253, v253, v193, v194\n" \
  "v_max_u32 v253, v253, v195\n" \
  "v_cmp_eq_u32 vcc, -1, v253\n" \
  "s_cbranch_vccz .Lv_shw0_ok\n" \
  "s_sleep 4\n" \
  "s_add_i32 s70, s70, 1\n" \
  "s_and_b32 s68, s70, 1023\n" \
  "s_cbranch_scc1 .Lv_shw0\n" \
  "buffer_load_dword v253, off, %[F], 0 offset:1024 sc1\n" \
  "s_waitcnt vmcnt(0)\n" \
  "v_readfirstlane_b32 s68, v253\n" \
  "s_cmp_lg_u32 s68, 0\n" \
  "s_cbranch_scc1 .Lv_shw0_ok\n" \
  "s_cmp_lt_u32 s70, 400000\n" \
  "s_cbranch_scc1 .Lv_shw0\n" \
  "v_mov_b32 v253, 1\n" \
  "buffer_store_dword v253, off, %[F], 0 offset:1024 sc1\n" \
  ".Lv_shw0_ok:\n" \
  "v_mfma_f32_16x16x32_bf16 v[56:59], v[148:151], a[0:3], 0\n" \
  "v_mfma_f32_16x16x32_bf16 v[60:63], v[148:151], a[8:11], 0\n" \
  "v_mfma_f32_16x16x32_bf16 v[64:67], v[148:151], a[16:19], 0\n" \
  "v_mfma_f32_16x16x32_bf16 v[68:71], v[148:151], a[24:27], 0\n" \
  "v_mfma_f32_16x16x32_bf16 v[72:75], v[152:155], a[0:3], 0\n" \
  "v_mfma_f32_16x16x32_bf16 v[76:79], v[152:155], a[8:11], 0\n" \
  "v_mfma_f32_16x16x32_bf16 v[80:83], v[152:155], a[16:19], 0\n" \
  "v_mfma_f32_16x16x32_bf16 v[84:87], v[152:155], a[24:27], 0\n" \
  "v_mfma_f32_16x16x32_bf16 v[56:59], v[156:159], a[4:7], v[56:59]\n" \
  "v_mfma_f32_16x16x32_bf16 v[60:63], v[156:159], a[12:15], v[60:63]\n" \
  "v_mfma_f32_16x16x32_bf16 v[64:67], v[156:159], a[20:23], v[64:67]\n" \
  "v_mfma_f32_16x16x32_bf16 v[68:71], v[156:159], a[28:31], v[68:71]\n" \
  "v_mfma_f32_16x16x32_bf16 v[72:75], v[160:163], a[4:7], v[72:75]\n" \
  "v_mfma_f32_16x16x32_bf16 v[76:79], v[160:163], a[12:15], v[76:79]\n" \
  "v_mfma_f32_16x16x32_bf16 v[80:83], v[160:163], a[20:23], v[80:83]\n" \
  "v_mfma_f32_16x16x32_bf16 v[84:87], v[160:163], a[28:31], v[84:87]\n" \
  "v_mfma_f32_16x16x32_bf16 v[56:59], v[164:167], a[32:35], v[56:59]\n" \
  "v_mfma_f32_16x16x32_bf16 v[60:63], v[164:167], a[40:43], v[60:63]\n" \
  "v_mfma_f32_16x16x32_bf16 v[64:67], v[164:167], a[48:51], v[64:67]\n" \
  "v_mfma_f32_16x16x32_bf16 v[68:71], v[164:167], a[56:59], v[68:71]\n" \
  "v_mfma_f32_16x16x32_bf16 v[72:75], v[168:171], a[32:35], v[72:75]\n" \
  "v_mfma_f32_16x16x32_bf16 v[76:79], v[168:171], a[40:43], v[76:79]\n" \
  "v_mfma_f32_16x16x32_bf16 v[80:83], v[168:171], a[48:51], v[80:83]\n" \
  "v_mfma_f32_16x16x32_bf16 v[84:87], v[168:171], a[56:59], v[84:87]\n" \
  "v_mfma_f32_16x16x32_bf16 v[56:59], v[172:175], a[36:39], v[56:59]\n" \
  "v_mfma_f32_16x16x32_bf16 v[60:63], v[172:175], a[44:47], v[60:63]\n" \
  "v_mfma_f32_16x16x32_bf16 v[64:67], v[172:175], a[52:55], v[64:67]\n" \
  "v_mfma_f32_16x16x32_bf16 v[68:71], v[172:175], a[60:63], v[68:71]\n" \
  "v_mfma_f32_16x16x32_bf16 v[72:75], v[176:179], a[36:39], v[72:75]\n" \
  "v_mfma_f32_16x16x32_bf16 v[76:79], v[176:179], a[44:47], v[76:79]\n" \
  "v_mfma_f32_16x16x32_bf16 v[80:83], v[176:179], a[52:55], v[80:83]\n" \
  "v_mfma_f32_16x16x32_bf16 v[84:87], v[176:179], a[60:63], v[84:87]\n" \
  "v_mfma_f32_16x16x32_bf16 v[56:59], v[180:183], a[64:67], v[56:59]\n" \
  "v_mfma_f32_16x16x32_bf16 v[60:63], v[180:183], a[72:75], v[60:63]\n" \
  "v_mfma_f32_16x16x32_bf16 v[64:67], v[180:183], a[80:83], v[64:67]\n" \
  "v_mfma_f32_16x16x32_bf16 v[68:71], v[180:183], a[88:91], v[68:71]\n" \
  "v_mfma_f32_16x16x32_bf16 v[72:75], v[184:187], a[64:67], v[72:75]\n" \
  "v_mfma_f32_16x16x32_bf16 v[76:79], v[184:187], a[72:75], v[76:79]\n" \
  "v_mfma_f32_16x16x32_bf16 v[80:83], v[184:187], a[80:83], v[80:83]\n" \
  "v_mfma_f32_16x16x32_bf16 v[84:87], v[184:187], a[88:91], v[84:87]\n" \
  "v_mfma_f32_16x16x32_bf16 v[56:59], v[188:191], a[68:71], v[56:59]\n" \
  "v_mfma_f32_16x16x32_bf16 v[60:63], v[188:191], a[76:79], v[60:63]\n" \
  "v_mfma_f32_16x16x32_bf16 v[64:67], v[188:191], a[84:87], v[64:67]\n" \
  "v_mfma_f32_16x16x32_bf16 v[68:71], v[188:191], a[92:95], v[68:71]\n" \
  "v_mfma_f32_16x16x32_bf16 v[72:75], v[192:195], a[68:71], v[72:75]\n" \
  "v_mfma_f32_16x16x32_bf16 v[76:79], v[192:195], a[76:79], v[76:79]\n" \
  "v_mfma_f32_16x16x32_bf16 v[80:83], v[192:195], a[84:87], v[80:83]\n" \
  "v_mfma_f32_16x16x32_bf16 v[84:87], v[192:195], a[92:95], v[84:87]\n" \
  "s_add_i32 s76, s76, 3072\n" \
  "s_add_i32 s77, s77, 3072\n" \
  "s_mov_b32 s70, 0\n" \
  ".Lv_shw1:\n" \
  "buffer_load_dwordx4 v[148:151], v251, %[HS], s76 offen offset:0 sc1\n" \
  "buffer_load_dwordx4 v[152:155], v251, %[HS], s77 offen offset:0 sc1\n" \
  "buffer_load_dwordx4 v[156:159], v251, %[HS], s76 offen offset:512 sc1\n" \
  "buffer_load_dwordx4 v[160:163], v251, %[HS], s77 offen offset:512 sc1\n" \
  "buffer_load_dwordx4 v[164:167], v251, %[HS], s76 offen offset:1024 sc1\n" \
  "buffer_load_dwordx4 v[168:171], v251, %[HS], s77 offen offset:1024 sc1\n" \
  "buffer_load_dwordx4 v[172:175], v251, %[HS], s76 offen offset:1536 sc1\n" \
  "buffer_load_dwordx4 v[176:179], v251, %[HS], s77 offen offset:1536 sc1\n" \
  "buffer_load_dwordx4 v[180:183], v251, %[HS], s76 offen offset:2048 sc1\n" \
  "buffer_load_dwordx4 v[184:187], v251, %[HS], s77 offen offset:2048 sc1\n" \
  "buffer_load_dwordx4 v[188:191], v251, %[HS], s76 offen offset:2560 sc1\n" \
  "buffer_load_dwordx4 v[192:195], v251, %[HS], s77 offen offset:2560 sc1\n" \
  "s_waitcnt vmcnt(0)\n" \
  "v_max3_u32 v253, v148, v149, v150\n" \
  "v_max3_u32 v253, v253, v151, v152\n" \
  "v_max3_u32 v253, v253, v153, v154\n" \
  "v_max3_u32 v253, v253, v155, v156\n" \
  "v_max3_u32 v253, v253, v157, v158\n" \
  "v_max3_u32 v253, v253, v159, v160\n" \
  "v_max3_u32 v253, v253, v161, v162\n" \
  "v_max3_u32 v253, v253, v163, v164\n" \
  "v_max3_u32 v253, v253, v165, v166\n" \
  "v_max3_u32 v253, v253, v167, v168\n" \
  "v_max3_u32 v253, v253, v169, v170\n" \
  "v_max3_u32 v253, v253, v171, v172\n" \
  "v_max3_u32 v253, v253, v173, v174\n" \
  "v_max3_u32 v253, v253, v175, v176\n" \
  "v_max3_u32 v253, v253, v177, v178\n" \
  "v_max3_u32 v253, v253, v179, v180\n" \
  "v_max3_u32 v253, v253, v181, v182\n" \
  "v_max3_u32 v253, v253, v183, v184\n" \
  "v_max3_u32 v253, v253, v185, v186\n" \
  "v_max3_u32 v253, v253, v187, v188\n" \
  "v_max3_u32 v253, v253, v189, v190\n" \
  "v_max3_u32 v253, v253, v191, v192\n" \
  "v_max3_u32 v253, v253, v193, v194\n" \
  "v_max_u32 v253, v253, v195\n" \
  "v_cmp_eq_u32 vcc, -1, v253\n" \
  "s_cbranch_vccz .Lv_shw1_ok\n" \
  "s_sleep 4\n" \
  "s_add_i32 s70, s70, 1\n" \
  "s_and_b32 s68, s70, 1023\n" \
  "s_cbranch_scc1 .Lv_shw1\n" \
  "buffer_load_dword v253, off, %[F], 0 offset:1024 sc1\n" \
  "s_waitcnt vmcnt(0)\n" \
  "v_readfirstlane_b32 s68, v253\n" \
  "s_cmp_lg_u32 s68, 0\n" \
  "s_cbranch_scc1 .Lv_shw1_ok\n" \
  "s_cmp_lt_u32 s70, 400000\n" \
  "s_cbranch_scc1 .Lv_shw1\n" \
  "v_mov_b32 v253, 1\n" \
  "buffer_store_dword v253, off, %[F], 0 offset:1024 sc1\n" \
  ".Lv_shw1_ok:\n" \
  "v_mfma_f32_16x16x32_bf16 v[56:59], v[148:151], a[96:99], v[56:59]\n" \
  "v_mfma_f32_16x16x32_bf16 v[60:63], v[148:151], a[104:107], v[60:63]\n" \
  "v_mfma_f32_16x16x32_bf16 v[64:67], v[148:151], a[112:115], v[64:67]\n" \
  "v_mfma_f32_16x16x32_bf16 v[68:71], v[148:151], a[120:123], v[68:71]\n" \
  "v_mfma_f32_16x16x32_bf16 v[72:75], v[152:155], a[96:99], v[72:75]\n" \
  "v_mfma_f32_16x16x32_bf16 v[76:79], v[152:155], a[104:107], v[76:79]\n" \
  "v_mfma_f32_16x16x32_bf16 v[80:83], v[152:155], a[112:115], v[80:83]\n" \
  "v_mfma_f32_16x16x32_bf16 v[84:87], v[152:155], a[120:123], v[84:87]\n" \
  "v_mfma_f32_16x16x32_bf16 v[56:59], v[156:159], a[100:103], v[56:59]\n" \
  "v_mfma_f32_16x16x32_bf16 v[60:63], v[156:159], a[108:111], v[60:63]\n" \
  "v_mfma_f32_16x16x32_bf16 v[64:67], v[156:159], a[116:119], v[64:67]\n" \
  "v_mfma_f32_16x16x32_bf16 v[68:71], v[156:159], a[124:127], v[68:71]\n" \
  "v_mfma_f32_16x16x32_bf16 v[72:75], v[160:163], a[100:103], v[72:75]\n" \
  "v_mfma_f32_16x16x32_bf16 v[76:79], v[160:163], a[108:111], v[76:79]\n" \
  "v_mfma_f32_16x16x32_bf16 v[80:83], v[160:163], a[116:119], v[80:83]\n" \
  "v_mfma_f32_16x16x32_bf16 v[84:87], v[160:163], a[124:127], v[84:87]\n" \
  "v_mfma_f32_16x16x32_bf16 v[56:59], v[164:167], a[128:131], v[56:59]\n" \
  "v_mfma_f32_16x16x32_bf16 v[60:63], v[164:167], a[136:139], v[60:63]\n" \
  "v_mfma_f32_16x16x32_bf16 v[64:67], v[164:167], a[144:147], v[64:67]\n" \
  "v_mfma_f32_16x16x32_bf16 v[68:71], v[164:167], a[152:155], v[68:71]\n" \
  "v_mfma_f32_16x16x32_bf16 v[72:75], v[168:171], a[128:131], v[72:75]\n" \
  "v_mfma_f32_16x16x32_bf16 v[76:79], v[168:171], a[136:139], v[76:79]\n" \
  "v_mfma_f32_16x16x32_bf16 v[80:83], v[168:171], a[144:147], v[80:83]\n" \
  "v_mfma_f32_16x16x32_bf16 v[84:87], v[168:171], a[152:155], v[84:87]\n" \
  "v_mfma_f32_16x16x32_bf16 v[56:59], v[172:175], a[132:135], v[56:59]\n" \
  "v_mfma_f32_16x16x32_bf16 v[60:63], v[172:175], a[140:143], v[60:63]\n" \
  "v_mfma_f32_16x16x32_bf16 v[64:67], v[172:175], a[148:151], v[64:67]\n" \
  "v_mfma_f32_16x16x32_bf16 v[68:71], v[172:175], a[156:159], v[68:71]\n" \
  "v_mfma_f32_16x16x32_bf16 v[72:75], v[176:179], a[132:135], v[72:75]\n" \
  "v_mfma_f32_16x16x32_bf16 v[76:79], v[176:179], a[140:143], v[76:79]\n" \
  "v_mfma_f32_16x16x32_bf16 v[80:83], v[176:179], a[148:151], v[80:83]\n" \
  "v_mfma_f32_16x16x32_bf16 v[84:87], v[176:179], a[156:159], v[84:87]\n" \
  "v_mfma_f32_16x16x32_bf16 v[56:59], v[180:183], a[160:163], v[56:59]\n" \
  "v_mfma_f32_16x16x32_bf16 v[60:63], v[180:183], a[168:171], v[60:63]\n" \
  "v_mfma_f32_16x16x32_bf16 v[64:67], v[180:183], a[176:179], v[64:67]\n" \
  "v_mfma_f32_16x16x32_bf16 v[68:71], v[180:183], a[184:187], v[68:71]\n" \
  "v_mfma_f32_16x16x32_bf16 v[72:75], v[184:187], a[160:163], v[72:75]\n" \
  "v_mfma_f32_16x16x32_bf16 v[76:79], v[184:187], a[168:171], v[76:79]\n" \
  "v_mfma_f32_16x16x32_bf16 v[80:83], v[184:187], a[176:179], v[80:83]\n" \
  "v_mfma_f32_16x16x32_bf16 v[84:87], v[184:187], a[184:187], v[84:87]\n" \
  "v_mfma_f32_16x16x32_bf16 v[56:59], v[188:191], a[164:167], v[56:59]\n" \
  "v_mfma_f32_16x16x32_bf16 v[60:63], v[188:191], a[172:175], v[60:63]\n" \
  "v_mfma_f32_16x16x32_bf16 v[64:67], v[188:191], a[180:183], v[64:67]\n" \
  "v_mfma_f32_16x16x32_bf16 v[68:71], v[188:191], a[188:191], v[68:71]\n" \
  "v_mfma_f32_16x16x32_bf16 v[72:75], v[192:195], a[164:167], v[72:75]\n" \
  "v_mfma_f32_16x16x32_bf16 v[76:79], v[192:195], a[172:175], v[76:79]\n" \
  "v_mfma_f32_16x16x32_bf16 v[80:83], v[192:195], a[180:183], v[80:83]\n" \
  "v_mfma_f32_16x16x32_bf16 v[84:87], v[192:195], a[188:191], v[84:87]\n" \
  "s_add_i32 s76, s76, 3072\n" \
  "s_add_i32 s77, s77, 3072\n" \
  "s_mov_b32 s70, 0\n" \
  ".Lv_shw2:\n" \
  "buffer_load_dwordx4 v[148:151], v251, %[HS], s76 offen offset:0 sc1\n" \
  "buffer_load_dwordx4 v[152:155], v251, %[HS], s77 offen offset:0 sc1\n" \
  "buffer_load_dwordx4 v[156:159], v251, %[HS], s76 offen offset:512 sc1\n" \
  "buffer_load_dwordx4 v[160:163], v251, %[HS], s77 offen offset:512 sc1\n" \
  "buffer_load_dwordx4 v[164:167], v251, %[HS], s76 offen offset:1024 sc1\n" \
  "buffer_load_dwordx4 v[168:171], v251, %[HS], s77 offen offset:1024 sc1\n" \
  "buffer_load_dwordx4 v[172:175], v251, %[HS], s76 offen offset:1536 sc1\n" \
  "buffer_load_dwordx4 v[176:179], v251, %[HS], s77 offen offset:1536 sc1\n" \
  "buffer_load_dwordx4 v[180:183], v251, %[HS], s76 offen offset:2048 sc1\n" \
  "buffer_load_dwordx4 v[184:187], v251, %[HS], s77 offen offset:2048 sc1\n" \
  "buffer_load_dwordx4 v[188:191], v251, %[HS], s76 offen offset:2560 sc1\n" \
  "buffer_load_dwordx4 v[192:195], v251, %[HS], s77 offen offset:2560 sc1\n" \
  "s_waitcnt vmcnt(0)\n" \
  "v_max3_u32 v253, v148, v149, v150\n" \
  "v_max3_u32 v253, v253, v151, v152\n" \
  "v_max3_u32 v253, v253, v153, v154\n" \
  "v_max3_u32 v253, v253, v155, v156\n" \
  "v_max3_u32 v253, v253, v157, v158\n" \
  "v_max3_u32 v253, v253, v159, v160\n" \
  "v_max3_u32 v253, v253, v161, v162\n" \
  "v_max3_u32 v253, v253, v163, v164\n" \
  "v_max3_u32 v253, v253, v165, v166\n" \
  "v_max3_u32 v253, v253, v167, v168\n" \
  "v_max3_u32 v253, v253, v169, v170\n" \
  "v_max3_u32 v253, v253, v171, v172\n" \
  "v_max3_u32 v253, v253, v173, v174\n" \
  "v_max3_u32 v253, v253, v175, v176\n" \
  "v_max3_u32 v253, v253, v177, v178\n" \
  "v_max3_u32 v253, v253, v179, v180\n" \
  "v_max3_u32 v253, v253, v181, v182\n" \
  "v_max3_u32 v253, v253, v183, v184\n" \
  "v_max3_u32 v253, v253, v185, v186\n" \
  "v_max3_u32 v253, v253, v187, v188\n" \
  "v_max3_u32 v253, v253, v189, v190\n" \
  "v_max3_u32 v253, v253, v191, v192\n" \
  "v_max3_u32 v253, v253, v193, v194\n" \
  "v_max_u32 v253, v253, v195\n" \
  "v_cmp_eq_u32 vcc, -1, v253\n" \
  "s_cbranch_vccz .Lv_shw2_ok\n" \
  "s_sleep 4\n" \
  "s_add_i32 s70, s70, 1\n" \
  "s_and_b32 s68, s70, 1023\n" \
  "s_cbranch_scc1 .Lv_shw2\n" \
  "buffer_load_dword v253, off, %[F], 0 offset:1024 sc1\n" \
  "s_waitcnt vmcnt(0)\n" \
  "v_readfirstlane_b32 s68, v253\n" \
  "s_cmp_lg_u32 s68, 0\n" \
  "s_cbranch_scc1 .Lv_shw2_ok\n" \
  "s_cmp_lt_u32 s70, 400000\n" \
  "s_cbranch_scc1 .Lv_shw2\n" \
  "v_mov_b32 v253, 1\n" \
  "buffer_store_dword v253, off, %[F], 0 offset:1024 sc1\n" \
  ".Lv_shw2_ok:\n" \
  "v_mfma_f32_16x16x32_bf16 v[56:59], v[148:151], a[192:195], v[56:59]\n" \
  "v_mfma_f32_16x16x32_bf16 v[60:63], v[148:151], a[200:203], v[60:63]\n" \
  "v_mfma_f32_16x16x32_bf16 v[64:67], v[148:151], a[208:211], v[64:67]\n" \
  "v_mfma_f32_16x16x32_bf16 v[68:71], v[148:151], a[216:219], v[68:71]\n" \
  "v_mfma_f32_16x16x32_bf16 v[72:75], v[152:155], a[192:195], v[72:75]\n" \
  "v_mfma_f32_16x16x32_bf16 v[76:79], v[152:155], a[200:203], v[76:79]\n" \
  "v_mfma_f32_16x16x32_bf16 v[80:83], v[152:155], a[208:211], v[80:83]\n" \
  "v_mfma_f32_16x16x32_bf16 v[84:87], v[152:155], a[216:219], v[84:87]\n" \
  "v_mfma_f32_16x16x32_bf16 v[56:59], v[156:159], a[196:199], v[56:59]\n" \
  "v_mfma_f32_16x16x32_bf16 v[60:63], v[156:159], a[204:207], v[60:63]\n" \
  "v_mfma_f32_16x16x32_bf16 v[64:67], v[156:159], a[212:215], v[64:67]\n" \
  "v_mfma_f32_16x16x32_bf16 v[68:71], v[156:159], a[220:223], v[68:71]\n" \
  "v_mfma_f32_16x16x32_bf16 v[72:75], v[160:163], a[196:199], v[72:75]\n" \
  "v_mfma_f32_16x16x32_bf16 v[76:79], v[160:163], a[204:207], v[76:79]\n" \
  "v_mfma_f32_16x16x32_bf16 v[80:83], v[160:163], a[212:215], v[80:83]\n" \
  "v_mfma_f32_16x16x32_bf16 v[84:87], v[160:163], a[220:223], v[84:87]\n" \
  "v_mfma_f32_16x16x32_bf16 v[56:59], v[164:167], a[224:227], v[56:59]\n" \
  "v_mfma_f32_16x16x32_bf16 v[60:63], v[164:167], a[232:235], v[60:63]\n" \
  "v_mfma_f32_16x16x32_bf16 v[64:67], v[164:167], a[240:243], v[64:67]\n" \
  "v_mfma_f32_16x16x32_bf16 v[68:71], v[164:167], a[248:251], v[68:71]\n" \
  "v_mfma_f32_16x16x32_bf16 v[72:75], v[168:171], a[224:227], v[72:75]\n" \
  "v_mfma_f32_16x16x32_bf16 v[76:79], v[168:171], a[232:235], v[76:79]\n" \
  "v_mfma_f32_16x16x32_bf16 v[80:83], v[168:171], a[240:243], v[80:83]\n" \
  "v_mfma_f32_16x16x32_bf16 v[84:87], v[168:171], a[248:251], v[84:87]\n" \
  "v_mfma_f32_16x16x32_bf16 v[56:59], v[172:175], a[228:231], v[56:59]\n" \
  "v_mfma_f32_16x16x32_bf16 v[60:63], v[172:175], a[236:239], v[60:63]\n" \
  "v_mfma_f32_16x16x32_bf16 v[64:67], v[172:175], a[244:247], v[64:67]\n" \
  "v_mfma_f32_16x16x32_bf16 v[68:71], v[172:175], a[252:255], v[68:71]\n" \
  "v_mfma_f32_16x16x32_bf16 v[72:75], v[176:179], a[228:231], v[72:75]\n" \
  "v_mfma_f32_16x16x32_bf16 v[76:79], v[176:179], a[236:239], v[76:79]\n" \
  "v_mfma_f32_16x16x32_bf16 v[80:83], v[176:179], a[244:247], v[80:83]\n" \
  "v_mfma_f32_16x16x32_bf16 v[84:87], v[176:179], a[252:255], v[84:87]\n" \
  "v_mfma_f32_16x16x32_bf16 v[56:59], v[180:183], v[24:27], v[56:59]\n" \
  "v_mfma_f32_16x16x32_bf16 v[60:63], v[180:183], v[32:35], v[60:63]\n" \
  "v_mfma_f32_16x16x32_bf16 v[64:67], v[180:183], v[40:43], v[64:67]\n" \
  "v_mfma_f32_16x16x32_bf16 v[68:71], v[180:183], v[48:51], v[68:71]\n" \
  "v_mfma_f32_16x16x32_bf16 v[72:75], v[184:187], v[24:27], v[72:75]\n" \
  "v_mfma_f32_16x16x32_bf16 v[76:79], v[184:187], v[32:35], v[76:79]\n" \
  "v_mfma_f32_16x16x32_bf16 v[80:83], v[184:187], v[40:43], v[80:83]\n" \
  "v_mfma_f32_16x16x32_bf16 v[84:87], v[184:187], v[48:51], v[84:87]\n" \
  "v_mfma_f32_16x16x32_bf16 v[56:59], v[188:191], v[28:31], v[56:59]\n" \
  "v_mfma_f32_16x16x32_bf16 v[60:63], v[188:191], v[36:39], v[60:63]\n" \
  "v_mfma_f32_16x16x32_bf16 v[64:67], v[188:191], v[44:47], v[64:67]\n" \
  "v_mfma_f32_16x16x32_bf16 v[68:71], v[188:191], v[52:55], v[68:71]\n" \
  "v_mfma_f32_16x16x32_bf16 v[72:75], v[192:195], v[28:31], v[72:75]\n" \
  "v_mfma_f32_16x16x32_bf16 v[76:79], v[192:195], v[36:39], v[76:79]\n" \
  "v_mfma_f32_16x16x32_bf16 v[80:83], v[192:195], v[44:47], v[80:83]\n" \
  "v_mfma_f32_16x16x32_bf16 v[84:87], v[192:195], v[52:55], v[84:87]\n" \
  "s_add_i32 s76, s76, 3072\n" \
  "s_add_i32 s77, s77, 3072\n" \
  "s_nop 7\n" \
  "v_cvt_pk_bf16_f32 v212, v56, v57\n" \
  "v_cvt_pk_bf16_f32 v213, v58, v59\n" \
  "v_cvt_pk_bf16_f32 v214, v72, v73\n" \
  "v_cvt_pk_bf16_f32 v215, v74, v75\n" \
  "v_cvt_pk_bf16_f32 v216, v60, v61\n" \
  "v_cvt_pk_bf16_f32 v217, v62, v63\n" \
  "v_cvt_pk_bf16_f32 v218, v76, v77\n" \
  "v_cvt_pk_bf16_f32 v219, v78, v79\n" \
  "v_cvt_pk_bf16_f32 v220, v64, v65\n" \
  "v_cvt_pk_bf16_f32 v221, v66, v67\n" \
  "v_cvt_pk_bf16_f32 v222, v80, v81\n" \
  "v_cvt_pk_bf16_f32 v223, v82, v83\n" \
  "v_cvt_pk_bf16_f32 v224, v68, v69\n" \
  "v_cvt_pk_bf16_f32 v225, v70, v71\n" \
  "v_cvt_pk_bf16_f32 v226, v84, v85\n" \
  "v_cvt_pk_bf16_f32 v227, v86, v87\n" \
  "s_nop 1\n" \
  "v_mfma_f32_16x16x32_bf16 v[240:243], v[212:215], v[128:131], 0\n" \
  "v_mfma_f32_16x16x32_bf16 v[240:243], v[216:219], v[132:135], v[240:243]\n" \
  "v_mfma_f32_16x16x32_bf16 v[240:243], v[220:223], v[136:139], v[240:243]\n" \
  "v_mfma_f32_16x16x32_bf16 v[240:243], v[224:227], v[140:143], v[240:243]\n" \
  "v_mfma_f32_16x16x32_bf16 v[240:243], v[244:247], v[144:147], v[240:243]\n" \
  "v_mfma_f32_16x16x32_bf16 v[56:59], v[196:199], a[0:3], 0\n" \
  "v_mfma_f32_16x16x32_bf16 v[60:63], v[196:199], a[8:11], 0\n" \
  "v_mfma_f32_16x16x32_bf16 v[64:67], v[196:199], a[16:19], 0\n" \
  "s_nop 7\n" \
  "ds_write_b128 %[rmW], v[240:243]\n" \
  "s_waitcnt lgkmcnt(0)\n" \
  "s_branch .Lv_sq_back\n" \
  ".Lv_late:\n" \
  "s_cmp_gt_u32 s91, -3000\n" \
  "s_cbranch_scc0 .Lv_late2\n" \
  ".Lv_wt:\n" \
  "s_memrealtime s[76:77]\n" \
  "s_waitcnt lgkmcnt(0)\n" \
  "s_sub_i32 s76, s76, s71\n" \
  "s_cmp_lt_i32 s76, 0\n" \
  "s_cbranch_scc1 .Lv_wt\n" \
  "s_branch .Lv_al_ok\n" \
  ".Lv_late2:\n" \
  "s_cmp_lt_i32 s56, 0\n" \
  "s_cbranch_scc1 .Lv_al_ok\n" \
  "s_cmp_lg_u32 s71, 0\n" \
  "s_cbranch_scc1 .Lv_late3\n" \
  "s_mov_b32 s70, 0\n" \
  ".Lv_tw:\n" \
  "buffer_load_dword v252, off, %[X], s94 offset:4 sc1\n" \
  "s_waitcnt vmcnt(0)\n" \
  "v_readfirstlane_b32 s71, v252\n" \
  "s_cmp_lg_u32 s71, 0\n" \
  "s_cbranch_scc1 .Lv_tw_ok\n" \
  "s_sleep 1\n" \
  "s_add_i32 s70, s70, 1\n" \
  "s_cmp_lt_u32 s70, 200\n" \
  "s_cbranch_scc1 .Lv_tw\n" \
  ".Lv_tw_ok:\n" \
  "s_sub_i32 s91, s74, s71\n" \
  "s_cmp_gt_u32 s91, -3000\n" \
  "s_cbranch_scc1 .Lv_wt\n" \
  ".Lv_late3:\n" \
  "s_branch .Lv_al_ok\n" \
  ".Lv_sm:\n" \
  "s_cmp_lt_i32 s56, 0\n" \
  "s_cbranch_scc1 .Lv_sm_back\n" \
  "s_add_i32 s76, s59, -0x4000\n" \
  "v_mov_b64 v[112:113], 0\n" \
  "v_mov_b64 v[114:115], 0\n" \
  "v_mov_b64 v[116:117], 0\n" \
  "v_mov_b64 v[118:119], 0\n" \
  "v_mov_b64 v[120:121], 0\n" \
  "v_mov_b64 v[122:123], 0\n" \
  "v_mov_b64 v[124:125], 0\n" \
  "v_mov_b64 v[126:127], 0\n" \
  "s_mov_b32 s70, 0\n" \
  ".Lv_smw0:\n" \
  "buffer_load_dwordx4 v[128:131], %[la], %[X], s76 offen offset:0 sc1\n" \
  "buffer_load_dwordx4 v[132:135], %[la], %[X], s76 offen offset:1024 sc1\n" \
  "buffer_load_dwordx4 v[136:139], %[la], %[X], s76 offen offset:2048 sc1\n" \
  "buffer_load_dwordx4 v[140:143], %[la], %[X], s76 offen offset:3072 sc1\n" \
  "buffer_load_dwordx4 v[144:147], v250, %[X], s76 offen offset:0 sc1\n" \
  "s_waitcnt vmcnt(0)\n" \
  "v_max3_u32 v253, v128, v129, v130\n" \
  "v_max3_u32 v253, v253, v131, v132\n" \
  "v_max3_u32 v253, v253, v133, v134\n" \
  "v_max3_u32 v253, v253, v135, v136\n" \
  "v_max3_u32 v253, v253, v137, v138\n" \
  "v_max3_u32 v253, v253, v139, v140\n" \
  "v_max3_u32 v253, v253, v141, v142\n" \
  "v_max3_u32 v253, v253, v143, v144\n" \
  "v_max3_u32 v253, v253, v145, v146\n" \
  "v_max_u32 v253, v253, v147\n" \
  "v_cmp_eq_u32 vcc, -1, v253\n" \
  "s_cbranch_vccz .Lv_smw0_ok\n" \
  "s_add_i32 s70, s70, 1\n" \
  "s_and_b32 s68, s70, 1023\n" \
  "s_cbranch_scc1 .Lv_smw0\n" \
  "buffer_load_dword v253, off, %[F], 0 offset:1024 sc1\n" \
  "s_waitcnt vmcnt(0)\n" \
  "v_readfirstlane_b32 s68, v253\n" \
  "s_cmp_lg_u32 s68, 0\n" \
  "s_cbranch_scc1 .Lv_smw0_ok\n" \
  "s_cmp_lt_u32 s70, 400000\n" \
  "s_cbranch_scc1 .Lv_smw0\n" \
  "v_mov_b32 v253, 1\n" \
  "buffer_store_dword v253, off, %[F], 0 offset:1024 sc1\n" \
  ".Lv_smw0_ok:\n" \
  "v_smfmac_f32_16x16x64_bf16 v[112:115], v[128:131], a[0:7], %[idx]\n" \
  "v_smfmac_f32_16x16x64_bf16 v[116:119], v[128:131], a[8:15], %[idx]\n" \
  "v_smfmac_f32_16x16x64_bf16 v[120:123], v[128:131], a[16:23], %[idx]\n" \
  "v_smfmac_f32_16x16x64_bf16 v[124:127], v[128:131], a[24:31], %[idx]\n" \
  "v_smfmac_f32_16x16x64_bf16 v[112:115], v[132:135], a[32:39], %[idx]\n" \
  "v_smfmac_f32_16x16x64_bf16 v[116:119], v[132:135], a[40:47], %[idx]\n" \
  "v_smfmac_f32_16x16x64_bf16 v[120:123], v[132:135], a[48:55], %[idx]\n" \
  "v_smfmac_f32_16x16x64_bf16 v[124:127], v[132:135], a[56:63], %[idx]\n" \
  "v_smfmac_f32_16x16x64_bf16 v[112:115], v[136:139], a[64:71], %[idx]\n" \
  "v_smfmac_f32_16x16x64_bf16 v[116:119], v[136:139], a[72:79], %[idx]\n" \
  "v_smfmac_f32_16x16x64_bf16 v[120:123], v[136:139], a[80:87], %[idx]\n" \
  "v_smfmac_f32_16x16x64_bf16 v[124:127], v[136:139], a[88:95], %[idx]\n" \
  "v_smfmac_f32_16x16x64_bf16 v[112:115], v[140:143], a[96:103], %[idx]\n" \
  "v_smfmac_f32_16x16x64_bf16 v[116:119], v[140:143], a[104:111], %[idx]\n" \
  "v_smfmac_f32_16x16x64_bf16 v[120:123], v[140:143], a[112:119], %[idx]\n" \
  "v_smfmac_f32_16x16x64_bf16 v[124:127], v[140:143], a[120:127], %[idx]\n" \
  "v_smfmac_f32_16x16x64_bf16 v[112:115], v[144:147], a[128:135], %[idx]\n" \
  "v_smfmac_f32_16x16x64_bf16 v[116:119], v[144:147], a[136:143], %[idx]\n" \
  "v_smfmac_f32_16x16x64_bf16 v[120:123], v[144:147], a[144:151], %[idx]\n" \
  "v_smfmac_f32_16x16x64_bf16 v[124:127], v[144:147], a[152:159], %[idx]\n" \
  "s_mov_b32 s70, 0\n" \
  ".Lv_smw1:\n" \
  "buffer_load_dwordx4 v[128:131], v250, %[X], s76 offen offset:1024 sc1\n" \
  "buffer_load_dwordx4 v[132:135], v250, %[X], s76 offen offset:2048 sc1\n" \
  "buffer_load_dwordx4 v[136:139], v250, %[X], s76 offen offset:3072 sc1\n" \
  "buffer_load_dwordx4 v[140:143], %[la], %[X], s59 offen offset:0 sc1\n" \
  "s_waitcnt vmcnt(0)\n" \
  "v_max3_u32 v253, v128, v129, v130\n" \
  "v_max3_u32 v253, v253, v131, v132\n" \
  "v_max3_u32 v253, v253, v133, v134\n" \
  "v_max3_u32 v253, v253, v135, v136\n" \
  "v_max3_u32 v253, v253, v137, v138\n" \
  "v_max3_u32 v253, v253, v139, v140\n" \
  "v_max3_u32 v253, v253, v141, v142\n" \
  "v_max_u32 v253, v253, v143\n" \
  "v_cmp_eq_u32 vcc, -1, v253\n" \
  "s_cbranch_vccz .Lv_smw1_ok\n" \
  "s_add_i32 s70, s70, 1\n" \
  "s_and_b32 s68, s70, 1023\n" \
  "s_cbranch_scc1 .Lv_smw1\n" \
  "buffer_load_dword v253, off, %[F], 0 offset:1024 sc1\n" \
  "s_waitcnt vmcnt(0)\n" \
  "v_readfirstlane_b32 s68, v253\n" \
  "s_cmp_lg_u32 s68, 0\n" \
  "s_cbranch_scc1 .Lv_smw1_ok\n" \
  "s_cmp_lt_u32 s70, 400000\n" \
  "s_cbranch_scc1 .Lv_smw1\n" \
  "v_mov_b32 v253, 1\n" \
  "buffer_store_dword v253, off, %[F], 0 offset:1024 sc1\n" \
  ".Lv_smw1_ok:\n" \
  "v_smfmac_f32_16x16x64_bf16 v[112:115], v[128:131], a[160:167], %[idx]\n" \
  "v_smfmac_f32_16x16x64_bf16 v[116:119], v[128:131], a[168:175], %[idx]\n" \
  "v_smfmac_f32_16x16x64_bf16 v[120:123], v[128:131], a[176:183], %[idx]\n" \
  "v_smfmac_f32_16x16x64_bf16 v[124:127], v[128:131], a[184:191], %[idx]\n" \
  "v_smfmac_f32_16x16x64_bf16 v[112:115], v[132:135], a[192:199], %[idx]\n" \
  "v_smfmac_f32_16x16x64_bf16 v[116:119], v[132:135], a[200:207], %[idx]\n" \
  "v_smfmac_f32_16x16x64_bf16 v[120:123], v[132:135], a[208:215], %[idx]\n" \
  "v_smfmac_f32_16x16x64_bf16 v[124:127], v[132:135], a[216:223], %[idx]\n" \
  "v_smfmac_f32_16x16x64_bf16 v[112:115], v[136:139], a[224:231], %[idx]\n" \
  "v_smfmac_f32_16x16x64_bf16 v[116:119], v[136:139], a[232:239], %[idx]\n" \
  "v_smfmac_f32_16x16x64_bf16 v[120:123], v[136:139], a[240:247], %[idx]\n" \
  "v_smfmac_f32_16x16x64_bf16 v[124:127], v[136:139], a[248:255], %[idx]\n" \
  "v_smfmac_f32_16x16x64_bf16 v[112:115], v[140:143], v[24:31], %[idx]\n" \
  "v_smfmac_f32_16x16x64_bf16 v[116:119], v[140:143], v[32:39], %[idx]\n" \
  "v_smfmac_f32_16x16x64_bf16 v[120:123], v[140:143], v[40:47], %[idx]\n" \
  "v_smfmac_f32_16x16x64_bf16 v[124:127], v[140:143], v[48:55], %[idx]\n" \
  "s_nop 7\n" \
  "v_add_f32 v112, v112, v114\n" \
  "v_add_f32 v113, v113, v115\n" \
  "v_add_f32 v116, v116, v118\n" \
  "v_add_f32 v117, v117, v119\n" \
  "v_add_f32 v120, v120, v122\n" \
  "v_add_f32 v121, v121, v123\n" \
  "s_branch .Lv_sm_back\n" \
  ".Lv_entry:\n" \
  "s_mov_b32 s81, 63\n" \
  "s_mov_b32 s87, %[mr0]\n" \
  "s_cmp_eq_u32 %[grp], 0\n" \
  "s_cbranch_scc1 .Lv_go\n" \
  "s_mov_b32 s70, 0\n" \
  "s_xor_b32 s75, %[bas], 0x2000\n" \
  ".Lv_g1:\n" \
  "buffer_load_dword v252, off, %[X], s75 offset:48 sc1\n" \
  "s_waitcnt vmcnt(0)\n" \
  "v_readfirstlane_b32 s74, v252\n" \
  "s_cmp_lg_u32 s74, 0\n" \
  "s_cbranch_scc1 .Lv_g2\n" \
  "s_sleep 8\n" \
  "s_add_i32 s70, s70, 1\n" \
  "s_cmp_lt_u32 s70, 400\n" \
  "s_cbranch_scc1 .Lv_g1\n" \
  ".Lv_g2:\n" \
  "s_add_i32 s74, s74, %[g1t]\n" \
  ".Lv_g3:\n" \
  "s_memrealtime s[76:77]\n" \
  "s_waitcnt lgkmcnt(0)\n" \
  "s_sub_i32 s76, s76, s74\n" \
  "s_cmp_gt_u32 s76, -3000\n" \
  "s_cbranch_scc1 .Lv_g3\n" \
  ".Lv_go:\n" \
  "s_waitcnt vmcnt(0) lgkmcnt(0)\n" \
  "s_barrier\n" \
  "s_mov_b32 s75, 0\n" \
  "s_movk_i32 s76, 0x1000\n" \
  "s_movk_i32 s77, 0x2000\n" \
  "v_mov_b32 v128, 1\n" \
  "buffer_atomic_add v128, %[rdy], %[X], %[bas] offen offset:1536 sc0 sc1\n" \
  "s_mov_b32 s70, 0\n" \
  ".Lv_enw:\n" \
  "buffer_load_dwordx4 v[148:151], %[hvo], %[HS], s75 offen offset:0 sc1\n" \
  "buffer_load_dwordx4 v[152:155], %[hvo], %[HS], s75 offen offset:1024 sc1\n" \
  "buffer_load_dwordx4 v[156:159], %[hvo], %[HS], s75 offen offset:2048 sc1\n" \
  "buffer_load_dwordx4 v[160:163], %[hvo], %[HS], s75 offen offset:3072 sc1\n" \
  "buffer_load_dwordx4 v[164:167], %[hvo], %[HS], s76 offen offset:0 sc1\n" \
  "buffer_load_dwordx4 v[168:171], %[hvo], %[HS], s76 offen offset:1024 sc1\n" \
  "buffer_load_dwordx4 v[172:175], %[hvo], %[HS], s76 offen offset:2048 sc1\n" \
  "buffer_load_dwordx4 v[176:179], %[hvo], %[HS], s76 offen offset:3072 sc1\n" \
  "buffer_load_dwordx4 v[180:183], %[hvo], %[HS], s77 offen offset:0 sc1\n" \
  "s_waitcnt vmcnt(0)\n" \
  "v_max3_u32 v253, v148, v149, v150\n" \
  "v_max3_u32 v253, v253, v151, v152\n" \
  "v_max3_u32 v253, v253, v153, v154\n" \
  "v_max3_u32 v253, v253, v155, v156\n" \
  "v_max3_u32 v253, v253, v157, v158\n" \
  "v_max3_u32 v253, v253, v159, v160\n" \
  "v_max3_u32 v253, v253, v161, v162\n" \
  "v_max3_u32 v253, v253, v163, v164\n" \
  "v_max3_u32 v253, v253, v165, v166\n" \
  "v_max3_u32 v253, v253, v167, v168\n" \
  "v_max3_u32 v253, v253, v169, v170\n" \
  "v_max3_u32 v253, v253, v171, v172\n" \
  "v_max3_u32 v253, v253, v173, v174\n" \
  "v_max3_u32 v253, v253, v175, v176\n" \
  "v_max3_u32 v253, v253, v177, v178\n" \
  "v_max3_u32 v253, v253, v179, v180\n" \
  "v_max3_u32 v253, v253, v181, v182\n" \
  "v_max_u32 v253, v253, v183\n" \
  "v_cmp_eq_u32 vcc, -1, v253\n" \
  "s_cbranch_vccz .Lv_enw_ok\n" \
  "s_sleep 4\n" \
  "s_add_i32 s70, s70, 1\n" \
  "s_and_b32 s68, s70, 1023\n" \
  "s_cbranch_scc1 .Lv_enw\n" \
  "buffer_load_dword v253, off, %[F], 0 offset:1024 sc1\n" \
  "s_waitcnt vmcnt(0)\n" \
  "v_readfirstlane_b32 s68, v253\n" \
  "s_cmp_lg_u32 s68, 0\n" \
  "s_cbranch_scc1 .Lv_enw_ok\n" \
  "s_cmp_lt_u32 s70, 400000\n" \
  "s_cbranch_scc1 .Lv_enw\n" \
  "v_mov_b32 v253, 1\n" \
  "buffer_store_dword v253, off, %[F], 0 offset:1024 sc1\n" \
  ".Lv_enw_ok:\n" \
  "ds_write_b128 %[hvo], v[148:151] offset:0\n" \
  "ds_write_b128 %[hvo], v[152:155] offset:1024\n" \
  "ds_write_b128 %[hvo], v[156:159] offset:2048\n" \
  "ds_write_b128 %[hvo], v[160:163] offset:3072\n" \
  "ds_write_b128 %[hvo], v[164:167] offset:4096\n" \
  "ds_write_b128 %[hvo], v[168:171] offset:5120\n" \
  "ds_write_b128 %[hvo], v[172:175] offset:6144\n" \
  "ds_write_b128 %[hvo], v[176:179] offset:7168\n" \
  "ds_write_b128 %[hvo], v[180:183] offset:8192\n" \
  "v_readfirstlane_b32 s92, v128\n" \
  "s_cmp_lg_u32 s92, 103\n" \
  "s_cbranch_scc1 .Lv_nrdy\n" \
  "s_memrealtime s[74:75]\n" \
  "s_waitcnt lgkmcnt(0)\n" \
  "s_add_i32 s74, s74, %[x0]\n" \
  "s_mul_i32 s75, %[tau], 162\n" \
  "s_lshr_b32 s75, s75, 8\n" \
  "s_add_i32 s75, s75, s74\n" \
  "v_mov_b32 v128, s74\n" \
  "v_mov_b32 v129, s75\n" \
  "buffer_store_dwordx2 v[128:129], %[rdy], %[X], %[bas] offen offset:0 sc1\n" \
  "s_add_i32 s74, s74, %[tau]\n" \
  "s_add_i32 s75, s75, %[tau]\n" \
  "v_mov_b32 v128, s74\n" \
  "v_mov_b32 v129, s75\n" \
  "buffer_store_dwordx2 v[128:129], %[rdy], %[X], %[bas] offen offset:16 sc1\n" \
  ".Lv_nrdy:\n" \
  "s_add_i32 s69, s56, 2\n" \
  "s_min_i32 s68, s69, 63\n" \
  "s_mul_i32 s57, s68, 36864\n" \
  "s_lshl_b32 s68, s69, 16\n" \
  "s_and_b32 s68, s68, 0x10000\n" \
  "s_add_i32 s58, %[hl0], s68\n" \
  "s_add_i32 s68, s56, -1\n" \
  "s_mul_i32 s63, s68, 36864\n" \
  "s_add_i32 s63, s63, %[hl0]\n" \
  "s_cmp_gt_i32 s56, 0\n" \
  "s_cselect_b32 s63, s63, 2.0\n" \
  "s_waitcnt lgkmcnt(0)\n" \
  "s_barrier\n" \
  "ds_read_b128 v[196:199], %[hA] offset:0\n" \
  "ds_read_b128 v[200:203], %[hA] offset:18432\n" \
  "ds_read_b128 v[204:207], %[hA] offset:512\n" \
  "ds_read_b128 v[208:211], %[hA] offset:18944\n" \
  "s_waitcnt lgkmcnt(0)\n" \
  "v_mfma_f32_16x16x32_bf16 v[56:59], v[196:199], a[0:3], 0\n" \
  "v_mfma_f32_16x16x32_bf16 v[60:63], v[196:199], a[8:11], 0\n" \
  "v_mfma_f32_16x16x32_bf16 v[64:67], v[196:199], a[16:19], 0\n" \
  "v_mfma_f32_16x16x32_bf16 v[68:71], v[196:199], a[24:27], 0\n" \
  "v_mfma_f32_16x16x32_bf16 v[72:75], v[200:203], a[0:3], 0\n" \
  "v_mfma_f32_16x16x32_bf16 v[76:79], v[200:203], a[8:11], 0\n" \
  "v_mfma_f32_16x16x32_bf16 v[80:83], v[200:203], a[16:19], 0\n" \
  "v_mfma_f32_16x16x32_bf16 v[84:87], v[200:203], a[24:27], 0\n" \
  "v_mfma_f32_16x16x32_bf16 v[56:59], v[204:207], a[4:7], v[56:59]\n" \
  "v_mfma_f32_16x16x32_bf16 v[60:63], v[204:207], a[12:15], v[60:63]\n" \
  "s_waitcnt lgkmcnt(0)\n" \
  "s_branch .Lv_top\n" \
  ".Lv_end:\n"
#define V_CLOB "v24", "v25", "v26", "v27", "v28", "v29", "v30", "v31", "v32", "v33", "v34", "v35", "v36", "v37", "v38", "v39", "v40", "v41", "v42", "v43", "v44", "v45", "v46", "v47", "v48", "v49", "v50", "v51", "v52", "v53", "v54", "v55", "v56", "v57", "v58", "v59", "v60", "v61", "v62", "v63", "v64", "v65", "v66", "v67", "v68", "v69", "v70", "v71", "v72", "v73", "v74", "v75", "v76", "v77", "v78", "v79", "v80", "v81", "v82", "v83", "v84", "v85", "v86", "v87", "v88", "v89", "v90", "v91", "v92", "v93", "v94", "v95", "v96", "v97", "v98", "v99", "v100", "v101", "v102", "v103", "v104", "v105", "v106", "v107", "v108", "v109", "v110", "v111", "v112", "v113", "v114", "v115", "v116", "v117", "v118", "v119", "v120", "v121", "v122", "v123", "v124", "v125", "v126", "v127", "v128", "v129", "v130", "v131", "v132", "v133", "v134", "v135", "v136", "v137", "v138", "v139", "v140", "v141", "v142", "v143", "v144", "v145", "v146", "v147", "v148", "v149", "v150", "v151", "v152", "v153", "v154", "v155", "v156", "v157", "v158", "v159", "v160", "v161", "v162", "v163", "v164", "v165", "v166", "v167", "v168", "v169", "v170", "v171", "v172", "v173", "v174", "v175", "v176", "v177", "v178", "v179", "v180", "v181", "v182", "v183", "v184", "v185", "v186", "v187", "v188", "v189", "v190", "v191", "v192", "v193", "v194", "v195", "v196", "v197", "v198", "v199", "v200", "v201", "v202", "v203", "v204", "v205", "v206", "v207", "v208", "v209", "v210", "v211", "v212", "v213", "v214", "v215", "v216", "v217", "v218", "v219", "v220", "v221", "v222", "v223", "v224", "v225", "v226", "v227", "v228", "v229", "v230", "v231", "v232", "v233", "v234", "v235", "v236", "v237", "v238", "v239", "v240", "v241", "v242", "v243", "v244", "v245", "v246", "v247", "v248", "v249", "v250", "v251", "v252", "v253", "v254", "v255", "s56", "s57", "s58", "s59", "s60", "s61", "s62", "s63", "s64", "s65", "s66", "s67", "s68", "s69", "s70", "s71", "s72", "s73", "s74", "s75", "s76", "s77", "s78", "s79", "s80", "s81", "s82", "s83", "s84", "s85", "s86", "s87", "s88", "s89", "s90", "s91", "s92", "s93", "s94", "a0", "a1", "a2", "a3", "a4", "a5", "a6", "a7", "a8", "a9", "a10", "a11", "a12", "a13", "a14", "a15", "a16", "a17", "a18", "a19", "a20", "a21", "a22", "a23", "a24", "a25", "a26", "a27", "a28", "a29", "a30", "a31", "a32", "a33", "a34", "a35", "a36", "a37", "a38", "a39", "a40", "a41", "a42", "a43", "a44", "a45", "a46", "a47", "a48", "a49", "a50", "a51", "a52", "a53", "a54", "a55", "a56", "a57", "a58", "a59", "a60", "a61", "a62", "a63", "a64", "a65", "a66", "a67", "a68", "a69", "a70", "a71", "a72", "a73", "a74", "a75", "a76", "a77", "a78", "a79", "a80", "a81", "a82", "a83", "a84", "a85", "a86", "a87", "a88", "a89", "a90", "a91", "a92", "a93", "a94", "a95", "a96", "a97", "a98", "a99", "a100", "a101", "a102", "a103", "a104", "a105", "a106", "a107", "a108", "a109", "a110", "a111", "a112", "a113", "a114", "a115", "a116", "a117", "a118", "a119", "a120", "a121", "a122", "a123", "a124", "a125", "a126", "a127", "a128", "a129", "a130", "a131", "a132", "a133", "a134", "a135", "a136", "a137", "a138", "a139", "a140", "a141", "a142", "a143", "a144", "a145", "a146", "a147", "a148", "a149", "a150", "a151", "a152", "a153", "a154", "a155", "a156", "a157", "a158", "a159", "a160", "a161", "a162", "a163", "a164", "a165", "a166", "a167", "a168", "a169", "a170", "a171", "a172", "a173", "a174", "a175", "a176", "a177", "a178", "a179", "a180", "a181", "a182", "a183", "a184", "a185", "a186", "a187", "a188", "a189", "a190", "a191", "a192", "a193", "a194", "a195", "a196", "a197", "a198", "a199", "a200", "a201", "a202", "a203", "a204", "a205", "a206", "a207", "a208", "a209", "a210", "a211", "a212", "a213", "a214", "a215", "a216", "a217", "a218", "a219", "a220", "a221", "a222", "a223", "a224", "a225", "a226", "a227", "a228", "a229", "a230", "a231", "a232", "a233", "a234", "a235", "a236", "a237", "a238", "a239", "a240", "a241", "a242", "a243", "a244", "a245", "a246", "a247", "a248", "a249", "a250", "a251", "a252", "a253", "a254", "a255", "vcc", "scc", "m0", "memory"
#define TAU 220
#define SH1 40
#define MRG 4
#define LAG 345
#define SCHED [tau] "s"(TAU), [x0] "s"(110), [g1t] "s"(195), [bas] "s"(gb + CTL_CH * CH + 64u), [rdy] "v"(tid == 0 ? 0u : DROP)
__device__ __forceinline__ unsigned qrow(int r) { return (unsigned)(L_QI + (r >> 4) * 2816 + (r & 15) * 160 + ((r >> 2) & 3) * 8); }
__device__ __forceinline__ void qk_wg(const MP& p, int role, int grp, int h, int rep, unsigned gb) {
  const int tid = threadIdx.x, lane = tid & 63, w = __builtin_amdgcn_readfirstlane(tid >> 6);
  const int fr = lane & 15, g = lane >> 4;
  const int jj = (h >> 1) & 3, hh = h & 1;
  const unsigned pb = (unsigned)(h >> 3) * CH + (unsigned)(((h >> 3) & 1) * 1024);
  const unsigned ptb = (unsigned)(PT_CH - PM_CH - 4 * w) * CH + (unsigned)(h >> 3) * 2u * CH;
  const int qc = 8 * (w & 1) + (fr & 7), T = w >> 1;
  const unsigned po1 = fr < 8 ? pb + (unsigned)(jj * 1024 + (g * 16 + hh * 8 + fr) * 16) : T ? ptb + phys((unsigned)((4 + (h & 7)) * 1024 + (g * 16 + qc) * 16)) : DROP;
  const unsigned po2 = fr < 8 ? pb + (unsigned)(4096 + (jj * 16 + hh * 8 + fr) * 16 + g * 4) : ptb + (unsigned)((T * 2 + hh) * 1024 + (jj * 16 + qc) * 16 + g * 4);
  const unsigned po3 = fr < 8 || T ? DROP : ptb + phys((unsigned)((12 + (jj & 2) + hh) * 1024 + (g * 16 + qc) * 16 + (jj & 1) * 8));
  const u16* wb = p.WP + (size_t)h * (16 * 18 * 512);
  unsigned hA = (unsigned)(65536 + (fr >> 3) * RGB + g * 128 + (lane & 7) * 16);
  asm volatile(QK_ASM
    : [hA] "+v"(hA)
    : [X] "s"(mkd(p.win, WIN_BYTES)), [HS] "s"(mkd(p.H + (size_t)grp * (NSEG * SEGB / 2), NSEG * SEGB)), [F] "s"(mkd(p.ct, CT_TOTAL * 4u)),
      [mr0] "s"(gb + (unsigned)(MEM_CH + 2 * rep) * CH), [pm0] "s"(gb + (unsigned)(PM_CH + 4 * w) * CH), [hl0] "s"((unsigned)w * RGB), [wp] "s"(wb), [grp] "s"(grp),
      SCHED, [mrg] "s"(MRG), [rpo] "v"(tid == 0 ? gb + CTL_CH * CH + (unsigned)(2048 + 256 * rep) : DROP),
      [cpo] "v"(h == 0 ? gb + CTL_CH * CH + (unsigned)(2048 + 256 * (lane & 7)) : DROP), [cpw] "v"(h == 0 && tid == 0 ? 0u : DROP),
      [cpa] "v"(h == 0 && grp ? 5u * 16u : DROP), [lag] "s"(LAG), [dn] "s"(3), [up] "s"(3), [sh1] "s"(SH1), [gg] "s"(grp * 130), [c2] "s"(41), [c4] "s"(23),
      [mo] "v"((unsigned)lane * 16u), [idx] "v"((lane & 2) ? 0xEEEEu : 0x4444u),
      [qW] "v"(qrow(32 * w + fr) + 4 * g), [kW] "v"(qrow(32 * w + fr) + 128 + 8 * g),
      [qR] "v"(qrow(4 * g + (fr >> 2)) + ((fr & 2) ? 16 + 32 * w : 0) + 8 * (fr & 1)), [kR] "v"(qrow(4 * g + (fr >> 2)) + 128 + 8 * (fr & 3)),
      [qtW] "v"(qrow(32 * w + fr) + 16 + 32 * (g >> 1) + 8 * (g & 1)), [ktW] "v"(L_KT + (unsigned)(w * 1024 + lane * 16)), [ktR] "v"(L_KT + (unsigned)lane * 16u),
      [hvo] "v"((unsigned)w * RGB + (unsigned)lane * 16u), [po1] "v"(po1), [po2] "v"(po2), [po3] "v"(po3), [pcl] "v"(fr == 0 ? po1 : lane == 1 ? po2 : DROP),
      [lim] "v"(fr >= 8 ? 16 * T + qc - 4 * g : 1000), [wo] "v"((unsigned)(w * (4 * 18 * 1024) + lane * 16))
    : QK_CLOB);
}

__device__ __forceinline__ void v_wg(const MP& p, int role, int grp, int j, int rep, unsigned gb) {
  const int tid = threadIdx.x, lane = tid & 63, w = __builtin_amdgcn_readfirstlane(tid >> 6);
  const int fr = lane & 15, g = lane >> 4;
  const int par = lane & 1, rq = lane >> 3, rgp = (2 * par + ((lane >> 2) & 1)) ^ ((rq >> 1) & 3), wsl = (fr * 4 + (g ^ ((fr >> 1) & 3))) * 16;
  const u16* wb = p.BP + (size_t)j * (16 * 18 * 512);
  if (!grp) load_w<0, 8>(wb);
  unsigned hA = (unsigned)(grp * 65536 + (fr >> 3) * RGB + g * 128 + (lane & 7) * 16);
  asm volatile(V_ASM
    : [hA] "+v"(hA)
    : [X] "s"(mkd(p.win, WIN_BYTES)), [HS] "s"(mkd(p.H + (size_t)grp * (NSEG * SEGB / 2), NSEG * SEGB)), [OS] "s"(mkd(p.OT + (size_t)grp * (NSEG * SEGB / 2), NSEG * SEGB)),
      [F] "s"(mkd(p.ct, CT_TOTAL * 4u)), [mw0] "s"(gb + (unsigned)(MEM_CH + 2 * w) * CH), [mr0] "s"(gb + (unsigned)(MEM_CH + 2 * rep) * CH), [pt0] "s"(gb + (unsigned)(PT_CH + 2 * w) * CH),
      [pm0] "s"(gb + (unsigned)(PM_CH + rep * 4 + w) * CH + (unsigned)(w & 1) * 1024u), [hl0] "s"((unsigned)w * RGB), [ml0] "s"(LV_MEM + (unsigned)w * 1024u),
      [wp] "s"(wb), [grp] "s"(grp), SCHED, [rpo] "v"(tid == 0 ? gb + CTL_CH * CH + (unsigned)(3072 + 256 * rep) : DROP),
      [la] "v"((unsigned)lane * 16u), [idx] "v"((lane & 2) ? 0xEEEEu : 0x4444u), [mA] "v"(LV_MEM + (unsigned)lane * 16u), [mA8] "v"(LV_MEM + (unsigned)(8192 + g * 1024 + fr * 16)),
      [c0] "v"((unsigned)(w * 1024 + lane * 16)), [c2] "v"(lane < 16 ? (unsigned)(w * 256 + lane * 16) : DROP), [hvo] "v"((unsigned)w * RGB + (unsigned)lane * 16u),
      [rmW] "v"(LV_RM + (unsigned)(w * 1024 + wsl)), [rmA] "v"(LV_RM + (unsigned)(((8 * par + rq) * 4 + rgp) * 16 + (lane & 2) * 4)),
      [rtW] "v"(LV_RT + (unsigned)(w * 2048 + wsl)), [rtA] "v"(LV_RT + (unsigned)((w >> 1) * 1024 + ((8 * (w & 1) + rq) * 4 + rgp) * 16 + (lane & 2) * 4)),
      [lo] "v"(par ? DROP : (unsigned)(j * 128 + lane * 2)), [lm] "v"(par ? DROP : phys((unsigned)((j >> 1) * 256 + (4 * (rq >> 1) + 2 * (j & 1) + (rq & 1)) * 16 + (lane & 6) * 2))),
      [wo] "v"((unsigned)(w * (4 * 18 * 1024) + lane * 16))
    : V_CLOB);
}

#define YSLP 2
struct Row9 { unsigned u[9]; };
__device__ __forceinline__ float rnd(float a) { return __uint_as_float(pk2(0.f, a)); }
__device__ __forceinline__ void ln18(const f32x2 (&x)[9], f32x2 (&o)[9], float* st, int lane, int w) {
  f32x2 s2 = x[0];
#pragma unroll
  for (int i = 1; i < 9; ++i) s2 += x[i];
  const float mw = i_red8add(s2[0] + s2[1]) * (1.0f / 144.0f);
  f32x2 q2 = {0.f, 0.f};
#pragma unroll
  for (int i = 0; i < 9; ++i) { const f32x2 d = x[i] - mw; q2 += d * d; }
  const float qw = i_red8add(q2[0] + q2[1]);
  if (lane < 8) *(f32x2*)(st + (lane * 4 + w) * 2) = (f32x2){mw, qw};
  BAR();
  const f32x4 a = *(const f32x4*)(st + (lane & 7) * 8), b = *(const f32x4*)(st + (lane & 7) * 8 + 4);
  const float mean = ((a[0] + a[2]) + (b[0] + b[2])) * 0.25f;
  const float d0 = a[0] - mean, d1 = a[2] - mean, d2 = b[0] - mean, d3 = b[2] - mean;
  const float m2 = ((a[1] + a[3]) + (b[1] + b[3])) + 144.0f * ((d0 * d0 + d1 * d1) + (d2 * d2 + d3 * d3));
  const float var = rnd(m2 * (1.0f / DM));
  const float m = rnd(mean), rden = __builtin_amdgcn_rcpf(rnd(__builtin_amdgcn_sqrtf(rnd(var + 1e-5f))));
#pragma unroll
  for (int i = 0; i < 9; ++i) { const f32x2 d = x[i] - m; o[i] = (f32x2){rnd(d[0]), rnd(d[1])} * rden; }
}
__device__ __forceinline__ void mix_rows(Row9& xa, const Row9& ya, const rsrc_t H, unsigned so, unsigned o0, unsigned oe, float* st, int lane, int w) {
  f32x2 x[9], y[9], ln[9];
#pragma unroll
  for (int i = 0; i < 9; ++i) { x[i] = (f32x2){lo2f(xa.u[i]), hi2f(xa.u[i])}; y[i] = (f32x2){lo2f(ya.u[i]), hi2f(ya.u[i])}; }
  ln18(x, ln, st, lane, w);
#pragma unroll
  for (int i = 0; i < 9; ++i) {
    const f32x2 m = (f32x2){rnd(ln[i][0]), rnd(ln[i][1])} * y[i];
    const f32x2 u = x[i] + (f32x2){rnd(m[0]), rnd(m[1])};
    x[i] = (f32x2){rnd(u[0]), rnd(u[1])};
  }
  ln18(x, ln, st + 64, lane, w);
  unsigned h[9];
#pragma unroll
  for (int i = 0; i < 9; ++i) h[i] = nosent(pk2(ln[i][0], ln[i][1]));
  st16(H, o0, so, (v4u){h[0], h[1], h[2], h[3]});
  st16(H, o0 + 4096u, so, (v4u){h[4], h[5], h[6], h[7]});
  st4(H, oe, so, h[8]);
#pragma unroll
  for (int i = 0; i < 9; ++i) xa.u[i] = pk2(x[i][0], x[i][1]);
}
template <int SLP>
__device__ __forceinline__ void wait9(const rsrc_t O, const rsrc_t F, unsigned so, unsigned o0, unsigned o1, unsigned oe, v4u (&v)[2], unsigned& e) {
  unsigned spins = 0;
#pragma nounroll
  while (true) {
    asm volatile("" ::: "memory");
    v[0] = ld16(O, o0, so); v[1] = ld16(O, o1, so); e = ld4(O, oe, so);
    if (__all(mx4(mx4(e, v[0]), v[1]) != SENT)) break;
    if (spin_fail(spins, F)) break;
    __builtin_amdgcn_s_sleep(SLP);
  }
}
__device__ __forceinline__ void add_rows(Row9& xa, const v4u (&v)[2], unsigned e) {
#pragma unroll
  for (int i = 0; i < 2; ++i) {
    const v4u o = {(unsigned)__builtin_amdgcn_update_dpp(v[i].x, v[i].z, 0x128, 0xF, 0xC, false), (unsigned)__builtin_amdgcn_update_dpp(v[i].z, v[i].x, 0x128, 0xF, 0x3, false),
                   (unsigned)__builtin_amdgcn_update_dpp(v[i].y, v[i].w, 0x128, 0xF, 0xC, false), (unsigned)__builtin_amdgcn_update_dpp(v[i].w, v[i].y, 0x128, 0xF, 0x3, false)};
#pragma unroll
    for (int k = 0; k < 4; ++k) xa.u[4 * i + k] = pk2(lo2f(xa.u[4 * i + k]) + lo2f(o[k]), hi2f(xa.u[4 * i + k]) + hi2f(o[k]));
  }
  xa.u[8] = pk2(lo2f(xa.u[8]) + lo2f(e), hi2f(xa.u[8]) + hi2f(e));
}
__device__ __forceinline__ void arm_slots(const MP& p, int s, int rg) {
  const rsrc_t O = mkr(p.OT, 2u * NSEG * SEGB), H = mkr(p.H, 2u * NSEG * SEGB);
  const unsigned so = __builtin_amdgcn_readfirstlane((unsigned)s * SEGB + (unsigned)rg * RGB), o0 = threadIdx.x * 16u;
  const v4u sent = {SENT, SENT, SENT, SENT};
#pragma unroll
  for (int i = 0; i < 3; ++i) {
    const unsigned o = i < 2 || threadIdx.x < 64 ? o0 + 4096u * i : DROP;
    st16(H, o, s + 12 < NSEG ? so + 12u * SEGB : DROP, sent);
    st16(O, o, so, sent); st16(O, o, NSEG * SEGB + so, sent); st16(H, o, s + 2 < NSEG ? (NSEG + 2) * SEGB + so : DROP, sent);
  }
}
#define LR_Y 1024
#define LR_SV 10240
#define RSLP 4
__device__ __forceinline__ void row_wg(const MP& p, char* smem, int role, int rw, unsigned flip) {
  const int tid = threadIdx.x, lane = tid & 63, w = __builtin_amdgcn_readfirstlane(tid >> 6);
  const rsrc_t F = mkr(p.ct, CT_TOTAL * 4u), O = mkr(p.OT, 2u * NSEG * SEGB), H = mkr(p.H, 2u * NSEG * SEGB), Y = mkr(p.Y, DM * YS), X = mkr(p.win, WIN_BYTES);
  float* st = (float*)smem;
  const int r8 = tid & 7, pj = (tid >> 3) & 7;
  const unsigned o0 = (unsigned)tid * 16u;
  const unsigned oe = (unsigned)(8192 + pj * 128 + r8 * 16 + w * 4);
  const unsigned oe2 = (unsigned)(8192 + ((pj & 6) + (w & 1)) * 128 + r8 * 16 + (2 * (pj & 1) + (w >> 1)) * 4);
  const int rg = rw & 3, s0 = rw >> 2;
  const char* yR = smem + LR_Y + (8 * ((tid >> 4) * 2 + ((lane >> 1) & 1)) + ((lane & 15) >> 2)) * 16 + (lane & 1) * 8;
  const char* yE = smem + LR_Y + (512 + 8 * pj + 2 * w) * 16 + r8 * 2;
#pragma nounroll
  for (int job = rw < 8 ? -1 : 0; job < 14; ++job) {
    const bool isA = job <= 0 || (job & 1);
    const int k = job <= 0 ? 0 : (isA ? (job + 1) >> 1 : (job >> 1) - 1);
    const int s = s0 + 12 * k;
    if (s >= NSEG) continue;
    const unsigned to = CTL_CH * CH + 64u + 16u * (unsigned)(s < 62 ? s + 2 : 64);
    const int tc = s < 63 ? 20 : 60;
    const unsigned so = __builtin_amdgcn_readfirstlane((unsigned)s * SEGB + (unsigned)rg * RGB);
    char* sv = smem + LR_SV + (k & 1) * 2 * RGB;
    const size_t go = (size_t)(s * SEG + rg * 8 + r8) * DM;
    Row9 xa, ya;
    v4u v[2];
    unsigned e;
    if (isA) {
      const v4u x0 = *(const v4u*)(p.x0 + go + (tid >> 3) * 8), x1 = *(const v4u*)(p.x0 + go + 256 + (tid >> 3) * 8);
      xa.u[8] = *(const unsigned*)(p.x0 + go + 512 + 8 * pj + 2 * w);
#pragma unroll
      for (int i = 0; i < 4; ++i) { xa.u[i] = x0[i]; xa.u[4 + i] = x1[i]; }
      if (job > 0) arm_slots(p, s, rg);
      if (job == 0) {
        unsigned spins = 0;
#pragma nounroll
        while (!__all(ld4(F, (unsigned)(CT_YC + (lane & 7) * 32) * 4u, 0) >= 72u)) { if (spin_fail(spins, F)) break; __builtin_amdgcn_s_sleep(4); asm volatile("" ::: "memory"); }
      }
      if (job >= 0) wait9<YSLP>(Y, F, (unsigned)(s * SEG + rg * 8) * 2u, (unsigned)tid * YS, (unsigned)(tid + 256) * YS, (unsigned)(512 + (tid >> 2)) * YS + (unsigned)(tid & 3) * 4u, v, e);
      else { v[0] = x0; v[1] = x1; e = xa.u[8]; }
      VM0();
      *(v4u*)(smem + LR_Y + o0) = v[0]; *(v4u*)(smem + LR_Y + o0 + 4096) = v[1]; *(unsigned*)(smem + LR_Y + 8192 + tid * 4) = e;
      BAR();
#pragma unroll
      for (int i = 0; i < 2; ++i) {
        const v2u x = trd(yR + 4096 * i), y = trd(yR + 4096 * i + 64);
        ya.u[4 * i] = x.x; ya.u[4 * i + 1] = x.y; ya.u[4 * i + 2] = y.x; ya.u[4 * i + 3] = y.y;
      }
      ya.u[8] = (unsigned)*(const u16*)yE | ((unsigned)*(const u16*)(yE + 16) << 16);
    } else {
#pragma unroll
      for (int i = 0; i < 2; ++i) {
        const v4u a = *(const v4u*)(sv + o0 + 4096 * i), b = *(const v4u*)(sv + RGB + o0 + 4096 * i);
#pragma unroll
        for (int q = 0; q < 4; ++q) { xa.u[4 * i + q] = a[q]; ya.u[4 * i + q] = b[q]; }
      }
      xa.u[8] = *(const unsigned*)(sv + 8192 + tid * 4); ya.u[8] = *(const unsigned*)(sv + RGB + 8192 + tid * 4);
      wait_time(X, flip * 8192u + to, tc);
      wait9<RSLP>(O, F, so, o0, o0 + 4096u, oe2, v, e);
      add_rows(xa, v, e);
    }
    mix_rows(xa, ya, H, job < 0 ? 0x40000000u : (isA ? so : NSEG * SEGB + so), o0, oe, st, lane, w);
    if (isA) {
#pragma unroll
      for (int i = 0; i < 2; ++i) {
        *(v4u*)(sv + o0 + 4096 * i) = (v4u){xa.u[4 * i], xa.u[4 * i + 1], xa.u[4 * i + 2], xa.u[4 * i + 3]};
        *(v4u*)(sv + RGB + o0 + 4096 * i) = (v4u){ya.u[4 * i], ya.u[4 * i + 1], ya.u[4 * i + 2], ya.u[4 * i + 3]};
      }
      *(unsigned*)(sv + 8192 + tid * 4) = xa.u[8]; *(unsigned*)(sv + RGB + 8192 + tid * 4) = ya.u[8];
    } else {
      wait_time(X, (flip ^ 1u) * 8192u + to, tc);
      wait9<RSLP>(O, F, NSEG * SEGB + so, o0, o0 + 4096u, oe2, v, e);
      add_rows(xa, v, e);
      *(v4u*)(p.out + go + (tid >> 3) * 8) = (v4u){xa.u[0], xa.u[1], xa.u[2], xa.u[3]};
      *(v4u*)(p.out + go + 256 + (tid >> 3) * 8) = (v4u){xa.u[4], xa.u[5], xa.u[6], xa.u[7]};
      *(unsigned*)(p.out + go + 512 + 8 * pj + 2 * w) = xa.u[8];
    }
  }
}

#define CREV_STRIDE 2208
#define LC_KVS 35328
#define LC_RED 43008
__device__ __forceinline__ unsigned rot16(unsigned u) { return __builtin_amdgcn_alignbit(u, u, 16); }
#define LC_IN 61440
__device__ __forceinline__ void crev_put(u16* dst, int stride, const v4u& c0, const v4u& c1) {
  const unsigned e[8] = {rot16(c0.w), rot16(c0.z), rot16(c0.y), rot16(c0.x), rot16(c1.w), rot16(c1.z), rot16(c1.y), rot16(c1.x)};
  unsigned u[7];
#pragma unroll
  for (int k = 0; k < 7; ++k) u[k] = __builtin_amdgcn_alignbit(e[k + 1], e[k], 16);
#pragma unroll
  for (int a = 0; a < 4; ++a) {
    *(v4u*)(dst + (2 * a) * stride) = (v4u){e[a], e[a + 1], e[a + 2], e[a + 3]};
    *(v4u*)(dst + (2 * a + 1) * stride) = (v4u){u[a], u[a + 1], u[a + 2], u[a + 3]};
  }
}
__device__ __forceinline__ unsigned y2pair(float y0, float y1, unsigned kv, float Dv) {
  const unsigned yy = pk2(y0, y1), kd = pk2(lo2f(kv) * Dv, hi2f(kv) * Dv);
  return nosent(pk2(lo2f(yy) + lo2f(kd), hi2f(yy) + hi2f(kd)));
}
__device__ __forceinline__ void conv_hi(char* smem, const char* in, float Dv, const rsrc_t Y, int d, int tid) {
  const int lane = tid & 63, w = tid >> 6;
  const v4u c0 = *(const v4u*)(in + tid * 16), kq = *(const v4u*)(in + 4096 + tid * 16);
  v4u c1 = *(const v4u*)(in + (tid ? tid * 16 - 16 : 0));
  if (tid == 0) c1 = (v4u){0u, 0u, 0u, 0u};
  u16* crev = (u16*)smem;
  u16* kvs = (u16*)(smem + LC_KVS);
  float* red = (float*)(smem + LC_RED);
  crev_put(crev + (263 - tid) * 8, CREV_STRIDE, c0, c1);
  *(v4u*)&kvs[(32 + (tid >> 2)) * 40 + (tid & 3) * 8] = kq;
  BAR();
  const int i = lane & 31, h = lane >> 5, X0 = 2048 + 63;
  const int rho = (X0 - i) & 7;
  const u16* aB = crev + rho * CREV_STRIDE + (X0 - (32 * w + i - 8 * h) - rho);
  const u16* bB = kvs + (64 + i - w) * 40 + 8 * h;
  f32x16 acc;
#pragma unroll
  for (int r = 0; r < 16; ++r) acc[r] = 0.f;
  v4u A[8], B[8];
#pragma unroll
  for (int q = 0; q < 8; ++q) { A[q] = *(const v4u*)(aB - 128 * (q >> 1) + 16 * (q & 1)); B[q] = *(const v4u*)(bB - 160 * (q >> 1) + 16 * (q & 1)); }
  asm volatile("s_nop 1" : "+v"(acc));
#pragma nounroll
  for (int m4 = 1; m4 <= 4; ++m4) {
    const u16* an = aB - 512 * (m4 & 3);
    const u16* bn = bB - 640 * (m4 & 3);
    PIN();
#pragma unroll
    for (int q = 0; q < 8; ++q) {
      asm volatile("v_mfma_f32_32x32x16_bf16 %0, %1, %2, %0" : "+v"(acc) : "v"(A[q]), "v"(B[q]));
      A[q] = *(const v4u*)(an - 128 * (q >> 1) + 16 * (q & 1)); B[q] = *(const v4u*)(bn - 160 * (q >> 1) + 16 * (q & 1));
      PIN();
    }
  }
  asm volatile("s_nop 7\n\ts_nop 7" : "+v"(acc));
#pragma unroll
  for (int r = 0; r < 4; ++r) *(f32x4*)&red[(w * 32 + i) * 36 + 8 * r + 4 * h] = (f32x4){acc[4 * r], acc[4 * r + 1], acc[4 * r + 2], acc[4 * r + 3]};
  BAR();
  if (tid >= 128) {
    const int l = tid & 127;
    const float* rr = red + (l >> 2) * 36 + (l & 3) * 8;
    f32x4 y[2];
#pragma unroll
    for (int e = 0; e < 2; ++e)
      y[e] = (*(const f32x4*)(rr + 4 * e) + *(const f32x4*)(rr + 32 * 36 + 4 * e)) + (*(const f32x4*)(rr + 64 * 36 + 4 * e) + *(const f32x4*)(rr + 96 * 36 + 4 * e));
    st16(Y, (unsigned)tid * 16u, (unsigned)d * YS,
         (v4u){y2pair(y[0][0], y[0][1], kq.x, Dv), y2pair(y[0][2], y[0][3], kq.y, Dv), y2pair(y[1][0], y[1][1], kq.z, Dv), y2pair(y[1][2], y[1][3], kq.w, Dv)});
  }
}
#define CL_STRIDE 1056
#define LC_WAVE 22016
__device__ __forceinline__ void conv_lo(char* sm, const v4u (&cw)[3], const v4u (&kw)[2], float Dv, const rsrc_t Y, int d, int lane) {
  u16* crev = (u16*)sm;
  u16* kvs = (u16*)(sm + 16 * CL_STRIDE);
  const v4u z = {0u, 0u, 0u, 0u};
  crev_put(crev + (127 - 2 * lane) * 8, CL_STRIDE, cw[1], lane ? cw[0] : z);
  crev_put(crev + (126 - 2 * lane) * 8, CL_STRIDE, cw[2], cw[1]);
  if (lane < 32) *(v4u*)(crev + (lane >> 2) * CL_STRIDE + (128 + (lane & 3)) * 8) = z;
  *(v4u*)&kvs[(lane >> 1) * 40 + (lane & 1) * 16] = z; *(v4u*)&kvs[(lane >> 1) * 40 + (lane & 1) * 16 + 8] = z;
  *(v4u*)&kvs[(32 + (lane >> 1)) * 40 + (lane & 1) * 16] = kw[0]; *(v4u*)&kvs[(32 + (lane >> 1)) * 40 + (lane & 1) * 16 + 8] = kw[1];
  asm volatile("" ::: "memory");
  const int i = lane & 31, h = lane >> 5;
  const int rho = (1023 - i) & 7;
  const u16* aB = crev + rho * CL_STRIDE + (1023 - (i - 8 * h) - rho);
  const u16* bB = kvs + (32 + i) * 40 + 8 * h;
  f32x16 acc;
#pragma unroll
  for (int r = 0; r < 16; ++r) acc[r] = 0.f;
  v4u A[8], B[8];
#pragma unroll
  for (int q = 0; q < 8; ++q) { A[q] = *(const v4u*)(aB - 32 * (q >> 1) + 16 * (q & 1)); B[q] = *(const v4u*)(bB - 40 * (q >> 1) + 16 * (q & 1)); }
  asm volatile("s_nop 1" : "+v"(acc));
#pragma nounroll
  for (int m4 = 1; m4 <= 8; ++m4) {
    const u16* an = aB - 128 * (m4 & 7);
    const u16* bn = bB - 160 * (m4 & 7);
    PIN();
#pragma unroll
    for (int q = 0; q < 8; ++q) {
      asm volatile("v_mfma_f32_32x32x16_bf16 %0, %1, %2, %0" : "+v"(acc) : "v"(A[q]), "v"(B[q]));
      A[q] = *(const v4u*)(an - 32 * (q >> 1) + 16 * (q & 1)); B[q] = *(const v4u*)(bn - 40 * (q >> 1) + 16 * (q & 1));
      PIN();
    }
  }
  v2u kv4[4];
#pragma unroll
  for (int r = 0; r < 4; ++r) kv4[r] = *(const v2u*)&kvs[(32 + i) * 40 + 8 * r + 4 * h];
  asm volatile("s_nop 7\n\ts_nop 7" : "+v"(acc));
  v2u o[4];
#pragma unroll
  for (int r = 0; r < 4; ++r) o[r] = (v2u){y2pair(acc[4 * r], acc[4 * r + 1], kv4[r].x, Dv), y2pair(acc[4 * r + 2], acc[4 * r + 3], kv4[r].y, Dv)};
#pragma unroll
  for (int r = 0; r < 4; ++r) *(v2u*)&kvs[i * 40 + 8 * r + 4 * h] = o[r];
  asm volatile("" ::: "memory");
#pragma unroll
  for (int k = 0; k < 2; ++k) st16(Y, (unsigned)(k * 64 + lane) * 16u, (unsigned)d * YS, *(const v4u*)&kvs[(16 * k + (lane >> 2)) * 40 + 8 * (lane & 3)]);
}

#define QN0 8
#define QN1 8
#define QN2 8
#define RW0 6
#define RW1 8
#define RW2 4
#define G0SLEEP 1
__global__ __launch_bounds__(256, 1) void k_main(MP p) {
  extern __shared__ __attribute__((aligned(16))) char smem[];
  const int tid = threadIdx.x;
  unsigned cpf = 0;
  {
    unsigned long long pc; unsigned len;
    asm volatile("s_getpc_b64 %0\n.Lcpf:\n\ts_mov_b32 %1, _Z5k_pre4PreP-.Lcpf" : "=s"(pc), "=s"(len));
    cpf = __builtin_amdgcn_raw_buffer_load_b32(mkr((const void*)pc, (int)len > 0 ? len & ~3u : 0u), (unsigned)(tid + 256 * ((blockIdx.x >> 3) & 1)) * 128u, 0, 0);
  }
  unsigned xcc; asm volatile("s_getreg_b32 %0, hwreg(HW_REG_XCC_ID)" : "=s"(xcc));
  xcc &= 7u;
  int* rl = (int*)(smem + L_ROLE);
  if (tid == 0) {
    const unsigned fl = p.ct[CT_FLIP];
    int r = 0;
#pragma nounroll
    for (unsigned k = 0; k < 8; ++k) {
      const unsigned x = xcc ^ k;
      const unsigned tk = __hip_atomic_fetch_add(&p.ct[CT_TICK + x * 32], 1u, __ATOMIC_RELAXED, __HIP_MEMORY_SCOPE_AGENT);
      if (tk < 32u) { r = (int)(x * 32u + tk); break; }
    }
    rl[0] = r; rl[1] = (int)(fl & 1u);
  }
  asm volatile("" :: "v"(cpf));
  BAR();
  int role = __builtin_amdgcn_readfirstlane(rl[0]);
  {
    const int xq = (role >> 5) & 3, tk = role & 31;
    const int nr = xq == 0 ? RW0 : xq == 1 ? RW1 : xq == 2 ? RW2 : 24 - RW0 - RW1 - RW2;
    const int rb = xq == 0 ? 0 : xq == 1 ? RW0 : xq == 2 ? RW0 + RW1 : RW0 + RW1 + RW2;
    const int nq = xq == 0 ? QN0 : xq == 1 ? QN1 : xq == 2 ? QN2 : 32 - QN0 - QN1 - QN2;
    const int qb = xq == 0 ? 0 : xq == 1 ? QN0 : xq == 2 ? QN0 + QN1 : QN0 + QN1 + QN2;
    if (tk < nq) { const int v = qb + tk; role = (role & 128) + (v >> 3) * 32 + (v & 7); }
    else if (tk < 32 - nr) { const int v = xq * 32 - rb - qb + tk - nq; role = (role & 128) + (v / 18) * 32 + 8 + v % 18; }
    else { const int v = rb + tk - (32 - nr); role = (role & 128) + (v / 6) * 32 + 26 + v % 6; }
  }
  const unsigned flip = (unsigned)__builtin_amdgcn_readfirstlane(rl[1]);
  const int x = role >> 5, i = role & 31, grp = x >> 2;
  const unsigned gb = (((unsigned)grp) ^ flip) * 8192u;
  const int rep = (int)(xcc & 3u);
  const int slot = i < 8 ? (x & 3) * 8 + i : (x & 3) * 18 + (i < 26 ? i - 8 : 0);
  const int rw = (x ^ 4) * 6 + (i - 26);
  if (grp || i >= 26) {
    const int lane = tid & 63, w = __builtin_amdgcn_readfirstlane(tid >> 6);
    const int e2 = (x & 3) * 26 + i, d1 = (i < 26 ? e2 : (rw < 8 ? 200 : 96 + rw)) * 4 + w;
    const int n2 = grp && i < 26 ? (e2 < 56 ? 6 : 5) : 0;
    const rsrc_t Y = mkr(p.Y, DM * YS);
    const int dl = lane == 0 ? d1 : e2 + 104 * (lane - 1);
    const unsigned dv = (unsigned)p.ssm_D[dl < DM ? dl : 0] << 16;
    v4u cw[3], kw[2], i2[6][2];
    {
      const size_t o = (size_t)(d1 < DM ? d1 : 0) * S_LEN + lane * 16;
      cw[0] = *(const v4u*)&p.ssm_k[lane ? o - 8 : o]; cw[1] = *(const v4u*)&p.ssm_k[o]; cw[2] = *(const v4u*)&p.ssm_k[o + 8];
      kw[0] = *(const v4u*)&p.kvT[o]; kw[1] = *(const v4u*)&p.kvT[o + 8];
    }
    VM0();
    if (i >= 26) arm_slots(p, rw >> 2, rw & 3);
    if (d1 < DM) conv_lo(smem + w * LC_WAVE, cw, kw, __uint_as_float(__builtin_amdgcn_readlane(dv, 0)), Y, d1, lane);
    if (d1 < DM && lane == 0) __hip_atomic_fetch_add(&p.ct[CT_YC + (d1 & 7) * 32], 1u, __ATOMIC_RELAXED, __HIP_MEMORY_SCOPE_AGENT);
    if (n2) {
#pragma unroll
      for (int c = 0; c < 6; ++c) {
        const int d = e2 + 104 * c < DM ? e2 + 104 * c : e2;
        i2[c][0] = *(const v4u*)&p.ssm_k[(size_t)d * S_LEN + tid * 8]; i2[c][1] = *(const v4u*)&p.kvT[(size_t)d * S_LEN + tid * 8];
      }
      BAR();
#pragma unroll
      for (int k = 0; k < 6; ++k) { *(v4u*)(smem + LC_IN + k * 8192 + tid * 16) = i2[k][0]; *(v4u*)(smem + LC_IN + 4096 + k * 8192 + tid * 16) = i2[k][1]; }
      if (tid < 64) *(v4u*)(smem + ((tid >> 3) * CREV_STRIDE + (264 + (tid & 7)) * 8) * 2) = (v4u){0u, 0u, 0u, 0u};
      if (tid < 128) *(v4u*)(smem + LC_KVS + ((tid >> 2) * 40 + (tid & 3) * 8) * 2) = (v4u){0u, 0u, 0u, 0u};
      BAR();
#pragma nounroll
      for (int c = 0; c < n2; ++c) {
        const u16* wb = (i < 8 ? p.WP : p.BP) + (size_t)slot * (16 * 18 * 512);
        if (c == 1) load_w<0, 2>(wb); else if (c == 2) load_w<2, 4>(wb); else if (c == 3) load_w<4, 6>(wb); else if (c == 4) load_w<6, 8>(wb);
        conv_hi(smem, smem + LC_IN + c * 8192, __uint_as_float(__builtin_amdgcn_readlane(dv, c + 1)), Y, e2 + 104 * c, tid);
        AOWN();
      }
    }
    BAR();
  } else if (i >= 8) {
    __builtin_amdgcn_s_sleep(G0SLEEP);
  }
  if (i < 8) qk_wg(p, role, grp, slot, rep, gb);
  else if (i < 26) v_wg(p, role, grp, slot, rep, gb);
  else row_wg(p, smem, role, rw, flip);
}

#define PRE_KV 144
#define PRE_PW 192
#define PRE_BG 384
#define PRE_FILL 40
#define BAUX 16
#define WAUX 16
#define FAUX 16
#define STW(base, bytes, idx16, v) __builtin_amdgcn_raw_buffer_store_b128(v, mkr(base, bytes), (unsigned)(idx16) * 16u, 0, FAUX)
struct PreP {
  const u16* kk; const u16* vv; const u16* shift_k; u16* kvT;
  const u16* Wq; const u16* Wk; const u16* Wv; const u16* Wo; u16* WP; u16* BP;
  const u16* mem0; v4u* win; v4u* Y; v4u* H; unsigned* ct;
};
__global__ __launch_bounds__(256) void k_pre(PreP p) {
  __shared__ __attribute__((aligned(16))) u16 sh[128 * 104 + 4 * 16 * 136];
  const int tid = threadIdx.x;
  int bid = blockIdx.x;
  if (bid < 16) {
    unsigned long long pc; int len, back;
    asm volatile("s_getpc_b64 %0\n.Lcpf0:\n\ts_mov_b32 %1, code_end-.Lcpf0\n\ts_mov_b32 %2, .Lcpf0-_Z6k_main2MP" : "=s"(pc), "=s"(len), "=s"(back));
    const bool ok = len > 0 && back > 0;
    const unsigned v = __builtin_amdgcn_raw_buffer_load_b32(mkr((const void*)pc, ok ? (unsigned)len & ~3u : 0u), (unsigned)tid * 128u, 0, 0);
    const unsigned u = __builtin_amdgcn_raw_buffer_load_b32(mkr((const void*)(pc - (unsigned)back), ok ? (unsigned)back & ~3u : 0u), (unsigned)(tid + 256 * (bid >> 3)) * 128u, 0, 0);
    asm volatile("" :: "v"(v), "v"(u));
  }
  if (bid == 0) {
    for (int i = tid; i < CT_TOTAL; i += 256) if (i < CT_FLIP || i >= CT_YC) p.ct[i] = 0u;
    const rsrc_t X = mkr(p.win, WIN_BYTES);
    unsigned xcc; asm volatile("s_getreg_b32 %0, hwreg(HW_REG_XCC_ID)" : "=s"(xcc));
    int* best = (int*)sh;
    int b0 = 1 << 30, b1 = 1 << 30;
    unsigned acc = 0;
    const unsigned ob = CAL_CH * CH + (unsigned)(tid >> 6) * 2048u;
    st16(X, ob + (unsigned)(((tid & 63) >> 3) * 256 + (tid & 7) * 16), 0, (v4u){0u, 0u, 0u, 0u});
    st16(X, ob + (unsigned)(((tid & 63) >> 3) * 256 + (tid & 7) * 16), 8192u, (v4u){0u, 0u, 0u, 0u});
    asm volatile("s_waitcnt vmcnt(0)" ::: "memory");
#pragma nounroll
    for (int i = 0; i < 16; ++i) {
      const unsigned o = ob + (unsigned)(i & 1) * 8192u + (unsigned)(i >> 1) * 256u;
      long long t0 = __builtin_amdgcn_s_memtime();
      unsigned v = ld4(X, o, 0);
      asm volatile("s_waitcnt vmcnt(0)" : "+v"(v) :: "memory");
      long long t1 = __builtin_amdgcn_s_memtime();
      acc += v;
      const int d = (int)(t1 - t0);
      if (i & 1) b1 = d < b1 ? d : b1; else b0 = d < b0 ? d : b0;
    }
    if ((tid & 63) == 0) { best[(tid >> 6) * 2] = b0; best[(tid >> 6) * 2 + 1] = b1; }
    __syncthreads();
    if (tid == 0) {
      for (int i = 1; i < 4; ++i) { b0 = best[2 * i] < b0 ? best[2 * i] : b0; b1 = best[2 * i + 1] < b1 ? best[2 * i + 1] : b1; }
      p.ct[CT_FLIP] = ((b1 < b0 ? 1u : 0u) ^ ((xcc >> 2) & 1u)) + acc;
    }
    return;
  }
  bid -= 1;
  bid = bid < PRE_KV + PRE_PW ? bid + PRE_BG : (bid < PRE_KV + PRE_PW + PRE_BG ? bid - (PRE_KV + PRE_PW) : bid);
  if (bid < PRE_BG) {
    const int r_ = bid >> 3, h = (bid & 7) + 8 * (r_ / 12), cq = (r_ % 12) >> 1, k0 = (r_ & 1) * 9;
    const int lane = tid & 63, w = __builtin_amdgcn_readfirstlane(tid >> 6), fr = lane & 15, g = lane >> 4;
    v4u af[3][2][4];
    {
      v4u ld[6];
#pragma unroll
      for (int i = 0; i < 6; ++i) { int c = tid + 256 * i, r = c / 12, q = c % 12; ld[i] = *(const v4u*)&p.Wo[(long)(h * DH + r) * DM + 96 * cq + 8 * q]; }
#pragma unroll
      for (int u = 0; u < 3; ++u)
#pragma unroll
        for (int T = 0; T < 2; ++T)
#pragma unroll
          for (int i = 0; i < 4; ++i)
            if (u < 2 || w == 0) af[u][T][i] = *(const v4u*)&p.Wv[(long)(32 * (k0 + w + 4 * u) + 8 * i + g + 4 * T) * HD + h * DH + 8 * fr];
#pragma unroll
      for (int i = 0; i < 6; ++i) { int c = tid + 256 * i, r = c / 12, q = c % 12; *(v4u*)&sh[r * 104 + 8 * q] = ld[i]; }
      u16* st = sh + 128 * 104 + w * (16 * 136);
#pragma unroll
      for (int u = 0; u < 3; ++u)
#pragma unroll
        for (int T = 0; T < 2; ++T)
          if (u < 2 || w == 0) {
#pragma unroll
            for (int i = 0; i < 4; ++i) *(v4u*)&st[(4 * i + g) * 136 + 8 * fr] = af[u][T][i];
            asm volatile("" ::: "memory");
#pragma unroll
            for (int es = 0; es < 4; ++es) af[u][T][es] = *(const v4u*)&st[fr * 136 + 32 * es + 8 * g];
            asm volatile("" ::: "memory");
          }
    }
    __syncthreads();
#pragma nounroll
    for (int ct = 0; ct < 6; ++ct) {
      v4u bf[4];
#pragma unroll
      for (int es = 0; es < 4; ++es) {
        const u16* a0 = &sh[(32 * es + 8 * g + (fr >> 2)) * 104 + 16 * ct + 4 * (fr & 3)];
        v2u x = __builtin_bit_cast(v2u, __builtin_amdgcn_ds_read_tr16_b64_v4i16((__attribute__((address_space(3))) s16x4*)a0));
        v2u y = __builtin_bit_cast(v2u, __builtin_amdgcn_ds_read_tr16_b64_v4i16((__attribute__((address_space(3))) s16x4*)(a0 + 4 * 104)));
        bf[es] = (v4u){x.x, x.y, y.x, y.y};
      }
      const int j = 12 * cq + 2 * ct + ((fr >> 1) & 1);
#pragma unroll
      for (int u = 0; u < 3; ++u) {
        if (u < 2 || w == 0) {
          f32x4 a = {0, 0, 0, 0}, b = {0, 0, 0, 0};
#pragma unroll
          for (int es = 0; es < 4; ++es) { a = MFMA32(af[u][0][es], bf[es], a); b = MFMA32(af[u][1][es], bf[es], b); }
          __builtin_amdgcn_raw_buffer_store_b128((v4u){pk2(a[0], a[1]), pk2(a[2], a[3]), pk2(b[0], b[1]), pk2(b[2], b[3])}, mkr(p.BP, NH * DM * DM * 2u),
              (unsigned)(((j * 16 + (h >> 1)) * 18 + k0 + w + 4 * u) * 64 + g * 16 + (h & 1) * 8 + 2 * (fr >> 2) + (fr & 1)) * 16u, 0, BAUX);
        }
      }
    }
    return;
  }
  bid -= PRE_BG;
  if (bid < PRE_KV) {
    const int d0 = (bid % 9) * 64, s0 = (bid / 9) * 128;
    const int dq = tid & 7, sr = tid >> 3;
    const int d = d0 + dq * 8, s = s0 + 4 * sr;
    v4u kr[7], vr[4], sw[4];
#pragma unroll
    for (int a = 0; a < 7; ++a) kr[a] = (s - 3 + a >= 0) ? *(const v4u*)&p.kk[(long)(s - 3 + a) * DM + d] : (v4u){0, 0, 0, 0};
#pragma unroll
    for (int a = 0; a < 4; ++a) { vr[a] = *(const v4u*)&p.vv[(long)(s + a) * DM + d]; sw[a] = *(const v4u*)&p.shift_k[d * 4 + 8 * a]; }
#pragma unroll
    for (int jp = 0; jp < 4; ++jp) {
      const float w0[4] = {lo2f(sw[jp].x), hi2f(sw[jp].x), lo2f(sw[jp].y), hi2f(sw[jp].y)}, w1[4] = {lo2f(sw[jp].z), hi2f(sw[jp].z), lo2f(sw[jp].w), hi2f(sw[jp].w)};
      unsigned o[4];
#pragma unroll
      for (int r = 0; r < 4; ++r) {
        unsigned acc = pk2(lo2f(kr[r][jp]) * w0[3], hi2f(kr[r][jp]) * w1[3]);
#pragma unroll
        for (int a = 1; a < 4; ++a) {
          const unsigned m = pk2(lo2f(kr[r + a][jp]) * w0[3 - a], hi2f(kr[r + a][jp]) * w1[3 - a]);
          acc = pk2(lo2f(acc) + lo2f(m), hi2f(acc) + hi2f(m));
        }
        o[r] = pk2(lo2f(acc) * lo2f(vr[r][jp]), hi2f(acc) * hi2f(vr[r][jp]));
      }
      *(v2u*)&sh[(dq * 8 + 2 * jp) * 136 + 4 * sr] = (v2u){(o[0] & 0xFFFFu) | (o[1] << 16), (o[2] & 0xFFFFu) | (o[3] << 16)};
      *(v2u*)&sh[(dq * 8 + 2 * jp + 1) * 136 + 4 * sr] = (v2u){(o[0] >> 16) | (o[1] & 0xFFFF0000u), (o[2] >> 16) | (o[3] & 0xFFFF0000u)};
    }
    __syncthreads();
#pragma unroll
    for (int i = 0; i < 4; ++i) {
      const int c = tid + 256 * i, dd = c >> 4, q = c & 15;
      STW(p.kvT, DM * S_LEN * 2u, (d0 + dd) * (S_LEN / 8) + s0 / 8 + q, *(const v4u*)&sh[dd * 136 + 8 * q]);
    }
    return;
  }
  bid -= PRE_KV;
  if (bid < PRE_PW) {
    u16 (*t)[136] = (u16 (*)[136])sh;
    const int kq = bid % 6, h = bid / 6;
    v4u ld[2][6];
#pragma unroll
    for (int i = 0; i < 6; ++i) {
      int c = tid + 256 * i, r = c >> 4, q = c & 15;
      ld[0][i] = *reinterpret_cast<const v4u*>(&p.Wq[(long)(96 * kq + r) * HD + h * DH + q * 8]);
      ld[1][i] = *reinterpret_cast<const v4u*>(&p.Wk[(long)(96 * kq + r) * HD + h * DH + q * 8]);
    }
#pragma unroll
    for (int which = 0; which < 2; ++which) {
      if (which) __syncthreads();
#pragma unroll
      for (int i = 0; i < 6; ++i) {
        int c = tid + 256 * i, r = c >> 4, q = c & 15;
        *reinterpret_cast<v4u*>(&t[r][q * 8]) = ld[which][i];
      }
      __syncthreads();
#pragma unroll
      for (int i = 0; i < 6; ++i) {
        int c = tid + 256 * i, k3 = c >> 9, tile = (c >> 6) & 7, l = c & 63, fr = l & 15, g = l >> 4;
        const u16* a0 = &t[32 * k3 + 8 * g + (fr >> 2)][16 * tile + 4 * (fr & 3)];
        v2u x = __builtin_bit_cast(v2u, __builtin_amdgcn_ds_read_tr16_b64_v4i16((__attribute__((address_space(3))) s16x4*)a0));
        v2u y = __builtin_bit_cast(v2u, __builtin_amdgcn_ds_read_tr16_b64_v4i16((__attribute__((address_space(3))) s16x4*)(a0 + 4 * 136)));
        __builtin_amdgcn_raw_buffer_store_b128((v4u){x.x, x.y, y.x, y.y}, mkr(p.WP, 2u * HD * DM * 2u), (unsigned)(((h * 16 + (tile >> 1) * 4 + which * 2 + (tile & 1)) * 18 + 3 * kq + k3) * 64 + l) * 16u, 0, WAUX);
      }
    }
    return;
  }
  bid -= PRE_PW;
  {
    const v4u ones = {0xFFFFFFFFu, 0xFFFFFFFFu, 0xFFFFFFFFu, 0xFFFFFFFFu};
    for (unsigned i = bid * 256 + tid; i < WIN_USED_CH * CH / 16u; i += PRE_FILL * 256) {
      if (i >= PT_CH * CH / 16u && (i & (8u * CH / 16u))) continue;
      v4u v = ones;
      const unsigned c = ((i >> 10) & 1u) * 512u + (i & 511u);
      if (i < 2u * NREP * CH / 16u && c < 576u) {
        const unsigned* s = (const unsigned*)&p.mem0[(2 * ((c >> 2) & 3) + (c & 1)) * DM + (c >> 4) * 16 + (c & 2)];
        v = (v4u){nosent(s[0]), nosent(s[2]), nosent(s[4]), nosent(s[6])};
      }
      STW(p.win, WIN_BYTES, i, v);
    }
    if (bid < 2) STW(p.win, WIN_BYTES, CTL_CH * CH / 16u + bid * 512 + tid, ((v4u){0u, 0u, 0u, 0u}));
    for (unsigned i = bid * 256 + tid; i < DM * 128; i += PRE_FILL * 256) STW(p.Y, DM * YS, (i >> 7) * (YS / 16) + (i & 127), ones);
    for (unsigned i = bid * 256 + tid; i < 12 * SEGB / 16; i += PRE_FILL * 256) STW(p.H, 2u * NSEG * SEGB, i, ones);
    if (bid * 256 + tid < 2 * SEGB / 16) STW(p.H, 2u * NSEG * SEGB, NSEG * SEGB / 16 + bid * 256 + tid, ones);
  }
}

extern "C" __device__ __attribute__((used, noinline)) void code_end() {}

extern "C" void kernel_launch(void* const* d_in, const int* in_sizes, int n_in, void* d_out, int out_size, void* d_ws,
                              size_t ws_size, hipStream_t stream) {
  const u16* A_q = (const u16*)d_in[0];
  const u16* A_k = (const u16*)d_in[1];
  const u16* A_v = (const u16*)d_in[2];
  const u16* A_shift = (const u16*)d_in[3];
  const u16* A_ssmk = (const u16*)d_in[4];
  const u16* A_ssmD = (const u16*)d_in[5];
  const u16* B_mem0 = (const u16*)d_in[7];
  const u16* B_Wq = (const u16*)d_in[8];
  const u16* B_Wk = (const u16*)d_in[9];
  const u16* B_Wv = (const u16*)d_in[10];
  const u16* B_Wo = (const u16*)d_in[11];

  char* ws = (char*)(((uintptr_t)d_ws + (WIN_BYTES - 1)) & ~(uintptr_t)(WIN_BYTES - 1));
  size_t off = 0;
  auto alloc = [&](size_t bytes) { char* q = ws + off; off += (bytes + 4095) & ~(size_t)4095; return q; };
  unsigned* win = (unsigned*)alloc(WIN_BYTES);
  unsigned* ct = (unsigned*)alloc(CT_TOTAL * 4);
  u16* Y = (u16*)alloc((size_t)DM * YS);
  u16* H = (u16*)alloc((size_t)2 * NSEG * SEGB);
  u16* OT = (u16*)alloc((size_t)2 * NSEG * SEGB);
  u16* WP = (u16*)alloc((size_t)2 * HD * DM * 2);
  u16* BP = (u16*)alloc((size_t)NH * DM * DM * 2);
  u16* kvT = (u16*)alloc((size_t)DM * S_LEN * 2);

  PreP pp;
  pp.kk = A_k; pp.vv = A_v; pp.shift_k = A_shift; pp.kvT = kvT;
  pp.Wq = B_Wq; pp.Wk = B_Wk; pp.Wv = B_Wv; pp.Wo = B_Wo; pp.WP = WP; pp.BP = BP;
  pp.mem0 = B_mem0; pp.win = (v4u*)win; pp.Y = (v4u*)Y; pp.H = (v4u*)H; pp.ct = ct;
  k_pre<<<1 + PRE_BG + PRE_KV + PRE_PW + PRE_FILL, 256, 0, stream>>>(pp);
  MP mp;
  mp.WP = WP; mp.BP = BP; mp.x0 = A_q; mp.kvT = kvT; mp.ssm_k = A_ssmk; mp.ssm_D = A_ssmD; mp.mem0 = B_mem0;
  mp.out = (u16*)d_out; mp.win = win; mp.ct = ct; mp.Y = Y; mp.H = H; mp.OT = OT;
  const int smem_bytes = L_TOTAL;
  (void)hipFuncSetAttribute((const void*)k_main, hipFuncAttributeMaxDynamicSharedMemorySize, smem_bytes);
  k_main<<<256, 256, smem_bytes, stream>>>(mp);
}
```

```cpp
#include <hip/hip_runtime.h>
#include <hip/hip_bf16.h>
#include <stdint.h>

typedef unsigned short u16;
using bf16x8 = __attribute__((ext_vector_type(8))) short;
using s16x4 = __attribute__((ext_vector_type(4))) short;
using f32x4 = __attribute__((ext_vector_type(4))) float;
using f32x2 = __attribute__((ext_vector_type(2))) float;
using f32x16 = __attribute__((ext_vector_type(16))) float;
using bfx2 = __attribute__((ext_vector_type(2))) __bf16;
typedef unsigned v4u __attribute__((ext_vector_type(4)));
typedef unsigned v2u __attribute__((ext_vector_type(2)));

#define S_LEN 2048
#define DM 576
#define NH 32
#define DH 128
#define HD (NH * DH)
#define SEG 32
#define NSEG 64
#define RGB 9216u
#define SEGB (4u * RGB)

__device__ __forceinline__ unsigned pk2(float a, float b) { f32x2 v = {a, b}; bfx2 r = __builtin_convertvector(v, bfx2); return __builtin_bit_cast(unsigned, r); }
__device__ __forceinline__ float lo2f(unsigned u) { return __uint_as_float(u << 16); }
__device__ __forceinline__ float hi2f(unsigned u) { return __uint_as_float(u & 0xFFFF0000u); }
__device__ __forceinline__ unsigned nosent(unsigned u) { return u < 0xFFFFFFFEu ? u : 0xFFFFFFFEu; }
__device__ __forceinline__ bf16x8 asb8(v4u v) { return __builtin_bit_cast(bf16x8, v); }
#define MFMA32(a, b, c) __builtin_amdgcn_mfma_f32_16x16x32_bf16(asb8(a), asb8(b), c, 0, 0, 0)

typedef __amdgpu_buffer_rsrc_t rsrc_t;
__device__ __forceinline__ rsrc_t mkr(const void* p, unsigned bytes) { return __builtin_amdgcn_make_buffer_rsrc((void*)p, 0, bytes, 0x00020000); }
__device__ __forceinline__ void st16(rsrc_t r, unsigned vo, unsigned so, v4u v) {
  asm volatile("" : "+v"(v));
  __builtin_amdgcn_raw_buffer_store_b128(v, r, vo, so, 16);
  asm volatile("s_nop 1" :: "v"(v));
}
__device__ __forceinline__ void st4(rsrc_t r, unsigned vo, unsigned so, unsigned v) { __builtin_amdgcn_raw_buffer_store_b32(v, r, vo, so, 16); }
__device__ __forceinline__ v4u ld16(rsrc_t r, unsigned vo, unsigned so) { return __builtin_amdgcn_raw_buffer_load_b128(r, vo, so, 16); }
__device__ __forceinline__ unsigned ld4(rsrc_t r, unsigned vo, unsigned so) { return __builtin_amdgcn_raw_buffer_load_b32(r, vo, so, 16); }

#define WIN_BYTES (2u << 20)
#define CH 16384u
#define NREP 4
#define MEM_CH 0
#define PM_CH 32
#define PT_CH 64
#define WIN_USED_CH 88
#define CTL_CH 96
#define CAL_CH 127
#define CT_TICK 0
#define CT_TMO 256
#define CT_FLIP 352
#define CT_YC 384
#define CT_TOTAL 640
#define YS 4352u
#define SENT 0xFFFFFFFFu
#define SPIN_CAP 400000u

struct MP {
  const u16* WP; const u16* BP; const u16* x0; const u16* kvT; const u16* ssm_k; const u16* ssm_D; const u16* mem0;
  u16* out; unsigned* win; unsigned* ct; u16* Y; u16* H; u16* OT;
};

#define BAR() do { asm volatile("s_waitcnt lgkmcnt(0)\n\ts_barrier" ::: "memory"); } while (0)
#define VM0() __builtin_amdgcn_s_waitcnt(0x0F70)
#define PIN() __builtin_amdgcn_sched_barrier(0)
__device__ __forceinline__ unsigned umax3(unsigned a, unsigned b, unsigned c) { return __builtin_elementwise_max(__builtin_elementwise_max(a, b), c); }
__device__ __forceinline__ unsigned mx4(unsigned m, v4u v) { return umax3(umax3(m, v.x, v.y), v.z, v.w); }
__device__ __forceinline__ unsigned phys(unsigned o) { return (o >> 13) * CH + (o & 8191u); }
#define AOWN() asm volatile("" ::: "a0", "a1", "a2", "a3", "a4", "a5", "a6", "a7", "a8", "a9", "a10", "a11", "a12", "a13", "a14", "a15", "a16", "a17", "a18", "a19", "a20", "a21", "a22", "a23", "a24", "a25", "a26", "a27", "a28", "a29", "a30", "a31", "a32", "a33", "a34", "a35", "a36", "a37", "a38", "a39", "a40", "a41", "a42", "a43", "a44", "a45", "a46", "a47", "a48", "a49", "a50", "a51", "a52", "a53", "a54", "a55", "a56", "a57", "a58", "a59", "a60", "a61", "a62", "a63", "a64", "a65", "a66", "a67", "a68", "a69", "a70", "a71", "a72", "a73", "a74", "a75", "a76", "a77", "a78", "a79", "a80", "a81", "a82", "a83", "a84", "a85", "a86", "a87", "a88", "a89", "a90", "a91", "a92", "a93", "a94", "a95", "a96", "a97", "a98", "a99", "a100", "a101", "a102", "a103", "a104", "a105", "a106", "a107", "a108", "a109", "a110", "a111", "a112", "a113", "a114", "a115", "a116", "a117", "a118", "a119", "a120", "a121", "a122", "a123", "a124", "a125", "a126", "a127", "a128", "a129", "a130", "a131", "a132", "a133", "a134", "a135", "a136", "a137", "a138", "a139", "a140", "a141", "a142", "a143", "a144", "a145", "a146", "a147", "a148", "a149", "a150", "a151", "a152", "a153", "a154", "a155", "a156", "a157", "a158", "a159", "a160", "a161", "a162", "a163", "a164", "a165", "a166", "a167", "a168", "a169", "a170", "a171", "a172", "a173", "a174", "a175", "a176", "a177", "a178", "a179", "a180", "a181", "a182", "a183", "a184", "a185", "a186", "a187", "a188", "a189", "a190", "a191", "a192", "a193", "a194", "a195", "a196", "a197", "a198", "a199", "a200", "a201", "a202", "a203", "a204", "a205", "a206", "a207", "a208", "a209", "a210", "a211", "a212", "a213", "a214", "a215", "a216", "a217", "a218", "a219", "a220", "a221", "a222", "a223", "a224", "a225", "a226", "a227", "a228", "a229", "a230", "a231", "a232", "a233", "a234", "a235", "a236", "a237", "a238", "a239", "a240", "a241", "a242", "a243", "a244", "a245", "a246", "a247", "a248", "a249", "a250", "a251", "a252", "a253", "a254", "a255")
template <int N> struct IC { static constexpr int v = N; };
template <int B, int E, typename F> __device__ __forceinline__ void sfor(F&& f) { if constexpr (B < E) { f(IC<B>{}); sfor<B + 1, E>(f); } }

__device__ __forceinline__ bool spin_fail(unsigned& spins, rsrc_t F) {
  if (__builtin_expect((++spins & 1023u) != 0u, 1)) return false;
  if (spins > SPIN_CAP) st4(F, CT_TMO * 4u, 0, 1u);
  VM0();
  return (__builtin_amdgcn_readfirstlane(ld4(F, CT_TMO * 4u, 0)) != 0) | (spins > SPIN_CAP);
}

#define RED8(name, ins) __device__ __forceinline__ float name(float a) { float d, t; asm volatile("s_nop 1\n\t" ins "_dpp %0, %2, %2 row_ror:8 row_mask:0xf bank_mask:0xf\n\t" \
    "v_mov_b32 %1, %0\n\ts_nop 1\n\tv_permlane16_swap_b32 %0, %1\n\t" ins " %0, %0, %1\n\tv_mov_b32 %1, %0\n\ts_nop 1\n\tv_permlane32_swap_b32 %0, %1\n\t" ins " %0, %0, %1" \
    : "=&v"(d), "=&v"(t) : "v"(a)); return d; }
RED8(i_red8add, "v_add_f32")

__device__ __forceinline__ void wait_time(const rsrc_t X, unsigned o, int c) {
  unsigned v = 0;
#pragma nounroll
  for (int i = 0; i < 2000; ++i) {
    asm volatile("" ::: "memory");
    v = __builtin_amdgcn_readfirstlane(ld4(X, o, 0));
    if (v) break;
    __builtin_amdgcn_s_sleep(12);
  }
  v += (unsigned)c;
#pragma nounroll
  while ((unsigned)__builtin_amdgcn_s_memrealtime() - v > 0xFFFFF000u) __builtin_amdgcn_s_sleep(1);
}
__device__ __forceinline__ v4u mkd(const void* p, unsigned bytes) { return (v4u){(unsigned)(uintptr_t)p, (unsigned)((uintptr_t)p >> 32) & 0xFFFFu, bytes, 0x00020000u}; }
typedef __attribute__((address_space(3))) char* lds_ptr;
#define DROP 0x40000000u

#define L_QI 36864
#define L_KT 102400
#define LV_MEM 36864
#define LV_RM 49152
#define LV_RT 53248
#define L_ROLE 137728
#define L_TOTAL 137792
__device__ __forceinline__ v2u trd(const char* a) { return __builtin_bit_cast(v2u, __builtin_amdgcn_ds_read_tr16_b64_v4i16((__attribute__((address_space(3))) s16x4*)a)); }
template <int A0>
__device__ __forceinline__ void ld_w(const char* sb, unsigned vo) {
  asm volatile("global_load_dwordx4 a[%c2:%c3], %0, %1\n\tglobal_load_dwordx4 a[%c4:%c5], %0, %1 offset:1024" :: "v"(vo), "s"(sb), "i"(A0), "i"(A0 + 3), "i"(A0 + 4), "i"(A0 + 7) : "memory");
}
template <int S0, int S1>
__device__ __forceinline__ void load_w(const u16* base) {
  unsigned vo = (threadIdx.x >> 6) * (4 * 18 * 1024) + (threadIdx.x & 63) * 16;
  asm volatile("" : "+v"(vo));
  AOWN();
  sfor<S0, S1>([&](auto S) {
    const char* Ws = (const char*)base + 2048 * S.v;
    sfor<0, 4>([&](auto K) { ld_w<(S.v * 4 + K.v) * 8>(Ws + K.v * 18 * 1024, vo); });
  });
}

#define QK_ASM \
  "s_nop 4\n" \
  "s_cmp_eq_u32 %[grp], 0\n" \
  "s_cbranch_scc1 .Lqk_w0\n" \
  "v_add_u32 v229, 16384, %[wo]\n" \
  "global_load_dwordx4 v[32:35], v229, %[wp] offset:0\n" \
  "global_load_dwordx4 v[36:39], v229, %[wp] offset:1024\n" \
  "v_add_u32 v229, 18432, v229\n" \
  "global_load_dwordx4 v[40:43], v229, %[wp] offset:0\n" \
  "global_load_dwordx4 v[44:47], v229, %[wp] offset:1024\n" \
  "v_add_u32 v229, 18432, v229\n" \
  "global_load_dwordx4 v[48:51], v229, %[wp] offset:0\n" \
  "global_load_dwordx4 v[52:55], v229, %[wp] offset:1024\n" \
  "v_add_u32 v229, 18432, v229\n" \
  "global_load_dwordx4 v[56:59], v229, %[wp] offset:0\n" \
  "global_load_dwordx4 v[60:63], v229, %[wp] offset:1024\n" \
  "s_branch .Lqk_w1\n" \
  ".Lqk_w0:\n" \
  "v_add_u32 v229, 0, %[wo]\n" \
  "global_load_dwordx4 a[0:3], v229, %[wp] offset:0\n" \
  "global_load_dwordx4 a[4:7], v229, %[wp] offset:1024\n" \
  "global_load_dwordx4 a[32:35], v229, %[wp] offset:2048\n" \
  "global_load_dwordx4 a[36:39], v229, %[wp] offset:3072\n" \
  "v_add_u32 v229, 4096, v229\n" \
  "global_load_dwordx4 a[64:67], v229, %[wp] offset:0\n" \
  "global_load_dwordx4 a[68:71], v229, %[wp] offset:1024\n" \
  "global_load_dwordx4 a[96:99], v229, %[wp] offset:2048\n" \
  "global_load_dwordx4 a[100:103], v229, %[wp] offset:3072\n" \
  "v_add_u32 v229, 4096, v229\n" \
  "global_load_dwordx4 a[128:131], v229, %[wp] offset:0\n" \
  "global_load_dwordx4 a[132:135], v229, %[wp] offset:1024\n" \
  "global_load_dwordx4 a[160:163], v229, %[wp] offset:2048\n" \
  "global_load_dwordx4 a[164:167], v229, %[wp] offset:3072\n" \
  "v_add_u32 v229, 4096, v229\n" \
  "global_load_dwordx4 a[192:195], v229, %[wp] offset:0\n" \
  "global_load_dwordx4 a[196:199], v229, %[wp] offset:1024\n" \
  "global_load_dwordx4 a[224:227], v229, %[wp] offset:2048\n" \
  "global_load_dwordx4 a[228:231], v229, %[wp] offset:3072\n" \
  "v_add_u32 v229, 4096, v229\n" \
  "global_load_dwordx4 v[32:35], v229, %[wp] offset:0\n" \
  "global_load_dwordx4 v[36:39], v229, %[wp] offset:1024\n" \
  "v_add_u32 v229, 2048, v229\n" \
  "global_load_dwordx4 a[8:11], v229, %[wp] offset:0\n" \
  "global_load_dwordx4 a[12:15], v229, %[wp] offset:1024\n" \
  "global_load_dwordx4 a[40:43], v229, %[wp] offset:2048\n" \
  "global_load_dwordx4 a[44:47], v229, %[wp] offset:3072\n" \
  "v_add_u32 v229, 4096, v229\n" \
  "global_load_dwordx4 a[72:75], v229, %[wp] offset:0\n" \
  "global_load_dwordx4 a[76:79], v229, %[wp] offset:1024\n" \
  "global_load_dwordx4 a[104:107], v229, %[wp] offset:2048\n" \
  "global_load_dwordx4 a[108:111], v229, %[wp] offset:3072\n" \
  "v_add_u32 v229, 4096, v229\n" \
  "global_load_dwordx4 a[136:139], v229, %[wp] offset:0\n" \
  "global_load_dwordx4 a[140:143], v229, %[wp] offset:1024\n" \
  "global_load_dwordx4 a[168:171], v229, %[wp] offset:2048\n" \
  "global_load_dwordx4 a[172:175], v229, %[wp] offset:3072\n" \
  "v_add_u32 v229, 4096, v229\n" \
  "global_load_dwordx4 a[200:203], v229, %[wp] offset:0\n" \
  "global_load_dwordx4 a[204:207], v229, %[wp] offset:1024\n" \
  "global_load_dwordx4 a[232:235], v229, %[wp] offset:2048\n" \
  "global_load_dwordx4 a[236:239], v229, %[wp] offset:3072\n" \
  "v_add_u32 v229, 4096, v229\n" \
  "global_load_dwordx4 v[40:43], v229, %[wp] offset:0\n" \
  "global_load_dwordx4 v[44:47], v229, %[wp] offset:1024\n" \
  "v_add_u32 v229, 2048, v229\n" \
  "global_load_dwordx4 a[16:19], v229, %[wp] offset:0\n" \
  "global_load_dwordx4 a[20:23], v229, %[wp] offset:1024\n" \
  "global_load_dwordx4 a[48:51], v229, %[wp] offset:2048\n" \
  "global_load_dwordx4 a[52:55], v229, %[wp] offset:3072\n" \
  "v_add_u32 v229, 4096, v229\n" \
  "global_load_dwordx4 a[80:83], v229, %[wp] offset:0\n" \
  "global_load_dwordx4 a[84:87], v229, %[wp] offset:1024\n" \
  "global_load_dwordx4 a[112:115], v229, %[wp] offset:2048\n" \
  "global_load_dwordx4 a[116:119], v229, %[wp] offset:3072\n" \
  "v_add_u32 v229, 4096, v229\n" \
  "global_load_dwordx4 a[144:147], v229, %[wp] offset:0\n" \
  "global_load_dwordx4 a[148:151], v229, %[wp] offset:1024\n" \
  "global_load_dwordx4 a[176:179], v229, %[wp] offset:2048\n" \
  "global_load_dwordx4 a[180:183], v229, %[wp] offset:3072\n" \
  "v_add_u32 v229, 4096, v229\n" \
  "global_load_dwordx4 a[208:211], v229, %[wp] offset:0\n" \
  "global_load_dwordx4 a[212:215], v229, %[wp] offset:1024\n" \
  "global_load_dwordx4 a[240:243], v229, %[wp] offset:2048\n" \
  "global_load_dwordx4 a[244:247], v229, %[wp] offset:3072\n" \
  "v_add_u32 v229, 4096, v229\n" \
  "global_load_dwordx4 v[48:51], v229, %[wp] offset:0\n" \
  "global_load_dwordx4 v[52:55], v229, %[wp] offset:1024\n" \
  "v_add_u32 v229, 2048, v229\n" \
  "global_load_dwordx4 a[24:27], v229, %[wp] offset:0\n" \
  "global_load_dwordx4 a[28:31], v229, %[wp] offset:1024\n" \
  "global_load_dwordx4 a[56:59], v229, %[wp] offset:2048\n" \
  "global_load_dwordx4 a[60:63], v229, %[wp] offset:3072\n" \
  "v_add_u32 v229, 4096, v229\n" \
  "global_load_dwordx4 a[88:91], v229, %[wp] offset:0\n" \
  "global_load_dwordx4 a[92:95], v229, %[wp] offset:1024\n" \
  "global_load_dwordx4 a[120:123], v229, %[wp] offset:2048\n" \
  "global_load_dwordx4 a[124:127], v229, %[wp] offset:3072\n" \
  "v_add_u32 v229, 4096, v229\n" \
  "global_load_dwordx4 a[152:155], v229, %[wp] offset:0\n" \
  "global_load_dwordx4 a[156:159], v229, %[wp] offset:1024\n" \
  "global_load_dwordx4 a[184:187], v229, %[wp] offset:2048\n" \
  "global_load_dwordx4 a[188:191], v229, %[wp] offset:3072\n" \
  "v_add_u32 v229, 4096, v229\n" \
  "global_load_dwordx4 a[216:219], v229, %[wp] offset:0\n" \
  "global_load_dwordx4 a[220:223], v229, %[wp] offset:1024\n" \
  "global_load_dwordx4 a[248:251], v229, %[wp] offset:2048\n" \
  "global_load_dwordx4 a[252:255], v229, %[wp] offset:3072\n" \
  "v_add_u32 v229, 4096, v229\n" \
  "global_load_dwordx4 v[56:59], v229, %[wp] offset:0\n" \
  "global_load_dwordx4 v[60:63], v229, %[wp] offset:1024\n" \
  "v_add_u32 v229, 2048, v229\n" \
  ".Lqk_w1:\n" \
  "s_mov_b32 s60, -2\n" \
  "s_mov_b32 s70, 0x3db504f3\n" \
  "s_mov_b32 s71, 0x3e0293ee\n" \
  "v_add_u32 v228, 0x1000, %[mo]\n" \
  "v_mov_b32 v230, 0x7fffffff\n" \
  "v_mov_b32 v232, 0xff800000\n" \
  "s_mov_b32 s78, 0\n" \
  "s_mov_b32 s79, 0\n" \
  "s_mov_b32 s80, 0\n" \
  "s_mov_b32 s65, 2.0\n" \
  "s_mov_b32 s81, -1\n" \
  "s_mov_b32 s87, 2.0\n" \
  "s_mov_b32 s93, 0\n" \
  "s_mov_b32 s94, 0\n" \
  "s_lshl_b32 s96, s60, 4\n" \
  "s_add_i32 s96, s96, %[bas]\n" \
  "v_cmp_lt_i32_e64 vcc, %[lim], 0\n" \
  "s_nop 1\n" \
  "v_cndmask_b32 v212, 0, v232, vcc\n" \
  "v_cmp_lt_i32_e64 vcc, %[lim], 1\n" \
  "s_nop 1\n" \
  "v_cndmask_b32 v213, 0, v232, vcc\n" \
  "v_cmp_lt_i32_e64 vcc, %[lim], 2\n" \
  "s_nop 1\n" \
  "v_cndmask_b32 v214, 0, v232, vcc\n" \
  "v_cmp_lt_i32_e64 vcc, %[lim], 3\n" \
  "s_nop 1\n" \
  "v_cndmask_b32 v215, 0, v232, vcc\n" \
  "v_cmp_lt_i32_e64 vcc, %[lim], 16\n" \
  "s_nop 1\n" \
  "v_cndmask_b32 v216, 0, v232, vcc\n" \
  "v_cmp_lt_i32_e64 vcc, %[lim], 17\n" \
  "s_nop 1\n" \
  "v_cndmask_b32 v217, 0, v232, vcc\n" \
  "v_cmp_lt_i32_e64 vcc, %[lim], 18\n" \
  "s_nop 1\n" \
  "v_cndmask_b32 v218, 0, v232, vcc\n" \
  "v_cmp_lt_i32_e64 vcc, %[lim], 19\n" \
  "s_nop 1\n" \
  "v_cndmask_b32 v219, 0, v232, vcc\n" \
  "s_mov_b32 s90, 0\n" \
  "s_waitcnt vmcnt(0)\n" \
  ".Lqk_top:\n" \
  "v_mfma_f32_16x16x32_bf16 v[116:119], v[164:167], a[32:35], v[116:119]\n" \
  "ds_read_b128 v[148:151], %[hA] offset:2048\n" \
  "buffer_store_dwordx2 v[232:233], %[po3], %[X], s65 offen sc1\n" \
  "v_mfma_f32_16x16x32_bf16 v[132:135], v[168:171], a[32:35], v[132:135]\n" \
  "ds_read_b128 v[152:155], %[hA] offset:20480\n" \
  "v_mfma_f32_16x16x32_bf16 v[120:123], v[164:167], a[40:43], v[120:123]\n" \
  "buffer_load_dword v251, %[cpo], %[X], s94 offen sc1\n" \
  "s_xor_b32 s76, s96, 0x2000\n" \
  "v_mfma_f32_16x16x32_bf16 v[136:139], v[168:171], a[40:43], v[136:139]\n" \
  "buffer_load_dword v255, %[cpa], %[X], s76 offen sc1\n" \
  "v_mfma_f32_16x16x32_bf16 v[124:127], a[48:51], v[164:167], v[124:127]\n" \
  "s_add_i32 s67, s60, 2\n" \
  "s_min_i32 s67, s67, 63\n" \
  "s_mul_i32 s61, s67, 36864\n" \
  "v_mfma_f32_16x16x32_bf16 v[140:143], a[48:51], v[168:171], v[140:143]\n" \
  "s_lshl_b32 s67, s60, 16\n" \
  "s_and_b32 s67, s67, 0x10000\n" \
  "s_add_i32 s62, %[hl0], s67\n" \
  "v_mfma_f32_16x16x32_bf16 v[128:131], a[56:59], v[164:167], v[128:131]\n" \
  "s_add_i32 m0, s62, 0\n" \
  "v_mfma_f32_16x16x32_bf16 v[144:147], a[56:59], v[168:171], v[144:147]\n" \
  "s_add_i32 s68, s60, 1\n" \
  "s_lshl_b32 s73, s68, 17\n" \
  "s_and_b32 s73, s73, 0x60000\n" \
  "v_mfma_f32_16x16x32_bf16 v[116:119], v[172:175], a[36:39], v[116:119]\n" \
  "ds_read_b128 v[156:159], %[hA] offset:2560\n" \
  "buffer_load_dword v250, off, %[X], s96 offset:16 sc1\n" \
  "v_mfma_f32_16x16x32_bf16 v[132:135], v[176:179], a[36:39], v[132:135]\n" \
  "ds_read_b128 v[160:163], %[hA] offset:20992\n" \
  "v_mov_b64 v[100:101], 0\n" \
  "v_mfma_f32_16x16x32_bf16 v[120:123], v[172:175], a[44:47], v[120:123]\n" \
  "buffer_load_dwordx4 %[hvo], %[HS], s61 offen offset:0 sc1 lds\n" \
  "v_mov_b64 v[102:103], 0\n" \
  "v_mfma_f32_16x16x32_bf16 v[136:139], v[176:179], a[44:47], v[136:139]\n" \
  "s_add_i32 s63, s87, s73\n" \
  "s_add_i32 s64, s63, 0x4000\n" \
  "s_lshl_b32 s73, s68, 18\n" \
  "v_mfma_f32_16x16x32_bf16 v[124:127], a[52:55], v[172:175], v[124:127]\n" \
  "s_and_b32 s73, s73, 0x40000\n" \
  "s_add_i32 s65, %[pm0], s73\n" \
  "s_lshl_b32 s73, s68, 30\n" \
  "v_mfma_f32_16x16x32_bf16 v[140:143], a[52:55], v[176:179], v[140:143]\n" \
  "s_and_b32 s66, s73, 0x80000000\n" \
  "s_cmp_lt_u32 s68, 64\n" \
  "s_cselect_b32 s65, s65, 2.0\n" \
  "v_mfma_f32_16x16x32_bf16 v[128:131], a[60:63], v[172:175], v[128:131]\n" \
  "buffer_load_dwordx4 %[hvo], %[HS], s61 offen offset:1024 sc1 lds\n" \
  "v_mov_b64 v[104:105], 0\n" \
  "v_mfma_f32_16x16x32_bf16 v[144:147], a[60:63], v[176:179], v[144:147]\n" \
  "v_mov_b64 v[106:107], 0\n" \
  "s_waitcnt lgkmcnt(2)\n" \
  "v_mfma_f32_16x16x32_bf16 v[116:119], v[148:151], a[64:67], v[116:119]\n" \
  "ds_read_b128 v[164:167], %[hA] offset:3072\n" \
  "v_mov_b64 v[108:109], 0\n" \
  "v_mfma_f32_16x16x32_bf16 v[132:135], v[152:155], a[64:67], v[132:135]\n" \
  "ds_read_b128 v[168:171], %[hA] offset:21504\n" \
  "v_mov_b64 v[110:111], 0\n" \
  "v_mfma_f32_16x16x32_bf16 v[120:123], v[148:151], a[72:75], v[120:123]\n" \
  "buffer_load_dwordx4 %[hvo], %[HS], s61 offen offset:2048 sc1 lds\n" \
  "v_mov_b64 v[112:113], 0\n" \
  "v_mfma_f32_16x16x32_bf16 v[136:139], v[152:155], a[72:75], v[136:139]\n" \
  "s_ashr_i32 s67, s66, 31\n" \
  "v_not_b32 v232, s67\n" \
  "v_mov_b32 v233, s90\n" \
  "v_mfma_f32_16x16x32_bf16 v[124:127], a[80:83], v[148:151], v[124:127]\n" \
  "v_mov_b64 v[114:115], 0\n" \
  "v_mfma_f32_16x16x32_bf16 v[140:143], a[80:83], v[152:155], v[140:143]\n" \
  "v_mfma_f32_16x16x32_bf16 v[128:131], a[88:91], v[148:151], v[128:131]\n" \
  "buffer_load_dwordx4 %[hvo], %[HS], s61 offen offset:3072 sc1 lds\n" \
  "v_mfma_f32_16x16x32_bf16 v[144:147], a[88:91], v[152:155], v[144:147]\n" \
  "s_add_i32 s61, s61, 0x1000\n" \
  "s_add_i32 m0, s62, 4096\n" \
  "s_waitcnt lgkmcnt(2)\n" \
  "v_mfma_f32_16x16x32_bf16 v[116:119], v[156:159], a[68:71], v[116:119]\n" \
  "ds_read_b128 v[172:175], %[hA] offset:3584\n" \
  "v_mfma_f32_16x16x32_bf16 v[132:135], v[160:163], a[68:71], v[132:135]\n" \
  "ds_read_b128 v[176:179], %[hA] offset:22016\n" \
  "v_mfma_f32_16x16x32_bf16 v[120:123], v[156:159], a[76:79], v[120:123]\n" \
  "buffer_load_dwordx4 %[hvo], %[HS], s61 offen offset:0 sc1 lds\n" \
  "v_mfma_f32_16x16x32_bf16 v[136:139], v[160:163], a[76:79], v[136:139]\n" \
  "v_mfma_f32_16x16x32_bf16 v[124:127], a[84:87], v[156:159], v[124:127]\n" \
  "v_mfma_f32_16x16x32_bf16 v[140:143], a[84:87], v[160:163], v[140:143]\n" \
  "v_mfma_f32_16x16x32_bf16 v[128:131], a[92:95], v[156:159], v[128:131]\n" \
  "buffer_load_dwordx4 %[hvo], %[HS], s61 offen offset:1024 sc1 lds\n" \
  "v_mfma_f32_16x16x32_bf16 v[144:147], a[92:95], v[160:163], v[144:147]\n" \
  "s_waitcnt lgkmcnt(2)\n" \
  "v_mfma_f32_16x16x32_bf16 v[116:119], v[164:167], a[96:99], v[116:119]\n" \
  "ds_read_b128 v[148:151], %[hA] offset:4096\n" \
  "v_mfma_f32_16x16x32_bf16 v[132:135], v[168:171], a[96:99], v[132:135]\n" \
  "ds_read_b128 v[152:155], %[hA] offset:22528\n" \
  "v_mfma_f32_16x16x32_bf16 v[120:123], v[164:167], a[104:107], v[120:123]\n" \
  "buffer_load_dwordx4 %[hvo], %[HS], s61 offen offset:2048 sc1 lds\n" \
  "v_mfma_f32_16x16x32_bf16 v[136:139], v[168:171], a[104:107], v[136:139]\n" \
  "v_mfma_f32_16x16x32_bf16 v[124:127], a[112:115], v[164:167], v[124:127]\n" \
  "v_mfma_f32_16x16x32_bf16 v[140:143], a[112:115], v[168:171], v[140:143]\n" \
  "v_mfma_f32_16x16x32_bf16 v[128:131], a[120:123], v[164:167], v[128:131]\n" \
  "buffer_load_dwordx4 %[hvo], %[HS], s61 offen offset:3072 sc1 lds\n" \
  "v_mfma_f32_16x16x32_bf16 v[144:147], a[120:123], v[168:171], v[144:147]\n" \
  "s_add_i32 s61, s61, 0x1000\n" \
  "s_add_i32 m0, s62, 8192\n" \
  "s_waitcnt lgkmcnt(2)\n" \
  "v_mfma_f32_16x16x32_bf16 v[116:119], v[172:175], a[100:103], v[116:119]\n" \
  "ds_read_b128 v[156:159], %[hA] offset:4608\n" \
  "v_mfma_f32_16x16x32_bf16 v[132:135], v[176:179], a[100:103], v[132:135]\n" \
  "ds_read_b128 v[160:163], %[hA] offset:23040\n" \
  "v_mfma_f32_16x16x32_bf16 v[120:123], v[172:175], a[108:111], v[120:123]\n" \
  "buffer_load_dwordx4 %[hvo], %[HS], s61 offen offset:0 sc1 lds\n" \
  "v_mfma_f32_16x16x32_bf16 v[136:139], v[176:179], a[108:111], v[136:139]\n" \
  "v_mfma_f32_16x16x32_bf16 v[124:127], a[116:119], v[172:175], v[124:127]\n" \
  "v_mfma_f32_16x16x32_bf16 v[140:143], a[116:119], v[176:179], v[140:143]\n" \
  "buffer_store_dword v232, %[pcl], %[X], s65 offen sc1\n" \
  "v_mfma_f32_16x16x32_bf16 v[128:131], a[124:127], v[172:175], v[128:131]\n" \
  "v_mfma_f32_16x16x32_bf16 v[144:147], a[124:127], v[176:179], v[144:147]\n" \
  "buffer_atomic_umax v233, %[rpo], %[X], s93 offen sc1\n" \
  "s_waitcnt lgkmcnt(2)\n" \
  "v_mfma_f32_16x16x32_bf16 v[116:119], v[148:151], a[128:131], v[116:119]\n" \
  "ds_read_b128 v[164:167], %[hA] offset:5120\n" \
  "v_mfma_f32_16x16x32_bf16 v[132:135], v[152:155], a[128:131], v[132:135]\n" \
  "ds_read_b128 v[168:171], %[hA] offset:23552\n" \
  "v_mfma_f32_16x16x32_bf16 v[120:123], v[148:151], a[136:139], v[120:123]\n" \
  "v_mfma_f32_16x16x32_bf16 v[136:139], v[152:155], a[136:139], v[136:139]\n" \
  "v_mfma_f32_16x16x32_bf16 v[124:127], a[144:147], v[148:151], v[124:127]\n" \
  "v_mfma_f32_16x16x32_bf16 v[140:143], a[144:147], v[152:155], v[140:143]\n" \
  "v_mfma_f32_16x16x32_bf16 v[128:131], a[152:155], v[148:151], v[128:131]\n" \
  "v_mfma_f32_16x16x32_bf16 v[144:147], a[152:155], v[152:155], v[144:147]\n" \
  "s_waitcnt lgkmcnt(2)\n" \
  "v_mfma_f32_16x16x32_bf16 v[116:119], v[156:159], a[132:135], v[116:119]\n" \
  "ds_read_b128 v[172:175], %[hA] offset:5632\n" \
  "v_mfma_f32_16x16x32_bf16 v[132:135], v[160:163], a[132:135], v[132:135]\n" \
  "ds_read_b128 v[176:179], %[hA] offset:24064\n" \
  "v_mfma_f32_16x16x32_bf16 v[120:123], v[156:159], a[140:143], v[120:123]\n" \
  "v_mfma_f32_16x16x32_bf16 v[136:139], v[160:163], a[140:143], v[136:139]\n" \
  "v_mfma_f32_16x16x32_bf16 v[124:127], a[148:151], v[156:159], v[124:127]\n" \
  "s_waitcnt vmcnt(11)\n" \
  "v_readfirstlane_b32 s72, v250\n" \
  "v_mfma_f32_16x16x32_bf16 v[140:143], a[148:151], v[160:163], v[140:143]\n" \
  "v_max_u32_dpp v251, v251, v251 quad_perm:[1,0,3,2] row_mask:0xf bank_mask:0xf\n" \
  "v_mfma_f32_16x16x32_bf16 v[128:131], a[156:159], v[156:159], v[128:131]\n" \
  "v_mfma_f32_16x16x32_bf16 v[144:147], a[156:159], v[160:163], v[144:147]\n" \
  "v_max_u32_dpp v251, v251, v251 quad_perm:[2,3,0,1] row_mask:0xf bank_mask:0xf\n" \
  "s_waitcnt lgkmcnt(2)\n" \
  "v_mfma_f32_16x16x32_bf16 v[116:119], v[164:167], a[160:163], v[116:119]\n" \
  "ds_read_b128 v[148:151], %[hA] offset:6144\n" \
  "v_mfma_f32_16x16x32_bf16 v[132:135], v[168:171], a[160:163], v[132:135]\n" \
  "ds_read_b128 v[152:155], %[hA] offset:24576\n" \
  "v_max_u32_dpp v251, v251, v251 row_half_mirror row_mask:0xf bank_mask:0xf\n" \
  "v_mfma_f32_16x16x32_bf16 v[120:123], v[164:167], a[168:171], v[120:123]\n" \
  "v_mfma_f32_16x16x32_bf16 v[136:139], v[168:171], a[168:171], v[136:139]\n" \
  "v_readfirstlane_b32 s92, v251\n" \
  "v_readfirstlane_b32 s73, v255\n" \
  "v_mfma_f32_16x16x32_bf16 v[124:127], a[176:179], v[164:167], v[124:127]\n" \
  "s_sub_i32 s92, s92, s95\n" \
  "s_add_i32 s94, s92, 1\n" \
  "s_ashr_i32 s94, s94, 2\n" \
  "v_mfma_f32_16x16x32_bf16 v[140:143], a[176:179], v[168:171], v[140:143]\n" \
  "s_max_i32 s92, s92, s94\n" \
  "s_max_i32 s92, s92, %[dn]\n" \
  "s_min_i32 s92, s92, %[up]\n" \
  "v_mfma_f32_16x16x32_bf16 v[128:131], a[184:187], v[164:167], v[128:131]\n" \
  "s_cmp_lt_i32 s60, s88\n" \
  "s_cselect_b32 s92, 0, s92\n" \
  "s_add_i32 s95, s95, s92\n" \
  "v_mfma_f32_16x16x32_bf16 v[144:147], a[184:187], v[168:171], v[144:147]\n" \
  "s_waitcnt lgkmcnt(2)\n" \
  "v_mfma_f32_16x16x32_bf16 v[116:119], v[172:175], a[164:167], v[116:119]\n" \
  "ds_read_b128 v[156:159], %[hA] offset:6656\n" \
  "v_mfma_f32_16x16x32_bf16 v[132:135], v[176:179], a[164:167], v[132:135]\n" \
  "ds_read_b128 v[160:163], %[hA] offset:25088\n" \
  "v_mfma_f32_16x16x32_bf16 v[120:123], v[172:175], a[172:175], v[120:123]\n" \
  "s_mul_i32 s76, s95, 209\n" \
  "s_lshr_b32 s76, s76, 8\n" \
  "s_add_i32 s76, s76, %[c2]\n" \
  "v_mfma_f32_16x16x32_bf16 v[136:139], v[176:179], a[172:175], v[136:139]\n" \
  "s_add_i32 s92, s95, %[mrg]\n" \
  "s_max_i32 s92, s92, s76\n" \
  "s_add_i32 s94, s72, s92\n" \
  "v_mfma_f32_16x16x32_bf16 v[124:127], a[180:183], v[172:175], v[124:127]\n" \
  "s_sub_i32 s76, s92, s76\n" \
  "s_lshr_b32 s76, s76, 1\n" \
  "s_add_i32 s76, s76, %[c4]\n" \
  "v_mfma_f32_16x16x32_bf16 v[140:143], a[180:183], v[176:179], v[140:143]\n" \
  "s_mul_i32 s92, s95, 135\n" \
  "s_lshr_b32 s92, s92, 8\n" \
  "s_add_i32 s76, s76, s92\n" \
  "v_mfma_f32_16x16x32_bf16 v[128:131], a[188:191], v[172:175], v[128:131]\n" \
  "s_add_i32 s92, s73, %[lag]\n" \
  "s_sub_i32 s92, s92, s94\n" \
  "s_max_i32 s92, s92, 0\n" \
  "v_mfma_f32_16x16x32_bf16 v[144:147], a[188:191], v[176:179], v[144:147]\n" \
  "s_cmp_lt_i32 s60, 59\n" \
  "s_cselect_b32 s77, %[gg], 0\n" \
  "s_waitcnt lgkmcnt(2)\n" \
  "v_mfma_f32_16x16x32_bf16 v[116:119], v[148:151], a[192:195], v[116:119]\n" \
  "ds_read_b128 v[164:167], %[hA] offset:7168\n" \
  "v_mfma_f32_16x16x32_bf16 v[132:135], v[152:155], a[192:195], v[132:135]\n" \
  "ds_read_b128 v[168:171], %[hA] offset:25600\n" \
  "v_mfma_f32_16x16x32_bf16 v[120:123], v[148:151], a[200:203], v[120:123]\n" \
  "s_cmp_lg_u32 s73, 0\n" \
  "s_cselect_b32 s92, s92, s77\n" \
  "s_add_i32 s86, s94, s92\n" \
  "v_mfma_f32_16x16x32_bf16 v[136:139], v[152:155], a[200:203], v[136:139]\n" \
  "s_add_i32 s76, s76, s86\n" \
  "v_mov_b32 v252, s86\n" \
  "v_mov_b32 v253, s76\n" \
  "v_mfma_f32_16x16x32_bf16 v[124:127], a[208:211], v[148:151], v[124:127]\n" \
  "s_cmp_lt_i32 s60, 0\n" \
  "s_cselect_b32 s73, 2.0, s96\n" \
  "v_mfma_f32_16x16x32_bf16 v[140:143], a[208:211], v[152:155], v[140:143]\n" \
  "v_mfma_f32_16x16x32_bf16 v[128:131], a[216:219], v[148:151], v[128:131]\n" \
  "v_mfma_f32_16x16x32_bf16 v[144:147], a[216:219], v[152:155], v[144:147]\n" \
  "s_waitcnt lgkmcnt(2)\n" \
  "v_mfma_f32_16x16x32_bf16 v[116:119], v[156:159], a[196:199], v[116:119]\n" \
  "ds_read_b128 v[172:175], %[hA] offset:7680\n" \
  "v_mfma_f32_16x16x32_bf16 v[132:135], v[160:163], a[196:199], v[132:135]\n" \
  "ds_read_b128 v[176:179], %[hA] offset:26112\n" \
  "v_mfma_f32_16x16x32_bf16 v[120:123], v[156:159], a[204:207], v[120:123]\n" \
  "ds_read_b128 v[148:151], %[hA] offset:8192\n" \
  "v_mfma_f32_16x16x32_bf16 v[136:139], v[160:163], a[204:207], v[136:139]\n" \
  "ds_read_b128 v[152:155], %[hA] offset:26624\n" \
  "v_mfma_f32_16x16x32_bf16 v[124:127], a[212:215], v[156:159], v[124:127]\n" \
  "v_mfma_f32_16x16x32_bf16 v[140:143], a[212:215], v[160:163], v[140:143]\n" \
  "v_mfma_f32_16x16x32_bf16 v[128:131], a[220:223], v[156:159], v[128:131]\n" \
  "v_mfma_f32_16x16x32_bf16 v[144:147], a[220:223], v[160:163], v[144:147]\n" \
  "s_memrealtime s[74:75]\n" \
  "s_waitcnt lgkmcnt(4)\n" \
  "v_mfma_f32_16x16x32_bf16 v[124:127], a[240:243], v[164:167], v[124:127]\n" \
  "ds_read_b128 v[156:159], %[hA] offset:8704\n" \
  "v_mfma_f32_16x16x32_bf16 v[140:143], a[240:243], v[168:171], v[140:143]\n" \
  "ds_read_b128 v[160:163], %[hA] offset:27136\n" \
  "v_mfma_f32_16x16x32_bf16 v[128:131], a[248:251], v[164:167], v[128:131]\n" \
  "v_mfma_f32_16x16x32_bf16 v[144:147], a[248:251], v[168:171], v[144:147]\n" \
  "s_waitcnt lgkmcnt(0)\n" \
  "s_sub_i32 s91, s74, s72\n" \
  "s_cmp_gt_u32 s91, 14\n" \
  "v_mfma_f32_16x16x32_bf16 v[124:127], a[244:247], v[172:175], v[124:127]\n" \
  "s_cbranch_scc1 .Lqk_late\n" \
  ".Lqk_al_ok:\n" \
  "v_mfma_f32_16x16x32_bf16 v[140:143], a[244:247], v[176:179], v[140:143]\n" \
  "buffer_load_dwordx4 v[64:67], %[mo], %[X], s63 offen offset:0 sc1\n" \
  "v_mfma_f32_16x16x32_bf16 v[128:131], a[252:255], v[172:175], v[128:131]\n" \
  "v_mfma_f32_16x16x32_bf16 v[144:147], a[252:255], v[176:179], v[144:147]\n" \
  "v_mfma_f32_16x16x32_bf16 v[124:127], v[48:51], v[148:151], v[124:127]\n" \
  "buffer_load_dwordx4 v[68:71], %[mo], %[X], s63 offen offset:1024 sc1\n" \
  "v_mfma_f32_16x16x32_bf16 v[140:143], v[48:51], v[152:155], v[140:143]\n" \
  "v_mfma_f32_16x16x32_bf16 v[128:131], v[56:59], v[148:151], v[128:131]\n" \
  "v_mfma_f32_16x16x32_bf16 v[144:147], v[56:59], v[152:155], v[144:147]\n" \
  "buffer_load_dwordx4 v[72:75], %[mo], %[X], s63 offen offset:2048 sc1\n" \
  "v_mfma_f32_16x16x32_bf16 v[124:127], v[52:55], v[156:159], v[124:127]\n" \
  "v_mfma_f32_16x16x32_bf16 v[140:143], v[52:55], v[160:163], v[140:143]\n" \
  "v_mfma_f32_16x16x32_bf16 v[128:131], v[60:63], v[156:159], v[128:131]\n" \
  "buffer_load_dwordx4 v[76:79], %[mo], %[X], s63 offen offset:3072 sc1\n" \
  "v_mfma_f32_16x16x32_bf16 v[144:147], v[60:63], v[160:163], v[144:147]\n" \
  "v_mfma_f32_16x16x32_bf16 v[116:119], v[164:167], a[224:227], v[116:119]\n" \
  "v_mfma_f32_16x16x32_bf16 v[132:135], v[168:171], a[224:227], v[132:135]\n" \
  "buffer_load_dwordx4 v[80:83], v228, %[X], s63 offen offset:0 sc1\n" \
  "v_mfma_f32_16x16x32_bf16 v[120:123], v[164:167], a[232:235], v[120:123]\n" \
  "v_cvt_pk_bf16_f32 v220, v124, v125\n" \
  "v_cvt_pk_bf16_f32 v221, v126, v127\n" \
  "v_mfma_f32_16x16x32_bf16 v[136:139], v[168:171], a[232:235], v[136:139]\n" \
  "v_cvt_pk_bf16_f32 v222, v128, v129\n" \
  "v_cvt_pk_bf16_f32 v223, v130, v131\n" \
  "v_mfma_f32_16x16x32_bf16 v[116:119], v[172:175], a[228:231], v[116:119]\n" \
  "buffer_load_dwordx4 v[84:87], v228, %[X], s63 offen offset:1024 sc1\n" \
  "v_cvt_pk_bf16_f32 v224, v140, v141\n" \
  "v_mfma_f32_16x16x32_bf16 v[132:135], v[176:179], a[228:231], v[132:135]\n" \
  "ds_write_b128 %[ktW], v[220:223] offset:0\n" \
  "v_mfma_f32_16x16x32_bf16 v[120:123], v[172:175], a[236:239], v[120:123]\n" \
  "v_cvt_pk_bf16_f32 v225, v142, v143\n" \
  "v_cvt_pk_bf16_f32 v226, v144, v145\n" \
  "v_mfma_f32_16x16x32_bf16 v[136:139], v[176:179], a[236:239], v[136:139]\n" \
  "buffer_load_dwordx4 v[88:91], v228, %[X], s63 offen offset:2048 sc1\n" \
  "v_cvt_pk_bf16_f32 v227, v146, v147\n" \
  "v_mfma_f32_16x16x32_bf16 v[116:119], v[148:151], v[32:35], v[116:119]\n" \
  "ds_write_b128 %[ktW], v[224:227] offset:4096\n" \
  "v_mfma_f32_16x16x32_bf16 v[132:135], v[152:155], v[32:35], v[132:135]\n" \
  "v_add_f32 v231, v124, v140\n" \
  "v_xor_b32 %[hA], 0x10000, %[hA]\n" \
  "v_mfma_f32_16x16x32_bf16 v[120:123], v[148:151], v[40:43], v[120:123]\n" \
  "buffer_load_dwordx4 v[92:95], v228, %[X], s63 offen offset:3072 sc1\n" \
  "v_mfma_f32_16x16x32_bf16 v[136:139], v[152:155], v[40:43], v[136:139]\n" \
  "v_mfma_f32_16x16x32_bf16 v[116:119], v[156:159], v[36:39], v[116:119]\n" \
  "s_waitcnt vmcnt(10)\n" \
  "s_waitcnt lgkmcnt(0)\n" \
  "s_barrier\n" \
  "v_mfma_f32_16x16x32_bf16 v[132:135], v[160:163], v[36:39], v[132:135]\n" \
  "buffer_load_dwordx4 v[96:99], %[mo], %[X], s64 offen offset:0 sc1\n" \
  "ds_read_b128 v[180:183], %[ktR] offset:0\n" \
  "v_mfma_f32_16x16x32_bf16 v[120:123], v[156:159], v[44:47], v[120:123]\n" \
  "ds_read_b128 v[184:187], %[ktR] offset:1024\n" \
  "v_mfma_f32_16x16x32_bf16 v[136:139], v[160:163], v[44:47], v[136:139]\n" \
  "ds_read_b128 v[188:191], %[ktR] offset:2048\n" \
  ".Lqk_m:\n" \
  "s_waitcnt vmcnt(8)\n" \
  "v_smfmac_f32_16x16x64_bf16 v[100:103], v[64:67], a[0:7], %[idx]\n" \
  "ds_read_b128 v[192:195], %[ktR] offset:3072\n" \
  "v_smfmac_f32_16x16x64_bf16 v[104:107], v[64:67], a[8:15], %[idx]\n" \
  "ds_read_b128 v[196:199], %[ktR] offset:4096\n" \
  "v_smfmac_f32_16x16x64_bf16 v[108:111], v[64:67], a[16:23], %[idx]\n" \
  "ds_read_b128 v[200:203], %[ktR] offset:5120\n" \
  "v_cvt_pk_bf16_f32 v220, v116, v117\n" \
  "v_smfmac_f32_16x16x64_bf16 v[112:115], v[64:67], a[24:31], %[idx]\n" \
  "ds_read_b128 v[204:207], %[ktR] offset:6144\n" \
  "s_waitcnt vmcnt(7)\n" \
  "v_smfmac_f32_16x16x64_bf16 v[100:103], v[68:71], a[32:39], %[idx]\n" \
  "ds_read_b128 v[208:211], %[ktR] offset:7168\n" \
  "v_cvt_pk_bf16_f32 v221, v118, v119\n" \
  "v_smfmac_f32_16x16x64_bf16 v[104:107], v[68:71], a[40:47], %[idx]\n" \
  "ds_read_b128 v[164:167], %[hA] offset:1024\n" \
  "v_cvt_pk_bf16_f32 v222, v120, v121\n" \
  "v_smfmac_f32_16x16x64_bf16 v[108:111], v[68:71], a[48:55], %[idx]\n" \
  "ds_read_b128 v[168:171], %[hA] offset:19456\n" \
  "v_cvt_pk_bf16_f32 v223, v122, v123\n" \
  "v_smfmac_f32_16x16x64_bf16 v[112:115], v[68:71], a[56:63], %[idx]\n" \
  "ds_read_b128 v[172:175], %[hA] offset:1536\n" \
  "s_waitcnt vmcnt(6)\n" \
  "v_smfmac_f32_16x16x64_bf16 v[100:103], v[72:75], a[64:71], %[idx]\n" \
  "ds_read_b128 v[176:179], %[hA] offset:19968\n" \
  "v_cvt_pk_bf16_f32 v224, v132, v133\n" \
  "v_smfmac_f32_16x16x64_bf16 v[104:107], v[72:75], a[72:79], %[idx]\n" \
  "ds_read_b128 v[148:151], %[hA] offset:0\n" \
  "v_cvt_pk_bf16_f32 v225, v134, v135\n" \
  "v_smfmac_f32_16x16x64_bf16 v[108:111], v[72:75], a[80:87], %[idx]\n" \
  "ds_read_b128 v[152:155], %[hA] offset:18432\n" \
  "v_cvt_pk_bf16_f32 v226, v136, v137\n" \
  "v_smfmac_f32_16x16x64_bf16 v[112:115], v[72:75], a[88:95], %[idx]\n" \
  "ds_read_b128 v[156:159], %[hA] offset:512\n" \
  "s_waitcnt vmcnt(5)\n" \
  "v_smfmac_f32_16x16x64_bf16 v[100:103], v[76:79], a[96:103], %[idx]\n" \
  "ds_read_b128 v[160:163], %[hA] offset:18944\n" \
  "v_cvt_pk_bf16_f32 v227, v138, v139\n" \
  "v_smfmac_f32_16x16x64_bf16 v[104:107], v[76:79], a[104:111], %[idx]\n" \
  "ds_write_b64 %[qtW], v[220:221] offset:0\n" \
  "v_smfmac_f32_16x16x64_bf16 v[108:111], v[76:79], a[112:119], %[idx]\n" \
  "ds_write_b64 %[qtW], v[222:223] offset:2816\n" \
  "v_smfmac_f32_16x16x64_bf16 v[112:115], v[76:79], a[120:127], %[idx]\n" \
  "s_waitcnt vmcnt(4)\n" \
  "v_smfmac_f32_16x16x64_bf16 v[100:103], v[80:83], a[128:135], %[idx]\n" \
  "ds_write_b64 %[qtW], v[224:225] offset:64\n" \
  "v_smfmac_f32_16x16x64_bf16 v[104:107], v[80:83], a[136:143], %[idx]\n" \
  "ds_write_b64 %[qtW], v[226:227] offset:2880\n" \
  "v_smfmac_f32_16x16x64_bf16 v[108:111], v[80:83], a[144:151], %[idx]\n" \
  "s_add_i32 s60, s60, 1\n" \
  "s_add_i32 s96, s96, 16\n" \
  "s_mov_b32 s94, s93\n" \
  "v_smfmac_f32_16x16x64_bf16 v[112:115], v[80:83], a[152:159], %[idx]\n" \
  "s_waitcnt vmcnt(3)\n" \
  "v_smfmac_f32_16x16x64_bf16 v[100:103], v[84:87], a[160:167], %[idx]\n" \
  "s_sub_i32 s90, s74, s89\n" \
  "s_max_i32 s92, s91, -2\n" \
  "s_add_i32 s89, s72, s92\n" \
  "v_smfmac_f32_16x16x64_bf16 v[104:107], v[84:87], a[168:175], %[idx]\n" \
  "s_ashr_i32 s92, s92, 1\n" \
  "s_add_i32 s90, s90, s92\n" \
  "v_smfmac_f32_16x16x64_bf16 v[108:111], v[84:87], a[176:183], %[idx]\n" \
  "s_mul_i32 s93, s60, 130\n" \
  "s_and_b32 s93, s93, 0xfc\n" \
  "v_smfmac_f32_16x16x64_bf16 v[112:115], v[84:87], a[184:191], %[idx]\n" \
  "s_waitcnt vmcnt(2)\n" \
  "v_smfmac_f32_16x16x64_bf16 v[100:103], v[88:91], a[192:199], %[idx]\n" \
  "v_smfmac_f32_16x16x64_bf16 v[104:107], v[88:91], a[200:207], %[idx]\n" \
  "v_smfmac_f32_16x16x64_bf16 v[108:111], v[88:91], a[208:215], %[idx]\n" \
  "v_smfmac_f32_16x16x64_bf16 v[112:115], v[88:91], a[216:223], %[idx]\n" \
  "s_waitcnt vmcnt(1)\n" \
  "v_smfmac_f32_16x16x64_bf16 v[100:103], v[92:95], a[224:231], %[idx]\n" \
  "v_smfmac_f32_16x16x64_bf16 v[104:107], v[92:95], a[232:239], %[idx]\n" \
  "s_waitcnt vmcnt(0)\n" \
  "v_smfmac_f32_16x16x64_bf16 v[100:103], v[96:99], v[32:39], %[idx]\n" \
  "v_smfmac_f32_16x16x64_bf16 v[104:107], v[96:99], v[40:47], %[idx]\n" \
  "s_nop 1\n" \
  "v_smfmac_f32_16x16x64_bf16 v[108:111], v[92:95], a[240:247], %[idx]\n" \
  "s_nop 1\n" \
  "v_smfmac_f32_16x16x64_bf16 v[112:115], v[92:95], a[248:255], %[idx]\n" \
  "s_waitcnt lgkmcnt(4)\n" \
  "s_nop 0\n" \
  "v_add_f32 v232, v100, v102\n" \
  "v_smfmac_f32_16x16x64_bf16 v[108:111], v[96:99], v[48:55], %[idx]\n" \
  "v_add_f32 v233, v101, v103\n" \
  "v_smfmac_f32_16x16x64_bf16 v[112:115], v[96:99], v[56:63], %[idx]\n" \
  "v_add_f32 v234, v104, v106\n" \
  "v_add_f32 v235, v105, v107\n" \
  "v_cvt_pk_bf16_f32 v236, v232, v233\n" \
  "v_cvt_pk_bf16_f32 v237, v234, v235\n" \
  "ds_write2st64_b32 %[qW], v236, v237 offset1:11\n" \
  "s_nop 0\n" \
  "v_pk_add_f32 v[232:233], v[108:109], v[110:111]\n" \
  "s_nop 0\n" \
  "v_pk_add_f32 v[234:235], v[112:113], v[114:115]\n" \
  "v_cvt_pk_bf16_f32 v236, v232, v233\n" \
  "v_cvt_pk_bf16_f32 v237, v234, v235\n" \
  "ds_write2st64_b32 %[kW], v236, v237 offset1:11\n" \
  "v_fma_f32 v232, v232, v233, v231\n" \
  "v_cmp_u_f32 vcc, v232, v232\n" \
  "v_mfma_f32_16x16x32_bf16 v[116:119], v[148:151], a[0:3], 0\n" \
  "v_mfma_f32_16x16x32_bf16 v[132:135], v[152:155], a[0:3], 0\n" \
  "s_cbranch_vccnz .Lqk_sm\n" \
  ".Lqk_sm_back:\n" \
  "s_waitcnt lgkmcnt(0)\n" \
  "s_barrier\n" \
  "ds_read_b64_tr_b16 v[64:65], %[qR] offset:0\n" \
  "ds_read_b64_tr_b16 v[66:67], %[qR] offset:2816\n" \
  "v_mfma_f32_16x16x32_bf16 v[120:123], v[148:151], a[8:11], 0\n" \
  "ds_read_b64_tr_b16 v[68:69], %[qR] offset:5632\n" \
  "ds_read_b64_tr_b16 v[70:71], %[qR] offset:8448\n" \
  "v_mfma_f32_16x16x32_bf16 v[136:139], v[152:155], a[8:11], 0\n" \
  "ds_read_b64_tr_b16 v[72:73], %[qR] offset:11264\n" \
  "ds_read_b64_tr_b16 v[74:75], %[qR] offset:14080\n" \
  "v_mfma_f32_16x16x32_bf16 v[124:127], a[16:19], v[148:151], 0\n" \
  "ds_read_b64_tr_b16 v[76:77], %[qR] offset:16896\n" \
  "ds_read_b64_tr_b16 v[78:79], %[qR] offset:19712\n" \
  "s_waitcnt lgkmcnt(6)\n" \
  "v_mfma_f32_16x16x32_bf16 v[100:103], v[180:183], v[64:67], v[212:215]\n" \
  "ds_read_b64_tr_b16 v[80:81], %[kR] offset:0\n" \
  "ds_read_b64_tr_b16 v[82:83], %[kR] offset:2816\n" \
  "s_waitcnt lgkmcnt(6)\n" \
  "v_mfma_f32_16x16x32_bf16 v[100:103], v[184:187], v[68:71], v[100:103]\n" \
  "ds_read_b64_tr_b16 v[84:85], %[kR] offset:5632\n" \
  "ds_read_b64_tr_b16 v[86:87], %[kR] offset:8448\n" \
  "s_waitcnt lgkmcnt(6)\n" \
  "v_mfma_f32_16x16x32_bf16 v[100:103], v[188:191], v[72:75], v[100:103]\n" \
  "ds_read_b64_tr_b16 v[88:89], %[kR] offset:11264\n" \
  "ds_read_b64_tr_b16 v[90:91], %[kR] offset:14080\n" \
  "s_waitcnt lgkmcnt(6)\n" \
  "v_mfma_f32_16x16x32_bf16 v[100:103], v[192:195], v[76:79], v[100:103]\n" \
  "ds_read_b64_tr_b16 v[92:93], %[kR] offset:16896\n" \
  "ds_read_b64_tr_b16 v[94:95], %[kR] offset:19712\n" \
  "v_mfma_f32_16x16x32_bf16 v[104:107], v[196:199], v[64:67], v[216:219]\n" \
  "buffer_store_dwordx2 v[252:253], %[cpw], %[X], s73 offen offset:32 sc1\n" \
  "v_mfma_f32_16x16x32_bf16 v[104:107], v[200:203], v[68:71], v[104:107]\n" \
  "v_mfma_f32_16x16x32_bf16 v[104:107], v[204:207], v[72:75], v[104:107]\n" \
  "v_mfma_f32_16x16x32_bf16 v[104:107], v[208:211], v[76:79], v[104:107]\n" \
  "s_waitcnt lgkmcnt(6)\n" \
  "v_mfma_f32_16x16x32_bf16 v[108:111], v[80:83], v[64:67], 0\n" \
  "s_waitcnt lgkmcnt(4)\n" \
  "v_mfma_f32_16x16x32_bf16 v[108:111], v[84:87], v[68:71], v[108:111]\n" \
  "v_max3_f32 v248, v100, v101, v102\n" \
  "s_waitcnt lgkmcnt(2)\n" \
  "v_mfma_f32_16x16x32_bf16 v[108:111], v[88:91], v[72:75], v[108:111]\n" \
  "s_waitcnt lgkmcnt(0)\n" \
  "v_mfma_f32_16x16x32_bf16 v[108:111], v[92:95], v[76:79], v[108:111]\n" \
  "v_max3_f32 v249, v103, v104, v105\n" \
  "v_max3_f32 v248, v248, v106, v107\n" \
  "v_mfma_f32_16x16x32_bf16 v[140:143], a[16:19], v[152:155], 0\n" \
  "s_nop 4\n" \
  "v_max3_f32 v249, v249, v108, v109\n" \
  "v_max_f32 v232, v248, v249\n" \
  "v_max_f32 v233, v248, v249\n" \
  "v_mfma_f32_16x16x32_bf16 v[128:131], a[24:27], v[148:151], 0\n" \
  "s_nop 0\n" \
  "v_permlane16_swap_b32 v232, v233\n" \
  "v_max_f32 v248, v232, v233\n" \
  "v_max_f32 v249, v232, v233\n" \
  "v_mfma_f32_16x16x32_bf16 v[144:147], a[24:27], v[152:155], 0\n" \
  "s_nop 0\n" \
  "v_permlane32_swap_b32 v248, v249\n" \
  "v_max_f32 v248, v248, v249\n" \
  "v_mul_f32_e64 v248, -s71, v248\n" \
  "v_pk_fma_f32 v[100:101], v[100:101], s[70:71], v[248:249] op_sel:[0,1,0] op_sel_hi:[1,1,0]\n" \
  "v_pk_fma_f32 v[102:103], v[102:103], s[70:71], v[248:249] op_sel:[0,1,0] op_sel_hi:[1,1,0]\n" \
  "v_pk_fma_f32 v[104:105], v[104:105], s[70:71], v[248:249] op_sel:[0,1,0] op_sel_hi:[1,1,0]\n" \
  "v_pk_fma_f32 v[106:107], v[106:107], s[70:71], v[248:249] op_sel:[0,1,0] op_sel_hi:[1,1,0]\n" \
  "v_pk_fma_f32 v[108:109], v[108:109], s[70:71], v[248:249] op_sel:[0,1,0] op_sel_hi:[1,1,0]\n" \
  "v_exp_f32 v100, v100\n" \
  "v_exp_f32 v101, v101\n" \
  "v_exp_f32 v102, v102\n" \
  "v_exp_f32 v103, v103\n" \
  "v_exp_f32 v104, v104\n" \
  "v_exp_f32 v105, v105\n" \
  "v_exp_f32 v106, v106\n" \
  "v_exp_f32 v107, v107\n" \
  "v_exp_f32 v108, v108\n" \
  "v_exp_f32 v109, v109\n" \
  "v_pk_add_f32 v[248:249], v[100:101], v[102:103]\n" \
  "v_pk_add_f32 v[232:233], v[104:105], v[106:107]\n" \
  "v_pk_add_f32 v[248:249], v[248:249], v[108:109]\n" \
  "v_pk_add_f32 v[248:249], v[248:249], v[232:233]\n" \
  "v_add_f32 v232, v248, v249\n" \
  "v_add_f32 v233, v248, v249\n" \
  "v_mfma_f32_16x16x32_bf16 v[116:119], v[156:159], a[4:7], v[116:119]\n" \
  "s_nop 0\n" \
  "v_permlane16_swap_b32 v232, v233\n" \
  "v_add_f32 v248, v232, v233\n" \
  "v_add_f32 v249, v232, v233\n" \
  "v_mfma_f32_16x16x32_bf16 v[132:135], v[160:163], a[4:7], v[132:135]\n" \
  "s_nop 0\n" \
  "v_permlane32_swap_b32 v248, v249\n" \
  "v_add_f32 v248, v248, v249\n" \
  "v_rcp_f32 v248, v248\n" \
  "s_cmp_lg_u32 s60, s81\n" \
  "v_bfi_b32 v248, v230, v248, s66\n" \
  "v_pk_mul_f32 v[100:101], v[248:249], v[100:101] op_sel_hi:[0,1]\n" \
  "v_pk_mul_f32 v[102:103], v[248:249], v[102:103] op_sel_hi:[0,1]\n" \
  "v_pk_mul_f32 v[104:105], v[248:249], v[104:105] op_sel_hi:[0,1]\n" \
  "v_pk_mul_f32 v[106:107], v[248:249], v[106:107] op_sel_hi:[0,1]\n" \
  "v_pk_mul_f32 v[108:109], v[248:249], v[108:109] op_sel_hi:[0,1]\n" \
  "v_cvt_pk_bf16_f32 v232, v100, v101\n" \
  "v_cvt_pk_bf16_f32 v233, v102, v103\n" \
  "v_cvt_pk_bf16_f32 v234, v104, v105\n" \
  "v_cvt_pk_bf16_f32 v235, v106, v107\n" \
  "v_cvt_pk_bf16_f32 v236, v108, v109\n" \
  "buffer_store_dwordx4 v[232:235], %[po1], %[X], s65 offen sc1\n" \
  "v_mfma_f32_16x16x32_bf16 v[120:123], v[156:159], a[12:15], v[120:123]\n" \
  "v_mfma_f32_16x16x32_bf16 v[136:139], v[160:163], a[12:15], v[136:139]\n" \
  "buffer_store_dword v236, %[po2], %[X], s65 offen sc1\n" \
  "v_mfma_f32_16x16x32_bf16 v[124:127], a[20:23], v[156:159], v[124:127]\n" \
  "v_mfma_f32_16x16x32_bf16 v[140:143], a[20:23], v[160:163], v[140:143]\n" \
  "v_mfma_f32_16x16x32_bf16 v[128:131], a[28:31], v[156:159], v[128:131]\n" \
  "v_mfma_f32_16x16x32_bf16 v[144:147], a[28:31], v[160:163], v[144:147]\n" \
  "s_cbranch_scc1 .Lqk_top\n" \
  "s_cmp_eq_u32 s60, -1\n" \
  "s_cbranch_scc1 .Lqk_entry\n" \
  "buffer_store_dwordx2 v[232:233], %[po3], %[X], s65 offen sc1\n" \
  "s_branch .Lqk_end\n" \
  ".Lqk_sm:\n" \
  "s_cmp_lt_i32 s60, 0\n" \
  "s_cbranch_scc1 .Lqk_sm_back\n" \
  "v_cmp_u_f32 vcc, v231, v231\n" \
  "s_cbranch_vccnz .Lqk_sh\n" \
  "s_mov_b32 s69, 0\n" \
  ".Lqk_smw:\n" \
  "buffer_load_dwordx4 v[64:67], %[mo], %[X], s63 offen offset:0 sc1\n" \
  "buffer_load_dwordx4 v[68:71], %[mo], %[X], s63 offen offset:1024 sc1\n" \
  "buffer_load_dwordx4 v[72:75], %[mo], %[X], s63 offen offset:2048 sc1\n" \
  "buffer_load_dwordx4 v[76:79], %[mo], %[X], s63 offen offset:3072 sc1\n" \
  "buffer_load_dwordx4 v[80:83], v228, %[X], s63 offen offset:0 sc1\n" \
  "buffer_load_dwordx4 v[84:87], v228, %[X], s63 offen offset:1024 sc1\n" \
  "buffer_load_dwordx4 v[88:91], v228, %[X], s63 offen offset:2048 sc1\n" \
  "buffer_load_dwordx4 v[92:95], v228, %[X], s63 offen offset:3072 sc1\n" \
  "buffer_load_dwordx4 v[96:99], %[mo], %[X], s64 offen offset:0 sc1\n" \
  "s_waitcnt vmcnt(0)\n" \
  "v_max3_u32 v229, v64, v65, v66\n" \
  "v_max3_u32 v229, v229, v67, v68\n" \
  "v_max3_u32 v229, v229, v69, v70\n" \
  "v_max3_u32 v229, v229, v71, v72\n" \
  "v_max3_u32 v229, v229, v73, v74\n" \
  "v_max3_u32 v229, v229, v75, v76\n" \
  "v_max3_u32 v229, v229, v77, v78\n" \
  "v_max3_u32 v229, v229, v79, v80\n" \
  "v_max3_u32 v229, v229, v81, v82\n" \
  "v_max3_u32 v229, v229, v83, v84\n" \
  "v_max3_u32 v229, v229, v85, v86\n" \
  "v_max3_u32 v229, v229, v87, v88\n" \
  "v_max3_u32 v229, v229, v89, v90\n" \
  "v_max3_u32 v229, v229, v91, v92\n" \
  "v_max3_u32 v229, v229, v93, v94\n" \
  "v_max3_u32 v229, v229, v95, v96\n" \
  "v_max3_u32 v229, v229, v97, v98\n" \
  "v_max_u32 v229, v229, v99\n" \
  "v_cmp_eq_u32 vcc, -1, v229\n" \
  "s_cbranch_vccz .Lqk_smw_ok\n" \
  "s_add_i32 s69, s69, 1\n" \
  "s_and_b32 s67, s69, 1023\n" \
  "s_cbranch_scc1 .Lqk_smw\n" \
  "buffer_load_dword v229, off, %[F], 0 offset:1024 sc1\n" \
  "s_waitcnt vmcnt(0)\n" \
  "v_readfirstlane_b32 s67, v229\n" \
  "s_cmp_lg_u32 s67, 0\n" \
  "s_cbranch_scc1 .Lqk_smw_ok\n" \
  "s_cmp_lt_u32 s69, 400000\n" \
  "s_cbranch_scc1 .Lqk_smw\n" \
  "v_mov_b32 v229, 1\n" \
  "buffer_store_dword v229, off, %[F], 0 offset:1024 sc1\n" \
  ".Lqk_smw_ok:\n" \
  "s_memrealtime s[76:77]\n" \
  "s_waitcnt lgkmcnt(0)\n" \
  "s_add_i32 s76, s76, s95\n" \
  "s_add_i32 s76, s76, %[sh1]\n" \
  "s_sub_i32 s76, s76, s86\n" \
  "s_cmp_gt_u32 s76, -3000\n" \
  "s_cselect_b32 s76, 0, s76\n" \
  "s_add_i32 s86, s86, s76\n" \
  "s_add_i32 s88, s60, 3\n" \
  "v_add_u32 v252, s76, v252\n" \
  "v_add_u32 v253, s76, v253\n" \
  "v_mov_b64 v[100:101], 0\n" \
  "v_mov_b64 v[102:103], 0\n" \
  "v_mov_b64 v[104:105], 0\n" \
  "v_mov_b64 v[106:107], 0\n" \
  "v_mov_b64 v[108:109], 0\n" \
  "v_mov_b64 v[110:111], 0\n" \
  "v_mov_b64 v[112:113], 0\n" \
  "v_mov_b64 v[114:115], 0\n" \
  "s_nop 1\n" \
  "v_smfmac_f32_16x16x64_bf16 v[100:103], v[64:67], a[0:7], %[idx]\n" \
  "v_smfmac_f32_16x16x64_bf16 v[104:107], v[64:67], a[8:15], %[idx]\n" \
  "v_smfmac_f32_16x16x64_bf16 v[108:111], v[64:67], a[16:23], %[idx]\n" \
  "v_smfmac_f32_16x16x64_bf16 v[112:115], v[64:67], a[24:31], %[idx]\n" \
  "v_smfmac_f32_16x16x64_bf16 v[100:103], v[68:71], a[32:39], %[idx]\n" \
  "v_smfmac_f32_16x16x64_bf16 v[104:107], v[68:71], a[40:47], %[idx]\n" \
  "v_smfmac_f32_16x16x64_bf16 v[108:111], v[68:71], a[48:55], %[idx]\n" \
  "v_smfmac_f32_16x16x64_bf16 v[112:115], v[68:71], a[56:63], %[idx]\n" \
  "v_smfmac_f32_16x16x64_bf16 v[100:103], v[72:75], a[64:71], %[idx]\n" \
  "v_smfmac_f32_16x16x64_bf16 v[104:107], v[72:75], a[72:79], %[idx]\n" \
  "v_smfmac_f32_16x16x64_bf16 v[108:111], v[72:75], a[80:87], %[idx]\n" \
  "v_smfmac_f32_16x16x64_bf16 v[112:115], v[72:75], a[88:95], %[idx]\n" \
  "v_smfmac_f32_16x16x64_bf16 v[100:103], v[76:79], a[96:103], %[idx]\n" \
  "v_smfmac_f32_16x16x64_bf16 v[104:107], v[76:79], a[104:111], %[idx]\n" \
  "v_smfmac_f32_16x16x64_bf16 v[108:111], v[76:79], a[112:119], %[idx]\n" \
  "v_smfmac_f32_16x16x64_bf16 v[112:115], v[76:79], a[120:127], %[idx]\n" \
  "v_smfmac_f32_16x16x64_bf16 v[100:103], v[80:83], a[128:135], %[idx]\n" \
  "v_smfmac_f32_16x16x64_bf16 v[104:107], v[80:83], a[136:143], %[idx]\n" \
  "v_smfmac_f32_16x16x64_bf16 v[108:111], v[80:83], a[144:151], %[idx]\n" \
  "v_smfmac_f32_16x16x64_bf16 v[112:115], v[80:83], a[152:159], %[idx]\n" \
  "v_smfmac_f32_16x16x64_bf16 v[100:103], v[84:87], a[160:167], %[idx]\n" \
  "v_smfmac_f32_16x16x64_bf16 v[104:107], v[84:87], a[168:175], %[idx]\n" \
  "v_smfmac_f32_16x16x64_bf16 v[108:111], v[84:87], a[176:183], %[idx]\n" \
  "v_smfmac_f32_16x16x64_bf16 v[112:115], v[84:87], a[184:191], %[idx]\n" \
  "v_smfmac_f32_16x16x64_bf16 v[100:103], v[88:91], a[192:199], %[idx]\n" \
  "v_smfmac_f32_16x16x64_bf16 v[104:107], v[88:91], a[200:207], %[idx]\n" \
  "v_smfmac_f32_16x16x64_bf16 v[108:111], v[88:91], a[208:215], %[idx]\n" \
  "v_smfmac_f32_16x16x64_bf16 v[112:115], v[88:91], a[216:223], %[idx]\n" \
  "v_smfmac_f32_16x16x64_bf16 v[100:103], v[92:95], a[224:231], %[idx]\n" \
  "v_smfmac_f32_16x16x64_bf16 v[104:107], v[92:95], a[232:239], %[idx]\n" \
  "v_smfmac_f32_16x16x64_bf16 v[108:111], v[92:95], a[240:247], %[idx]\n" \
  "v_smfmac_f32_16x16x64_bf16 v[112:115], v[92:95], a[248:255], %[idx]\n" \
  "v_smfmac_f32_16x16x64_bf16 v[100:103], v[96:99], v[32:39], %[idx]\n" \
  "v_smfmac_f32_16x16x64_bf16 v[104:107], v[96:99], v[40:47], %[idx]\n" \
  "v_smfmac_f32_16x16x64_bf16 v[108:111], v[96:99], v[48:55], %[idx]\n" \
  "v_smfmac_f32_16x16x64_bf16 v[112:115], v[96:99], v[56:63], %[idx]\n" \
  "s_nop 7\n" \
  "v_pk_add_f32 v[232:233], v[100:101], v[102:103]\n" \
  "v_pk_add_f32 v[234:235], v[104:105], v[106:107]\n" \
  "v_cvt_pk_bf16_f32 v236, v232, v233\n" \
  "v_cvt_pk_bf16_f32 v237, v234, v235\n" \
  "ds_write2st64_b32 %[qW], v236, v237 offset1:11\n" \
  "v_pk_add_f32 v[232:233], v[108:109], v[110:111]\n" \
  "v_pk_add_f32 v[234:235], v[112:113], v[114:115]\n" \
  "v_cvt_pk_bf16_f32 v236, v232, v233\n" \
  "v_cvt_pk_bf16_f32 v237, v234, v235\n" \
  "ds_write2st64_b32 %[kW], v236, v237 offset1:11\n" \
  "s_branch .Lqk_sm_back\n" \
  ".Lqk_late:\n" \
  "s_cmp_lt_i32 s60, 0\n" \
  "s_cbranch_scc1 .Lqk_al_ok\n" \
  "s_cmp_gt_u32 s91, -3000\n" \
  "s_cbranch_scc0 .Lqk_late2\n" \
  ".Lqk_wt:\n" \
  "s_memrealtime s[76:77]\n" \
  "s_waitcnt lgkmcnt(0)\n" \
  "s_sub_i32 s76, s76, s72\n" \
  "s_cmp_lt_i32 s76, 0\n" \
  "s_cbranch_scc1 .Lqk_wt\n" \
  "s_branch .Lqk_al_ok\n" \
  ".Lqk_late2:\n" \
  "s_cmp_lg_u32 s72, 0\n" \
  "s_cbranch_scc1 .Lqk_late3\n" \
  "s_mov_b32 s69, 0\n" \
  ".Lqk_tw:\n" \
  "buffer_load_dword v229, off, %[X], s96 offset:16 sc1\n" \
  "s_waitcnt vmcnt(0)\n" \
  "v_readfirstlane_b32 s72, v229\n" \
  "s_cmp_lg_u32 s72, 0\n" \
  "s_cbranch_scc1 .Lqk_tw_ok\n" \
  "s_sleep 1\n" \
  "s_add_i32 s69, s69, 1\n" \
  "s_cmp_lt_u32 s69, 200\n" \
  "s_cbranch_scc1 .Lqk_tw\n" \
  ".Lqk_tw_ok:\n" \
  "s_add_i32 s76, s72, s95\n" \
  "s_add_i32 s76, s76, 8\n" \
  "s_sub_i32 s76, s76, s86\n" \
  "s_cmp_gt_u32 s76, -3000\n" \
  "s_cselect_b32 s76, 0, s76\n" \
  "s_add_i32 s86, s86, s76\n" \
  "s_add_i32 s88, s60, 3\n" \
  "v_add_u32 v252, s76, v252\n" \
  "v_add_u32 v253, s76, v253\n" \
  "s_sub_i32 s91, s74, s72\n" \
  "s_cmp_gt_u32 s91, -3000\n" \
  "s_cbranch_scc1 .Lqk_wt\n" \
  ".Lqk_late3:\n" \
  "s_memrealtime s[76:77]\n" \
  "s_waitcnt lgkmcnt(0)\n" \
  "s_add_i32 s76, s76, s95\n" \
  "s_add_i32 s76, s76, 90\n" \
  "s_sub_i32 s76, s76, s86\n" \
  "s_cmp_gt_u32 s76, -3000\n" \
  "s_cselect_b32 s76, 0, s76\n" \
  "s_add_i32 s86, s86, s76\n" \
  "s_add_i32 s88, s60, 3\n" \
  "v_add_u32 v252, s76, v252\n" \
  "v_add_u32 v253, s76, v253\n" \
  "s_branch .Lqk_al_ok\n" \
  ".Lqk_sh:\n" \
  "s_mul_i32 s75, s60, 36864\n" \
  "s_add_i32 s76, s75, 0x1000\n" \
  "s_add_i32 s77, s75, 0x2000\n" \
  "s_add_i32 s60, s60, -1\n" \
  "s_add_i32 s96, s96, -16\n" \
  "s_mov_b32 s93, s94\n" \
  "v_xor_b32 %[hA], 0x10000, %[hA]\n" \
  "s_waitcnt vmcnt(0) lgkmcnt(0)\n" \
  "s_barrier\n" \
  "s_mov_b32 s69, 0\n" \
  ".Lqk_shw:\n" \
  "buffer_load_dwordx4 v[64:67], %[hvo], %[HS], s75 offen offset:0 sc1\n" \
  "buffer_load_dwordx4 v[68:71], %[hvo], %[HS], s75 offen offset:1024 sc1\n" \
  "buffer_load_dwordx4 v[72:75], %[hvo], %[HS], s75 offen offset:2048 sc1\n" \
  "buffer_load_dwordx4 v[76:79], %[hvo], %[HS], s75 offen offset:3072 sc1\n" \
  "buffer_load_dwordx4 v[80:83], %[hvo], %[HS], s76 offen offset:0 sc1\n" \
  "buffer_load_dwordx4 v[84:87], %[hvo], %[HS], s76 offen offset:1024 sc1\n" \
  "buffer_load_dwordx4 v[88:91], %[hvo], %[HS], s76 offen offset:2048 sc1\n" \
  "buffer_load_dwordx4 v[92:95], %[hvo], %[HS], s76 offen offset:3072 sc1\n" \
  "buffer_load_dwordx4 v[96:99], %[hvo], %[HS], s77 offen offset:0 sc1\n" \
  "s_waitcnt vmcnt(0)\n" \
  "v_max3_u32 v229, v64, v65, v66\n" \
  "v_max3_u32 v229, v229, v67, v68\n" \
  "v_max3_u32 v229, v229, v69, v70\n" \
  "v_max3_u32 v229, v229, v71, v72\n" \
  "v_max3_u32 v229, v229, v73, v74\n" \
  "v_max3_u32 v229, v229, v75, v76\n" \
  "v_max3_u32 v229, v229, v77, v78\n" \
  "v_max3_u32 v229, v229, v79, v80\n" \
  "v_max3_u32 v229, v229, v81, v82\n" \
  "v_max3_u32 v229, v229, v83, v84\n" \
  "v_max3_u32 v229, v229, v85, v86\n" \
  "v_max3_u32 v229, v229, v87, v88\n" \
  "v_max3_u32 v229, v229, v89, v90\n" \
  "v_max3_u32 v229, v229, v91, v92\n" \
  "v_max3_u32 v229, v229, v93, v94\n" \
  "v_max3_u32 v229, v229, v95, v96\n" \
  "v_max3_u32 v229, v229, v97, v98\n" \
  "v_max_u32 v229, v229, v99\n" \
  "v_cmp_eq_u32 vcc, -1, v229\n" \
  "s_cbranch_vccz .Lqk_shw_ok\n" \
  "s_sleep 4\n" \
  "s_add_i32 s69, s69, 1\n" \
  "s_and_b32 s67, s69, 1023\n" \
  "s_cbranch_scc1 .Lqk_shw\n" \
  "buffer_load_dword v229, off, %[F], 0 offset:1024 sc1\n" \
  "s_waitcnt vmcnt(0)\n" \
  "v_readfirstlane_b32 s67, v229\n" \
  "s_cmp_lg_u32 s67, 0\n" \
  "s_cbranch_scc1 .Lqk_shw_ok\n" \
  "s_cmp_lt_u32 s69, 400000\n" \
  "s_cbranch_scc1 .Lqk_shw\n" \
  "v_mov_b32 v229, 1\n" \
  "buffer_store_dword v229, off, %[F], 0 offset:1024 sc1\n" \
  ".Lqk_shw_ok:\n" \
  "s_xor_b32 s67, s62, 0x10000\n" \
  "v_add_u32 v229, s67, %[mo]\n" \
  "ds_write_b128 v229, v[64:67] offset:0\n" \
  "ds_write_b128 v229, v[68:71] offset:1024\n" \
  "ds_write_b128 v229, v[72:75] offset:2048\n" \
  "ds_write_b128 v229, v[76:79] offset:3072\n" \
  "ds_write_b128 v229, v[80:83] offset:4096\n" \
  "ds_write_b128 v229, v[84:87] offset:5120\n" \
  "ds_write_b128 v229, v[88:91] offset:6144\n" \
  "ds_write_b128 v229, v[92:95] offset:7168\n" \
  "ds_write_b128 v229, v[96:99] offset:8192\n" \
  "s_waitcnt lgkmcnt(0)\n" \
  "s_barrier\n" \
  "ds_read_b128 v[148:151], %[hA] offset:0\n" \
  "ds_read_b128 v[152:155], %[hA] offset:18432\n" \
  "ds_read_b128 v[156:159], %[hA] offset:512\n" \
  "ds_read_b128 v[160:163], %[hA] offset:18944\n" \
  "ds_read_b128 v[164:167], %[hA] offset:1024\n" \
  "ds_read_b128 v[168:171], %[hA] offset:19456\n" \
  "ds_read_b128 v[172:175], %[hA] offset:1536\n" \
  "ds_read_b128 v[176:179], %[hA] offset:19968\n" \
  "s_waitcnt lgkmcnt(0)\n" \
  "v_mfma_f32_16x16x32_bf16 v[116:119], v[148:151], a[0:3], 0\n" \
  "v_mfma_f32_16x16x32_bf16 v[132:135], v[152:155], a[0:3], 0\n" \
  "v_mfma_f32_16x16x32_bf16 v[120:123], v[148:151], a[8:11], 0\n" \
  "v_mfma_f32_16x16x32_bf16 v[136:139], v[152:155], a[8:11], 0\n" \
  "v_mfma_f32_16x16x32_bf16 v[124:127], a[16:19], v[148:151], 0\n" \
  "v_mfma_f32_16x16x32_bf16 v[140:143], a[16:19], v[152:155], 0\n" \
  "v_mfma_f32_16x16x32_bf16 v[128:131], a[24:27], v[148:151], 0\n" \
  "v_mfma_f32_16x16x32_bf16 v[144:147], a[24:27], v[152:155], 0\n" \
  "v_mfma_f32_16x16x32_bf16 v[116:119], v[156:159], a[4:7], v[116:119]\n" \
  "v_mfma_f32_16x16x32_bf16 v[132:135], v[160:163], a[4:7], v[132:135]\n" \
  "v_mfma_f32_16x16x32_bf16 v[120:123], v[156:159], a[12:15], v[120:123]\n" \
  "v_mfma_f32_16x16x32_bf16 v[136:139], v[160:163], a[12:15], v[136:139]\n" \
  "v_mfma_f32_16x16x32_bf16 v[124:127], a[20:23], v[156:159], v[124:127]\n" \
  "v_mfma_f32_16x16x32_bf16 v[140:143], a[20:23], v[160:163], v[140:143]\n" \
  "v_mfma_f32_16x16x32_bf16 v[128:131], a[28:31], v[156:159], v[128:131]\n" \
  "v_mfma_f32_16x16x32_bf16 v[144:147], a[28:31], v[160:163], v[144:147]\n" \
  "v_mfma_f32_16x16x32_bf16 v[116:119], v[164:167], a[32:35], v[116:119]\n" \
  "ds_read_b128 v[148:151], %[hA] offset:2048\n" \
  "v_mfma_f32_16x16x32_bf16 v[132:135], v[168:171], a[32:35], v[132:135]\n" \
  "ds_read_b128 v[152:155], %[hA] offset:20480\n" \
  "v_mfma_f32_16x16x32_bf16 v[120:123], v[164:167], a[40:43], v[120:123]\n" \
  "v_mfma_f32_16x16x32_bf16 v[136:139], v[168:171], a[40:43], v[136:139]\n" \
  "v_mfma_f32_16x16x32_bf16 v[124:127], a[48:51], v[164:167], v[124:127]\n" \
  "v_mfma_f32_16x16x32_bf16 v[140:143], a[48:51], v[168:171], v[140:143]\n" \
  "v_mfma_f32_16x16x32_bf16 v[128:131], a[56:59], v[164:167], v[128:131]\n" \
  "v_mfma_f32_16x16x32_bf16 v[144:147], a[56:59], v[168:171], v[144:147]\n" \
  "v_mfma_f32_16x16x32_bf16 v[116:119], v[172:175], a[36:39], v[116:119]\n" \
  "ds_read_b128 v[156:159], %[hA] offset:2560\n" \
  "v_mfma_f32_16x16x32_bf16 v[132:135], v[176:179], a[36:39], v[132:135]\n" \
  "ds_read_b128 v[160:163], %[hA] offset:20992\n" \
  "v_mfma_f32_16x16x32_bf16 v[120:123], v[172:175], a[44:47], v[120:123]\n" \
  "v_mfma_f32_16x16x32_bf16 v[136:139], v[176:179], a[44:47], v[136:139]\n" \
  "v_mfma_f32_16x16x32_bf16 v[124:127], a[52:55], v[172:175], v[124:127]\n" \
  "v_mfma_f32_16x16x32_bf16 v[140:143], a[52:55], v[176:179], v[140:143]\n" \
  "v_mfma_f32_16x16x32_bf16 v[128:131], a[60:63], v[172:175], v[128:131]\n" \
  "v_mfma_f32_16x16x32_bf16 v[144:147], a[60:63], v[176:179], v[144:147]\n" \
  "s_waitcnt lgkmcnt(2)\n" \
  "v_mfma_f32_16x16x32_bf16 v[116:119], v[148:151], a[64:67], v[116:119]\n" \
  "ds_read_b128 v[164:167], %[hA] offset:3072\n" \
  "v_mfma_f32_16x16x32_bf16 v[132:135], v[152:155], a[64:67], v[132:135]\n" \
  "ds_read_b128 v[168:171], %[hA] offset:21504\n" \
  "v_mfma_f32_16x16x32_bf16 v[120:123], v[148:151], a[72:75], v[120:123]\n" \
  "v_mfma_f32_16x16x32_bf16 v[136:139], v[152:155], a[72:75], v[136:139]\n" \
  "v_mfma_f32_16x16x32_bf16 v[124:127], a[80:83], v[148:151], v[124:127]\n" \
  "v_mfma_f32_16x16x32_bf16 v[140:143], a[80:83], v[152:155], v[140:143]\n" \
  "v_mfma_f32_16x16x32_bf16 v[128:131], a[88:91], v[148:151], v[128:131]\n" \
  "v_mfma_f32_16x16x32_bf16 v[144:147], a[88:91], v[152:155], v[144:147]\n" \
  "s_waitcnt lgkmcnt(2)\n" \
  "v_mfma_f32_16x16x32_bf16 v[116:119], v[156:159], a[68:71], v[116:119]\n" \
  "ds_read_b128 v[172:175], %[hA] offset:3584\n" \
  "v_mfma_f32_16x16x32_bf16 v[132:135], v[160:163], a[68:71], v[132:135]\n" \
  "ds_read_b128 v[176:179], %[hA] offset:22016\n" \
  "v_mfma_f32_16x16x32_bf16 v[120:123], v[156:159], a[76:79], v[120:123]\n" \
  "v_mfma_f32_16x16x32_bf16 v[136:139], v[160:163], a[76:79], v[136:139]\n" \
  "v_mfma_f32_16x16x32_bf16 v[124:127], a[84:87], v[156:159], v[124:127]\n" \
  "v_mfma_f32_16x16x32_bf16 v[140:143], a[84:87], v[160:163], v[140:143]\n" \
  "v_mfma_f32_16x16x32_bf16 v[128:131], a[92:95], v[156:159], v[128:131]\n" \
  "v_mfma_f32_16x16x32_bf16 v[144:147], a[92:95], v[160:163], v[144:147]\n" \
  "s_waitcnt lgkmcnt(2)\n" \
  "v_mfma_f32_16x16x32_bf16 v[116:119], v[164:167], a[96:99], v[116:119]\n" \
  "ds_read_b128 v[148:151], %[hA] offset:4096\n" \
  "v_mfma_f32_16x16x32_bf16 v[132:135], v[168:171], a[96:99], v[132:135]\n" \
  "ds_read_b128 v[152:155], %[hA] offset:22528\n" \
  "v_mfma_f32_16x16x32_bf16 v[120:123], v[164:167], a[104:107], v[120:123]\n" \
  "v_mfma_f32_16x16x32_bf16 v[136:139], v[168:171], a[104:107], v[136:139]\n" \
  "v_mfma_f32_16x16x32_bf16 v[124:127], a[112:115], v[164:167], v[124:127]\n" \
  "v_mfma_f32_16x16x32_bf16 v[140:143], a[112:115], v[168:171], v[140:143]\n" \
  "v_mfma_f32_16x16x32_bf16 v[128:131], a[120:123], v[164:167], v[128:131]\n" \
  "v_mfma_f32_16x16x32_bf16 v[144:147], a[120:123], v[168:171], v[144:147]\n" \
  "s_waitcnt lgkmcnt(2)\n" \
  "v_mfma_f32_16x16x32_bf16 v[116:119], v[172:175], a[100:103], v[116:119]\n" \
  "ds_read_b128 v[156:159], %[hA] offset:4608\n" \
  "v_mfma_f32_16x16x32_bf16 v[132:135], v[176:179], a[100:103], v[132:135]\n" \
  "ds_read_b128 v[160:163], %[hA] offset:23040\n" \
  "v_mfma_f32_16x16x32_bf16 v[120:123], v[172:175], a[108:111], v[120:123]\n" \
  "v_mfma_f32_16x16x32_bf16 v[136:139], v[176:179], a[108:111], v[136:139]\n" \
  "v_mfma_f32_16x16x32_bf16 v[124:127], a[116:119], v[172:175], v[124:127]\n" \
  "v_mfma_f32_16x16x32_bf16 v[140:143], a[116:119], v[176:179], v[140:143]\n" \
  "v_mfma_f32_16x16x32_bf16 v[128:131], a[124:127], v[172:175], v[128:131]\n" \
  "v_mfma_f32_16x16x32_bf16 v[144:147], a[124:127], v[176:179], v[144:147]\n" \
  "s_waitcnt lgkmcnt(2)\n" \
  "v_mfma_f32_16x16x32_bf16 v[116:119], v[148:151], a[128:131], v[116:119]\n" \
  "ds_read_b128 v[164:167], %[hA] offset:5120\n" \
  "v_mfma_f32_16x16x32_bf16 v[132:135], v[152:155], a[128:131], v[132:135]\n" \
  "ds_read_b128 v[168:171], %[hA] offset:23552\n" \
  "v_mfma_f32_16x16x32_bf16 v[120:123], v[148:151], a[136:139], v[120:123]\n" \
  "v_mfma_f32_16x16x32_bf16 v[136:139], v[152:155], a[136:139], v[136:139]\n" \
  "v_mfma_f32_16x16x32_bf16 v[124:127], a[144:147], v[148:151], v[124:127]\n" \
  "v_mfma_f32_16x16x32_bf16 v[140:143], a[144:147], v[152:155], v[140:143]\n" \
  "v_mfma_f32_16x16x32_bf16 v[128:131], a[152:155], v[148:151], v[128:131]\n" \
  "v_mfma_f32_16x16x32_bf16 v[144:147], a[152:155], v[152:155], v[144:147]\n" \
  "s_waitcnt lgkmcnt(2)\n" \
  "v_mfma_f32_16x16x32_bf16 v[116:119], v[156:159], a[132:135], v[116:119]\n" \
  "ds_read_b128 v[172:175], %[hA] offset:5632\n" \
  "v_mfma_f32_16x16x32_bf16 v[132:135], v[160:163], a[132:135], v[132:135]\n" \
  "ds_read_b128 v[176:179], %[hA] offset:24064\n" \
  "v_mfma_f32_16x16x32_bf16 v[120:123], v[156:159], a[140:143], v[120:123]\n" \
  "v_mfma_f32_16x16x32_bf16 v[136:139], v[160:163], a[140:143], v[136:139]\n" \
  "v_mfma_f32_16x16x32_bf16 v[124:127], a[148:151], v[156:159], v[124:127]\n" \
  "v_mfma_f32_16x16x32_bf16 v[140:143], a[148:151], v[160:163], v[140:143]\n" \
  "v_mfma_f32_16x16x32_bf16 v[128:131], a[156:159], v[156:159], v[128:131]\n" \
  "v_mfma_f32_16x16x32_bf16 v[144:147], a[156:159], v[160:163], v[144:147]\n" \
  "s_waitcnt lgkmcnt(2)\n" \
  "v_mfma_f32_16x16x32_bf16 v[116:119], v[164:167], a[160:163], v[116:119]\n" \
  "ds_read_b128 v[148:151], %[hA] offset:6144\n" \
  "v_mfma_f32_16x16x32_bf16 v[132:135], v[168:171], a[160:163], v[132:135]\n" \
  "ds_read_b128 v[152:155], %[hA] offset:24576\n" \
  "v_mfma_f32_16x16x32_bf16 v[120:123], v[164:167], a[168:171], v[120:123]\n" \
  "v_mfma_f32_16x16x32_bf16 v[136:139], v[168:171], a[168:171], v[136:139]\n" \
  "v_mfma_f32_16x16x32_bf16 v[124:127], a[176:179], v[164:167], v[124:127]\n" \
  "v_mfma_f32_16x16x32_bf16 v[140:143], a[176:179], v[168:171], v[140:143]\n" \
  "v_mfma_f32_16x16x32_bf16 v[128:131], a[184:187], v[164:167], v[128:131]\n" \
  "v_mfma_f32_16x16x32_bf16 v[144:147], a[184:187], v[168:171], v[144:147]\n" \
  "s_waitcnt lgkmcnt(2)\n" \
  "v_mfma_f32_16x16x32_bf16 v[116:119], v[172:175], a[164:167], v[116:119]\n" \
  "ds_read_b128 v[156:159], %[hA] offset:6656\n" \
  "v_mfma_f32_16x16x32_bf16 v[132:135], v[176:179], a[164:167], v[132:135]\n" \
  "ds_read_b128 v[160:163], %[hA] offset:25088\n" \
  "v_mfma_f32_16x16x32_bf16 v[120:123], v[172:175], a[172:175], v[120:123]\n" \
  "v_mfma_f32_16x16x32_bf16 v[136:139], v[176:179], a[172:175], v[136:139]\n" \
  "v_mfma_f32_16x16x32_bf16 v[124:127], a[180:183], v[172:175], v[124:127]\n" \
  "v_mfma_f32_16x16x32_bf16 v[140:143], a[180:183], v[176:179], v[140:143]\n" \
  "v_mfma_f32_16x16x32_bf16 v[128:131], a[188:191], v[172:175], v[128:131]\n" \
  "v_mfma_f32_16x16x32_bf16 v[144:147], a[188:191], v[176:179], v[144:147]\n" \
  "s_waitcnt lgkmcnt(2)\n" \
  "v_mfma_f32_16x16x32_bf16 v[116:119], v[148:151], a[192:195], v[116:119]\n" \
  "ds_read_b128 v[164:167], %[hA] offset:7168\n" \
  "v_mfma_f32_16x16x32_bf16 v[132:135], v[152:155], a[192:195], v[132:135]\n" \
  "ds_read_b128 v[168:171], %[hA] offset:25600\n" \
  "v_mfma_f32_16x16x32_bf16 v[120:123], v[148:151], a[200:203], v[120:123]\n" \
  "v_mfma_f32_16x16x32_bf16 v[136:139], v[152:155], a[200:203], v[136:139]\n" \
  "v_mfma_f32_16x16x32_bf16 v[124:127], a[208:211], v[148:151], v[124:127]\n" \
  "v_mfma_f32_16x16x32_bf16 v[140:143], a[208:211], v[152:155], v[140:143]\n" \
  "v_mfma_f32_16x16x32_bf16 v[128:131], a[216:219], v[148:151], v[128:131]\n" \
  "v_mfma_f32_16x16x32_bf16 v[144:147], a[216:219], v[152:155], v[144:147]\n" \
  "s_waitcnt lgkmcnt(2)\n" \
  "v_mfma_f32_16x16x32_bf16 v[116:119], v[156:159], a[196:199], v[116:119]\n" \
  "ds_read_b128 v[172:175], %[hA] offset:7680\n" \
  "v_mfma_f32_16x16x32_bf16 v[132:135], v[160:163], a[196:199], v[132:135]\n" \
  "ds_read_b128 v[176:179], %[hA] offset:26112\n" \
  "v_mfma_f32_16x16x32_bf16 v[120:123], v[156:159], a[204:207], v[120:123]\n" \
  "ds_read_b128 v[148:151], %[hA] offset:8192\n" \
  "v_mfma_f32_16x16x32_bf16 v[136:139], v[160:163], a[204:207], v[136:139]\n" \
  "ds_read_b128 v[152:155], %[hA] offset:26624\n" \
  "v_mfma_f32_16x16x32_bf16 v[124:127], a[212:215], v[156:159], v[124:127]\n" \
  "v_mfma_f32_16x16x32_bf16 v[140:143], a[212:215], v[160:163], v[140:143]\n" \
  "v_mfma_f32_16x16x32_bf16 v[128:131], a[220:223], v[156:159], v[128:131]\n" \
  "v_mfma_f32_16x16x32_bf16 v[144:147], a[220:223], v[160:163], v[144:147]\n" \
  "s_waitcnt lgkmcnt(4)\n" \
  "v_mfma_f32_16x16x32_bf16 v[124:127], a[240:243], v[164:167], v[124:127]\n" \
  "ds_read_b128 v[156:159], %[hA] offset:8704\n" \
  "v_mfma_f32_16x16x32_bf16 v[140:143], a[240:243], v[168:171], v[140:143]\n" \
  "ds_read_b128 v[160:163], %[hA] offset:27136\n" \
  "v_mfma_f32_16x16x32_bf16 v[128:131], a[248:251], v[164:167], v[128:131]\n" \
  "v_mfma_f32_16x16x32_bf16 v[144:147], a[248:251], v[168:171], v[144:147]\n" \
  "s_waitcnt lgkmcnt(4)\n" \
  "v_mfma_f32_16x16x32_bf16 v[124:127], a[244:247], v[172:175], v[124:127]\n" \
  "v_mfma_f32_16x16x32_bf16 v[140:143], a[244:247], v[176:179], v[140:143]\n" \
  "v_mfma_f32_16x16x32_bf16 v[128:131], a[252:255], v[172:175], v[128:131]\n" \
  "v_mfma_f32_16x16x32_bf16 v[144:147], a[252:255], v[176:179], v[144:147]\n" \
  "s_waitcnt lgkmcnt(2)\n" \
  "v_mfma_f32_16x16x32_bf16 v[124:127], v[48:51], v[148:151], v[124:127]\n" \
  "v_mfma_f32_16x16x32_bf16 v[140:143], v[48:51], v[152:155], v[140:143]\n" \
  "v_mfma_f32_16x16x32_bf16 v[128:131], v[56:59], v[148:151], v[128:131]\n" \
  "v_mfma_f32_16x16x32_bf16 v[144:147], v[56:59], v[152:155], v[144:147]\n" \
  "s_waitcnt lgkmcnt(0)\n" \
  "v_mfma_f32_16x16x32_bf16 v[124:127], v[52:55], v[156:159], v[124:127]\n" \
  "v_mfma_f32_16x16x32_bf16 v[140:143], v[52:55], v[160:163], v[140:143]\n" \
  "v_mfma_f32_16x16x32_bf16 v[128:131], v[60:63], v[156:159], v[128:131]\n" \
  "v_mfma_f32_16x16x32_bf16 v[144:147], v[60:63], v[160:163], v[144:147]\n" \
  "v_mfma_f32_16x16x32_bf16 v[116:119], v[164:167], a[224:227], v[116:119]\n" \
  "v_mfma_f32_16x16x32_bf16 v[132:135], v[168:171], a[224:227], v[132:135]\n" \
  "v_mfma_f32_16x16x32_bf16 v[120:123], v[164:167], a[232:235], v[120:123]\n" \
  "v_mfma_f32_16x16x32_bf16 v[136:139], v[168:171], a[232:235], v[136:139]\n" \
  "v_mfma_f32_16x16x32_bf16 v[116:119], v[172:175], a[228:231], v[116:119]\n" \
  "v_mfma_f32_16x16x32_bf16 v[132:135], v[176:179], a[228:231], v[132:135]\n" \
  "v_mfma_f32_16x16x32_bf16 v[120:123], v[172:175], a[236:239], v[120:123]\n" \
  "v_mfma_f32_16x16x32_bf16 v[136:139], v[176:179], a[236:239], v[136:139]\n" \
  "v_mfma_f32_16x16x32_bf16 v[116:119], v[148:151], v[32:35], v[116:119]\n" \
  "v_mfma_f32_16x16x32_bf16 v[132:135], v[152:155], v[32:35], v[132:135]\n" \
  "v_mfma_f32_16x16x32_bf16 v[120:123], v[148:151], v[40:43], v[120:123]\n" \
  "v_mfma_f32_16x16x32_bf16 v[136:139], v[152:155], v[40:43], v[136:139]\n" \
  "v_mfma_f32_16x16x32_bf16 v[116:119], v[156:159], v[36:39], v[116:119]\n" \
  "v_mfma_f32_16x16x32_bf16 v[132:135], v[160:163], v[36:39], v[132:135]\n" \
  "v_mfma_f32_16x16x32_bf16 v[120:123], v[156:159], v[44:47], v[120:123]\n" \
  "v_mfma_f32_16x16x32_bf16 v[136:139], v[160:163], v[44:47], v[136:139]\n" \
  "s_memrealtime s[76:77]\n" \
  "s_waitcnt lgkmcnt(0)\n" \
  "s_add_i32 s76, s76, s95\n" \
  "s_add_i32 s76, s76, 90\n" \
  "s_sub_i32 s76, s76, s86\n" \
  "s_cmp_gt_u32 s76, -3000\n" \
  "s_cselect_b32 s76, 0, s76\n" \
  "s_add_i32 s86, s86, s76\n" \
  "s_add_i32 s88, s60, 3\n" \
  "v_add_u32 v252, s76, v252\n" \
  "v_add_u32 v253, s76, v253\n" \
  "buffer_load_dwordx4 v[64:67], %[mo], %[X], s63 offen offset:0 sc1\n" \
  "buffer_load_dwordx4 v[68:71], %[mo], %[X], s63 offen offset:1024 sc1\n" \
  "buffer_load_dwordx4 v[72:75], %[mo], %[X], s63 offen offset:2048 sc1\n" \
  "buffer_load_dwordx4 v[76:79], %[mo], %[X], s63 offen offset:3072 sc1\n" \
  "buffer_load_dwordx4 v[80:83], v228, %[X], s63 offen offset:0 sc1\n" \
  "buffer_load_dwordx4 v[84:87], v228, %[X], s63 offen offset:1024 sc1\n" \
  "buffer_load_dwordx4 v[88:91], v228, %[X], s63 offen offset:2048 sc1\n" \
  "buffer_load_dwordx4 v[92:95], v228, %[X], s63 offen offset:3072 sc1\n" \
  "buffer_load_dwordx4 v[96:99], %[mo], %[X], s64 offen offset:0 sc1\n" \
  "v_mov_b64 v[100:101], 0\n" \
  "v_mov_b64 v[102:103], 0\n" \
  "v_mov_b64 v[104:105], 0\n" \
  "v_mov_b64 v[106:107], 0\n" \
  "v_mov_b64 v[108:109], 0\n" \
  "v_mov_b64 v[110:111], 0\n" \
  "v_mov_b64 v[112:113], 0\n" \
  "v_mov_b64 v[114:115], 0\n" \
  "v_cvt_pk_bf16_f32 v220, v124, v125\n" \
  "v_cvt_pk_bf16_f32 v221, v126, v127\n" \
  "v_cvt_pk_bf16_f32 v222, v128, v129\n" \
  "v_cvt_pk_bf16_f32 v223, v130, v131\n" \
  "v_cvt_pk_bf16_f32 v224, v140, v141\n" \
  "v_cvt_pk_bf16_f32 v225, v142, v143\n" \
  "v_cvt_pk_bf16_f32 v226, v144, v145\n" \
  "v_cvt_pk_bf16_f32 v227, v146, v147\n" \
  "ds_write_b128 %[ktW], v[220:223] offset:0\n" \
  "ds_write_b128 %[ktW], v[224:227] offset:4096\n" \
  "v_xor_b32 %[hA], 0x10000, %[hA]\n" \
  "v_mov_b32 v231, 0\n" \
  "s_waitcnt lgkmcnt(0)\n" \
  "s_barrier\n" \
  "ds_read_b128 v[180:183], %[ktR] offset:0\n" \
  "ds_read_b128 v[184:187], %[ktR] offset:1024\n" \
  "ds_read_b128 v[188:191], %[ktR] offset:2048\n" \
  "ds_read_b128 v[192:195], %[ktR] offset:3072\n" \
  "ds_read_b128 v[196:199], %[ktR] offset:4096\n" \
  "ds_read_b128 v[200:203], %[ktR] offset:5120\n" \
  "ds_read_b128 v[204:207], %[ktR] offset:6144\n" \
  "ds_read_b128 v[208:211], %[ktR] offset:7168\n" \
  "s_waitcnt lgkmcnt(0)\n" \
  "s_branch .Lqk_m\n" \
  ".Lqk_entry:\n" \
  "s_mov_b32 s81, 63\n" \
  "s_mov_b32 s87, %[mr0]\n" \
  "s_cmp_eq_u32 %[grp], 0\n" \
  "s_cbranch_scc1 .Lqk_go\n" \
  "s_mov_b32 s69, 0\n" \
  "s_xor_b32 s75, %[bas], 0x2000\n" \
  ".Lqk_g1:\n" \
  "buffer_load_dword v229, off, %[X], s75 offset:48 sc1\n" \
  "s_waitcnt vmcnt(0)\n" \
  "v_readfirstlane_b32 s74, v229\n" \
  "s_cmp_lg_u32 s74, 0\n" \
  "s_cbranch_scc1 .Lqk_g2\n" \
  "s_sleep 8\n" \
  "s_add_i32 s69, s69, 1\n" \
  "s_cmp_lt_u32 s69, 400\n" \
  "s_cbranch_scc1 .Lqk_g1\n" \
  ".Lqk_g2:\n" \
  "s_add_i32 s74, s74, %[g1t]\n" \
  ".Lqk_g3:\n" \
  "s_memrealtime s[76:77]\n" \
  "s_waitcnt lgkmcnt(0)\n" \
  "s_sub_i32 s76, s76, s74\n" \
  "s_cmp_gt_u32 s76, -3000\n" \
  "s_cbranch_scc1 .Lqk_g3\n" \
  ".Lqk_go:\n" \
  "s_waitcnt vmcnt(0) lgkmcnt(0)\n" \
  "s_barrier\n" \
  "s_mov_b32 s75, 0\n" \
  "s_movk_i32 s76, 0x1000\n" \
  "s_movk_i32 s77, 0x2000\n" \
  "v_mov_b32 v236, 1\n" \
  "buffer_atomic_add v236, %[rdy], %[X], %[bas] offen offset:1536 sc0 sc1\n" \
  "s_mov_b32 s69, 0\n" \
  ".Lqk_enw:\n" \
  "buffer_load_dwordx4 v[64:67], %[hvo], %[HS], s75 offen offset:0 sc1\n" \
  "buffer_load_dwordx4 v[68:71], %[hvo], %[HS], s75 offen offset:1024 sc1\n" \
  "buffer_load_dwordx4 v[72:75], %[hvo], %[HS], s75 offen offset:2048 sc1\n" \
  "buffer_load_dwordx4 v[76:79], %[hvo], %[HS], s75 offen offset:3072 sc1\n" \
  "buffer_load_dwordx4 v[80:83], %[hvo], %[HS], s76 offen offset:0 sc1\n" \
  "buffer_load_dwordx4 v[84:87], %[hvo], %[HS], s76 offen offset:1024 sc1\n" \
  "buffer_load_dwordx4 v[88:91], %[hvo], %[HS], s76 offen offset:2048 sc1\n" \
  "buffer_load_dwordx4 v[92:95], %[hvo], %[HS], s76 offen offset:3072 sc1\n" \
  "buffer_load_dwordx4 v[96:99], %[hvo], %[HS], s77 offen offset:0 sc1\n" \
  "s_waitcnt vmcnt(0)\n" \
  "v_max3_u32 v229, v64, v65, v66\n" \
  "v_max3_u32 v229, v229, v67, v68\n" \
  "v_max3_u32 v229, v229, v69, v70\n" \
  "v_max3_u32 v229, v229, v71, v72\n" \
  "v_max3_u32 v229, v229, v73, v74\n" \
  "v_max3_u32 v229, v229, v75, v76\n" \
  "v_max3_u32 v229, v229, v77, v78\n" \
  "v_max3_u32 v229, v229, v79, v80\n" \
  "v_max3_u32 v229, v229, v81, v82\n" \
  "v_max3_u32 v229, v229, v83, v84\n" \
  "v_max3_u32 v229, v229, v85, v86\n" \
  "v_max3_u32 v229, v229, v87, v88\n" \
  "v_max3_u32 v229, v229, v89, v90\n" \
  "v_max3_u32 v229, v229, v91, v92\n" \
  "v_max3_u32 v229, v229, v93, v94\n" \
  "v_max3_u32 v229, v229, v95, v96\n" \
  "v_max3_u32 v229, v229, v97, v98\n" \
  "v_max_u32 v229, v229, v99\n" \
  "v_cmp_eq_u32 vcc, -1, v229\n" \
  "s_cbranch_vccz .Lqk_enw_ok\n" \
  "s_sleep 4\n" \
  "s_add_i32 s69, s69, 1\n" \
  "s_and_b32 s67, s69, 1023\n" \
  "s_cbranch_scc1 .Lqk_enw\n" \
  "buffer_load_dword v229, off, %[F], 0 offset:1024 sc1\n" \
  "s_waitcnt vmcnt(0)\n" \
  "v_readfirstlane_b32 s67, v229\n" \
  "s_cmp_lg_u32 s67, 0\n" \
  "s_cbranch_scc1 .Lqk_enw_ok\n" \
  "s_cmp_lt_u32 s69, 400000\n" \
  "s_cbranch_scc1 .Lqk_enw\n" \
  "v_mov_b32 v229, 1\n" \
  "buffer_store_dword v229, off, %[F], 0 offset:1024 sc1\n" \
  ".Lqk_enw_ok:\n" \
  "ds_write_b128 %[hvo], v[64:67] offset:0\n" \
  "ds_write_b128 %[hvo], v[68:71] offset:1024\n" \
  "ds_write_b128 %[hvo], v[72:75] offset:2048\n" \
  "ds_write_b128 %[hvo], v[76:79] offset:3072\n" \
  "ds_write_b128 %[hvo], v[80:83] offset:4096\n" \
  "ds_write_b128 %[hvo], v[84:87] offset:5120\n" \
  "ds_write_b128 %[hvo], v[88:91] offset:6144\n" \
  "ds_write_b128 %[hvo], v[92:95] offset:7168\n" \
  "ds_write_b128 %[hvo], v[96:99] offset:8192\n" \
  "v_readfirstlane_b32 s92, v236\n" \
  "s_cmp_lg_u32 s92, 103\n" \
  "s_cbranch_scc1 .Lqk_nrdy\n" \
  "s_memrealtime s[74:75]\n" \
  "s_waitcnt lgkmcnt(0)\n" \
  "s_add_i32 s74, s74, %[x0]\n" \
  "s_mul_i32 s75, %[tau], 162\n" \
  "s_lshr_b32 s75, s75, 8\n" \
  "s_add_i32 s75, s75, s74\n" \
  "v_mov_b32 v236, s74\n" \
  "v_mov_b32 v237, s75\n" \
  "buffer_store_dwordx2 v[236:237], %[rdy], %[X], %[bas] offen offset:0 sc1\n" \
  "s_add_i32 s74, s74, %[tau]\n" \
  "s_add_i32 s75, s75, %[tau]\n" \
  "v_mov_b32 v236, s74\n" \
  "v_mov_b32 v237, s75\n" \
  "buffer_store_dwordx2 v[236:237], %[rdy], %[X], %[bas] offen offset:16 sc1\n" \
  ".Lqk_nrdy:\n" \
  "s_mov_b32 s95, %[tau]\n" \
  "s_mov_b32 s88, 3\n" \
  "s_mov_b32 s90, 0\n" \
  "s_mov_b32 s78, 0\n" \
  "s_waitcnt lgkmcnt(0)\n" \
  "s_barrier\n" \
  "ds_read_b128 v[148:151], %[hA] offset:0\n" \
  "ds_read_b128 v[152:155], %[hA] offset:18432\n" \
  "ds_read_b128 v[156:159], %[hA] offset:512\n" \
  "ds_read_b128 v[160:163], %[hA] offset:18944\n" \
  "ds_read_b128 v[164:167], %[hA] offset:1024\n" \
  "ds_read_b128 v[168:171], %[hA] offset:19456\n" \
  "ds_read_b128 v[172:175], %[hA] offset:1536\n" \
  "ds_read_b128 v[176:179], %[hA] offset:19968\n" \
  "s_waitcnt lgkmcnt(0)\n" \
  "v_mfma_f32_16x16x32_bf16 v[116:119], v[148:151], a[0:3], 0\n" \
  "v_mfma_f32_16x16x32_bf16 v[132:135], v[152:155], a[0:3], 0\n" \
  "v_mfma_f32_16x16x32_bf16 v[120:123], v[148:151], a[8:11], 0\n" \
  "v_mfma_f32_16x16x32_bf16 v[136:139], v[152:155], a[8:11], 0\n" \
  "v_mfma_f32_16x16x32_bf16 v[124:127], a[16:19], v[148:151], 0\n" \
  "v_mfma_f32_16x16x32_bf16 v[140:143], a[16:19], v[152:155], 0\n" \
  "v_mfma_f32_16x16x32_bf16 v[128:131], a[24:27], v[148:151], 0\n" \
  "v_mfma_f32_16x16x32_bf16 v[144:147], a[24:27], v[152:155], 0\n" \
  "v_mfma_f32_16x16x32_bf16 v[116:119], v[156:159], a[4:7], v[116:119]\n" \
  "v_mfma_f32_16x16x32_bf16 v[132:135], v[160:163], a[4:7], v[132:135]\n" \
  "v_mfma_f32_16x16x32_bf16 v[120:123], v[156:159], a[12:15], v[120:123]\n" \
  "v_mfma_f32_16x16x32_bf16 v[136:139], v[160:163], a[12:15], v[136:139]\n" \
  "v_mfma_f32_16x16x32_bf16 v[124:127], a[20:23], v[156:159], v[124:127]\n" \
  "v_mfma_f32_16x16x32_bf16 v[140:143], a[20:23], v[160:163], v[140:143]\n" \
  "v_mfma_f32_16x16x32_bf16 v[128:131], a[28:31], v[156:159], v[128:131]\n" \
  "v_mfma_f32_16x16x32_bf16 v[144:147], a[28:31], v[160:163], v[144:147]\n" \
  "s_waitcnt lgkmcnt(0)\n" \
  "s_branch .Lqk_top\n" \
  ".Lqk_end:\n"
#define QK_CLOB "v32", "v33", "v34", "v35", "v36", "v37", "v38", "v39", "v40", "v41", "v42", "v43", "v44", "v45", "v46", "v47", "v48", "v49", "v50", "v51", "v52", "v53", "v54", "v55", "v56", "v57", "v58", "v59", "v60", "v61", "v62", "v63", "v64", "v65", "v66", "v67", "v68", "v69", "v70", "v71", "v72", "v73", "v74", "v75", "v76", "v77", "v78", "v79", "v80", "v81", "v82", "v83", "v84", "v85", "v86", "v87", "v88", "v89", "v90", "v91", "v92", "v93", "v94", "v95", "v96", "v97", "v98", "v99", "v100", "v101", "v102", "v103", "v104", "v105", "v106", "v107", "v108", "v109", "v110", "v111", "v112", "v113", "v114", "v115", "v116", "v117", "v118", "v119", "v120", "v121", "v122", "v123", "v124", "v125", "v126", "v127", "v128", "v129", "v130", "v131", "v132", "v133", "v134", "v135", "v136", "v137", "v138", "v139", "v140", "v141", "v142", "v143", "v144", "v145", "v146", "v147", "v148", "v149", "v150", "v151", "v152", "v153", "v154", "v155", "v156", "v157", "v158", "v159", "v160", "v161", "v162", "v163", "v164", "v165", "v166", "v167", "v168", "v169", "v170", "v171", "v172", "v173", "v174", "v175", "v176", "v177", "v178", "v179", "v180", "v181", "v182", "v183", "v184", "v185", "v186", "v187", "v188", "v189", "v190", "v191", "v192", "v193", "v194", "v195", "v196", "v197", "v198", "v199", "v200", "v201", "v202", "v203", "v204", "v205", "v206", "v207", "v208", "v209", "v210", "v211", "v212", "v213", "v214", "v215", "v216", "v217", "v218", "v219", "v220", "v221", "v222", "v223", "v224", "v225", "v226", "v227", "v228", "v229", "v230", "v231", "v232", "v233", "v234", "v235", "v236", "v237", "v238", "v239", "v240", "v241", "v242", "v243", "v244", "v245", "v246", "v247", "v248", "v249", "v250", "v251", "v252", "v253", "v254", "v255", "s60", "s61", "s62", "s63", "s64", "s65", "s66", "s67", "s68", "s69", "s70", "s71", "s72", "s73", "s74", "s75", "s76", "s77", "s78", "s79", "s80", "s81", "s82", "s83", "s84", "s85", "s86", "s87", "s88", "s89", "s90", "s91", "s92", "s93", "s94", "s95", "s96", "a0", "a1", "a2", "a3", "a4", "a5", "a6", "a7", "a8", "a9", "a10", "a11", "a12", "a13", "a14", "a15", "a16", "a17", "a18", "a19", "a20", "a21", "a22", "a23", "a24", "a25", "a26", "a27", "a28", "a29", "a30", "a31", "a32", "a33", "a34", "a35", "a36", "a37", "a38", "a39", "a40", "a41", "a42", "a43", "a44", "a45", "a46", "a47", "a48", "a49", "a50", "a51", "a52", "a53", "a54", "a55", "a56", "a57", "a58", "a59", "a60", "a61", "a62", "a63", "a64", "a65", "a66", "a67", "a68", "a69", "a70", "a71", "a72", "a73", "a74", "a75", "a76", "a77", "a78", "a79", "a80", "a81", "a82", "a83", "a84", "a85", "a86", "a87", "a88", "a89", "a90", "a91", "a92", "a93", "a94", "a95", "a96", "a97", "a98", "a99", "a100", "a101", "a102", "a103", "a104", "a105", "a106", "a107", "a108", "a109", "a110", "a111", "a112", "a113", "a114", "a115", "a116", "a117", "a118", "a119", "a120", "a121", "a122", "a123", "a124", "a125", "a126", "a127", "a128", "a129", "a130", "a131", "a132", "a133", "a134", "a135", "a136", "a137", "a138", "a139", "a140", "a141", "a142", "a143", "a144", "a145", "a146", "a147", "a148", "a149", "a150", "a151", "a152", "a153", "a154", "a155", "a156", "a157", "a158", "a159", "a160", "a161", "a162", "a163", "a164", "a165", "a166", "a167", "a168", "a169", "a170", "a171", "a172", "a173", "a174", "a175", "a176", "a177", "a178", "a179", "a180", "a181", "a182", "a183", "a184", "a185", "a186", "a187", "a188", "a189", "a190", "a191", "a192", "a193", "a194", "a195", "a196", "a197", "a198", "a199", "a200", "a201", "a202", "a203", "a204", "a205", "a206", "a207", "a208", "a209", "a210", "a211", "a212", "a213", "a214", "a215", "a216", "a217", "a218", "a219", "a220", "a221", "a222", "a223", "a224", "a225", "a226", "a227", "a228", "a229", "a230", "a231", "a232", "a233", "a234", "a235", "a236", "a237", "a238", "a239", "a240", "a241", "a242", "a243", "a244", "a245", "a246", "a247", "a248", "a249", "a250", "a251", "a252", "a253", "a254", "a255", "vcc", "scc", "m0", "memory"
#define V_ASM \
  "s_nop 4\n" \
  "s_cmp_eq_u32 %[grp], 0\n" \
  "s_cbranch_scc1 .Lv_w0\n" \
  "v_add_u32 v251, 16384, %[wo]\n" \
  "global_load_dwordx4 v[24:27], v251, %[wp] offset:0\n" \
  "global_load_dwordx4 v[28:31], v251, %[wp] offset:1024\n" \
  "v_add_u32 v251, 18432, v251\n" \
  "global_load_dwordx4 v[32:35], v251, %[wp] offset:0\n" \
  "global_load_dwordx4 v[36:39], v251, %[wp] offset:1024\n" \
  "v_add_u32 v251, 18432, v251\n" \
  "global_load_dwordx4 v[40:43], v251, %[wp] offset:0\n" \
  "global_load_dwordx4 v[44:47], v251, %[wp] offset:1024\n" \
  "v_add_u32 v251, 18432, v251\n" \
  "global_load_dwordx4 v[48:51], v251, %[wp] offset:0\n" \
  "global_load_dwordx4 v[52:55], v251, %[wp] offset:1024\n" \
  "s_waitcnt vmcnt(0)\n" \
  "s_branch .Lv_w2\n" \
  ".Lv_w0:\n" \
  "v_add_u32 v251, 0, %[wo]\n" \
  ".Lv_w2:\n" \
  "s_mov_b32 s56, -2\n" \
  "v_add_u32 v250, 0x1000, %[la]\n" \
  "s_mov_b32 s72, -1\n" \
  "s_mov_b32 s73, 0\n" \
  "s_mov_b32 s78, 0\n" \
  "s_mov_b32 s79, 0\n" \
  "s_mov_b32 s80, 0\n" \
  "s_mov_b32 s66, 0\n" \
  "s_mov_b32 s81, -1\n" \
  "s_mov_b32 s87, 2.0\n" \
  "s_add_i32 s69, s56, 2\n" \
  "s_min_i32 s68, s69, 63\n" \
  "s_mul_i32 s57, s68, 36864\n" \
  "s_lshl_b32 s68, s69, 16\n" \
  "s_and_b32 s68, s68, 0x10000\n" \
  "s_add_i32 s58, %[hl0], s68\n" \
  "s_add_i32 s68, s56, -1\n" \
  "s_mul_i32 s63, s68, 36864\n" \
  "s_add_i32 s63, s63, %[hl0]\n" \
  "s_cmp_gt_i32 s56, 0\n" \
  "s_cselect_b32 s63, s63, 2.0\n" \
  "s_mov_b32 s57, 2.0\n" \
  ".Lv_top:\n" \
  "v_mfma_f32_16x16x32_bf16 v[60:63], v[204:207], a[12:15], v[60:63]\n" \
  "ds_read_b128 v[104:107], %[hA] offset:1024\n" \
  "s_add_i32 m0, s58, 0\n" \
  "v_mfma_f32_16x16x32_bf16 v[76:79], v[208:211], a[12:15], v[76:79]\n" \
  "ds_read_b128 v[108:111], %[hA] offset:19456\n" \
  "v_mfma_f32_16x16x32_bf16 v[64:67], v[204:207], a[20:23], v[64:67]\n" \
  "ds_read_b128 v[88:91], %[hA] offset:1536\n" \
  "v_mfma_f32_16x16x32_bf16 v[80:83], v[208:211], a[20:23], v[80:83]\n" \
  "ds_read_b128 v[92:95], %[hA] offset:19968\n" \
  "v_mfma_f32_16x16x32_bf16 v[68:71], v[204:207], a[28:31], v[68:71]\n" \
  "ds_read2st64_b64 v[128:131], %[rtA] offset0:0 offset1:4\n" \
  "buffer_load_dwordx4 %[hvo], %[HS], s57 offen offset:0 sc1 lds\n" \
  "v_mfma_f32_16x16x32_bf16 v[84:87], v[208:211], a[28:31], v[84:87]\n" \
  "ds_read2st64_b64 v[132:135], %[rtA] offset0:8 offset1:12\n" \
  "s_waitcnt lgkmcnt(4)\n" \
  "v_mfma_f32_16x16x32_bf16 v[56:59], v[104:107], a[32:35], v[56:59]\n" \
  "ds_read_b128 v[96:99], %[hA] offset:2048\n" \
  "v_mfma_f32_16x16x32_bf16 v[72:75], v[108:111], a[32:35], v[72:75]\n" \
  "ds_read_b128 v[100:103], %[hA] offset:20480\n" \
  "v_mfma_f32_16x16x32_bf16 v[60:63], v[104:107], a[40:43], v[60:63]\n" \
  "buffer_load_dwordx4 %[hvo], %[HS], s57 offen offset:1024 sc1 lds\n" \
  "v_mfma_f32_16x16x32_bf16 v[76:79], v[108:111], a[40:43], v[76:79]\n" \
  "s_add_i32 s56, s56, 1\n" \
  "s_lshl_b32 s94, s56, 4\n" \
  "s_add_i32 s94, s94, %[bas]\n" \
  "v_mfma_f32_16x16x32_bf16 v[64:67], v[104:107], a[48:51], v[64:67]\n" \
  "s_waitcnt lgkmcnt(2)\n" \
  "v_add_f32 v128, v128, v130\n" \
  "v_add_f32 v129, v129, v131\n" \
  "v_mfma_f32_16x16x32_bf16 v[80:83], v[108:111], a[48:51], v[80:83]\n" \
  "v_add_f32 v132, v132, v134\n" \
  "v_add_f32 v133, v133, v135\n" \
  "v_mfma_f32_16x16x32_bf16 v[68:71], v[104:107], a[56:59], v[68:71]\n" \
  "v_add_f32 v128, v128, v132\n" \
  "v_add_f32 v129, v129, v133\n" \
  "v_mfma_f32_16x16x32_bf16 v[84:87], v[108:111], a[56:59], v[84:87]\n" \
  "buffer_load_dwordx4 %[hvo], %[HS], s57 offen offset:2048 sc1 lds\n" \
  "v_mfma_f32_16x16x32_bf16 v[56:59], v[88:91], a[36:39], v[56:59]\n" \
  "ds_read_b128 v[104:107], %[hA] offset:2560\n" \
  "v_add_f32_dpp v128, v128, v128 quad_perm:[1,0,3,2] row_mask:0xf bank_mask:0xf\n" \
  "v_mfma_f32_16x16x32_bf16 v[72:75], v[92:95], a[36:39], v[72:75]\n" \
  "ds_read_b128 v[108:111], %[hA] offset:20992\n" \
  "v_add_f32_dpp v129, v129, v129 quad_perm:[1,0,3,2] row_mask:0xf bank_mask:0xf\n" \
  "v_mfma_f32_16x16x32_bf16 v[60:63], v[88:91], a[44:47], v[60:63]\n" \
  "v_cvt_pk_bf16_f32 v128, v128, v129\n" \
  "v_xor_b32 v128, s62, v128\n" \
  "v_mfma_f32_16x16x32_bf16 v[76:79], v[92:95], a[44:47], v[76:79]\n" \
  "buffer_load_dwordx4 %[hvo], %[HS], s57 offen offset:3072 sc1 lds\n" \
  "v_min_u32 v128, -2, v128\n" \
  "v_mfma_f32_16x16x32_bf16 v[64:67], v[88:91], a[52:55], v[64:67]\n" \
  "buffer_store_dword v128, %[lo], %[OS], s63 offen sc1\n" \
  "v_mov_b32 v251, -1\n" \
  "v_mfma_f32_16x16x32_bf16 v[80:83], v[92:95], a[52:55], v[80:83]\n" \
  "s_add_i32 s57, s57, 0x1000\n" \
  "s_add_i32 m0, s58, 4096\n" \
  "v_mov_b64 v[228:229], v[212:213]\n" \
  "v_mfma_f32_16x16x32_bf16 v[68:71], v[88:91], a[60:63], v[68:71]\n" \
  "s_lshl_b32 s68, s56, 17\n" \
  "s_and_b32 s68, s68, 0x60000\n" \
  "s_add_i32 s59, s87, s68\n" \
  "v_mfma_f32_16x16x32_bf16 v[84:87], v[92:95], a[60:63], v[84:87]\n" \
  "s_xor_b32 s68, s68, 0x40000\n" \
  "s_add_i32 s64, %[mw0], s68\n" \
  "s_waitcnt lgkmcnt(2)\n" \
  "v_mfma_f32_16x16x32_bf16 v[56:59], v[96:99], a[64:67], v[56:59]\n" \
  "ds_read_b128 v[88:91], %[hA] offset:3072\n" \
  "buffer_load_dwordx4 %[hvo], %[HS], s57 offen offset:0 sc1 lds\n" \
  "v_mfma_f32_16x16x32_bf16 v[72:75], v[100:103], a[64:67], v[72:75]\n" \
  "ds_read_b128 v[92:95], %[hA] offset:21504\n" \
  "v_mov_b64 v[230:231], v[216:217]\n" \
  "v_mfma_f32_16x16x32_bf16 v[60:63], v[96:99], a[72:75], v[60:63]\n" \
  "s_cmp_gt_i32 s56, 1\n" \
  "s_cselect_b32 s64, s64, 2.0\n" \
  "s_add_i32 s69, s56, 1\n" \
  "v_mfma_f32_16x16x32_bf16 v[76:79], v[100:103], a[72:75], v[76:79]\n" \
  "s_lshl_b32 s68, s69, 17\n" \
  "s_and_b32 s68, s68, 0x60000\n" \
  "s_add_i32 s67, %[mw0], s68\n" \
  "v_mfma_f32_16x16x32_bf16 v[64:67], v[96:99], a[80:83], v[64:67]\n" \
  "buffer_load_dwordx4 %[hvo], %[HS], s57 offen offset:1024 sc1 lds\n" \
  "v_mov_b64 v[232:233], v[220:221]\n" \
  "v_mfma_f32_16x16x32_bf16 v[80:83], v[100:103], a[80:83], v[80:83]\n" \
  "s_cmp_lt_u32 s56, 63\n" \
  "s_cselect_b32 s67, s67, 2.0\n" \
  "s_lshl_b32 s69, s56, 18\n" \
  "v_mfma_f32_16x16x32_bf16 v[68:71], v[96:99], a[88:91], v[68:71]\n" \
  "s_and_b32 s69, s69, 0x40000\n" \
  "s_add_i32 s65, %[pm0], s69\n" \
  "s_xor_b32 s69, s69, 0x40000\n" \
  "v_mfma_f32_16x16x32_bf16 v[84:87], v[100:103], a[88:91], v[84:87]\n" \
  "v_mov_b64 v[234:235], v[224:225]\n" \
  "s_waitcnt lgkmcnt(2)\n" \
  "v_mfma_f32_16x16x32_bf16 v[56:59], v[104:107], a[68:71], v[56:59]\n" \
  "ds_read_b128 v[96:99], %[hA] offset:3584\n" \
  "v_mov_b64 v[236:237], v[244:245]\n" \
  "v_mfma_f32_16x16x32_bf16 v[72:75], v[108:111], a[68:71], v[72:75]\n" \
  "ds_read_b128 v[100:103], %[hA] offset:22016\n" \
  "buffer_load_dwordx4 %[hvo], %[HS], s57 offen offset:2048 sc1 lds\n" \
  "v_mfma_f32_16x16x32_bf16 v[60:63], v[104:107], a[76:79], v[60:63]\n" \
  "s_add_i32 s60, %[pt0], s69\n" \
  "s_cmp_gt_i32 s56, 0\n" \
  "s_cselect_b32 s60, s60, 2.0\n" \
  "v_mfma_f32_16x16x32_bf16 v[76:79], v[108:111], a[76:79], v[76:79]\n" \
  "s_add_i32 s61, s60, 0x4000\n" \
  "v_mov_b64 v[238:239], v[246:247]\n" \
  "v_mov_b64 v[112:113], 0\n" \
  "v_mfma_f32_16x16x32_bf16 v[64:67], v[104:107], a[84:87], v[64:67]\n" \
  "s_mov_b32 s62, s66\n" \
  "s_bfe_i32 s68, s56, 0x10001\n" \
  "s_and_b32 s66, s68, 0x80008000\n" \
  "v_mfma_f32_16x16x32_bf16 v[80:83], v[108:111], a[84:87], v[80:83]\n" \
  "buffer_load_dwordx4 %[hvo], %[HS], s57 offen offset:3072 sc1 lds\n" \
  "v_mov_b64 v[114:115], 0\n" \
  "v_mfma_f32_16x16x32_bf16 v[68:71], v[104:107], a[92:95], v[68:71]\n" \
  "s_add_i32 s57, s57, 0x1000\n" \
  "s_add_i32 m0, s58, 8192\n" \
  "v_mov_b64 v[116:117], 0\n" \
  "v_mfma_f32_16x16x32_bf16 v[84:87], v[108:111], a[92:95], v[84:87]\n" \
  "v_mov_b64 v[118:119], 0\n" \
  "s_waitcnt lgkmcnt(2)\n" \
  "v_mfma_f32_16x16x32_bf16 v[56:59], v[88:91], a[96:99], v[56:59]\n" \
  "ds_read_b128 v[104:107], %[hA] offset:4096\n" \
  "v_mov_b64 v[120:121], 0\n" \
  "v_mfma_f32_16x16x32_bf16 v[72:75], v[92:95], a[96:99], v[72:75]\n" \
  "ds_read_b128 v[108:111], %[hA] offset:22528\n" \
  "v_mov_b64 v[122:123], 0\n" \
  "v_mfma_f32_16x16x32_bf16 v[60:63], v[88:91], a[104:107], v[60:63]\n" \
  "buffer_load_dwordx4 %[hvo], %[HS], s57 offen offset:0 sc1 lds\n" \
  "v_mov_b64 v[124:125], 0\n" \
  "v_mfma_f32_16x16x32_bf16 v[76:79], v[92:95], a[104:107], v[76:79]\n" \
  "s_mov_b32 m0, %[ml0]\n" \
  "v_mov_b64 v[126:127], 0\n" \
  "v_mfma_f32_16x16x32_bf16 v[64:67], v[88:91], a[112:115], v[64:67]\n" \
  "v_mfma_f32_16x16x32_bf16 v[80:83], v[92:95], a[112:115], v[80:83]\n" \
  "v_mfma_f32_16x16x32_bf16 v[68:71], v[88:91], a[120:123], v[68:71]\n" \
  "buffer_load_dwordx4 %[c0], %[X], s59 offen sc1 lds\n" \
  "v_mfma_f32_16x16x32_bf16 v[84:87], v[92:95], a[120:123], v[84:87]\n" \
  "s_add_i32 s59, s59, 0x1000\n" \
  "s_add_i32 m0, %[ml0], 0x1000\n" \
  "s_waitcnt lgkmcnt(2)\n" \
  "v_mfma_f32_16x16x32_bf16 v[56:59], v[96:99], a[100:103], v[56:59]\n" \
  "ds_read_b128 v[88:91], %[hA] offset:4608\n" \
  "v_mfma_f32_16x16x32_bf16 v[72:75], v[100:103], a[100:103], v[72:75]\n" \
  "ds_read_b128 v[92:95], %[hA] offset:23040\n" \
  "v_mfma_f32_16x16x32_bf16 v[60:63], v[96:99], a[108:111], v[60:63]\n" \
  "v_mfma_f32_16x16x32_bf16 v[76:79], v[100:103], a[108:111], v[76:79]\n" \
  "buffer_load_dwordx4 %[c0], %[X], s59 offen sc1 lds\n" \
  "v_mfma_f32_16x16x32_bf16 v[64:67], v[96:99], a[116:119], v[64:67]\n" \
  "s_add_i32 s59, s59, 0x3000\n" \
  "s_add_i32 m0, %[ml0], 0x2000\n" \
  "v_mfma_f32_16x16x32_bf16 v[80:83], v[100:103], a[116:119], v[80:83]\n" \
  "v_mfma_f32_16x16x32_bf16 v[68:71], v[96:99], a[124:127], v[68:71]\n" \
  "v_mfma_f32_16x16x32_bf16 v[84:87], v[100:103], a[124:127], v[84:87]\n" \
  "buffer_load_dwordx4 %[c2], %[X], s59 offen sc1 lds\n" \
  "s_waitcnt lgkmcnt(2)\n" \
  "v_mfma_f32_16x16x32_bf16 v[56:59], v[104:107], a[128:131], v[56:59]\n" \
  "ds_read_b128 v[96:99], %[hA] offset:5120\n" \
  "buffer_load_dword v248, off, %[X], s94 offset:4 sc1\n" \
  "v_mfma_f32_16x16x32_bf16 v[72:75], v[108:111], a[128:131], v[72:75]\n" \
  "ds_read_b128 v[100:103], %[hA] offset:23552\n" \
  "v_mfma_f32_16x16x32_bf16 v[60:63], v[104:107], a[136:139], v[60:63]\n" \
  "s_add_i32 s69, s56, 2\n" \
  "s_min_i32 s68, s69, 63\n" \
  "s_mul_i32 s57, s68, 36864\n" \
  "v_mfma_f32_16x16x32_bf16 v[76:79], v[108:111], a[136:139], v[76:79]\n" \
  "s_lshl_b32 s68, s69, 16\n" \
  "s_and_b32 s68, s68, 0x10000\n" \
  "s_add_i32 s58, %[hl0], s68\n" \
  "v_mfma_f32_16x16x32_bf16 v[64:67], v[104:107], a[144:147], v[64:67]\n" \
  "buffer_load_dwordx4 v[148:151], %[la], %[X], s60 offen offset:0 sc1\n" \
  "v_mfma_f32_16x16x32_bf16 v[80:83], v[108:111], a[144:147], v[80:83]\n" \
  "s_add_i32 s68, s56, -1\n" \
  "s_mul_i32 s63, s68, 36864\n" \
  "s_add_i32 s63, s63, %[hl0]\n" \
  "v_mfma_f32_16x16x32_bf16 v[68:71], v[104:107], a[152:155], v[68:71]\n" \
  "s_cmp_gt_i32 s56, 0\n" \
  "s_cselect_b32 s63, s63, 2.0\n" \
  "v_mfma_f32_16x16x32_bf16 v[84:87], v[108:111], a[152:155], v[84:87]\n" \
  "s_waitcnt lgkmcnt(2)\n" \
  "v_mfma_f32_16x16x32_bf16 v[56:59], v[88:91], a[132:135], v[56:59]\n" \
  "ds_read_b128 v[104:107], %[hA] offset:5632\n" \
  "buffer_load_dwordx4 v[152:155], %[la], %[X], s60 offen offset:1024 sc1\n" \
  "v_mfma_f32_16x16x32_bf16 v[72:75], v[92:95], a[132:135], v[72:75]\n" \
  "ds_read_b128 v[108:111], %[hA] offset:24064\n" \
  "v_mfma_f32_16x16x32_bf16 v[60:63], v[88:91], a[140:143], v[60:63]\n" \
  "v_mfma_f32_16x16x32_bf16 v[76:79], v[92:95], a[140:143], v[76:79]\n" \
  "v_mfma_f32_16x16x32_bf16 v[64:67], v[88:91], a[148:151], v[64:67]\n" \
  "v_mfma_f32_16x16x32_bf16 v[80:83], v[92:95], a[148:151], v[80:83]\n" \
  "buffer_load_dwordx4 v[156:159], %[la], %[X], s60 offen offset:2048 sc1\n" \
  "v_mfma_f32_16x16x32_bf16 v[68:71], v[88:91], a[156:159], v[68:71]\n" \
  "v_mfma_f32_16x16x32_bf16 v[84:87], v[92:95], a[156:159], v[84:87]\n" \
  "s_waitcnt lgkmcnt(2)\n" \
  "v_mfma_f32_16x16x32_bf16 v[56:59], v[96:99], a[160:163], v[56:59]\n" \
  "ds_read_b128 v[88:91], %[hA] offset:6144\n" \
  "v_mfma_f32_16x16x32_bf16 v[72:75], v[100:103], a[160:163], v[72:75]\n" \
  "ds_read_b128 v[92:95], %[hA] offset:24576\n" \
  "buffer_load_dwordx4 v[160:163], %[la], %[X], s60 offen offset:3072 sc1\n" \
  "v_mfma_f32_16x16x32_bf16 v[60:63], v[96:99], a[168:171], v[60:63]\n" \
  "v_mfma_f32_16x16x32_bf16 v[76:79], v[100:103], a[168:171], v[76:79]\n" \
  "v_mfma_f32_16x16x32_bf16 v[64:67], v[96:99], a[176:179], v[64:67]\n" \
  "v_mfma_f32_16x16x32_bf16 v[80:83], v[100:103], a[176:179], v[80:83]\n" \
  "v_mfma_f32_16x16x32_bf16 v[68:71], v[96:99], a[184:187], v[68:71]\n" \
  "buffer_load_dwordx4 v[164:167], v250, %[X], s60 offen offset:0 sc1\n" \
  "v_mfma_f32_16x16x32_bf16 v[84:87], v[100:103], a[184:187], v[84:87]\n" \
  "s_waitcnt lgkmcnt(2)\n" \
  "v_mfma_f32_16x16x32_bf16 v[56:59], v[104:107], a[164:167], v[56:59]\n" \
  "ds_read_b128 v[96:99], %[hA] offset:6656\n" \
  "v_mfma_f32_16x16x32_bf16 v[72:75], v[108:111], a[164:167], v[72:75]\n" \
  "ds_read_b128 v[100:103], %[hA] offset:25088\n" \
  "v_mfma_f32_16x16x32_bf16 v[60:63], v[104:107], a[172:175], v[60:63]\n" \
  "buffer_load_dwordx4 v[168:171], v250, %[X], s60 offen offset:1024 sc1\n" \
  "v_mfma_f32_16x16x32_bf16 v[76:79], v[108:111], a[172:175], v[76:79]\n" \
  "v_mfma_f32_16x16x32_bf16 v[64:67], v[104:107], a[180:183], v[64:67]\n" \
  "v_mfma_f32_16x16x32_bf16 v[80:83], v[108:111], a[180:183], v[80:83]\n" \
  "v_mfma_f32_16x16x32_bf16 v[68:71], v[104:107], a[188:191], v[68:71]\n" \
  "v_mfma_f32_16x16x32_bf16 v[84:87], v[108:111], a[188:191], v[84:87]\n" \
  "buffer_load_dwordx4 v[172:175], v250, %[X], s60 offen offset:2048 sc1\n" \
  "s_waitcnt lgkmcnt(2)\n" \
  "v_mfma_f32_16x16x32_bf16 v[56:59], v[88:91], a[192:195], v[56:59]\n" \
  "ds_read_b128 v[104:107], %[hA] offset:7168\n" \
  "v_mfma_f32_16x16x32_bf16 v[72:75], v[92:95], a[192:195], v[72:75]\n" \
  "ds_read_b128 v[108:111], %[hA] offset:25600\n" \
  "v_mfma_f32_16x16x32_bf16 v[60:63], v[88:91], a[200:203], v[60:63]\n" \
  "v_mfma_f32_16x16x32_bf16 v[76:79], v[92:95], a[200:203], v[76:79]\n" \
  "buffer_load_dwordx4 v[176:179], v250, %[X], s60 offen offset:3072 sc1\n" \
  "v_mfma_f32_16x16x32_bf16 v[64:67], v[88:91], a[208:211], v[64:67]\n" \
  "v_mfma_f32_16x16x32_bf16 v[80:83], v[92:95], a[208:211], v[80:83]\n" \
  "v_mfma_f32_16x16x32_bf16 v[68:71], v[88:91], a[216:219], v[68:71]\n" \
  "s_waitcnt vmcnt(9)\n" \
  "s_barrier\n" \
  "v_mfma_f32_16x16x32_bf16 v[84:87], v[92:95], a[216:219], v[84:87]\n" \
  "ds_read_b128 v[128:131], %[mA] offset:0\n" \
  "s_waitcnt lgkmcnt(3)\n" \
  "v_mfma_f32_16x16x32_bf16 v[56:59], v[96:99], a[196:199], v[56:59]\n" \
  "ds_read_b128 v[88:91], %[hA] offset:7680\n" \
  "buffer_load_dwordx4 v[180:183], %[la], %[X], s61 offen offset:0 sc1\n" \
  "ds_read_b128 v[132:135], %[mA] offset:1024\n" \
  "v_mfma_f32_16x16x32_bf16 v[72:75], v[100:103], a[196:199], v[72:75]\n" \
  "ds_read_b128 v[92:95], %[hA] offset:26112\n" \
  "v_mfma_f32_16x16x32_bf16 v[60:63], v[96:99], a[204:207], v[60:63]\n" \
  "ds_read_b128 v[136:139], %[mA] offset:2048\n" \
  "v_mfma_f32_16x16x32_bf16 v[76:79], v[100:103], a[204:207], v[76:79]\n" \
  "ds_read_b128 v[140:143], %[mA] offset:3072\n" \
  "v_mfma_f32_16x16x32_bf16 v[64:67], v[96:99], a[212:215], v[64:67]\n" \
  "buffer_load_dwordx4 v[184:187], %[la], %[X], s61 offen offset:1024 sc1\n" \
  "s_waitcnt lgkmcnt(5)\n" \
  "v_smfmac_f32_16x16x64_bf16 v[112:115], v[128:131], a[0:7], %[idx]\n" \
  "ds_read_b128 v[144:147], %[mA] offset:4096\n" \
  "v_smfmac_f32_16x16x64_bf16 v[116:119], v[128:131], a[8:15], %[idx]\n" \
  "s_waitcnt vmcnt(10)\n" \
  "v_readfirstlane_b32 s71, v248\n" \
  "v_smfmac_f32_16x16x64_bf16 v[120:123], v[128:131], a[16:23], %[idx]\n" \
  "s_waitcnt vmcnt(9)\n" \
  "v_bitop3_b32 v240, v148, v149, s62 bitop3:0x7e\n" \
  "v_smfmac_f32_16x16x64_bf16 v[124:127], v[128:131], a[24:31], %[idx]\n" \
  "s_waitcnt lgkmcnt(4)\n" \
  "v_smfmac_f32_16x16x64_bf16 v[112:115], v[132:135], a[32:39], %[idx]\n" \
  "ds_read_b128 v[128:131], %[mA] offset:5120\n" \
  "buffer_load_dwordx4 v[188:191], %[la], %[X], s61 offen offset:2048 sc1\n" \
  "v_smfmac_f32_16x16x64_bf16 v[116:119], v[132:135], a[40:47], %[idx]\n" \
  "v_bitop3_b32 v241, v150, v151, s62 bitop3:0x7e\n" \
  "v_smfmac_f32_16x16x64_bf16 v[120:123], v[132:135], a[48:55], %[idx]\n" \
  "s_waitcnt vmcnt(9)\n" \
  "v_bitop3_b32 v242, v152, v153, s62 bitop3:0x7e\n" \
  "v_smfmac_f32_16x16x64_bf16 v[124:127], v[132:135], a[56:63], %[idx]\n" \
  "s_waitcnt lgkmcnt(3)\n" \
  "v_smfmac_f32_16x16x64_bf16 v[112:115], v[136:139], a[64:71], %[idx]\n" \
  "ds_read_b128 v[132:135], %[mA] offset:6144\n" \
  "buffer_load_dwordx4 v[192:195], %[la], %[X], s61 offen offset:3072 sc1\n" \
  "v_smfmac_f32_16x16x64_bf16 v[116:119], v[136:139], a[72:79], %[idx]\n" \
  "v_or3_b32 v240, v240, v241, v242\n" \
  "v_smfmac_f32_16x16x64_bf16 v[120:123], v[136:139], a[80:87], %[idx]\n" \
  "v_bitop3_b32 v241, v154, v155, s62 bitop3:0x7e\n" \
  "v_smfmac_f32_16x16x64_bf16 v[124:127], v[136:139], a[88:95], %[idx]\n" \
  "s_waitcnt lgkmcnt(3)\n" \
  "v_smfmac_f32_16x16x64_bf16 v[112:115], v[140:143], a[96:103], %[idx]\n" \
  "ds_read_b128 v[136:139], %[mA] offset:7168\n" \
  "v_smfmac_f32_16x16x64_bf16 v[116:119], v[140:143], a[104:111], %[idx]\n" \
  "buffer_load_dwordx4 v[196:199], v250, %[X], s61 offen offset:0 sc1\n" \
  "v_smfmac_f32_16x16x64_bf16 v[120:123], v[140:143], a[112:119], %[idx]\n" \
  "s_waitcnt vmcnt(10)\n" \
  "v_bitop3_b32 v242, v156, v157, s62 bitop3:0x7e\n" \
  "v_smfmac_f32_16x16x64_bf16 v[124:127], v[140:143], a[120:127], %[idx]\n" \
  "s_waitcnt lgkmcnt(3)\n" \
  "v_smfmac_f32_16x16x64_bf16 v[112:115], v[144:147], a[128:135], %[idx]\n" \
  "ds_read_b128 v[140:143], %[mA8] offset:0\n" \
  "v_or3_b32 v240, v240, v241, v242\n" \
  "v_smfmac_f32_16x16x64_bf16 v[116:119], v[144:147], a[136:143], %[idx]\n" \
  "buffer_load_dwordx4 v[200:203], v250, %[X], s61 offen offset:1024 sc1\n" \
  "v_bitop3_b32 v241, v158, v159, s62 bitop3:0x7e\n" \
  "v_smfmac_f32_16x16x64_bf16 v[120:123], v[144:147], a[144:151], %[idx]\n" \
  "s_waitcnt vmcnt(10)\n" \
  "v_bitop3_b32 v242, v160, v161, s62 bitop3:0x7e\n" \
  "v_smfmac_f32_16x16x64_bf16 v[124:127], v[144:147], a[152:159], %[idx]\n" \
  "s_waitcnt lgkmcnt(3)\n" \
  "v_smfmac_f32_16x16x64_bf16 v[112:115], v[128:131], a[160:167], %[idx]\n" \
  "v_or3_b32 v240, v240, v241, v242\n" \
  "v_smfmac_f32_16x16x64_bf16 v[116:119], v[128:131], a[168:175], %[idx]\n" \
  "v_bitop3_b32 v241, v162, v163, s62 bitop3:0x7e\n" \
  "v_smfmac_f32_16x16x64_bf16 v[120:123], v[128:131], a[176:183], %[idx]\n" \
  "buffer_load_dwordx4 v[204:207], v250, %[X], s61 offen offset:2048 sc1\n" \
  "v_smfmac_f32_16x16x64_bf16 v[124:127], v[128:131], a[184:191], %[idx]\n" \
  "s_waitcnt lgkmcnt(2)\n" \
  "v_smfmac_f32_16x16x64_bf16 v[112:115], v[132:135], a[192:199], %[idx]\n" \
  "s_waitcnt vmcnt(9)\n" \
  "v_bitop3_b32 v242, v164, v168, s62 bitop3:0x7e\n" \
  "v_smfmac_f32_16x16x64_bf16 v[116:119], v[132:135], a[200:207], %[idx]\n" \
  "v_or3_b32 v240, v240, v241, v242\n" \
  "v_smfmac_f32_16x16x64_bf16 v[120:123], v[132:135], a[208:215], %[idx]\n" \
  "buffer_load_dwordx4 v[208:211], v250, %[X], s61 offen offset:3072 sc1\n" \
  "v_smfmac_f32_16x16x64_bf16 v[124:127], v[132:135], a[216:223], %[idx]\n" \
  "s_waitcnt lgkmcnt(1)\n" \
  "v_smfmac_f32_16x16x64_bf16 v[112:115], v[136:139], a[224:231], %[idx]\n" \
  "s_waitcnt vmcnt(8)\n" \
  "v_bitop3_b32 v241, v172, v176, s62 bitop3:0x7e\n" \
  "v_smfmac_f32_16x16x64_bf16 v[116:119], v[136:139], a[232:239], %[idx]\n" \
  "v_smfmac_f32_16x16x64_bf16 v[120:123], v[136:139], a[240:247], %[idx]\n" \
  "v_smfmac_f32_16x16x64_bf16 v[124:127], v[136:139], a[248:255], %[idx]\n" \
  "s_waitcnt lgkmcnt(0)\n" \
  "v_smfmac_f32_16x16x64_bf16 v[112:115], v[140:143], v[24:31], %[idx]\n" \
  "v_smfmac_f32_16x16x64_bf16 v[116:119], v[140:143], v[32:39], %[idx]\n" \
  "v_smfmac_f32_16x16x64_bf16 v[120:123], v[140:143], v[40:47], %[idx]\n" \
  "s_nop 1\n" \
  "v_smfmac_f32_16x16x64_bf16 v[124:127], v[140:143], v[48:55], %[idx]\n" \
  "s_waitcnt vmcnt(6)\n" \
  "v_bitop3_b32 v242, v180, v184, s62 bitop3:0x7e\n" \
  "v_mfma_f32_16x16x32_bf16 v[80:83], v[100:103], a[212:215], v[80:83]\n" \
  "v_add_f32 v112, v112, v114\n" \
  "v_add_f32 v113, v113, v115\n" \
  "v_mfma_f32_16x16x32_bf16 v[68:71], v[96:99], a[220:223], v[68:71]\n" \
  "v_cmp_u_f32 vcc, v112, v113\n" \
  "v_add_f32 v116, v116, v118\n" \
  "v_mfma_f32_16x16x32_bf16 v[84:87], v[100:103], a[220:223], v[84:87]\n" \
  "v_add_f32 v117, v117, v119\n" \
  "v_mfma_f32_16x16x32_bf16 v[56:59], v[104:107], a[224:227], v[56:59]\n" \
  "ds_read_b128 v[96:99], %[hA] offset:8192\n" \
  "v_add_f32 v120, v120, v122\n" \
  "v_mfma_f32_16x16x32_bf16 v[72:75], v[108:111], a[224:227], v[72:75]\n" \
  "ds_read_b128 v[100:103], %[hA] offset:26624\n" \
  "v_add_f32 v121, v121, v123\n" \
  "v_mfma_f32_16x16x32_bf16 v[60:63], v[104:107], a[232:235], v[60:63]\n" \
  "s_cbranch_vccnz .Lv_sm\n" \
  ".Lv_sm_back:\n" \
  "v_mfma_f32_16x16x32_bf16 v[76:79], v[108:111], a[232:235], v[76:79]\n" \
  "v_add_f32 v124, v124, v126\n" \
  "v_add_f32 v125, v125, v127\n" \
  "v_mfma_f32_16x16x32_bf16 v[64:67], v[104:107], a[240:243], v[64:67]\n" \
  "v_cvt_pk_bf16_f32 v244, v112, v113\n" \
  "v_cvt_pk_bf16_f32 v246, v120, v121\n" \
  "v_mfma_f32_16x16x32_bf16 v[80:83], v[108:111], a[240:243], v[80:83]\n" \
  "v_cvt_pk_bf16_f32 v245, v116, v117\n" \
  "v_cvt_pk_bf16_f32 v247, v124, v125\n" \
  "v_mfma_f32_16x16x32_bf16 v[68:71], v[104:107], a[248:251], v[68:71]\n" \
  "v_permlane32_swap_b32 v244, v246\n" \
  "v_mfma_f32_16x16x32_bf16 v[84:87], v[108:111], a[248:251], v[84:87]\n" \
  "v_or3_b32 v240, v240, v241, v242\n" \
  "s_waitcnt vmcnt(5)\n" \
  "v_mfma_f32_16x16x32_bf16 v[112:115], v[236:239], v[148:151], 0\n" \
  "s_memrealtime s[74:75]\n" \
  "v_permlane32_swap_b32 v245, v247\n" \
  "v_mfma_f32_16x16x32_bf16 v[116:119], v[236:239], v[152:155], 0\n" \
  "v_mfma_f32_16x16x32_bf16 v[120:123], v[236:239], v[156:159], 0\n" \
  "v_permlane16_swap_b32 v244, v245\n" \
  "v_mfma_f32_16x16x32_bf16 v[124:127], v[236:239], v[160:163], 0\n" \
  "v_permlane16_swap_b32 v246, v247\n" \
  "v_mfma_f32_16x16x32_bf16 v[120:123], v[212:215], v[164:167], v[120:123]\n" \
  "s_waitcnt lgkmcnt(0)\n" \
  "s_sub_i32 s91, s74, s71\n" \
  "s_cmp_gt_u32 s91, 14\n" \
  "v_mfma_f32_16x16x32_bf16 v[124:127], v[212:215], v[168:171], v[124:127]\n" \
  "s_cbranch_scc1 .Lv_late\n" \
  ".Lv_al_ok:\n" \
  "v_mfma_f32_16x16x32_bf16 v[120:123], v[216:219], v[172:175], v[120:123]\n" \
  "buffer_load_dwordx4 v[128:131], %[la], %[X], s65 offen offset:0 sc1\n" \
  "s_sub_i32 s90, s74, s89\n" \
  "v_mfma_f32_16x16x32_bf16 v[124:127], v[216:219], v[176:179], v[124:127]\n" \
  "s_max_i32 s92, s91, -2\n" \
  "s_add_i32 s89, s71, s92\n" \
  "s_ashr_i32 s92, s92, 1\n" \
  "v_mfma_f32_16x16x32_bf16 v[120:123], v[220:223], v[180:183], v[120:123]\n" \
  "s_add_i32 s90, s90, s92\n" \
  "s_mul_i32 s93, s56, 130\n" \
  "s_and_b32 s93, s93, 0xfc\n" \
  "v_mfma_f32_16x16x32_bf16 v[124:127], v[220:223], v[184:187], v[124:127]\n" \
  "buffer_load_dwordx4 v[132:135], %[la], %[X], s65 offen offset:1024 sc1\n" \
  "v_mov_b32 v249, s90\n" \
  "v_mfma_f32_16x16x32_bf16 v[120:123], v[224:227], v[188:191], v[120:123]\n" \
  "s_waitcnt vmcnt(6)\n" \
  "v_mfma_f32_16x16x32_bf16 v[124:127], v[224:227], v[192:195], v[124:127]\n" \
  "v_bitop3_b32 v241, v188, v192, s62 bitop3:0x7e\n" \
  "s_waitcnt vmcnt(5)\n" \
  "v_mfma_f32_16x16x32_bf16 v[112:115], v[228:231], v[196:199], v[112:115]\n" \
  "buffer_load_dwordx4 v[136:139], %[la], %[X], s65 offen offset:2048 sc1\n" \
  "s_waitcnt vmcnt(5)\n" \
  "v_mfma_f32_16x16x32_bf16 v[116:119], v[228:231], v[200:203], v[116:119]\n" \
  "v_bitop3_b32 v242, v196, v198, s62 bitop3:0x7e\n" \
  "s_waitcnt vmcnt(4)\n" \
  "v_mfma_f32_16x16x32_bf16 v[112:115], v[232:235], v[204:207], v[112:115]\n" \
  "v_or3_b32 v240, v240, v241, v242\n" \
  "s_waitcnt vmcnt(3)\n" \
  "v_mfma_f32_16x16x32_bf16 v[116:119], v[232:235], v[208:211], v[116:119]\n" \
  "buffer_load_dwordx4 v[140:143], %[la], %[X], s65 offen offset:3072 sc1\n" \
  "v_mfma_f32_16x16x32_bf16 v[56:59], v[88:91], a[228:231], v[56:59]\n" \
  "ds_read_b128 v[104:107], %[hA] offset:8704\n" \
  "v_bitop3_b32 v241, v200, v202, s62 bitop3:0x7e\n" \
  "v_mfma_f32_16x16x32_bf16 v[72:75], v[92:95], a[228:231], v[72:75]\n" \
  "ds_read_b128 v[108:111], %[hA] offset:27136\n" \
  "v_mfma_f32_16x16x32_bf16 v[56:59], v[96:99], v[24:27], v[56:59]\n" \
  "buffer_load_dwordx4 v[144:147], v250, %[X], s65 offen offset:0 sc1\n" \
  "v_bitop3_b32 v242, v204, v206, s62 bitop3:0x7e\n" \
  "v_mfma_f32_16x16x32_bf16 v[72:75], v[100:103], v[24:27], v[72:75]\n" \
  "v_or3_b32 v240, v240, v241, v242\n" \
  "v_bitop3_b32 v240, v240, v208, s62 bitop3:0xf6\n" \
  "v_mfma_f32_16x16x32_bf16 v[60:63], v[88:91], a[236:239], v[60:63]\n" \
  "v_bitop3_b32 v240, v240, v210, s62 bitop3:0xf6\n" \
  "v_cmp_gt_i32 vcc, 0, v240\n" \
  "v_mfma_f32_16x16x32_bf16 v[76:79], v[92:95], a[236:239], v[76:79]\n" \
  "v_cndmask_b32 v120, v124, v120, s[72:73]\n" \
  "v_cndmask_b32 v121, v125, v121, s[72:73]\n" \
  "v_mfma_f32_16x16x32_bf16 v[60:63], v[96:99], v[32:35], v[60:63]\n" \
  "v_cndmask_b32 v122, v126, v122, s[72:73]\n" \
  "v_cndmask_b32 v123, v127, v123, s[72:73]\n" \
  "v_mfma_f32_16x16x32_bf16 v[76:79], v[100:103], v[32:35], v[76:79]\n" \
  "v_xor_b32 %[hA], 0x10000, %[hA]\n" \
  "s_waitcnt lgkmcnt(0)\n" \
  "v_mfma_f32_16x16x32_bf16 v[56:59], v[104:107], v[28:31], v[56:59]\n" \
  "s_cbranch_vccnz .Lv_sp\n" \
  ".Lv_sp_back:\n" \
  "v_mfma_f32_16x16x32_bf16 v[72:75], v[108:111], v[28:31], v[72:75]\n" \
  "ds_write_b128 %[rtW], v[120:123] offset:1024\n" \
  "v_mfma_f32_16x16x32_bf16 v[60:63], v[104:107], v[36:39], v[60:63]\n" \
  "v_cndmask_b32 v112, v116, v112, s[72:73]\n" \
  "v_cndmask_b32 v113, v117, v113, s[72:73]\n" \
  "v_mfma_f32_16x16x32_bf16 v[76:79], v[108:111], v[36:39], v[76:79]\n" \
  "v_cndmask_b32 v114, v118, v114, s[72:73]\n" \
  "v_cndmask_b32 v115, v119, v115, s[72:73]\n" \
  "v_mfma_f32_16x16x32_bf16 v[64:67], v[88:91], a[244:247], v[64:67]\n" \
  "buffer_atomic_umax v249, %[rpo], %[X], s93 offen sc1\n" \
  "buffer_store_dword v251, %[lm], %[X], s64 offen sc1\n" \
  "ds_read_b128 v[196:199], %[hA] offset:0\n" \
  "v_mfma_f32_16x16x32_bf16 v[80:83], v[92:95], a[244:247], v[80:83]\n" \
  "ds_write_b128 %[rtW], v[112:115] offset:0\n" \
  "v_mfma_f32_16x16x32_bf16 v[64:67], v[96:99], v[40:43], v[64:67]\n" \
  "ds_read_b128 v[200:203], %[hA] offset:18432\n" \
  "v_cvt_pk_bf16_f32 v212, v56, v57\n" \
  "v_mfma_f32_16x16x32_bf16 v[80:83], v[100:103], v[40:43], v[80:83]\n" \
  "ds_read_b128 v[204:207], %[hA] offset:512\n" \
  "v_cvt_pk_bf16_f32 v213, v58, v59\n" \
  "v_mfma_f32_16x16x32_bf16 v[64:67], v[104:107], v[44:47], v[64:67]\n" \
  "ds_read_b128 v[208:211], %[hA] offset:18944\n" \
  "v_cvt_pk_bf16_f32 v214, v72, v73\n" \
  "v_mfma_f32_16x16x32_bf16 v[80:83], v[108:111], v[44:47], v[80:83]\n" \
  "v_cvt_pk_bf16_f32 v215, v74, v75\n" \
  "v_cvt_pk_bf16_f32 v216, v60, v61\n" \
  "v_mfma_f32_16x16x32_bf16 v[68:71], v[88:91], a[252:255], v[68:71]\n" \
  "v_cvt_pk_bf16_f32 v217, v62, v63\n" \
  "v_cvt_pk_bf16_f32 v218, v76, v77\n" \
  "v_mfma_f32_16x16x32_bf16 v[84:87], v[92:95], a[252:255], v[84:87]\n" \
  "v_cvt_pk_bf16_f32 v219, v78, v79\n" \
  "v_mfma_f32_16x16x32_bf16 v[68:71], v[96:99], v[48:51], v[68:71]\n" \
  "v_mfma_f32_16x16x32_bf16 v[84:87], v[100:103], v[48:51], v[84:87]\n" \
  "v_cvt_pk_bf16_f32 v220, v64, v65\n" \
  "v_cvt_pk_bf16_f32 v221, v66, v67\n" \
  "v_mfma_f32_16x16x32_bf16 v[68:71], v[104:107], v[52:55], v[68:71]\n" \
  "v_cvt_pk_bf16_f32 v222, v80, v81\n" \
  "v_cvt_pk_bf16_f32 v223, v82, v83\n" \
  "v_mfma_f32_16x16x32_bf16 v[84:87], v[108:111], v[52:55], v[84:87]\n" \
  "s_waitcnt vmcnt(6)\n" \
  "v_mfma_f32_16x16x32_bf16 v[240:243], v[212:215], v[128:131], 0\n" \
  "s_waitcnt lgkmcnt(0)\n" \
  "s_nop 0\n" \
  "s_waitcnt vmcnt(5)\n" \
  "v_mfma_f32_16x16x32_bf16 v[240:243], v[216:219], v[132:135], v[240:243]\n" \
  "v_bitop3_b32 v251, v128, v132, s66 bitop3:0x7e\n" \
  "s_waitcnt vmcnt(4)\n" \
  "v_mfma_f32_16x16x32_bf16 v[240:243], v[220:223], v[136:139], v[240:243]\n" \
  "v_bitop3_b32 v251, v251, v136, s66 bitop3:0xf6\n" \
  "v_cvt_pk_bf16_f32 v224, v68, v69\n" \
  "v_cvt_pk_bf16_f32 v225, v70, v71\n" \
  "v_cvt_pk_bf16_f32 v226, v84, v85\n" \
  "v_cvt_pk_bf16_f32 v227, v86, v87\n" \
  "s_waitcnt vmcnt(3)\n" \
  "v_bitop3_b32 v251, v251, v140, s66 bitop3:0xf6\n" \
  "s_nop 0\n" \
  "v_mfma_f32_16x16x32_bf16 v[240:243], v[224:227], v[140:143], v[240:243]\n" \
  "s_waitcnt vmcnt(2)\n" \
  "v_mfma_f32_16x16x32_bf16 v[240:243], v[244:247], v[144:147], v[240:243]\n" \
  "v_bitop3_b32 v252, v144, v145, s66 bitop3:0x7e\n" \
  "v_bitop3_b32 v253, v146, v147, s66 bitop3:0x7e\n" \
  "v_or3_b32 v251, v251, v252, v253\n" \
  "v_ashrrev_i32 v252, 31, v251\n" \
  "v_mfma_f32_16x16x32_bf16 v[56:59], v[196:199], a[0:3], 0\n" \
  "s_nop 2\n" \
  "ds_write_b128 %[rmW], v[240:243]\n" \
  "v_cmp_u_f32 vcc, v240, v252\n" \
  "v_mfma_f32_16x16x32_bf16 v[72:75], v[200:203], a[0:3], 0\n" \
  "v_mfma_f32_16x16x32_bf16 v[60:63], v[196:199], a[8:11], 0\n" \
  "s_cbranch_vccnz .Lv_sq\n" \
  ".Lv_sq_back:\n" \
  "s_waitcnt lgkmcnt(0)\n" \
  "s_barrier\n" \
  "ds_read2st64_b64 v[136:139], %[rmA] offset0:0 offset1:2\n" \
  "ds_read2st64_b64 v[140:143], %[rmA] offset0:4 offset1:6\n" \
  "v_mfma_f32_16x16x32_bf16 v[76:79], v[200:203], a[8:11], 0\n" \
  "v_mfma_f32_16x16x32_bf16 v[64:67], v[196:199], a[16:19], 0\n" \
  "v_mfma_f32_16x16x32_bf16 v[80:83], v[200:203], a[16:19], 0\n" \
  "v_mfma_f32_16x16x32_bf16 v[68:71], v[196:199], a[24:27], 0\n" \
  "v_mfma_f32_16x16x32_bf16 v[84:87], v[200:203], a[24:27], 0\n" \
  "s_cmp_lg_u32 s56, s81\n" \
  "s_waitcnt lgkmcnt(0)\n" \
  "v_pk_add_f32 v[136:137], v[136:137], v[138:139]\n" \
  "v_pk_add_f32 v[140:141], v[140:141], v[142:143]\n" \
  "v_pk_add_f32 v[136:137], v[136:137], v[140:141]\n" \
  "v_mfma_f32_16x16x32_bf16 v[56:59], v[204:207], a[4:7], v[56:59]\n" \
  "s_nop 0\n" \
  "v_add_f32_dpp v136, v136, v136 quad_perm:[1,0,3,2] row_mask:0xf bank_mask:0xf\n" \
  "v_add_f32_dpp v137, v137, v137 quad_perm:[1,0,3,2] row_mask:0xf bank_mask:0xf\n" \
  "v_cvt_pk_bf16_f32 v136, v136, v137\n" \
  "v_xor_b32 v136, s66, v136\n" \
  "v_min_u32 v136, -2, v136\n" \
  "buffer_store_dword v136, %[lm], %[X], s67 offen sc1\n" \
  "v_mfma_f32_16x16x32_bf16 v[72:75], v[208:211], a[4:7], v[72:75]\n" \
  "s_cbranch_scc1 .Lv_top\n" \
  "s_cmp_eq_u32 s56, -1\n" \
  "s_cbranch_scc1 .Lv_entry\n" \
  "ds_read2st64_b64 v[128:131], %[rtA] offset0:0 offset1:4\n" \
  "ds_read2st64_b64 v[132:135], %[rtA] offset0:8 offset1:12\n" \
  "s_waitcnt lgkmcnt(0)\n" \
  "v_pk_add_f32 v[128:129], v[128:129], v[130:131]\n" \
  "v_pk_add_f32 v[132:133], v[132:133], v[134:135]\n" \
  "v_pk_add_f32 v[128:129], v[128:129], v[132:133]\n" \
  "s_nop 1\n" \
  "v_add_f32_dpp v128, v128, v128 quad_perm:[1,0,3,2] row_mask:0xf bank_mask:0xf\n" \
  "v_add_f32_dpp v129, v129, v129 quad_perm:[1,0,3,2] row_mask:0xf bank_mask:0xf\n" \
  "v_cvt_pk_bf16_f32 v128, v128, v129\n" \
  "v_xor_b32 v128, s62, v128\n" \
  "v_min_u32 v128, -2, v128\n" \
  "buffer_store_dword v128, %[lo], %[OS], s63 offen sc1\n" \
  "v_mov_b64 v[228:229], v[212:213]\n" \
  "v_mov_b64 v[230:231], v[216:217]\n" \
  "v_mov_b64 v[232:233], v[220:221]\n" \
  "v_mov_b64 v[234:235], v[224:225]\n" \
  "v_mov_b64 v[236:237], v[244:245]\n" \
  "v_mov_b64 v[238:239], v[246:247]\n" \
  "s_mov_b32 s62, s66\n" \
  "s_add_i32 s60, %[pt0], 0x40000\n" \
  "s_add_i32 s61, s60, 0x4000\n" \
  "s_add_i32 s63, %[hl0], 2322432\n" \
  "s_mov_b32 s70, 0\n" \
  ".Lv_fin:\n" \
  "buffer_load_dwordx4 v[148:151], %[la], %[X], s60 offen offset:0 sc1\n" \
  "buffer_load_dwordx4 v[152:155], %[la], %[X], s60 offen offset:1024 sc1\n" \
  "buffer_load_dwordx4 v[156:159], %[la], %[X], s60 offen offset:2048 sc1\n" \
  "buffer_load_dwordx4 v[160:163], %[la], %[X], s60 offen offset:3072 sc1\n" \
  "buffer_load_dwordx4 v[164:167], v250, %[X], s60 offen offset:0 sc1\n" \
  "buffer_load_dwordx4 v[168:171], v250, %[X], s60 offen offset:1024 sc1\n" \
  "buffer_load_dwordx4 v[172:175], v250, %[X], s60 offen offset:2048 sc1\n" \
  "buffer_load_dwordx4 v[176:179], v250, %[X], s60 offen offset:3072 sc1\n" \
  "buffer_load_dwordx4 v[180:183], %[la], %[X], s61 offen offset:0 sc1\n" \
  "buffer_load_dwordx4 v[184:187], %[la], %[X], s61 offen offset:1024 sc1\n" \
  "buffer_load_dwordx4 v[188:191], %[la], %[X], s61 offen offset:2048 sc1\n" \
  "buffer_load_dwordx4 v[192:195], %[la], %[X], s61 offen offset:3072 sc1\n" \
  "buffer_load_dwordx4 v[196:199], v250, %[X], s61 offen offset:0 sc1\n" \
  "buffer_load_dwordx4 v[200:203], v250, %[X], s61 offen offset:1024 sc1\n" \
  "buffer_load_dwordx4 v[204:207], v250, %[X], s61 offen offset:2048 sc1\n" \
  "buffer_load_dwordx4 v[208:211], v250, %[X], s61 offen offset:3072 sc1\n" \
  "s_waitcnt vmcnt(0)\n" \
  "v_bitop3_b32 v240, v148, v149, s62 bitop3:0x7e\n" \
  "v_bitop3_b32 v241, v150, v151, s62 bitop3:0x7e\n" \
  "v_bitop3_b32 v242, v152, v153, s62 bitop3:0x7e\n" \
  "v_or3_b32 v240, v240, v241, v242\n" \
  "v_bitop3_b32 v241, v154, v155, s62 bitop3:0x7e\n" \
  "v_bitop3_b32 v242, v156, v157, s62 bitop3:0x7e\n" \
  "v_or3_b32 v240, v240, v241, v242\n" \
  "v_bitop3_b32 v241, v158, v159, s62 bitop3:0x7e\n" \
  "v_bitop3_b32 v242, v160, v161, s62 bitop3:0x7e\n" \
  "v_or3_b32 v240, v240, v241, v242\n" \
  "v_bitop3_b32 v241, v162, v163, s62 bitop3:0x7e\n" \
  "v_bitop3_b32 v242, v164, v168, s62 bitop3:0x7e\n" \
  "v_or3_b32 v240, v240, v241, v242\n" \
  "v_bitop3_b32 v241, v172, v176, s62 bitop3:0x7e\n" \
  "v_bitop3_b32 v242, v180, v184, s62 bitop3:0x7e\n" \
  "v_or3_b32 v240, v240, v241, v242\n" \
  "v_bitop3_b32 v241, v188, v192, s62 bitop3:0x7e\n" \
  "v_bitop3_b32 v242, v196, v198, s62 bitop3:0x7e\n" \
  "v_or3_b32 v240, v240, v241, v242\n" \
  "v_bitop3_b32 v241, v200, v202, s62 bitop3:0x7e\n" \
  "v_bitop3_b32 v242, v204, v206, s62 bitop3:0x7e\n" \
  "v_or3_b32 v240, v240, v241, v242\n" \
  "v_bitop3_b32 v240, v240, v208, s62 bitop3:0xf6\n" \
  "v_bitop3_b32 v240, v240, v210, s62 bitop3:0xf6\n" \
  "v_cmp_gt_i32 vcc, 0, v240\n" \
  "s_cbranch_vccz .Lv_fin_ok\n" \
  "s_add_i32 s70, s70, 1\n" \
  "s_and_b32 s68, s70, 1023\n" \
  "s_cbranch_scc1 .Lv_fin\n" \
  "buffer_load_dword v253, off, %[F], 0 offset:1024 sc1\n" \
  "s_waitcnt vmcnt(0)\n" \
  "v_readfirstlane_b32 s68, v253\n" \
  "s_cmp_lg_u32 s68, 0\n" \
  "s_cbranch_scc1 .Lv_fin_ok\n" \
  "s_cmp_lt_u32 s70, 400000\n" \
  "s_cbranch_scc1 .Lv_fin\n" \
  "v_mov_b32 v253, 1\n" \
  "buffer_store_dword v253, off, %[F], 0 offset:1024 sc1\n" \
  ".Lv_fin_ok:\n" \
  "v_mfma_f32_16x16x32_bf16 v[112:115], v[236:239], v[148:151], 0\n" \
  "v_mfma_f32_16x16x32_bf16 v[116:119], v[236:239], v[152:155], 0\n" \
  "v_mfma_f32_16x16x32_bf16 v[120:123], v[236:239], v[156:159], 0\n" \
  "v_mfma_f32_16x16x32_bf16 v[124:127], v[236:239], v[160:163], 0\n" \
  "v_mfma_f32_16x16x32_bf16 v[120:123], v[212:215], v[164:167], v[120:123]\n" \
  "v_mfma_f32_16x16x32_bf16 v[124:127], v[212:215], v[168:171], v[124:127]\n" \
  "v_mfma_f32_16x16x32_bf16 v[120:123], v[216:219], v[172:175], v[120:123]\n" \
  "v_mfma_f32_16x16x32_bf16 v[124:127], v[216:219], v[176:179], v[124:127]\n" \
  "v_mfma_f32_16x16x32_bf16 v[120:123], v[220:223], v[180:183], v[120:123]\n" \
  "v_mfma_f32_16x16x32_bf16 v[124:127], v[220:223], v[184:187], v[124:127]\n" \
  "v_mfma_f32_16x16x32_bf16 v[120:123], v[224:227], v[188:191], v[120:123]\n" \
  "v_mfma_f32_16x16x32_bf16 v[124:127], v[224:227], v[192:195], v[124:127]\n" \
  "v_mfma_f32_16x16x32_bf16 v[112:115], v[228:231], v[196:199], v[112:115]\n" \
  "v_mfma_f32_16x16x32_bf16 v[116:119], v[228:231], v[200:203], v[116:119]\n" \
  "v_mfma_f32_16x16x32_bf16 v[112:115], v[232:235], v[204:207], v[112:115]\n" \
  "v_mfma_f32_16x16x32_bf16 v[116:119], v[232:235], v[208:211], v[116:119]\n" \
  "s_nop 7\n" \
  "v_cndmask_b32 v120, v124, v120, s[72:73]\n" \
  "v_cndmask_b32 v121, v125, v121, s[72:73]\n" \
  "v_cndmask_b32 v122, v126, v122, s[72:73]\n" \
  "v_cndmask_b32 v123, v127, v123, s[72:73]\n" \
  "v_cndmask_b32 v112, v116, v112, s[72:73]\n" \
  "v_cndmask_b32 v113, v117, v113, s[72:73]\n" \
  "v_cndmask_b32 v114, v118, v114, s[72:73]\n" \
  "v_cndmask_b32 v115, v119, v115, s[72:73]\n" \
  "s_barrier\n" \
  "ds_write_b128 %[rtW], v[120:123] offset:1024\n" \
  "ds_write_b128 %[rtW], v[112:115] offset:0\n" \
  "s_waitcnt lgkmcnt(0)\n" \
  "s_barrier\n" \
  "ds_read2st64_b64 v[128:131], %[rtA] offset0:0 offset1:4\n" \
  "ds_read2st64_b64 v[132:135], %[rtA] offset0:8 offset1:12\n" \
  "s_waitcnt lgkmcnt(0)\n" \
  "v_pk_add_f32 v[128:129], v[128:129], v[130:131]\n" \
  "v_pk_add_f32 v[132:133], v[132:133], v[134:135]\n" \
  "v_pk_add_f32 v[128:129], v[128:129], v[132:133]\n" \
  "s_nop 1\n" \
  "v_add_f32_dpp v128, v128, v128 quad_perm:[1,0,3,2] row_mask:0xf bank_mask:0xf\n" \
  "v_add_f32_dpp v129, v129, v129 quad_perm:[1,0,3,2] row_mask:0xf bank_mask:0xf\n" \
  "v_cvt_pk_bf16_f32 v128, v128, v129\n" \
  "v_xor_b32 v128, s62, v128\n" \
  "v_min_u32 v128, -2, v128\n" \
  "buffer_store_dword v128, %[lo], %[OS], s63 offen sc1\n" \
  "s_branch .Lv_end\n" \
  ".Lv_sp:\n" \
  "s_cmp_lt_i32 s56, 1\n" \
  "s_cbranch_scc1 .Lv_sp_back\n" \
  "s_nop 7\n" \
  "s_mov_b32 s70, 0\n" \
  ".Lv_spw:\n" \
  "buffer_load_dwordx4 v[148:151], %[la], %[X], s60 offen offset:0 sc1\n" \
  "buffer_load_dwordx4 v[152:155], %[la], %[X], s60 offen offset:1024 sc1\n" \
  "buffer_load_dwordx4 v[156:159], %[la], %[X], s60 offen offset:2048 sc1\n" \
  "buffer_load_dwordx4 v[160:163], %[la], %[X], s60 offen offset:3072 sc1\n" \
  "buffer_load_dwordx4 v[164:167], v250, %[X], s60 offen offset:0 sc1\n" \
  "buffer_load_dwordx4 v[168:171], v250, %[X], s60 offen offset:1024 sc1\n" \
  "buffer_load_dwordx4 v[172:175], v250, %[X], s60 offen offset:2048 sc1\n" \
  "buffer_load_dwordx4 v[176:179], v250, %[X], s60 offen offset:3072 sc1\n" \
  "buffer_load_dwordx4 v[180:183], %[la], %[X], s61 offen offset:0 sc1\n" \
  "buffer_load_dwordx4 v[184:187], %[la], %[X], s61 offen offset:1024 sc1\n" \
  "buffer_load_dwordx4 v[188:191], %[la], %[X], s61 offen offset:2048 sc1\n" \
  "buffer_load_dwordx4 v[192:195], %[la], %[X], s61 offen offset:3072 sc1\n" \
  "buffer_load_dwordx4 v[196:199], v250, %[X], s61 offen offset:0 sc1\n" \
  "buffer_load_dwordx4 v[200:203], v250, %[X], s61 offen offset:1024 sc1\n" \
  "buffer_load_dwordx4 v[204:207], v250, %[X], s61 offen offset:2048 sc1\n" \
  "buffer_load_dwordx4 v[208:211], v250, %[X], s61 offen offset:3072 sc1\n" \
  "s_waitcnt vmcnt(0)\n" \
  "v_bitop3_b32 v240, v148, v149, s62 bitop3:0x7e\n" \
  "v_bitop3_b32 v241, v150, v151, s62 bitop3:0x7e\n" \
  "v_bitop3_b32 v242, v152, v153, s62 bitop3:0x7e\n" \
  "v_or3_b32 v240, v240, v241, v242\n" \
  "v_bitop3_b32 v241, v154, v155, s62 bitop3:0x7e\n" \
  "v_bitop3_b32 v242, v156, v157, s62 bitop3:0x7e\n" \
  "v_or3_b32 v240, v240, v241, v242\n" \
  "v_bitop3_b32 v241, v158, v159, s62 bitop3:0x7e\n" \
  "v_bitop3_b32 v242, v160, v161, s62 bitop3:0x7e\n" \
  "v_or3_b32 v240, v240, v241, v242\n" \
  "v_bitop3_b32 v241, v162, v163, s62 bitop3:0x7e\n" \
  "v_bitop3_b32 v242, v164, v168, s62 bitop3:0x7e\n" \
  "v_or3_b32 v240, v240, v241, v242\n" \
  "v_bitop3_b32 v241, v172, v176, s62 bitop3:0x7e\n" \
  "v_bitop3_b32 v242, v180, v184, s62 bitop3:0x7e\n" \
  "v_or3_b32 v240, v240, v241, v242\n" \
  "v_bitop3_b32 v241, v188, v192, s62 bitop3:0x7e\n" \
  "v_bitop3_b32 v242, v196, v198, s62 bitop3:0x7e\n" \
  "v_or3_b32 v240, v240, v241, v242\n" \
  "v_bitop3_b32 v241, v200, v202, s62 bitop3:0x7e\n" \
  "v_bitop3_b32 v242, v204, v206, s62 bitop3:0x7e\n" \
  "v_or3_b32 v240, v240, v241, v242\n" \
  "v_bitop3_b32 v240, v240, v208, s62 bitop3:0xf6\n" \
  "v_bitop3_b32 v240, v240, v210, s62 bitop3:0xf6\n" \
  "v_cmp_gt_i32 vcc, 0, v240\n" \
  "s_cbranch_vccz .Lv_spw_ok\n" \
  "s_add_i32 s70, s70, 1\n" \
  "s_and_b32 s68, s70, 1023\n" \
  "s_cbranch_scc1 .Lv_spw\n" \
  "buffer_load_dword v253, off, %[F], 0 offset:1024 sc1\n" \
  "s_waitcnt vmcnt(0)\n" \
  "v_readfirstlane_b32 s68, v253\n" \
  "s_cmp_lg_u32 s68, 0\n" \
  "s_cbranch_scc1 .Lv_spw_ok\n" \
  "s_cmp_lt_u32 s70, 400000\n" \
  "s_cbranch_scc1 .Lv_spw\n" \
  "v_mov_b32 v253, 1\n" \
  "buffer_store_dword v253, off, %[F], 0 offset:1024 sc1\n" \
  ".Lv_spw_ok:\n" \
  "v_mfma_f32_16x16x32_bf16 v[112:115], v[236:239], v[148:151], 0\n" \
  "v_mfma_f32_16x16x32_bf16 v[116:119], v[236:239], v[152:155], 0\n" \
  "v_mfma_f32_16x16x32_bf16 v[120:123], v[236:239], v[156:159], 0\n" \
  "v_mfma_f32_16x16x32_bf16 v[124:127], v[236:239], v[160:163], 0\n" \
  "v_mfma_f32_16x16x32_bf16 v[120:123], v[212:215], v[164:167], v[120:123]\n" \
  "v_mfma_f32_16x16x32_bf16 v[124:127], v[212:215], v[168:171], v[124:127]\n" \
  "v_mfma_f32_16x16x32_bf16 v[120:123], v[216:219], v[172:175], v[120:123]\n" \
  "v_mfma_f32_16x16x32_bf16 v[124:127], v[216:219], v[176:179], v[124:127]\n" \
  "v_mfma_f32_16x16x32_bf16 v[120:123], v[220:223], v[180:183], v[120:123]\n" \
  "v_mfma_f32_16x16x32_bf16 v[124:127], v[220:223], v[184:187], v[124:127]\n" \
  "v_mfma_f32_16x16x32_bf16 v[120:123], v[224:227], v[188:191], v[120:123]\n" \
  "v_mfma_f32_16x16x32_bf16 v[124:127], v[224:227], v[192:195], v[124:127]\n" \
  "v_mfma_f32_16x16x32_bf16 v[112:115], v[228:231], v[196:199], v[112:115]\n" \
  "v_mfma_f32_16x16x32_bf16 v[116:119], v[228:231], v[200:203], v[116:119]\n" \
  "v_mfma_f32_16x16x32_bf16 v[112:115], v[232:235], v[204:207], v[112:115]\n" \
  "v_mfma_f32_16x16x32_bf16 v[116:119], v[232:235], v[208:211], v[116:119]\n" \
  "s_nop 7\n" \
  "v_cndmask_b32 v120, v124, v120, s[72:73]\n" \
  "v_cndmask_b32 v121, v125, v121, s[72:73]\n" \
  "v_cndmask_b32 v122, v126, v122, s[72:73]\n" \
  "v_cndmask_b32 v123, v127, v123, s[72:73]\n" \
  "s_branch .Lv_sp_back\n" \
  ".Lv_sq:\n" \
  "s_cmp_lt_i32 s56, 0\n" \
  "s_cbranch_scc1 .Lv_sq_back\n" \
  "v_cmp_gt_i32 vcc, 0, v251\n" \
  "s_cbranch_vccz .Lv_sv\n" \
  "s_mov_b32 s70, 0\n" \
  ".Lv_sqw:\n" \
  "buffer_load_dwordx4 v[128:131], %[la], %[X], s65 offen offset:0 sc1\n" \
  "buffer_load_dwordx4 v[132:135], %[la], %[X], s65 offen offset:1024 sc1\n" \
  "buffer_load_dwordx4 v[136:139], %[la], %[X], s65 offen offset:2048 sc1\n" \
  "buffer_load_dwordx4 v[140:143], %[la], %[X], s65 offen offset:3072 sc1\n" \
  "buffer_load_dwordx4 v[144:147], v250, %[X], s65 offen offset:0 sc1\n" \
  "s_waitcnt vmcnt(4)\n" \
  "v_mfma_f32_16x16x32_bf16 v[240:243], v[212:215], v[128:131], 0\n" \
  "s_waitcnt vmcnt(3)\n" \
  "v_mfma_f32_16x16x32_bf16 v[240:243], v[216:219], v[132:135], v[240:243]\n" \
  "v_bitop3_b32 v251, v128, v132, s66 bitop3:0x7e\n" \
  "s_waitcnt vmcnt(2)\n" \
  "v_mfma_f32_16x16x32_bf16 v[240:243], v[220:223], v[136:139], v[240:243]\n" \
  "v_bitop3_b32 v251, v251, v136, s66 bitop3:0xf6\n" \
  "s_waitcnt vmcnt(1)\n" \
  "v_mfma_f32_16x16x32_bf16 v[240:243], v[224:227], v[140:143], v[240:243]\n" \
  "v_bitop3_b32 v251, v251, v140, s66 bitop3:0xf6\n" \
  "s_waitcnt vmcnt(0)\n" \
  "v_mfma_f32_16x16x32_bf16 v[240:243], v[244:247], v[144:147], v[240:243]\n" \
  "v_bitop3_b32 v252, v144, v145, s66 bitop3:0x7e\n" \
  "v_bitop3_b32 v253, v146, v147, s66 bitop3:0x7e\n" \
  "v_or3_b32 v251, v251, v252, v253\n" \
  "v_cmp_gt_i32 vcc, 0, v251\n" \
  "s_cbranch_vccz .Lv_sqw_ok\n" \
  "s_add_i32 s70, s70, 1\n" \
  "s_and_b32 s68, s70, 1023\n" \
  "s_cbranch_scc1 .Lv_sqw\n" \
  "buffer_load_dword v253, off, %[F], 0 offset:1024 sc1\n" \
  "s_waitcnt vmcnt(0)\n" \
  "v_readfirstlane_b32 s68, v253\n" \
  "s_cmp_lg_u32 s68, 0\n" \
  "s_cbranch_scc1 .Lv_sqw_ok\n" \
  "s_cmp_lt_u32 s70, 400000\n" \
  "s_cbranch_scc1 .Lv_sqw\n" \
  "v_mov_b32 v253, 1\n" \
  "buffer_store_dword v253, off, %[F], 0 offset:1024 sc1\n" \
  ".Lv_sqw_ok:\n" \
  "s_nop 2\n" \
  "ds_write_b128 %[rmW], v[240:243]\n" \
  "v_cmp_u_f32 vcc, v240, v240\n" \
  "s_waitcnt lgkmcnt(0)\n" \
  "s_cbranch_vccz .Lv_sq_back\n" \
  ".Lv_sv:\n" \
  "v_and_b32 v251, 0xffff, %[hA]\n" \
  "s_mul_i32 s76, s56, 36864\n" \
  "s_add_i32 s77, s76, 18432\n" \
  "s_mov_b32 s70, 0\n" \
  ".Lv_shw0:\n" \
  "buffer_load_dwordx4 v[148:151], v251, %[HS], s76 offen offset:0 sc1\n" \
  "buffer_load_dwordx4 v[152:155], v251, %[HS], s77 offen offset:0 sc1\n" \
  "buffer_load_dwordx4 v[156:159], v251, %[HS], s76 offen offset:512 sc1\n" \
  "buffer_load_dwordx4 v[160:163], v251, %[HS], s77 offen offset:512 sc1\n" \
  "buffer_load_dwordx4 v[164:167], v251, %[HS], s76 offen offset:1024 sc1\n" \
  "buffer_load_dwordx4 v[168:171], v251, %[HS], s77 offen offset:1024 sc1\n" \
  "buffer_load_dwordx4 v[172:175], v251, %[HS], s76 offen offset:1536 sc1\n" \
  "buffer_load_dwordx4 v[176:179], v251, %[HS], s77 offen offset:1536 sc1\n" \
  "buffer_load_dwordx4 v[180:183], v251, %[HS], s76 offen offset:2048 sc1\n" \
  "buffer_load_dwordx4 v[184:187], v251, %[HS], s77 offen offset:2048 sc1\n" \
  "buffer_load_dwordx4 v[188:191], v251, %[HS], s76 offen offset:2560 sc1\n" \
  "buffer_load_dwordx4 v[192:195], v251, %[HS], s77 offen offset:2560 sc1\n" \
  "s_waitcnt vmcnt(0)\n" \
  "v_max3_u32 v253, v148, v149, v150\n" \
  "v_max3_u32 v253, v253, v151, v152\n" \
  "v_max3_u32 v253, v253, v153, v154\n" \
  "v_max3_u32 v253, v253, v155, v156\n" \
  "v_max3_u32 v253, v253, v157, v158\n" \
  "v_max3_u32 v253, v253, v159, v160\n" \
  "v_max3_u32 v253, v253, v161, v162\n" \
  "v_max3_u32 v253, v253, v163, v164\n" \
  "v_max3_u32 v253, v253, v165, v166\n" \
  "v_max3_u32 v253, v253, v167, v168\n" \
  "v_max3_u32 v253, v253, v169, v170\n" \
  "v_max3_u32 v253, v253, v171, v172\n" \
  "v_max3_u32 v253, v253, v173, v174\n" \
  "v_max3_u32 v253, v253, v175, v176\n" \
  "v_max3_u32 v253, v253, v177, v178\n" \
  "v_max3_u32 v253, v253, v179, v180\n" \
  "v_max3_u32 v253, v253, v181, v182\n" \
  "v_max3_u32 v253, v253, v183, v184\n" \
  "v_max3_u32 v253, v253, v185, v186\n" \
  "v_max3_u32 v253, v253, v187, v188\n" \
  "v_max3_u32 v253, v253, v189, v190\n" \
  "v_max3_u32 v253, v253, v191, v192\n" \
  "v_max3_u32 v253, v253, v193, v194\n" \
  "v_max_u32 v253, v253, v195\n" \
  "v_cmp_eq_u32 vcc, -1, v253\n" \
  "s_cbranch_vccz .Lv_shw0_ok\n" \
  "s_sleep 4\n" \
  "s_add_i32 s70, s70, 1\n" \
  "s_and_b32 s68, s70, 1023\n" \
  "s_cbranch_scc1 .Lv_shw0\n" \
  "buffer_load_dword v253, off, %[F], 0 offset:1024 sc1\n" \
  "s_waitcnt vmcnt(0)\n" \
  "v_readfirstlane_b32 s68, v253\n" \
  "s_cmp_lg_u32 s68, 0\n" \
  "s_cbranch_scc1 .Lv_shw0_ok\n" \
  "s_cmp_lt_u32 s70, 400000\n" \
  "s_cbranch_scc1 .Lv_shw0\n" \
  "v_mov_b32 v253, 1\n" \
  "buffer_store_dword v253, off, %[F], 0 offset:1024 sc1\n" \
  ".Lv_shw0_ok:\n" \
  "v_mfma_f32_16x16x32_bf16 v[56:59], v[148:151], a[0:3], 0\n" \
  "v_mfma_f32_16x16x32_bf16 v[60:63], v[148:151], a[8:11], 0\n" \
  "v_mfma_f32_16x16x32_bf16 v[64:67], v[148:151], a[16:19], 0\n" \
  "v_mfma_f32_16x16x32_bf16 v[68:71], v[148:151], a[24:27], 0\n" \
  "v_mfma_f32_16x16x32_bf16 v[72:75], v[152:155], a[0:3], 0\n" \
  "v_mfma_f32_16x16x32_bf16 v[76:79], v[152:155], a[8:11], 0\n" \
  "v_mfma_f32_16x16x32_bf16 v[80:83], v[152:155], a[16:19], 0\n" \
  "v_mfma_f32_16x16x32_bf16 v[84:87], v[152:155], a[24:27], 0\n" \
  "v_mfma_f32_16x16x32_bf16 v[56:59], v[156:159], a[4:7], v[56:59]\n" \
  "v_mfma_f32_16x16x32_bf16 v[60:63], v[156:159], a[12:15], v[60:63]\n" \
  "v_mfma_f32_16x16x32_bf16 v[64:67], v[156:159], a[20:23], v[64:67]\n" \
  "v_mfma_f32_16x16x32_bf16 v[68:71], v[156:159], a[28:31], v[68:71]\n" \
  "v_mfma_f32_16x16x32_bf16 v[72:75], v[160:163], a[4:7], v[72:75]\n" \
  "v_mfma_f32_16x16x32_bf16 v[76:79], v[160:163], a[12:15], v[76:79]\n" \
  "v_mfma_f32_16x16x32_bf16 v[80:83], v[160:163], a[20:23], v[80:83]\n" \
  "v_mfma_f32_16x16x32_bf16 v[84:87], v[160:163], a[28:31], v[84:87]\n" \
  "v_mfma_f32_16x16x32_bf16 v[56:59], v[164:167], a[32:35], v[56:59]\n" \
  "v_mfma_f32_16x16x32_bf16 v[60:63], v[164:167], a[40:43], v[60:63]\n" \
  "v_mfma_f32_16x16x32_bf16 v[64:67], v[164:167], a[48:51], v[64:67]\n" \
  "v_mfma_f32_16x16x32_bf16 v[68:71], v[164:167], a[56:59], v[68:71]\n" \
  "v_mfma_f32_16x16x32_bf16 v[72:75], v[168:171], a[32:35], v[72:75]\n" \
  "v_mfma_f32_16x16x32_bf16 v[76:79], v[168:171], a[40:43], v[76:79]\n" \
  "v_mfma_f32_16x16x32_bf16 v[80:83], v[168:171], a[48:51], v[80:83]\n" \
  "v_mfma_f32_16x16x32_bf16 v[84:87], v[168:171], a[56:59], v[84:87]\n" \
  "v_mfma_f32_16x16x32_bf16 v[56:59], v[172:175], a[36:39], v[56:59]\n" \
  "v_mfma_f32_16x16x32_bf16 v[60:63], v[172:175], a[44:47], v[60:63]\n" \
  "v_mfma_f32_16x16x32_bf16 v[64:67], v[172:175], a[52:55], v[64:67]\n" \
  "v_mfma_f32_16x16x32_bf16 v[68:71], v[172:175], a[60:63], v[68:71]\n" \
  "v_mfma_f32_16x16x32_bf16 v[72:75], v[176:179], a[36:39], v[72:75]\n" \
  "v_mfma_f32_16x16x32_bf16 v[76:79], v[176:179], a[44:47], v[76:79]\n" \
  "v_mfma_f32_16x16x32_bf16 v[80:83], v[176:179], a[52:55], v[80:83]\n" \
  "v_mfma_f32_16x16x32_bf16 v[84:87], v[176:179], a[60:63], v[84:87]\n" \
  "v_mfma_f32_16x16x32_bf16 v[56:59], v[180:183], a[64:67], v[56:59]\n" \
  "v_mfma_f32_16x16x32_bf16 v[60:63], v[180:183], a[72:75], v[60:63]\n" \
  "v_mfma_f32_16x16x32_bf16 v[64:67], v[180:183], a[80:83], v[64:67]\n" \
  "v_mfma_f32_16x16x32_bf16 v[68:71], v[180:183], a[88:91], v[68:71]\n" \
  "v_mfma_f32_16x16x32_bf16 v[72:75], v[184:187], a[64:67], v[72:75]\n" \
  "v_mfma_f32_16x16x32_bf16 v[76:79], v[184:187], a[72:75], v[76:79]\n" \
  "v_mfma_f32_16x16x32_bf16 v[80:83], v[184:187], a[80:83], v[80:83]\n" \
  "v_mfma_f32_16x16x32_bf16 v[84:87], v[184:187], a[88:91], v[84:87]\n" \
  "v_mfma_f32_16x16x32_bf16 v[56:59], v[188:191], a[68:71], v[56:59]\n" \
  "v_mfma_f32_16x16x32_bf16 v[60:63], v[188:191], a[76:79], v[60:63]\n" \
  "v_mfma_f32_16x16x32_bf16 v[64:67], v[188:191], a[84:87], v[64:67]\n" \
  "v_mfma_f32_16x16x32_bf16 v[68:71], v[188:191], a[92:95], v[68:71]\n" \
  "v_mfma_f32_16x16x32_bf16 v[72:75], v[192:195], a[68:71], v[72:75]\n" \
  "v_mfma_f32_16x16x32_bf16 v[76:79], v[192:195], a[76:79], v[76:79]\n" \
  "v_mfma_f32_16x16x32_bf16 v[80:83], v[192:195], a[84:87], v[80:83]\n" \
  "v_mfma_f32_16x16x32_bf16 v[84:87], v[192:195], a[92:95], v[84:87]\n" \
  "s_add_i32 s76, s76, 3072\n" \
  "s_add_i32 s77, s77, 3072\n" \
  "s_mov_b32 s70, 0\n" \
  ".Lv_shw1:\n" \
  "buffer_load_dwordx4 v[148:151], v251, %[HS], s76 offen offset:0 sc1\n" \
  "buffer_load_dwordx4 v[152:155], v251, %[HS], s77 offen offset:0 sc1\n" \
  "buffer_load_dwordx4 v[156:159], v251, %[HS], s76 offen offset:512 sc1\n" \
  "buffer_load_dwordx4 v[160:163], v251, %[HS], s77 offen offset:512 sc1\n" \
  "buffer_load_dwordx4 v[164:167], v251, %[HS], s76 offen offset:1024 sc1\n" \
  "buffer_load_dwordx4 v[168:171], v251, %[HS], s77 offen offset:1024 sc1\n" \
  "buffer_load_dwordx4 v[172:175], v251, %[HS], s76 offen offset:1536 sc1\n" \
  "buffer_load_dwordx4 v[176:179], v251, %[HS], s77 offen offset:1536 sc1\n" \
  "buffer_load_dwordx4 v[180:183], v251, %[HS], s76 offen offset:2048 sc1\n" \
  "buffer_load_dwordx4 v[184:187], v251, %[HS], s77 offen offset:2048 sc1\n" \
  "buffer_load_dwordx4 v[188:191], v251, %[HS], s76 offen offset:2560 sc1\n" \
  "buffer_load_dwordx4 v[192:195], v251, %[HS], s77 offen offset:2560 sc1\n" \
  "s_waitcnt vmcnt(0)\n" \
  "v_max3_u32 v253, v148, v149, v150\n" \
  "v_max3_u32 v253, v253, v151, v152\n" \
  "v_max3_u32 v253, v253, v153, v154\n" \
  "v_max3_u32 v253, v253, v155, v156\n" \
  "v_max3_u32 v253, v253, v157, v158\n" \
  "v_max3_u32 v253, v253, v159, v160\n" \
  "v_max3_u32 v253, v253, v161, v162\n" \
  "v_max3_u32 v253, v253, v163, v164\n" \
  "v_max3_u32 v253, v253, v165, v166\n" \
  "v_max3_u32 v253, v253, v167, v168\n" \
  "v_max3_u32 v253, v253, v169, v170\n" \
  "v_max3_u32 v253, v253, v171, v172\n" \
  "v_max3_u32 v253, v253, v173, v174\n" \
  "v_max3_u32 v253, v253, v175, v176\n" \
  "v_max3_u32 v253, v253, v177, v178\n" \
  "v_max3_u32 v253, v253, v179, v180\n" \
  "v_max3_u32 v253, v253, v181, v182\n" \
  "v_max3_u32 v253, v253, v183, v184\n" \
  "v_max3_u32 v253, v253, v185, v186\n" \
  "v_max3_u32 v253, v253, v187, v188\n" \
  "v_max3_u32 v253, v253, v189, v190\n" \
  "v_max3_u32 v253, v253, v191, v192\n" \
  "v_max3_u32 v253, v253, v193, v194\n" \
  "v_max_u32 v253, v253, v195\n" \
  "v_cmp_eq_u32 vcc, -1, v253\n" \
  "s_cbranch_vccz .Lv_shw1_ok\n" \
  "s_sleep 4\n" \
  "s_add_i32 s70, s70, 1\n" \
  "s_and_b32 s68, s70, 1023\n" \
  "s_cbranch_scc1 .Lv_shw1\n" \
  "buffer_load_dword v253, off, %[F], 0 offset:1024 sc1\n" \
  "s_waitcnt vmcnt(0)\n" \
  "v_readfirstlane_b32 s68, v253\n" \
  "s_cmp_lg_u32 s68, 0\n" \
  "s_cbranch_scc1 .Lv_shw1_ok\n" \
  "s_cmp_lt_u32 s70, 400000\n" \
  "s_cbranch_scc1 .Lv_shw1\n" \
  "v_mov_b32 v253, 1\n" \
  "buffer_store_dword v253, off, %[F], 0 offset:1024 sc1\n" \
  ".Lv_shw1_ok:\n" \
  "v_mfma_f32_16x16x32_bf16 v[56:59], v[148:151], a[96:99], v[56:59]\n" \
  "v_mfma_f32_16x16x32_bf16 v[60:63], v[148:151], a[104:107], v[60:63]\n" \
  "v_mfma_f32_16x16x32_bf16 v[64:67], v[148:151], a[112:115], v[64:67]\n" \
  "v_mfma_f32_16x16x32_bf16 v[68:71], v[148:151], a[120:123], v[68:71]\n" \
  "v_mfma_f32_16x16x32_bf16 v[72:75], v[152:155], a[96:99], v[72:75]\n" \
  "v_mfma_f32_16x16x32_bf16 v[76:79], v[152:155], a[104:107], v[76:79]\n" \
  "v_mfma_f32_16x16x32_bf16 v[80:83], v[152:155], a[112:115], v[80:83]\n" \
  "v_mfma_f32_16x16x32_bf16 v[84:87], v[152:155], a[120:123], v[84:87]\n" \
  "v_mfma_f32_16x16x32_bf16 v[56:59], v[156:159], a[100:103], v[56:59]\n" \
  "v_mfma_f32_16x16x32_bf16 v[60:63], v[156:159], a[108:111], v[60:63]\n" \
  "v_mfma_f32_16x16x32_bf16 v[64:67], v[156:159], a[116:119], v[64:67]\n" \
  "v_mfma_f32_16x16x32_bf16 v[68:71], v[156:159], a[124:127], v[68:71]\n" \
  "v_mfma_f32_16x16x32_bf16 v[72:75], v[160:163], a[100:103], v[72:75]\n" \
  "v_mfma_f32_16x16x32_bf16 v[76:79], v[160:163], a[108:111], v[76:79]\n" \
  "v_mfma_f32_16x16x32_bf16 v[80:83], v[160:163], a[116:119], v[80:83]\n" \
  "v_mfma_f32_16x16x32_bf16 v[84:87], v[160:163], a[124:127], v[84:87]\n" \
  "v_mfma_f32_16x16x32_bf16 v[56:59], v[164:167], a[128:131], v[56:59]\n" \
  "v_mfma_f32_16x16x32_bf16 v[60:63], v[164:167], a[136:139], v[60:63]\n" \
  "v_mfma_f32_16x16x32_bf16 v[64:67], v[164:167], a[144:147], v[64:67]\n" \
  "v_mfma_f32_16x16x32_bf16 v[68:71], v[164:167], a[152:155], v[68:71]\n" \
  "v_mfma_f32_16x16x32_bf16 v[72:75], v[168:171], a[128:131], v[72:75]\n" \
  "v_mfma_f32_16x16x32_bf16 v[76:79], v[168:171], a[136:139], v[76:79]\n" \
  "v_mfma_f32_16x16x32_bf16 v[80:83], v[168:171], a[144:147], v[80:83]\n" \
  "v_mfma_f32_16x16x32_bf16 v[84:87], v[168:171], a[152:155], v[84:87]\n" \
  "v_mfma_f32_16x16x32_bf16 v[56:59], v[172:175], a[132:135], v[56:59]\n" \
  "v_mfma_f32_16x16x32_bf16 v[60:63], v[172:175], a[140:143], v[60:63]\n" \
  "v_mfma_f32_16x16x32_bf16 v[64:67], v[172:175], a[148:151], v[64:67]\n" \
  "v_mfma_f32_16x16x32_bf16 v[68:71], v[172:175], a[156:159], v[68:71]\n" \
  "v_mfma_f32_16x16x32_bf16 v[72:75], v[176:179], a[132:135], v[72:75]\n" \
  "v_mfma_f32_16x16x32_bf16 v[76:79], v[176:179], a[140:143], v[76:79]\n" \
  "v_mfma_f32_16x16x32_bf16 v[80:83], v[176:179], a[148:151], v[80:83]\n" \
  "v_mfma_f32_16x16x32_bf16 v[84:87], v[176:179], a[156:159], v[84:87]\n" \
  "v_mfma_f32_16x16x32_bf16 v[56:59], v[180:183], a[160:163], v[56:59]\n" \
  "v_mfma_f32_16x16x32_bf16 v[60:63], v[180:183], a[168:171], v[60:63]\n" \
  "v_mfma_f32_16x16x32_bf16 v[64:67], v[180:183], a[176:179], v[64:67]\n" \
  "v_mfma_f32_16x16x32_bf16 v[68:71], v[180:183], a[184:187], v[68:71]\n" \
  "v_mfma_f32_16x16x32_bf16 v[72:75], v[184:187], a[160:163], v[72:75]\n" \
  "v_mfma_f32_16x16x32_bf16 v[76:79], v[184:187], a[168:171], v[76:79]\n" \
  "v_mfma_f32_16x16x32_bf16 v[80:83], v[184:187], a[176:179], v[80:83]\n" \
  "v_mfma_f32_16x16x32_bf16 v[84:87], v[184:187], a[184:187], v[84:87]\n" \
  "v_mfma_f32_16x16x32_bf16 v[56:59], v[188:191], a[164:167], v[56:59]\n" \
  "v_mfma_f32_16x16x32_bf16 v[60:63], v[188:191], a[172:175], v[60:63]\n" \
  "v_mfma_f32_16x16x32_bf16 v[64:67], v[188:191], a[180:183], v[64:67]\n" \
  "v_mfma_f32_16x16x32_bf16 v[68:71], v[188:191], a[188:191], v[68:71]\n" \
  "v_mfma_f32_16x16x32_bf16 v[72:75], v[192:195], a[164:167], v[72:75]\n" \
  "v_mfma_f32_16x16x32_bf16 v[76:79], v[192:195], a[172:175], v[76:79]\n" \
  "v_mfma_f32_16x16x32_bf16 v[80:83], v[192:195], a[180:183], v[80:83]\n" \
  "v_mfma_f32_16x16x32_bf16 v[84:87], v[192:195], a[188:191], v[84:87]\n" \
  "s_add_i32 s76, s76, 3072\n" \
  "s_add_i32 s77, s77, 3072\n" \
  "s_mov_b32 s70, 0\n" \
  ".Lv_shw2:\n" \
  "buffer_load_dwordx4 v[148:151], v251, %[HS], s76 offen offset:0 sc1\n" \
  "buffer_load_dwordx4 v[152:155], v251, %[HS], s77 offen offset:0 sc1\n" \
  "buffer_load_dwordx4 v[156:159], v251, %[HS], s76 offen offset:512 sc1\n" \
  "buffer_load_dwordx4 v[160:163], v251, %[HS], s77 offen offset:512 sc1\n" \
  "buffer_load_dwordx4 v[164:167], v251, %[HS], s76 offen offset:1024 sc1\n" \
  "buffer_load_dwordx4 v[168:171], v251, %[HS], s77 offen offset:1024 sc1\n" \
  "buffer_load_dwordx4 v[172:175], v251, %[HS], s76 offen offset:1536 sc1\n" \
  "buffer_load_dwordx4 v[176:179], v251, %[HS], s77 offen offset:1536 sc1\n" \
  "buffer_load_dwordx4 v[180:183], v251, %[HS], s76 offen offset:2048 sc1\n" \
  "buffer_load_dwordx4 v[184:187], v251, %[HS], s77 offen offset:2048 sc1\n" \
  "buffer_load_dwordx4 v[188:191], v251, %[HS], s76 offen offset:2560 sc1\n" \
  "buffer_load_dwordx4 v[192:195], v251, %[HS], s77 offen offset:2560 sc1\n" \
  "s_waitcnt vmcnt(0)\n" \
  "v_max3_u32 v253, v148, v149, v150\n" \
  "v_max3_u32 v253, v253, v151, v152\n" \
  "v_max3_u32 v253, v253, v153, v154\n" \
  "v_max3_u32 v253, v253, v155, v156\n" \
  "v_max3_u32 v253, v253, v157, v158\n" \
  "v_max3_u32 v253, v253, v159, v160\n" \
  "v_max3_u32 v253, v253, v161, v162\n" \
  "v_max3_u32 v253, v253, v163, v164\n" \
  "v_max3_u32 v253, v253, v165, v166\n" \
  "v_max3_u32 v253, v253, v167, v168\n" \
  "v_max3_u32 v253, v253, v169, v170\n" \
  "v_max3_u32 v253, v253, v171, v172\n" \
  "v_max3_u32 v253, v253, v173, v174\n" \
  "v_max3_u32 v253, v253, v175, v176\n" \
  "v_max3_u32 v253, v253, v177, v178\n" \
  "v_max3_u32 v253, v253, v179, v180\n" \
  "v_max3_u32 v253, v253, v181, v182\n" \
  "v_max3_u32 v253, v253, v183, v184\n" \
  "v_max3_u32 v253, v253, v185, v186\n" \
  "v_max3_u32 v253, v253, v187, v188\n" \
  "v_max3_u32 v253, v253, v189, v190\n" \
  "v_max3_u32 v253, v253, v191, v192\n" \
  "v_max3_u32 v253, v253, v193, v194\n" \
  "v_max_u32 v253, v253, v195\n" \
  "v_cmp_eq_u32 vcc, -1, v253\n" \
  "s_cbranch_vccz .Lv_shw2_ok\n" \
  "s_sleep 4\n" \
  "s_add_i32 s70, s70, 1\n" \
  "s_and_b32 s68, s70, 1023\n" \
  "s_cbranch_scc1 .Lv_shw2\n" \
  "buffer_load_dword v253, off, %[F], 0 offset:1024 sc1\n" \
  "s_waitcnt vmcnt(0)\n" \
  "v_readfirstlane_b32 s68, v253\n" \
  "s_cmp_lg_u32 s68, 0\n" \
  "s_cbranch_scc1 .Lv_shw2_ok\n" \
  "s_cmp_lt_u32 s70, 400000\n" \
  "s_cbranch_scc1 .Lv_shw2\n" \
  "v_mov_b32 v253, 1\n" \
  "buffer_store_dword v253, off, %[F], 0 offset:1024 sc1\n" \
  ".Lv_shw2_ok:\n" \
  "v_mfma_f32_16x16x32_bf16 v[56:59], v[148:151], a[192:195], v[56:59]\n" \
  "v_mfma_f32_16x16x32_bf16 v[60:63], v[148:151], a[200:203], v[60:63]\n" \
  "v_mfma_f32_16x16x32_bf16 v[64:67], v[148:151], a[208:211], v[64:67]\n" \
  "v_mfma_f32_16x16x32_bf16 v[68:71], v[148:151], a[216:219], v[68:71]\n" \
  "v_mfma_f32_16x16x32_bf16 v[72:75], v[152:155], a[192:195], v[72:75]\n" \
  "v_mfma_f32_16x16x32_bf16 v[76:79], v[152:155], a[200:203], v[76:79]\n" \
  "v_mfma_f32_16x16x32_bf16 v[80:83], v[152:155], a[208:211], v[80:83]\n" \
  "v_mfma_f32_16x16x32_bf16 v[84:87], v[152:155], a[216:219], v[84:87]\n" \
  "v_mfma_f32_16x16x32_bf16 v[56:59], v[156:159], a[196:199], v[56:59]\n" \
  "v_mfma_f32_16x16x32_bf16 v[60:63], v[156:159], a[204:207], v[60:63]\n" \
  "v_mfma_f32_16x16x32_bf16 v[64:67], v[156:159], a[212:215], v[64:67]\n" \
  "v_mfma_f32_16x16x32_bf16 v[68:71], v[156:159], a[220:223], v[68:71]\n" \
  "v_mfma_f32_16x16x32_bf16 v[72:75], v[160:163], a[196:199], v[72:75]\n" \
  "v_mfma_f32_16x16x32_bf16 v[76:79], v[160:163], a[204:207], v[76:79]\n" \
  "v_mfma_f32_16x16x32_bf16 v[80:83], v[160:163], a[212:215], v[80:83]\n" \
  "v_mfma_f32_16x16x32_bf16 v[84:87], v[160:163], a[220:223], v[84:87]\n" \
  "v_mfma_f32_16x16x32_bf16 v[56:59], v[164:167], a[224:227], v[56:59]\n" \
  "v_mfma_f32_16x16x32_bf16 v[60:63], v[164:167], a[232:235], v[60:63]\n" \
  "v_mfma_f32_16x16x32_bf16 v[64:67], v[164:167], a[240:243], v[64:67]\n" \
  "v_mfma_f32_16x16x32_bf16 v[68:71], v[164:167], a[248:251], v[68:71]\n" \
  "v_mfma_f32_16x16x32_bf16 v[72:75], v[168:171], a[224:227], v[72:75]\n" \
  "v_mfma_f32_16x16x32_bf16 v[76:79], v[168:171], a[232:235], v[76:79]\n" \
  "v_mfma_f32_16x16x32_bf16 v[80:83], v[168:171], a[240:243], v[80:83]\n" \
  "v_mfma_f32_16x16x32_bf16 v[84:87], v[168:171], a[248:251], v[84:87]\n" \
  "v_mfma_f32_16x16x32_bf16 v[56:59], v[172:175], a[228:231], v[56:59]\n" \
  "v_mfma_f32_16x16x32_bf16 v[60:63], v[172:175], a[236:239], v[60:63]\n" \
  "v_mfma_f32_16x16x32_bf16 v[64:67], v[172:175], a[244:247], v[64:67]\n" \
  "v_mfma_f32_16x16x32_bf16 v[68:71], v[172:175], a[252:255], v[68:71]\n" \
  "v_mfma_f32_16x16x32_bf16 v[72:75], v[176:179], a[228:231], v[72:75]\n" \
  "v_mfma_f32_16x16x32_bf16 v[76:79], v[176:179], a[236:239], v[76:79]\n" \
  "v_mfma_f32_16x16x32_bf16 v[80:83], v[176:179], a[244:247], v[80:83]\n" \
  "v_mfma_f32_16x16x32_bf16 v[84:87], v[176:179], a[252:255], v[84:87]\n" \
  "v_mfma_f32_16x16x32_bf16 v[56:59], v[180:183], v[24:27], v[56:59]\n" \
  "v_mfma_f32_16x16x32_bf16 v[60:63], v[180:183], v[32:35], v[60:63]\n" \
  "v_mfma_f32_16x16x32_bf16 v[64:67], v[180:183], v[40:43], v[64:67]\n" \
  "v_mfma_f32_16x16x32_bf16 v[68:71], v[180:183], v[48:51], v[68:71]\n" \
  "v_mfma_f32_16x16x32_bf16 v[72:75], v[184:187], v[24:27], v[72:75]\n" \
  "v_mfma_f32_16x16x32_bf16 v[76:79], v[184:187], v[32:35], v[76:79]\n" \
  "v_mfma_f32_16x16x32_bf16 v[80:83], v[184:187], v[40:43], v[80:83]\n" \
  "v_mfma_f32_16x16x32_bf16 v[84:87], v[184:187], v[48:51], v[84:87]\n" \
  "v_mfma_f32_16x16x32_bf16 v[56:59], v[188:191], v[28:31], v[56:59]\n" \
  "v_mfma_f32_16x16x32_bf16 v[60:63], v[188:191], v[36:39], v[60:63]\n" \
  "v_mfma_f32_16x16x32_bf16 v[64:67], v[188:191], v[44:47], v[64:67]\n" \
  "v_mfma_f32_16x16x32_bf16 v[68:71], v[188:191], v[52:55], v[68:71]\n" \
  "v_mfma_f32_16x16x32_bf16 v[72:75], v[192:195], v[28:31], v[72:75]\n" \
  "v_mfma_f32_16x16x32_bf16 v[76:79], v[192:195], v[36:39], v[76:79]\n" \
  "v_mfma_f32_16x16x32_bf16 v[80:83], v[192:195], v[44:47], v[80:83]\n" \
  "v_mfma_f32_16x16x32_bf16 v[84:87], v[192:195], v[52:55], v[84:87]\n" \
  "s_add_i32 s76, s76, 3072\n" \
  "s_add_i32 s77, s77, 3072\n" \
  "s_nop 7\n" \
  "v_cvt_pk_bf16_f32 v212, v56, v57\n" \
  "v_cvt_pk_bf16_f32 v213, v58, v59\n" \
  "v_cvt_pk_bf16_f32 v214, v72, v73\n" \
  "v_cvt_pk_bf16_f32 v215, v74, v75\n" \
  "v_cvt_pk_bf16_f32 v216, v60, v61\n" \
  "v_cvt_pk_bf16_f32 v217, v62, v63\n" \
  "v_cvt_pk_bf16_f32 v218, v76, v77\n" \
  "v_cvt_pk_bf16_f32 v219, v78, v79\n" \
  "v_cvt_pk_bf16_f32 v220, v64, v65\n" \
  "v_cvt_pk_bf16_f32 v221, v66, v67\n" \
  "v_cvt_pk_bf16_f32 v222, v80, v81\n" \
  "v_cvt_pk_bf16_f32 v223, v82, v83\n" \
  "v_cvt_pk_bf16_f32 v224, v68, v69\n" \
  "v_cvt_pk_bf16_f32 v225, v70, v71\n" \
  "v_cvt_pk_bf16_f32 v226, v84, v85\n" \
  "v_cvt_pk_bf16_f32 v227, v86, v87\n" \
  "s_nop 1\n" \
  "v_mfma_f32_16x16x32_bf16 v[240:243], v[212:215], v[128:131], 0\n" \
  "v_mfma_f32_16x16x32_bf16 v[240:243], v[216:219], v[132:135], v[240:243]\n" \
  "v_mfma_f32_16x16x32_bf16 v[240:243], v[220:223], v[136:139], v[240:243]\n" \
  "v_mfma_f32_16x16x32_bf16 v[240:243], v[224:227], v[140:143], v[240:243]\n" \
  "v_mfma_f32_16x16x32_bf16 v[240:243], v[244:247], v[144:147], v[240:243]\n" \
  "v_mfma_f32_16x16x32_bf16 v[56:59], v[196:199], a[0:3], 0\n" \
  "v_mfma_f32_16x16x32_bf16 v[72:75], v[200:203], a[0:3], 0\n" \
  "v_mfma_f32_16x16x32_bf16 v[60:63], v[196:199], a[8:11], 0\n" \
  "s_nop 7\n" \
  "ds_write_b128 %[rmW], v[240:243]\n" \
  "s_waitcnt lgkmcnt(0)\n" \
  "s_branch .Lv_sq_back\n" \
  ".Lv_late:\n" \
  "s_cmp_lt_i32 s56, 0\n" \
  "s_cbranch_scc1 .Lv_al_ok\n" \
  "s_cmp_gt_u32 s91, -3000\n" \
  "s_cbranch_scc0 .Lv_late2\n" \
  ".Lv_wt:\n" \
  "s_memrealtime s[76:77]\n" \
  "s_waitcnt lgkmcnt(0)\n" \
  "s_sub_i32 s76, s76, s71\n" \
  "s_cmp_lt_i32 s76, 0\n" \
  "s_cbranch_scc1 .Lv_wt\n" \
  "s_branch .Lv_al_ok\n" \
  ".Lv_late2:\n" \
  "s_cmp_lg_u32 s71, 0\n" \
  "s_cbranch_scc1 .Lv_late3\n" \
  "s_mov_b32 s70, 0\n" \
  ".Lv_tw:\n" \
  "buffer_load_dword v252, off, %[X], s94 offset:4 sc1\n" \
  "s_waitcnt vmcnt(0)\n" \
  "v_readfirstlane_b32 s71, v252\n" \
  "s_cmp_lg_u32 s71, 0\n" \
  "s_cbranch_scc1 .Lv_tw_ok\n" \
  "s_sleep 1\n" \
  "s_add_i32 s70, s70, 1\n" \
  "s_cmp_lt_u32 s70, 200\n" \
  "s_cbranch_scc1 .Lv_tw\n" \
  ".Lv_tw_ok:\n" \
  "s_sub_i32 s91, s74, s71\n" \
  "s_cmp_gt_u32 s91, -3000\n" \
  "s_cbranch_scc1 .Lv_wt\n" \
  ".Lv_late3:\n" \
  "s_branch .Lv_al_ok\n" \
  ".Lv_sm:\n" \
  "s_cmp_lt_i32 s56, 0\n" \
  "s_cbranch_scc1 .Lv_sm_back\n" \
  "s_add_i32 s76, s59, -0x4000\n" \
  "v_mov_b64 v[112:113], 0\n" \
  "v_mov_b64 v[114:115], 0\n" \
  "v_mov_b64 v[116:117], 0\n" \
  "v_mov_b64 v[118:119], 0\n" \
  "v_mov_b64 v[120:121], 0\n" \
  "v_mov_b64 v[122:123], 0\n" \
  "v_mov_b64 v[124:125], 0\n" \
  "v_mov_b64 v[126:127], 0\n" \
  "s_mov_b32 s70, 0\n" \
  ".Lv_smw0:\n" \
  "buffer_load_dwordx4 v[128:131], %[la], %[X], s76 offen offset:0 sc1\n" \
  "buffer_load_dwordx4 v[132:135], %[la], %[X], s76 offen offset:1024 sc1\n" \
  "buffer_load_dwordx4 v[136:139], %[la], %[X], s76 offen offset:2048 sc1\n" \
  "buffer_load_dwordx4 v[140:143], %[la], %[X], s76 offen offset:3072 sc1\n" \
  "buffer_load_dwordx4 v[144:147], v250, %[X], s76 offen offset:0 sc1\n" \
  "s_waitcnt vmcnt(0)\n" \
  "v_max3_u32 v253, v128, v129, v130\n" \
  "v_max3_u32 v253, v253, v131, v132\n" \
  "v_max3_u32 v253, v253, v133, v134\n" \
  "v_max3_u32 v253, v253, v135, v136\n" \
  "v_max3_u32 v253, v253, v137, v138\n" \
  "v_max3_u32 v253, v253, v139, v140\n" \
  "v_max3_u32 v253, v253, v141, v142\n" \
  "v_max3_u32 v253, v253, v143, v144\n" \
  "v_max3_u32 v253, v253, v145, v146\n" \
  "v_max_u32 v253, v253, v147\n" \
  "v_cmp_eq_u32 vcc, -1, v253\n" \
  "s_cbranch_vccz .Lv_smw0_ok\n" \
  "s_add_i32 s70, s70, 1\n" \
  "s_and_b32 s68, s70, 1023\n" \
  "s_cbranch_scc1 .Lv_smw0\n" \
  "buffer_load_dword v253, off, %[F], 0 offset:1024 sc1\n" \
  "s_waitcnt vmcnt(0)\n" \
  "v_readfirstlane_b32 s68, v253\n" \
  "s_cmp_lg_u32 s68, 0\n" \
  "s_cbranch_scc1 .Lv_smw0_ok\n" \
  "s_cmp_lt_u32 s70, 400000\n" \
  "s_cbranch_scc1 .Lv_smw0\n" \
  "v_mov_b32 v253, 1\n" \
  "buffer_store_dword v253, off, %[F], 0 offset:1024 sc1\n" \
  ".Lv_smw0_ok:\n" \
  "v_smfmac_f32_16x16x64_bf16 v[112:115], v[128:131], a[0:7], %[idx]\n" \
  "v_smfmac_f32_16x16x64_bf16 v[116:119], v[128:131], a[8:15], %[idx]\n" \
  "v_smfmac_f32_16x16x64_bf16 v[120:123], v[128:131], a[16:23], %[idx]\n" \
  "v_smfmac_f32_16x16x64_bf16 v[124:127], v[128:131], a[24:31], %[idx]\n" \
  "v_smfmac_f32_16x16x64_bf16 v[112:115], v[132:135], a[32:39], %[idx]\n" \
  "v_smfmac_f32_16x16x64_bf16 v[116:119], v[132:135], a[40:47], %[idx]\n" \
  "v_smfmac_f32_16x16x64_bf16 v[120:123], v[132:135], a[48:55], %[idx]\n" \
  "v_smfmac_f32_16x16x64_bf16 v[124:127], v[132:135], a[56:63], %[idx]\n" \
  "v_smfmac_f32_16x16x64_bf16 v[112:115], v[136:139], a[64:71], %[idx]\n" \
  "v_smfmac_f32_16x16x64_bf16 v[116:119], v[136:139], a[72:79], %[idx]\n" \
  "v_smfmac_f32_16x16x64_bf16 v[120:123], v[136:139], a[80:87], %[idx]\n" \
  "v_smfmac_f32_16x16x64_bf16 v[124:127], v[136:139], a[88:95], %[idx]\n" \
  "v_smfmac_f32_16x16x64_bf16 v[112:115], v[140:143], a[96:103], %[idx]\n" \
  "v_smfmac_f32_16x16x64_bf16 v[116:119], v[140:143], a[104:111], %[idx]\n" \
  "v_smfmac_f32_16x16x64_bf16 v[120:123], v[140:143], a[112:119], %[idx]\n" \
  "v_smfmac_f32_16x16x64_bf16 v[124:127], v[140:143], a[120:127], %[idx]\n" \
  "v_smfmac_f32_16x16x64_bf16 v[112:115], v[144:147], a[128:135], %[idx]\n" \
  "v_smfmac_f32_16x16x64_bf16 v[116:119], v[144:147], a[136:143], %[idx]\n" \
  "v_smfmac_f32_16x16x64_bf16 v[120:123], v[144:147], a[144:151], %[idx]\n" \
  "v_smfmac_f32_16x16x64_bf16 v[124:127], v[144:147], a[152:159], %[idx]\n" \
  "s_mov_b32 s70, 0\n" \
  ".Lv_smw1:\n" \
  "buffer_load_dwordx4 v[128:131], v250, %[X], s76 offen offset:1024 sc1\n" \
  "buffer_load_dwordx4 v[132:135], v250, %[X], s76 offen offset:2048 sc1\n" \
  "buffer_load_dwordx4 v[136:139], v250, %[X], s76 offen offset:3072 sc1\n" \
  "buffer_load_dwordx4 v[140:143], %[la], %[X], s59 offen offset:0 sc1\n" \
  "s_waitcnt vmcnt(0)\n" \
  "v_max3_u32 v253, v128, v129, v130\n" \
  "v_max3_u32 v253, v253, v131, v132\n" \
  "v_max3_u32 v253, v253, v133, v134\n" \
  "v_max3_u32 v253, v253, v135, v136\n" \
  "v_max3_u32 v253, v253, v137, v138\n" \
  "v_max3_u32 v253, v253, v139, v140\n" \
  "v_max3_u32 v253, v253, v141, v142\n" \
  "v_max_u32 v253, v253, v143\n" \
  "v_cmp_eq_u32 vcc, -1, v253\n" \
  "s_cbranch_vccz .Lv_smw1_ok\n" \
  "s_add_i32 s70, s70, 1\n" \
  "s_and_b32 s68, s70, 1023\n" \
  "s_cbranch_scc1 .Lv_smw1\n" \
  "buffer_load_dword v253, off, %[F], 0 offset:1024 sc1\n" \
  "s_waitcnt vmcnt(0)\n" \
  "v_readfirstlane_b32 s68, v253\n" \
  "s_cmp_lg_u32 s68, 0\n" \
  "s_cbranch_scc1 .Lv_smw1_ok\n" \
  "s_cmp_lt_u32 s70, 400000\n" \
  "s_cbranch_scc1 .Lv_smw1\n" \
  "v_mov_b32 v253, 1\n" \
  "buffer_store_dword v253, off, %[F], 0 offset:1024 sc1\n" \
  ".Lv_smw1_ok:\n" \
  "v_smfmac_f32_16x16x64_bf16 v[112:115], v[128:131], a[160:167], %[idx]\n" \
  "v_smfmac_f32_16x16x64_bf16 v[116:119], v[128:131], a[168:175], %[idx]\n" \
  "v_smfmac_f32_16x16x64_bf16 v[120:123], v[128:131], a[176:183], %[idx]\n" \
  "v_smfmac_f32_16x16x64_bf16 v[124:127], v[128:131], a[184:191], %[idx]\n" \
  "v_smfmac_f32_16x16x64_bf16 v[112:115], v[132:135], a[192:199], %[idx]\n" \
  "v_smfmac_f32_16x16x64_bf16 v[116:119], v[132:135], a[200:207], %[idx]\n" \
  "v_smfmac_f32_16x16x64_bf16 v[120:123], v[132:135], a[208:215], %[idx]\n" \
  "v_smfmac_f32_16x16x64_bf16 v[124:127], v[132:135], a[216:223], %[idx]\n" \
  "v_smfmac_f32_16x16x64_bf16 v[112:115], v[136:139], a[224:231], %[idx]\n" \
  "v_smfmac_f32_16x16x64_bf16 v[116:119], v[136:139], a[232:239], %[idx]\n" \
  "v_smfmac_f32_16x16x64_bf16 v[120:123], v[136:139], a[240:247], %[idx]\n" \
  "v_smfmac_f32_16x16x64_bf16 v[124:127], v[136:139], a[248:255], %[idx]\n" \
  "v_smfmac_f32_16x16x64_bf16 v[112:115], v[140:143], v[24:31], %[idx]\n" \
  "v_smfmac_f32_16x16x64_bf16 v[116:119], v[140:143], v[32:39], %[idx]\n" \
  "v_smfmac_f32_16x16x64_bf16 v[120:123], v[140:143], v[40:47], %[idx]\n" \
  "v_smfmac_f32_16x16x64_bf16 v[124:127], v[140:143], v[48:55], %[idx]\n" \
  "s_nop 7\n" \
  "v_add_f32 v112, v112, v114\n" \
  "v_add_f32 v113, v113, v115\n" \
  "v_add_f32 v116, v116, v118\n" \
  "v_add_f32 v117, v117, v119\n" \
  "v_add_f32 v120, v120, v122\n" \
  "v_add_f32 v121, v121, v123\n" \
  "s_branch .Lv_sm_back\n" \
  ".Lv_entry:\n" \
  "s_mov_b32 s81, 63\n" \
  "s_mov_b32 s87, %[mr0]\n" \
  "s_cmp_lg_u32 %[grp], 0\n" \
  "s_cbranch_scc1 .Lv_w1\n" \
  "v_add_u32 v251, 0, %[wo]\n" \
  "global_load_dwordx4 a[0:3], v251, %[wp] offset:0\n" \
  "global_load_dwordx4 a[4:7], v251, %[wp] offset:1024\n" \
  "global_load_dwordx4 a[32:35], v251, %[wp] offset:2048\n" \
  "global_load_dwordx4 a[36:39], v251, %[wp] offset:3072\n" \
  "v_add_u32 v251, 4096, v251\n" \
  "global_load_dwordx4 a[64:67], v251, %[wp] offset:0\n" \
  "global_load_dwordx4 a[68:71], v251, %[wp] offset:1024\n" \
  "global_load_dwordx4 a[96:99], v251, %[wp] offset:2048\n" \
  "global_load_dwordx4 a[100:103], v251, %[wp] offset:3072\n" \
  "v_add_u32 v251, 4096, v251\n" \
  "global_load_dwordx4 a[128:131], v251, %[wp] offset:0\n" \
  "global_load_dwordx4 a[132:135], v251, %[wp] offset:1024\n" \
  "global_load_dwordx4 a[160:163], v251, %[wp] offset:2048\n" \
  "global_load_dwordx4 a[164:167], v251, %[wp] offset:3072\n" \
  "v_add_u32 v251, 4096, v251\n" \
  "global_load_dwordx4 a[192:195], v251, %[wp] offset:0\n" \
  "global_load_dwordx4 a[196:199], v251, %[wp] offset:1024\n" \
  "global_load_dwordx4 a[224:227], v251, %[wp] offset:2048\n" \
  "global_load_dwordx4 a[228:231], v251, %[wp] offset:3072\n" \
  "v_add_u32 v251, 4096, v251\n" \
  "global_load_dwordx4 v[24:27], v251, %[wp] offset:0\n" \
  "global_load_dwordx4 v[28:31], v251, %[wp] offset:1024\n" \
  "v_add_u32 v251, 2048, v251\n" \
  "global_load_dwordx4 a[8:11], v251, %[wp] offset:0\n" \
  "global_load_dwordx4 a[12:15], v251, %[wp] offset:1024\n" \
  "global_load_dwordx4 a[40:43], v251, %[wp] offset:2048\n" \
  "global_load_dwordx4 a[44:47], v251, %[wp] offset:3072\n" \
  "v_add_u32 v251, 4096, v251\n" \
  "global_load_dwordx4 a[72:75], v251, %[wp] offset:0\n" \
  "global_load_dwordx4 a[76:79], v251, %[wp] offset:1024\n" \
  "global_load_dwordx4 a[104:107], v251, %[wp] offset:2048\n" \
  "global_load_dwordx4 a[108:111], v251, %[wp] offset:3072\n" \
  "v_add_u32 v251, 4096, v251\n" \
  "global_load_dwordx4 a[136:139], v251, %[wp] offset:0\n" \
  "global_load_dwordx4 a[140:143], v251, %[wp] offset:1024\n" \
  "global_load_dwordx4 a[168:171], v251, %[wp] offset:2048\n" \
  "global_load_dwordx4 a[172:175], v251, %[wp] offset:3072\n" \
  "v_add_u32 v251, 4096, v251\n" \
  "global_load_dwordx4 a[200:203], v251, %[wp] offset:0\n" \
  "global_load_dwordx4 a[204:207], v251, %[wp] offset:1024\n" \
  "global_load_dwordx4 a[232:235], v251, %[wp] offset:2048\n" \
  "global_load_dwordx4 a[236:239], v251, %[wp] offset:3072\n" \
  "v_add_u32 v251, 4096, v251\n" \
  "global_load_dwordx4 v[32:35], v251, %[wp] offset:0\n" \
  "global_load_dwordx4 v[36:39], v251, %[wp] offset:1024\n" \
  "v_add_u32 v251, 2048, v251\n" \
  "global_load_dwordx4 a[16:19], v251, %[wp] offset:0\n" \
  "global_load_dwordx4 a[20:23], v251, %[wp] offset:1024\n" \
  "global_load_dwordx4 a[48:51], v251, %[wp] offset:2048\n" \
  "global_load_dwordx4 a[52:55], v251, %[wp] offset:3072\n" \
  "v_add_u32 v251, 4096, v251\n" \
  "global_load_dwordx4 a[80:83], v251, %[wp] offset:0\n" \
  "global_load_dwordx4 a[84:87], v251, %[wp] offset:1024\n" \
  "global_load_dwordx4 a[112:115], v251, %[wp] offset:2048\n" \
  "global_load_dwordx4 a[116:119], v251, %[wp] offset:3072\n" \
  "v_add_u32 v251, 4096, v251\n" \
  "global_load_dwordx4 a[144:147], v251, %[wp] offset:0\n" \
  "global_load_dwordx4 a[148:151], v251, %[wp] offset:1024\n" \
  "global_load_dwordx4 a[176:179], v251, %[wp] offset:2048\n" \
  "global_load_dwordx4 a[180:183], v251, %[wp] offset:3072\n" \
  "v_add_u32 v251, 4096, v251\n" \
  "global_load_dwordx4 a[208:211], v251, %[wp] offset:0\n" \
  "global_load_dwordx4 a[212:215], v251, %[wp] offset:1024\n" \
  "global_load_dwordx4 a[240:243], v251, %[wp] offset:2048\n" \
  "global_load_dwordx4 a[244:247], v251, %[wp] offset:3072\n" \
  "v_add_u32 v251, 4096, v251\n" \
  "global_load_dwordx4 v[40:43], v251, %[wp] offset:0\n" \
  "global_load_dwordx4 v[44:47], v251, %[wp] offset:1024\n" \
  "v_add_u32 v251, 2048, v251\n" \
  "global_load_dwordx4 a[24:27], v251, %[wp] offset:0\n" \
  "global_load_dwordx4 a[28:31], v251, %[wp] offset:1024\n" \
  "global_load_dwordx4 a[56:59], v251, %[wp] offset:2048\n" \
  "global_load_dwordx4 a[60:63], v251, %[wp] offset:3072\n" \
  "v_add_u32 v251, 4096, v251\n" \
  "global_load_dwordx4 a[88:91], v251, %[wp] offset:0\n" \
  "global_load_dwordx4 a[92:95], v251, %[wp] offset:1024\n" \
  "global_load_dwordx4 a[120:123], v251, %[wp] offset:2048\n" \
  "global_load_dwordx4 a[124:127], v251, %[wp] offset:3072\n" \
  "v_add_u32 v251, 4096, v251\n" \
  "global_load_dwordx4 a[152:155], v251, %[wp] offset:0\n" \
  "global_load_dwordx4 a[156:159], v251, %[wp] offset:1024\n" \
  "global_load_dwordx4 a[184:187], v251, %[wp] offset:2048\n" \
  "global_load_dwordx4 a[188:191], v251, %[wp] offset:3072\n" \
  "v_add_u32 v251, 4096, v251\n" \
  "global_load_dwordx4 a[216:219], v251, %[wp] offset:0\n" \
  "global_load_dwordx4 a[220:223], v251, %[wp] offset:1024\n" \
  "global_load_dwordx4 a[248:251], v251, %[wp] offset:2048\n" \
  "global_load_dwordx4 a[252:255], v251, %[wp] offset:3072\n" \
  "v_add_u32 v251, 4096, v251\n" \
  "global_load_dwordx4 v[48:51], v251, %[wp] offset:0\n" \
  "global_load_dwordx4 v[52:55], v251, %[wp] offset:1024\n" \
  "v_add_u32 v251, 2048, v251\n" \
  ".Lv_w1:\n" \
  "s_cmp_eq_u32 %[grp], 0\n" \
  "s_cbranch_scc1 .Lv_go\n" \
  "s_mov_b32 s70, 0\n" \
  "s_xor_b32 s75, %[bas], 0x2000\n" \
  ".Lv_g1:\n" \
  "buffer_load_dword v252, off, %[X], s75 offset:48 sc1\n" \
  "s_waitcnt vmcnt(0)\n" \
  "v_readfirstlane_b32 s74, v252\n" \
  "s_cmp_lg_u32 s74, 0\n" \
  "s_cbranch_scc1 .Lv_g2\n" \
  "s_sleep 8\n" \
  "s_add_i32 s70, s70, 1\n" \
  "s_cmp_lt_u32 s70, 400\n" \
  "s_cbranch_scc1 .Lv_g1\n" \
  ".Lv_g2:\n" \
  "s_add_i32 s74, s74, %[g1t]\n" \
  ".Lv_g3:\n" \
  "s_memrealtime s[76:77]\n" \
  "s_waitcnt lgkmcnt(0)\n" \
  "s_sub_i32 s76, s76, s74\n" \
  "s_cmp_gt_u32 s76, -3000\n" \
  "s_cbranch_scc1 .Lv_g3\n" \
  ".Lv_go:\n" \
  "s_waitcnt vmcnt(0) lgkmcnt(0)\n" \
  "s_barrier\n" \
  "s_mov_b32 s75, 0\n" \
  "s_movk_i32 s76, 0x1000\n" \
  "s_movk_i32 s77, 0x2000\n" \
  "v_mov_b32 v128, 1\n" \
  "buffer_atomic_add v128, %[rdy], %[X], %[bas] offen offset:1536 sc0 sc1\n" \
  "s_mov_b32 s70, 0\n" \
  ".Lv_enw:\n" \
  "buffer_load_dwordx4 v[148:151], %[hvo], %[HS], s75 offen offset:0 sc1\n" \
  "buffer_load_dwordx4 v[152:155], %[hvo], %[HS], s75 offen offset:1024 sc1\n" \
  "buffer_load_dwordx4 v[156:159], %[hvo], %[HS], s75 offen offset:2048 sc1\n" \
  "buffer_load_dwordx4 v[160:163], %[hvo], %[HS], s75 offen offset:3072 sc1\n" \
  "buffer_load_dwordx4 v[164:167], %[hvo], %[HS], s76 offen offset:0 sc1\n" \
  "buffer_load_dwordx4 v[168:171], %[hvo], %[HS], s76 offen offset:1024 sc1\n" \
  "buffer_load_dwordx4 v[172:175], %[hvo], %[HS], s76 offen offset:2048 sc1\n" \
  "buffer_load_dwordx4 v[176:179], %[hvo], %[HS], s76 offen offset:3072 sc1\n" \
  "buffer_load_dwordx4 v[180:183], %[hvo], %[HS], s77 offen offset:0 sc1\n" \
  "s_waitcnt vmcnt(0)\n" \
  "v_max3_u32 v253, v148, v149, v150\n" \
  "v_max3_u32 v253, v253, v151, v152\n" \
  "v_max3_u32 v253, v253, v153, v154\n" \
  "v_max3_u32 v253, v253, v155, v156\n" \
  "v_max3_u32 v253, v253, v157, v158\n" \
  "v_max3_u32 v253, v253, v159, v160\n" \
  "v_max3_u32 v253, v253, v161, v162\n" \
  "v_max3_u32 v253, v253, v163, v164\n" \
  "v_max3_u32 v253, v253, v165, v166\n" \
  "v_max3_u32 v253, v253, v167, v168\n" \
  "v_max3_u32 v253, v253, v169, v170\n" \
  "v_max3_u32 v253, v253, v171, v172\n" \
  "v_max3_u32 v253, v253, v173, v174\n" \
  "v_max3_u32 v253, v253, v175, v176\n" \
  "v_max3_u32 v253, v253, v177, v178\n" \
  "v_max3_u32 v253, v253, v179, v180\n" \
  "v_max3_u32 v253, v253, v181, v182\n" \
  "v_max_u32 v253, v253, v183\n" \
  "v_cmp_eq_u32 vcc, -1, v253\n" \
  "s_cbranch_vccz .Lv_enw_ok\n" \
  "s_sleep 4\n" \
  "s_add_i32 s70, s70, 1\n" \
  "s_and_b32 s68, s70, 1023\n" \
  "s_cbranch_scc1 .Lv_enw\n" \
  "buffer_load_dword v253, off, %[F], 0 offset:1024 sc1\n" \
  "s_waitcnt vmcnt(0)\n" \
  "v_readfirstlane_b32 s68, v253\n" \
  "s_cmp_lg_u32 s68, 0\n" \
  "s_cbranch_scc1 .Lv_enw_ok\n" \
  "s_cmp_lt_u32 s70, 400000\n" \
  "s_cbranch_scc1 .Lv_enw\n" \
  "v_mov_b32 v253, 1\n" \
  "buffer_store_dword v253, off, %[F], 0 offset:1024 sc1\n" \
  ".Lv_enw_ok:\n" \
  "ds_write_b128 %[hvo], v[148:151] offset:0\n" \
  "ds_write_b128 %[hvo], v[152:155] offset:1024\n" \
  "ds_write_b128 %[hvo], v[156:159] offset:2048\n" \
  "ds_write_b128 %[hvo], v[160:163] offset:3072\n" \
  "ds_write_b128 %[hvo], v[164:167] offset:4096\n" \
  "ds_write_b128 %[hvo], v[168:171] offset:5120\n" \
  "ds_write_b128 %[hvo], v[172:175] offset:6144\n" \
  "ds_write_b128 %[hvo], v[176:179] offset:7168\n" \
  "ds_write_b128 %[hvo], v[180:183] offset:8192\n" \
  "v_readfirstlane_b32 s92, v128\n" \
  "s_cmp_lg_u32 s92, 103\n" \
  "s_cbranch_scc1 .Lv_nrdy\n" \
  "s_memrealtime s[74:75]\n" \
  "s_waitcnt lgkmcnt(0)\n" \
  "s_add_i32 s74, s74, %[x0]\n" \
  "s_mul_i32 s75, %[tau], 162\n" \
  "s_lshr_b32 s75, s75, 8\n" \
  "s_add_i32 s75, s75, s74\n" \
  "v_mov_b32 v128, s74\n" \
  "v_mov_b32 v129, s75\n" \
  "buffer_store_dwordx2 v[128:129], %[rdy], %[X], %[bas] offen offset:0 sc1\n" \
  "s_add_i32 s74, s74, %[tau]\n" \
  "s_add_i32 s75, s75, %[tau]\n" \
  "v_mov_b32 v128, s74\n" \
  "v_mov_b32 v129, s75\n" \
  "buffer_store_dwordx2 v[128:129], %[rdy], %[X], %[bas] offen offset:16 sc1\n" \
  ".Lv_nrdy:\n" \
  "s_add_i32 s69, s56, 2\n" \
  "s_min_i32 s68, s69, 63\n" \
  "s_mul_i32 s57, s68, 36864\n" \
  "s_lshl_b32 s68, s69, 16\n" \
  "s_and_b32 s68, s68, 0x10000\n" \
  "s_add_i32 s58, %[hl0], s68\n" \
  "s_add_i32 s68, s56, -1\n" \
  "s_mul_i32 s63, s68, 36864\n" \
  "s_add_i32 s63, s63, %[hl0]\n" \
  "s_cmp_gt_i32 s56, 0\n" \
  "s_cselect_b32 s63, s63, 2.0\n" \
  "s_waitcnt lgkmcnt(0)\n" \
  "s_barrier\n" \
  "ds_read_b128 v[196:199], %[hA] offset:0\n" \
  "ds_read_b128 v[200:203], %[hA] offset:18432\n" \
  "ds_read_b128 v[204:207], %[hA] offset:512\n" \
  "ds_read_b128 v[208:211], %[hA] offset:18944\n" \
  "s_waitcnt lgkmcnt(0)\n" \
  "v_mfma_f32_16x16x32_bf16 v[56:59], v[196:199], a[0:3], 0\n" \
  "v_mfma_f32_16x16x32_bf16 v[72:75], v[200:203], a[0:3], 0\n" \
  "v_mfma_f32_16x16x32_bf16 v[60:63], v[196:199], a[8:11], 0\n" \
  "v_mfma_f32_16x16x32_bf16 v[76:79], v[200:203], a[8:11], 0\n" \
  "v_mfma_f32_16x16x32_bf16 v[64:67], v[196:199], a[16:19], 0\n" \
  "v_mfma_f32_16x16x32_bf16 v[80:83], v[200:203], a[16:19], 0\n" \
  "v_mfma_f32_16x16x32_bf16 v[68:71], v[196:199], a[24:27], 0\n" \
  "v_mfma_f32_16x16x32_bf16 v[84:87], v[200:203], a[24:27], 0\n" \
  "v_mfma_f32_16x16x32_bf16 v[56:59], v[204:207], a[4:7], v[56:59]\n" \
  "v_mfma_f32_16x16x32_bf16 v[72:75], v[208:211], a[4:7], v[72:75]\n" \
  "s_waitcnt lgkmcnt(0)\n" \
  "s_branch .Lv_top\n" \
  ".Lv_end:\n"
#define V_CLOB "v24", "v25", "v26", "v27", "v28", "v29", "v30", "v31", "v32", "v33", "v34", "v35", "v36", "v37", "v38", "v39", "v40", "v41", "v42", "v43", "v44", "v45", "v46", "v47", "v48", "v49", "v50", "v51", "v52", "v53", "v54", "v55", "v56", "v57", "v58", "v59", "v60", "v61", "v62", "v63", "v64", "v65", "v66", "v67", "v68", "v69", "v70", "v71", "v72", "v73", "v74", "v75", "v76", "v77", "v78", "v79", "v80", "v81", "v82", "v83", "v84", "v85", "v86", "v87", "v88", "v89", "v90", "v91", "v92", "v93", "v94", "v95", "v96", "v97", "v98", "v99", "v100", "v101", "v102", "v103", "v104", "v105", "v106", "v107", "v108", "v109", "v110", "v111", "v112", "v113", "v114", "v115", "v116", "v117", "v118", "v119", "v120", "v121", "v122", "v123", "v124", "v125", "v126", "v127", "v128", "v129", "v130", "v131", "v132", "v133", "v134", "v135", "v136", "v137", "v138", "v139", "v140", "v141", "v142", "v143", "v144", "v145", "v146", "v147", "v148", "v149", "v150", "v151", "v152", "v153", "v154", "v155", "v156", "v157", "v158", "v159", "v160", "v161", "v162", "v163", "v164", "v165", "v166", "v167", "v168", "v169", "v170", "v171", "v172", "v173", "v174", "v175", "v176", "v177", "v178", "v179", "v180", "v181", "v182", "v183", "v184", "v185", "v186", "v187", "v188", "v189", "v190", "v191", "v192", "v193", "v194", "v195", "v196", "v197", "v198", "v199", "v200", "v201", "v202", "v203", "v204", "v205", "v206", "v207", "v208", "v209", "v210", "v211", "v212", "v213", "v214", "v215", "v216", "v217", "v218", "v219", "v220", "v221", "v222", "v223", "v224", "v225", "v226", "v227", "v228", "v229", "v230", "v231", "v232", "v233", "v234", "v235", "v236", "v237", "v238", "v239", "v240", "v241", "v242", "v243", "v244", "v245", "v246", "v247", "v248", "v249", "v250", "v251", "v252", "v253", "v254", "v255", "s56", "s57", "s58", "s59", "s60", "s61", "s62", "s63", "s64", "s65", "s66", "s67", "s68", "s69", "s70", "s71", "s72", "s73", "s74", "s75", "s76", "s77", "s78", "s79", "s80", "s81", "s82", "s83", "s84", "s85", "s86", "s87", "s88", "s89", "s90", "s91", "s92", "s93", "s94", "a0", "a1", "a2", "a3", "a4", "a5", "a6", "a7", "a8", "a9", "a10", "a11", "a12", "a13", "a14", "a15", "a16", "a17", "a18", "a19", "a20", "a21", "a22", "a23", "a24", "a25", "a26", "a27", "a28", "a29", "a30", "a31", "a32", "a33", "a34", "a35", "a36", "a37", "a38", "a39", "a40", "a41", "a42", "a43", "a44", "a45", "a46", "a47", "a48", "a49", "a50", "a51", "a52", "a53", "a54", "a55", "a56", "a57", "a58", "a59", "a60", "a61", "a62", "a63", "a64", "a65", "a66", "a67", "a68", "a69", "a70", "a71", "a72", "a73", "a74", "a75", "a76", "a77", "a78", "a79", "a80", "a81", "a82", "a83", "a84", "a85", "a86", "a87", "a88", "a89", "a90", "a91", "a92", "a93", "a94", "a95", "a96", "a97", "a98", "a99", "a100", "a101", "a102", "a103", "a104", "a105", "a106", "a107", "a108", "a109", "a110", "a111", "a112", "a113", "a114", "a115", "a116", "a117", "a118", "a119", "a120", "a121", "a122", "a123", "a124", "a125", "a126", "a127", "a128", "a129", "a130", "a131", "a132", "a133", "a134", "a135", "a136", "a137", "a138", "a139", "a140", "a141", "a142", "a143", "a144", "a145", "a146", "a147", "a148", "a149", "a150", "a151", "a152", "a153", "a154", "a155", "a156", "a157", "a158", "a159", "a160", "a161", "a162", "a163", "a164", "a165", "a166", "a167", "a168", "a169", "a170", "a171", "a172", "a173", "a174", "a175", "a176", "a177", "a178", "a179", "a180", "a181", "a182", "a183", "a184", "a185", "a186", "a187", "a188", "a189", "a190", "a191", "a192", "a193", "a194", "a195", "a196", "a197", "a198", "a199", "a200", "a201", "a202", "a203", "a204", "a205", "a206", "a207", "a208", "a209", "a210", "a211", "a212", "a213", "a214", "a215", "a216", "a217", "a218", "a219", "a220", "a221", "a222", "a223", "a224", "a225", "a226", "a227", "a228", "a229", "a230", "a231", "a232", "a233", "a234", "a235", "a236", "a237", "a238", "a239", "a240", "a241", "a242", "a243", "a244", "a245", "a246", "a247", "a248", "a249", "a250", "a251", "a252", "a253", "a254", "a255", "vcc", "scc", "m0", "memory"
#define TAU 230
#define SH1 40
#define MRG 4
#define LAG 345
#define SCHED [tau] "s"(236), [g1t] "s"(195), [bas] "s"(gb + CTL_CH * CH + 64u), [rdy] "v"(tid == 0 ? 0u : DROP)
__device__ __forceinline__ unsigned qrow(int r) { return (unsigned)(L_QI + (r >> 4) * 2816 + (r & 15) * 160 + ((r >> 2) & 3) * 8); }
__device__ __forceinline__ void qk_wg(const MP& p, int role, int grp, int h, int rep, unsigned gb) {
  const int tid = threadIdx.x, lane = tid & 63, w = __builtin_amdgcn_readfirstlane(tid >> 6);
  const int fr = lane & 15, g = lane >> 4;
  const int jj = (h >> 1) & 3, hh = h & 1;
  const unsigned pb = (unsigned)(h >> 3) * CH + (unsigned)(((h >> 3) & 1) * 1024);
  const unsigned ptb = (unsigned)(PT_CH - PM_CH - 4 * w) * CH + (unsigned)(h >> 3) * 2u * CH;
  const int qc = 8 * (w & 1) + (fr & 7), T = w >> 1;
  const unsigned po1 = fr < 8 ? pb + (unsigned)(jj * 1024 + (g * 16 + hh * 8 + fr) * 16) : T ? ptb + phys((unsigned)((4 + (h & 7)) * 1024 + (g * 16 + qc) * 16)) : DROP;
  const unsigned po2 = fr < 8 ? pb + (unsigned)(4096 + (jj * 16 + hh * 8 + fr) * 16 + g * 4) : ptb + (unsigned)((T * 2 + hh) * 1024 + (jj * 16 + qc) * 16 + g * 4);
  const unsigned po3 = fr < 8 || T ? DROP : ptb + phys((unsigned)((12 + (jj & 2) + hh) * 1024 + (g * 16 + qc) * 16 + (jj & 1) * 8));
  const u16* wb = p.WP + (size_t)h * (16 * 18 * 512);
  unsigned hA = (unsigned)(65536 + (fr >> 3) * RGB + g * 128 + (lane & 7) * 16);
  asm volatile(QK_ASM
    : [hA] "+v"(hA)
    : [X] "s"(mkd(p.win, WIN_BYTES)), [HS] "s"(mkd(p.H + (size_t)grp * (NSEG * SEGB / 2), NSEG * SEGB)), [F] "s"(mkd(p.ct, CT_TOTAL * 4u)),
      [mr0] "s"(gb + (unsigned)(MEM_CH + 2 * rep) * CH), [pm0] "s"(gb + (unsigned)(PM_CH + 4 * w) * CH), [hl0] "s"((unsigned)w * RGB), [wp] "s"(wb), [grp] "s"(grp),
      SCHED, [x0] "s"(95), [mrg] "s"(2), [rpo] "v"(tid == 0 ? gb + CTL_CH * CH + (unsigned)(2048 + 256 * rep) : DROP),
      [cpo] "v"(h == 0 ? gb + CTL_CH * CH + (unsigned)(2048 + 256 * (lane & 7)) : DROP), [cpw] "v"(h == 0 && tid == 0 ? 0u : DROP),
      [cpa] "v"(h == 0 && grp ? 5u * 16u : DROP), [lag] "s"(355), [dn] "s"(-5), [up] "s"(8), [sh1] "s"(40), [gg] "s"(grp * 130), [c2] "s"(38), [c4] "s"(22),
      [mo] "v"((unsigned)lane * 16u), [idx] "v"((lane & 2) ? 0xEEEEu : 0x4444u),
      [qW] "v"(qrow(32 * w + fr) + 4 * g), [kW] "v"(qrow(32 * w + fr) + 128 + 8 * g),
      [qR] "v"(qrow(4 * g + (fr >> 2)) + ((fr & 2) ? 16 + 32 * w : 0) + 8 * (fr & 1)), [kR] "v"(qrow(4 * g + (fr >> 2)) + 128 + 8 * (fr & 3)),
      [qtW] "v"(qrow(32 * w + fr) + 16 + 32 * (g >> 1) + 8 * (g & 1)), [ktW] "v"(L_KT + (unsigned)(w * 1024 + lane * 16)), [ktR] "v"(L_KT + (unsigned)lane * 16u),
      [hvo] "v"((unsigned)w * RGB + (unsigned)lane * 16u), [po1] "v"(po1), [po2] "v"(po2), [po3] "v"(po3), [pcl] "v"(fr == 0 ? po1 : lane == 1 ? po2 : DROP),
      [lim] "v"(fr >= 8 ? 16 * T + qc - 4 * g : 1000), [wo] "v"((unsigned)(w * (4 * 18 * 1024) + lane * 16))
    : QK_CLOB);
}

__device__ __forceinline__ void v_wg(const MP& p, int role, int grp, int j, int rep, unsigned gb) {
  const int tid = threadIdx.x, lane = tid & 63, w = __builtin_amdgcn_readfirstlane(tid >> 6);
  const int fr = lane & 15, g = lane >> 4;
  const int par = lane & 1, rq = lane >> 3, rgp = (2 * par + ((lane >> 2) & 1)) ^ ((rq >> 1) & 3), wsl = (fr * 4 + (g ^ ((fr >> 1) & 3))) * 16;
  const u16* wb = p.BP + (size_t)j * (16 * 18 * 512);
  unsigned hA = (unsigned)(65536 + (fr >> 3) * RGB + g * 128 + (lane & 7) * 16);
  asm volatile(V_ASM
    : [hA] "+v"(hA)
    : [X] "s"(mkd(p.win, WIN_BYTES)), [HS] "s"(mkd(p.H + (size_t)grp * (NSEG * SEGB / 2), NSEG * SEGB)), [OS] "s"(mkd(p.OT + (size_t)grp * (NSEG * SEGB / 2), NSEG * SEGB)),
      [F] "s"(mkd(p.ct, CT_TOTAL * 4u)), [mw0] "s"(gb + (unsigned)(MEM_CH + 2 * w) * CH), [mr0] "s"(gb + (unsigned)(MEM_CH + 2 * rep) * CH), [pt0] "s"(gb + (unsigned)(PT_CH + 2 * w) * CH),
      [pm0] "s"(gb + (unsigned)(PM_CH + rep * 4 + w) * CH + (unsigned)(w & 1) * 1024u), [hl0] "s"((unsigned)w * RGB), [ml0] "s"(LV_MEM + (unsigned)w * 1024u),
      [wp] "s"(wb), [grp] "s"(grp), SCHED, [x0] "s"(grp ? 95 : 40), [rpo] "v"(tid == 0 ? gb + CTL_CH * CH + (unsigned)(3072 + 256 * rep) : DROP),
      [la] "v"((unsigned)lane * 16u), [idx] "v"((lane & 2) ? 0xEEEEu : 0x4444u), [mA] "v"(LV_MEM + (unsigned)lane * 16u), [mA8] "v"(LV_MEM + (unsigned)(8192 + g * 1024 + fr * 16)),
      [c0] "v"((unsigned)(w * 1024 + lane * 16)), [c2] "v"(lane < 16 ? (unsigned)(w * 256 + lane * 16) : DROP), [hvo] "v"((unsigned)w * RGB + (unsigned)lane * 16u),
      [rmW] "v"(LV_RM + (unsigned)(w * 1024 + wsl)), [rmA] "v"(LV_RM + (unsigned)(((8 * par + rq) * 4 + rgp) * 16 + (lane & 2) * 4)),
      [rtW] "v"(LV_RT + (unsigned)(w * 2048 + wsl)), [rtA] "v"(LV_RT + (unsigned)((w >> 1) * 1024 + ((8 * (w & 1) + rq) * 4 + rgp) * 16 + (lane & 2) * 4)),
      [lo] "v"(par ? DROP : (unsigned)(j * 128 + lane * 2)), [lm] "v"(par ? DROP : phys((unsigned)((j >> 1) * 256 + (4 * (rq >> 1) + 2 * (j & 1) + (rq & 1)) * 16 + (lane & 6) * 2))),
      [wo] "v"((unsigned)(w * (4 * 18 * 1024) + lane * 16))
    : V_CLOB);
}

#define YSLP 2
struct Row9 { unsigned u[9]; };
__device__ __forceinline__ float rnd(float a) { return __uint_as_float(pk2(0.f, a)); }
__device__ __forceinline__ void ln18(const f32x2 (&x)[9], f32x2 (&o)[9], float* st, int lane, int w) {
  f32x2 s2 = x[0];
#pragma unroll
  for (int i = 1; i < 9; ++i) s2 += x[i];
  const float mw = i_red8add(s2[0] + s2[1]) * (1.0f / 144.0f);
  f32x2 q2 = {0.f, 0.f};
#pragma unroll
  for (int i = 0; i < 9; ++i) { const f32x2 d = x[i] - mw; q2 += d * d; }
  const float qw = i_red8add(q2[0] + q2[1]);
  if (lane < 8) *(f32x2*)(st + (lane * 4 + w) * 2) = (f32x2){mw, qw};
  BAR();
  const f32x4 a = *(const f32x4*)(st + (lane & 7) * 8), b = *(const f32x4*)(st + (lane & 7) * 8 + 4);
  const float mean = ((a[0] + a[2]) + (b[0] + b[2])) * 0.25f;
  const float d0 = a[0] - mean, d1 = a[2] - mean, d2 = b[0] - mean, d3 = b[2] - mean;
  const float m2 = ((a[1] + a[3]) + (b[1] + b[3])) + 144.0f * ((d0 * d0 + d1 * d1) + (d2 * d2 + d3 * d3));
  const float var = rnd(m2 * (1.0f / DM));
  const float m = rnd(mean), rden = __builtin_amdgcn_rcpf(rnd(__builtin_amdgcn_sqrtf(rnd(var + 1e-5f))));
#pragma unroll
  for (int i = 0; i < 9; ++i) { const f32x2 d = x[i] - m; o[i] = (f32x2){rnd(d[0]), rnd(d[1])} * rden; }
}
__device__ __forceinline__ void mix_rows(Row9& xa, const Row9& ya, const rsrc_t H, unsigned so, unsigned o0, unsigned oe, float* st, int lane, int w) {
  f32x2 x[9], y[9], ln[9];
#pragma unroll
  for (int i = 0; i < 9; ++i) { x[i] = (f32x2){lo2f(xa.u[i]), hi2f(xa.u[i])}; y[i] = (f32x2){lo2f(ya.u[i]), hi2f(ya.u[i])}; }
  ln18(x, ln, st, lane, w);
#pragma unroll
  for (int i = 0; i < 9; ++i) {
    const f32x2 m = (f32x2){rnd(ln[i][0]), rnd(ln[i][1])} * y[i];
    const f32x2 u = x[i] + (f32x2){rnd(m[0]), rnd(m[1])};
    x[i] = (f32x2){rnd(u[0]), rnd(u[1])};
  }
  ln18(x, ln, st + 64, lane, w);
  unsigned h[9];
#pragma unroll
  for (int i = 0; i < 9; ++i) h[i] = nosent(pk2(ln[i][0], ln[i][1]));
  st16(H, o0, so, (v4u){h[0], h[1], h[2], h[3]});
  st16(H, o0 + 4096u, so, (v4u){h[4], h[5], h[6], h[7]});
  st4(H, oe, so, h[8]);
#pragma unroll
  for (int i = 0; i < 9; ++i) xa.u[i] = pk2(x[i][0], x[i][1]);
}
template <int SLP>
__device__ __forceinline__ void wait9(const rsrc_t O, const rsrc_t F, unsigned so, unsigned o0, unsigned o1, unsigned oe, v4u (&v)[2], unsigned& e) {
  unsigned spins = 0;
#pragma nounroll
  while (true) {
    asm volatile("" ::: "memory");
    v[0] = ld16(O, o0, so); v[1] = ld16(O, o1, so); e = ld4(O, oe, so);
    if (__all(mx4(mx4(e, v[0]), v[1]) != SENT)) break;
    if (spin_fail(spins, F)) break;
    __builtin_amdgcn_s_sleep(SLP);
  }
}
__device__ __forceinline__ void add_rows(Row9& xa, const v4u (&v)[2], unsigned e) {
#pragma unroll
  for (int i = 0; i < 2; ++i) {
    const v4u o = {(unsigned)__builtin_amdgcn_update_dpp(v[i].x, v[i].z, 0x128, 0xF, 0xC, false), (unsigned)__builtin_amdgcn_update_dpp(v[i].z, v[i].x, 0x128, 0xF, 0x3, false),
                   (unsigned)__builtin_amdgcn_update_dpp(v[i].y, v[i].w, 0x128, 0xF, 0xC, false), (unsigned)__builtin_amdgcn_update_dpp(v[i].w, v[i].y, 0x128, 0xF, 0x3, false)};
#pragma unroll
    for (int k = 0; k < 4; ++k) xa.u[4 * i + k] = pk2(lo2f(xa.u[4 * i + k]) + lo2f(o[k]), hi2f(xa.u[4 * i + k]) + hi2f(o[k]));
  }
  xa.u[8] = pk2(lo2f(xa.u[8]) + lo2f(e), hi2f(xa.u[8]) + hi2f(e));
}
__device__ __forceinline__ void arm_slots(const MP& p, int s, int rg) {
  const rsrc_t O = mkr(p.OT, 2u * NSEG * SEGB), H = mkr(p.H, 2u * NSEG * SEGB);
  const unsigned so = __builtin_amdgcn_readfirstlane((unsigned)s * SEGB + (unsigned)rg * RGB), o0 = threadIdx.x * 16u;
  const v4u sent = {SENT, SENT, SENT, SENT};
#pragma unroll
  for (int i = 0; i < 3; ++i) {
    const unsigned o = i < 2 || threadIdx.x < 64 ? o0 + 4096u * i : DROP;
    st16(H, o, s + 12 < NSEG ? so + 12u * SEGB : DROP, sent);
    st16(O, o, so, sent); st16(O, o, NSEG * SEGB + so, sent); st16(H, o, s + 2 < NSEG ? (NSEG + 2) * SEGB + so : DROP, sent);
  }
}
#define LR_Y 1024
#define LR_SV 10240
#define RSLP 4
__device__ __forceinline__ void row_wg(const MP& p, char* smem, int role, int rw, unsigned flip) {
  const int tid = threadIdx.x, lane = tid & 63, w = __builtin_amdgcn_readfirstlane(tid >> 6);
  const rsrc_t F = mkr(p.ct, CT_TOTAL * 4u), O = mkr(p.OT, 2u * NSEG * SEGB), H = mkr(p.H, 2u * NSEG * SEGB), Y = mkr(p.Y, DM * YS), X = mkr(p.win, WIN_BYTES);
  float* st = (float*)smem;
  const int r8 = tid & 7, pj = (tid >> 3) & 7;
  const unsigned o0 = (unsigned)tid * 16u;
  const unsigned oe = (unsigned)(8192 + pj * 128 + r8 * 16 + w * 4);
  const unsigned oe2 = (unsigned)(8192 + ((pj & 6) + (w & 1)) * 128 + r8 * 16 + (2 * (pj & 1) + (w >> 1)) * 4);
  const int rg = rw & 3, s0 = rw >> 2;
  const char* yR = smem + LR_Y + (8 * ((tid >> 4) * 2 + ((lane >> 1) & 1)) + ((lane & 15) >> 2)) * 16 + (lane & 1) * 8;
  const char* yE = smem + LR_Y + (512 + 8 * pj + 2 * w) * 16 + r8 * 2;
#pragma nounroll
  for (int job = rw < 8 ? -1 : 0; job < 14; ++job) {
    const bool isA = job <= 0 || (job & 1);
    const int k = job <= 0 ? 0 : (isA ? (job + 1) >> 1 : (job >> 1) - 1);
    const int s = s0 + 12 * k;
    if (s >= NSEG) continue;
    const unsigned to = CTL_CH * CH + 64u + 16u * (unsigned)(s < 62 ? s + 2 : 64);
    const int tc = s < 63 ? 20 : 60;
    const unsigned so = __builtin_amdgcn_readfirstlane((unsigned)s * SEGB + (unsigned)rg * RGB);
    char* sv = smem + LR_SV + (k & 1) * 2 * RGB;
    const size_t go = (size_t)(s * SEG + rg * 8 + r8) * DM;
    Row9 xa, ya;
    v4u v[2];
    unsigned e;
    if (isA) {
      const v4u x0 = *(const v4u*)(p.x0 + go + (tid >> 3) * 8), x1 = *(const v4u*)(p.x0 + go + 256 + (tid >> 3) * 8);
      xa.u[8] = *(const unsigned*)(p.x0 + go + 512 + 8 * pj + 2 * w);
#pragma unroll
      for (int i = 0; i < 4; ++i) { xa.u[i] = x0[i]; xa.u[4 + i] = x1[i]; }
      if (job > 0) arm_slots(p, s, rg);
      if (job == 0) {
        unsigned spins = 0;
#pragma nounroll
        while (!__all(ld4(F, (unsigned)(CT_YC + (lane & 7) * 32) * 4u, 0) >= 72u)) { if (spin_fail(spins, F)) break; __builtin_amdgcn_s_sleep(4); asm volatile("" ::: "memory"); }
      }
      if (job >= 0) wait9<YSLP>(Y, F, (unsigned)(s * SEG + rg * 8) * 2u, (unsigned)tid * YS, (unsigned)(tid + 256) * YS, (unsigned)(512 + (tid >> 2)) * YS + (unsigned)(tid & 3) * 4u, v, e);
      else { v[0] = x0; v[1] = x1; e = xa.u[8]; }
      VM0();
      *(v4u*)(smem + LR_Y + o0) = v[0]; *(v4u*)(smem + LR_Y + o0 + 4096) = v[1]; *(unsigned*)(smem + LR_Y + 8192 + tid * 4) = e;
      BAR();
#pragma unroll
      for (int i = 0; i < 2; ++i) {
        const v2u x = trd(yR + 4096 * i), y = trd(yR + 4096 * i + 64);
        ya.u[4 * i] = x.x; ya.u[4 * i + 1] = x.y; ya.u[4 * i + 2] = y.x; ya.u[4 * i + 3] = y.y;
      }
      ya.u[8] = (unsigned)*(const u16*)yE | ((unsigned)*(const u16*)(yE + 16) << 16);
    } else {
#pragma unroll
      for (int i = 0; i < 2; ++i) {
        const v4u a = *(const v4u*)(sv + o0 + 4096 * i), b = *(const v4u*)(sv + RGB + o0 + 4096 * i);
#pragma unroll
        for (int q = 0; q < 4; ++q) { xa.u[4 * i + q] = a[q]; ya.u[4 * i + q] = b[q]; }
      }
      xa.u[8] = *(const unsigned*)(sv + 8192 + tid * 4); ya.u[8] = *(const unsigned*)(sv + RGB + 8192 + tid * 4);
      wait_time(X, flip * 8192u + to, tc);
      wait9<RSLP>(O, F, so, o0, o0 + 4096u, oe2, v, e);
      add_rows(xa, v, e);
    }
    mix_rows(xa, ya, H, job < 0 ? 0x40000000u : (isA ? so : NSEG * SEGB + so), o0, oe, st, lane, w);
    if (isA) {
#pragma unroll
      for (int i = 0; i < 2; ++i) {
        *(v4u*)(sv + o0 + 4096 * i) = (v4u){xa.u[4 * i], xa.u[4 * i + 1], xa.u[4 * i + 2], xa.u[4 * i + 3]};
        *(v4u*)(sv + RGB + o0 + 4096 * i) = (v4u){ya.u[4 * i], ya.u[4 * i + 1], ya.u[4 * i + 2], ya.u[4 * i + 3]};
      }
      *(unsigned*)(sv + 8192 + tid * 4) = xa.u[8]; *(unsigned*)(sv + RGB + 8192 + tid * 4) = ya.u[8];
    } else {
      wait_time(X, (flip ^ 1u) * 8192u + to, tc);
      wait9<RSLP>(O, F, NSEG * SEGB + so, o0, o0 + 4096u, oe2, v, e);
      add_rows(xa, v, e);
      *(v4u*)(p.out + go + (tid >> 3) * 8) = (v4u){xa.u[0], xa.u[1], xa.u[2], xa.u[3]};
      *(v4u*)(p.out + go + 256 + (tid >> 3) * 8) = (v4u){xa.u[4], xa.u[5], xa.u[6], xa.u[7]};
      *(unsigned*)(p.out + go + 512 + 8 * pj + 2 * w) = xa.u[8];
    }
  }
}

#define CREV_STRIDE 2208
#define LC_KVS 35328
#define LC_RED 43008
__device__ __forceinline__ unsigned rot16(unsigned u) { return __builtin_amdgcn_alignbit(u, u, 16); }
#define LC_IN 61440
__device__ __forceinline__ void crev_put(u16* dst, int stride, const v4u& c0, const v4u& c1) {
  const unsigned e[8] = {rot16(c0.w), rot16(c0.z), rot16(c0.y), rot16(c0.x), rot16(c1.w), rot16(c1.z), rot16(c1.y), rot16(c1.x)};
  unsigned u[7];
#pragma unroll
  for (int k = 0; k < 7; ++k) u[k] = __builtin_amdgcn_alignbit(e[k + 1], e[k], 16);
#pragma unroll
  for (int a = 0; a < 4; ++a) {
    *(v4u*)(dst + (2 * a) * stride) = (v4u){e[a], e[a + 1], e[a + 2], e[a + 3]};
    *(v4u*)(dst + (2 * a + 1) * stride) = (v4u){u[a], u[a + 1], u[a + 2], u[a + 3]};
  }
}
__device__ __forceinline__ unsigned y2pair(float y0, float y1, unsigned kv, float Dv) {
  const unsigned yy = pk2(y0, y1), kd = pk2(lo2f(kv) * Dv, hi2f(kv) * Dv);
  return nosent(pk2(lo2f(yy) + lo2f(kd), hi2f(yy) + hi2f(kd)));
}
__device__ __forceinline__ void conv_hi(char* smem, const char* in, float Dv, const rsrc_t Y, int d, int tid) {
  const int lane = tid & 63, w = tid >> 6;
  const v4u c0 = *(const v4u*)(in + tid * 16), kq = *(const v4u*)(in + 4096 + tid * 16);
  v4u c1 = *(const v4u*)(in + (tid ? tid * 16 - 16 : 0));
  if (tid == 0) c1 = (v4u){0u, 0u, 0u, 0u};
  u16* crev = (u16*)smem;
  u16* kvs = (u16*)(smem + LC_KVS);
  float* red = (float*)(smem + LC_RED);
  crev_put(crev + (263 - tid) * 8, CREV_STRIDE, c0, c1);
  *(v4u*)&kvs[(32 + (tid >> 2)) * 40 + (tid & 3) * 8] = kq;
  BAR();
  const int i = lane & 31, h = lane >> 5, X0 = 2048 + 63;
  const int rho = (X0 - i) & 7;
  const u16* aB = crev + rho * CREV_STRIDE + (X0 - (32 * w + i - 8 * h) - rho);
  const u16* bB = kvs + (64 + i - w) * 40 + 8 * h;
  f32x16 acc;
#pragma unroll
  for (int r = 0; r < 16; ++r) acc[r] = 0.f;
  v4u A[8], B[8];
#pragma unroll
  for (int q = 0; q < 8; ++q) { A[q] = *(const v4u*)(aB - 128 * (q >> 1) + 16 * (q & 1)); B[q] = *(const v4u*)(bB - 160 * (q >> 1) + 16 * (q & 1)); }
  asm volatile("s_nop 1" : "+v"(acc));
#pragma nounroll
  for (int m4 = 1; m4 <= 4; ++m4) {
    const u16* an = aB - 512 * (m4 & 3);
    const u16* bn = bB - 640 * (m4 & 3);
    PIN();
#pragma unroll
    for (int q = 0; q < 8; ++q) {
      asm volatile("v_mfma_f32_32x32x16_bf16 %0, %1, %2, %0" : "+v"(acc) : "v"(A[q]), "v"(B[q]));
      A[q] = *(const v4u*)(an - 128 * (q >> 1) + 16 * (q & 1)); B[q] = *(const v4u*)(bn - 160 * (q >> 1) + 16 * (q & 1));
      PIN();
    }
  }
  asm volatile("s_nop 7\n\ts_nop 7" : "+v"(acc));
#pragma unroll
  for (int r = 0; r < 4; ++r) *(f32x4*)&red[(w * 32 + i) * 36 + 8 * r + 4 * h] = (f32x4){acc[4 * r], acc[4 * r + 1], acc[4 * r + 2], acc[4 * r + 3]};
  BAR();
  if (tid >= 128) {
    const int l = tid & 127;
    const float* rr = red + (l >> 2) * 36 + (l & 3) * 8;
    f32x4 y[2];
#pragma unroll
    for (int e = 0; e < 2; ++e)
      y[e] = (*(const f32x4*)(rr + 4 * e) + *(const f32x4*)(rr + 32 * 36 + 4 * e)) + (*(const f32x4*)(rr + 64 * 36 + 4 * e) + *(const f32x4*)(rr + 96 * 36 + 4 * e));
    st16(Y, (unsigned)tid * 16u, (unsigned)d * YS,
         (v4u){y2pair(y[0][0], y[0][1], kq.x, Dv), y2pair(y[0][2], y[0][3], kq.y, Dv), y2pair(y[1][0], y[1][1], kq.z, Dv), y2pair(y[1][2], y[1][3], kq.w, Dv)});
  }
}
#define CL_STRIDE 1056
#define LC_WAVE 22016
__device__ __forceinline__ void conv_lo(char* sm, const v4u (&cw)[3], const v4u (&kw)[2], float Dv, const rsrc_t Y, int d, int lane) {
  u16* crev = (u16*)sm;
  u16* kvs = (u16*)(sm + 16 * CL_STRIDE);
  const v4u z = {0u, 0u, 0u, 0u};
  crev_put(crev + (127 - 2 * lane) * 8, CL_STRIDE, cw[1], lane ? cw[0] : z);
  crev_put(crev + (126 - 2 * lane) * 8, CL_STRIDE, cw[2], cw[1]);
  if (lane < 32) *(v4u*)(crev + (lane >> 2) * CL_STRIDE + (128 + (lane & 3)) * 8) = z;
  *(v4u*)&kvs[(lane >> 1) * 40 + (lane & 1) * 16] = z; *(v4u*)&kvs[(lane >> 1) * 40 + (lane & 1) * 16 + 8] = z;
  *(v4u*)&kvs[(32 + (lane >> 1)) * 40 + (lane & 1) * 16] = kw[0]; *(v4u*)&kvs[(32 + (lane >> 1)) * 40 + (lane & 1) * 16 + 8] = kw[1];
  asm volatile("" ::: "memory");
  const int i = lane & 31, h = lane >> 5;
  const int rho = (1023 - i) & 7;
  const u16* aB = crev + rho * CL_STRIDE + (1023 - (i - 8 * h) - rho);
  const u16* bB = kvs + (32 + i) * 40 + 8 * h;
  f32x16 acc;
#pragma unroll
  for (int r = 0; r < 16; ++r) acc[r] = 0.f;
  v4u A[8], B[8];
#pragma unroll
  for (int q = 0; q < 8; ++q) { A[q] = *(const v4u*)(aB - 32 * (q >> 1) + 16 * (q & 1)); B[q] = *(const v4u*)(bB - 40 * (q >> 1) + 16 * (q & 1)); }
  asm volatile("s_nop 1" : "+v"(acc));
#pragma nounroll
  for (int m4 = 1; m4 <= 8; ++m4) {
    const u16* an = aB - 128 * (m4 & 7);
    const u16* bn = bB - 160 * (m4 & 7);
    PIN();
#pragma unroll
    for (int q = 0; q < 8; ++q) {
      asm volatile("v_mfma_f32_32x32x16_bf16 %0, %1, %2, %0" : "+v"(acc) : "v"(A[q]), "v"(B[q]));
      A[q] = *(const v4u*)(an - 32 * (q >> 1) + 16 * (q & 1)); B[q] = *(const v4u*)(bn - 40 * (q >> 1) + 16 * (q & 1));
      PIN();
    }
  }
  v2u kv4[4];
#pragma unroll
  for (int r = 0; r < 4; ++r) kv4[r] = *(const v2u*)&kvs[(32 + i) * 40 + 8 * r + 4 * h];
  asm volatile("s_nop 7\n\ts_nop 7" : "+v"(acc));
  v2u o[4];
#pragma unroll
  for (int r = 0; r < 4; ++r) o[r] = (v2u){y2pair(acc[4 * r], acc[4 * r + 1], kv4[r].x, Dv), y2pair(acc[4 * r + 2], acc[4 * r + 3], kv4[r].y, Dv)};
#pragma unroll
  for (int r = 0; r < 4; ++r) *(v2u*)&kvs[i * 40 + 8 * r + 4 * h] = o[r];
  asm volatile("" ::: "memory");
#pragma unroll
  for (int k = 0; k < 2; ++k) st16(Y, (unsigned)(k * 64 + lane) * 16u, (unsigned)d * YS, *(const v4u*)&kvs[(16 * k + (lane >> 2)) * 40 + 8 * (lane & 3)]);
}

#define QN0 16
#define QN1 0
#define QN2 16
#define RW0 6
#define RW1 6
#define RW2 6
__global__ __launch_bounds__(256, 1) void k_main(MP p) {
  extern __shared__ __attribute__((aligned(16))) char smem[];
  const int tid = threadIdx.x;
  unsigned cpf = 0;
  {
    unsigned long long pc; unsigned len;
    asm volatile("s_getpc_b64 %0\n.Lcpf:\n\ts_mov_b32 %1, _Z5k_pre4PreP-.Lcpf" : "=s"(pc), "=s"(len));
    cpf = __builtin_amdgcn_raw_buffer_load_b32(mkr((const void*)pc, (int)len > 0 ? len & ~3u : 0u), (unsigned)(tid + 256 * ((blockIdx.x >> 3) & 1)) * 128u, 0, 0);
  }
  unsigned xcc; asm volatile("s_getreg_b32 %0, hwreg(HW_REG_XCC_ID)" : "=s"(xcc));
  xcc &= 7u;
  int* rl = (int*)(smem + L_ROLE);
  if (tid == 0) {
    const unsigned fl = p.ct[CT_FLIP];
    int r = 0;
#pragma nounroll
    for (unsigned k = 0; k < 8; ++k) {
      const unsigned x = xcc ^ k;
      const unsigned tk = __hip_atomic_fetch_add(&p.ct[CT_TICK + x * 32], 1u, __ATOMIC_RELAXED, __HIP_MEMORY_SCOPE_AGENT);
      if (tk < 32u) { r = (int)(x * 32u + tk); break; }
    }
    rl[0] = r; rl[1] = (int)(fl & 1u);
  }
  asm volatile("" :: "v"(cpf));
  BAR();
  int role = __builtin_amdgcn_readfirstlane(rl[0]);
  {
    const int xq = (role >> 5) & 3, tk = role & 31;
    const int nr = xq == 0 ? RW0 : xq == 1 ? RW1 : xq == 2 ? RW2 : 24 - RW0 - RW1 - RW2;
    const int rb = xq == 0 ? 0 : xq == 1 ? RW0 : xq == 2 ? RW0 + RW1 : RW0 + RW1 + RW2;
    const int nq = xq == 0 ? QN0 : xq == 1 ? QN1 : xq == 2 ? QN2 : 32 - QN0 - QN1 - QN2;
    const int qb = xq == 0 ? 0 : xq == 1 ? QN0 : xq == 2 ? QN0 + QN1 : QN0 + QN1 + QN2;
    if (tk < nq) { const int v = qb + tk; role = (role & 128) + (v >> 3) * 32 + (v & 7); }
    else if (tk < 32 - nr) { const int v = xq * 32 - rb - qb + tk - nq; role = (role & 128) + (v / 18) * 32 + 8 + v % 18; }
    else { const int v = rb + tk - (32 - nr); role = (role & 128) + (v / 6) * 32 + 26 + v % 6; }
  }
  const unsigned flip = (unsigned)__builtin_amdgcn_readfirstlane(rl[1]);
  const int x = role >> 5, i = role & 31, grp = x >> 2;
  const unsigned gb = (((unsigned)grp) ^ flip) * 8192u;
  const int rep = (int)(xcc & 3u);
  const int slot = i < 8 ? (x & 3) * 8 + i : (x & 3) * 18 + (i < 26 ? i - 8 : 0);
  const int rw = (x ^ 4) * 6 + (i - 26);
  if (grp || i >= 26) {
    const int lane = tid & 63, w = __builtin_amdgcn_readfirstlane(tid >> 6);
    const int e2 = (x & 3) * 26 + i, d1 = (i < 26 ? e2 : (rw < 8 ? 200 : 96 + rw)) * 4 + w;
    const int n2 = grp && i < 26 ? (e2 < 56 ? 6 : 5) : 0;
    const rsrc_t Y = mkr(p.Y, DM * YS);
    const int dl = lane == 0 ? d1 : e2 + 104 * (lane - 1);
    const unsigned dv = (unsigned)p.ssm_D[dl < DM ? dl : 0] << 16;
    v4u cw[3], kw[2], i2[6][2];
    {
      const size_t o = (size_t)(d1 < DM ? d1 : 0) * S_LEN + lane * 16;
      cw[0] = *(const v4u*)&p.ssm_k[lane ? o - 8 : o]; cw[1] = *(const v4u*)&p.ssm_k[o]; cw[2] = *(const v4u*)&p.ssm_k[o + 8];
      kw[0] = *(const v4u*)&p.kvT[o]; kw[1] = *(const v4u*)&p.kvT[o + 8];
    }
    VM0();
    if (i >= 26) arm_slots(p, rw >> 2, rw & 3);
    if (d1 < DM) conv_lo(smem + w * LC_WAVE, cw, kw, __uint_as_float(__builtin_amdgcn_readlane(dv, 0)), Y, d1, lane);
    if (d1 < DM && lane == 0) __hip_atomic_fetch_add(&p.ct[CT_YC + (d1 & 7) * 32], 1u, __ATOMIC_RELAXED, __HIP_MEMORY_SCOPE_AGENT);
    if (n2) {
#pragma unroll
      for (int c = 0; c < 6; ++c) {
        const int d = e2 + 104 * c < DM ? e2 + 104 * c : e2;
        i2[c][0] = *(const v4u*)&p.ssm_k[(size_t)d * S_LEN + tid * 8]; i2[c][1] = *(const v4u*)&p.kvT[(size_t)d * S_LEN + tid * 8];
      }
      BAR();
#pragma unroll
      for (int k = 0; k < 6; ++k) { *(v4u*)(smem + LC_IN + k * 8192 + tid * 16) = i2[k][0]; *(v4u*)(smem + LC_IN + 4096 + k * 8192 + tid * 16) = i2[k][1]; }
      if (tid < 64) *(v4u*)(smem + ((tid >> 3) * CREV_STRIDE + (264 + (tid & 7)) * 8) * 2) = (v4u){0u, 0u, 0u, 0u};
      if (tid < 128) *(v4u*)(smem + LC_KVS + ((tid >> 2) * 40 + (tid & 3) * 8) * 2) = (v4u){0u, 0u, 0u, 0u};
      BAR();
#pragma nounroll
      for (int c = 0; c < n2; ++c) {
        const u16* wb = (i < 8 ? p.WP : p.BP) + (size_t)slot * (16 * 18 * 512);
        if (c == 1) load_w<0, 2>(wb); else if (c == 2) load_w<2, 4>(wb); else if (c == 3) load_w<4, 6>(wb); else if (c == 4) load_w<6, 8>(wb);
        conv_hi(smem, smem + LC_IN + c * 8192, __uint_as_float(__builtin_amdgcn_readlane(dv, c + 1)), Y, e2 + 104 * c, tid);
        AOWN();
      }
    }
    BAR();
  }
  if (i < 8) qk_wg(p, role, grp, slot, rep, gb);
  else if (i < 26) v_wg(p, role, grp, slot, rep, gb);
  else row_wg(p, smem, role, rw, flip);
}

#define PRE_KV 144
#define PRE_PW 192
#define PRE_BG 384
#define PRE_FILL 40
#define BAUX 16
#define WAUX 16
#define FAUX 16
#define STW(base, bytes, idx16, v) __builtin_amdgcn_raw_buffer_store_b128(v, mkr(base, bytes), (unsigned)(idx16) * 16u, 0, FAUX)
struct PreP {
  const u16* kk; const u16* vv; const u16* shift_k; u16* kvT;
  const u16* Wq; const u16* Wk; const u16* Wv; const u16* Wo; u16* WP; u16* BP;
  const u16* mem0; v4u* win; v4u* Y; v4u* H; unsigned* ct;
};
__global__ __launch_bounds__(256) void k_pre(PreP p) {
  __shared__ __attribute__((aligned(16))) u16 sh[128 * 104 + 4 * 16 * 136];
  const int tid = threadIdx.x;
  int bid = blockIdx.x;
  if (bid < 16) {
    unsigned long long pc; int len, back;
    asm volatile("s_getpc_b64 %0\n.Lcpf0:\n\ts_mov_b32 %1, code_end-.Lcpf0\n\ts_mov_b32 %2, .Lcpf0-_Z6k_main2MP" : "=s"(pc), "=s"(len), "=s"(back));
    const bool ok = len > 0 && back > 0;
    const unsigned v = __builtin_amdgcn_raw_buffer_load_b32(mkr((const void*)pc, ok ? (unsigned)len & ~3u : 0u), (unsigned)tid * 128u, 0, 0);
    const unsigned u = __builtin_amdgcn_raw_buffer_load_b32(mkr((const void*)(pc - (unsigned)back), ok ? (unsigned)back & ~3u : 0u), (unsigned)(tid + 256 * (bid >> 3)) * 128u, 0, 0);
    asm volatile("" :: "v"(v), "v"(u));
  }
  if (bid == 0) {
    for (int i = tid; i < CT_TOTAL; i += 256) if (i < CT_FLIP || i >= CT_YC) p.ct[i] = 0u;
    const rsrc_t X = mkr(p.win, WIN_BYTES);
    unsigned xcc; asm volatile("s_getreg_b32 %0, hwreg(HW_REG_XCC_ID)" : "=s"(xcc));
    int* best = (int*)sh;
    int b0 = 1 << 30, b1 = 1 << 30;
    unsigned acc = 0;
    const unsigned ob = CAL_CH * CH + (unsigned)(tid >> 6) * 2048u;
    st16(X, ob + (unsigned)(((tid & 63) >> 3) * 256 + (tid & 7) * 16), 0, (v4u){0u, 0u, 0u, 0u});
    st16(X, ob + (unsigned)(((tid & 63) >> 3) * 256 + (tid & 7) * 16), 8192u, (v4u){0u, 0u, 0u, 0u});
    asm volatile("s_waitcnt vmcnt(0)" ::: "memory");
#pragma nounroll
    for (int i = 0; i < 16; ++i) {
      const unsigned o = ob + (unsigned)(i & 1) * 8192u + (unsigned)(i >> 1) * 256u;
      long long t0 = __builtin_amdgcn_s_memtime();
      unsigned v = ld4(X, o, 0);
      asm volatile("s_waitcnt vmcnt(0)" : "+v"(v) :: "memory");
      long long t1 = __builtin_amdgcn_s_memtime();
      acc += v;
      const int d = (int)(t1 - t0);
      if (i & 1) b1 = d < b1 ? d : b1; else b0 = d < b0 ? d : b0;
    }
    if ((tid & 63) == 0) { best[(tid >> 6) * 2] = b0; best[(tid >> 6) * 2 + 1] = b1; }
    __syncthreads();
    if (tid == 0) {
      for (int i = 1; i < 4; ++i) { b0 = best[2 * i] < b0 ? best[2 * i] : b0; b1 = best[2 * i + 1] < b1 ? best[2 * i + 1] : b1; }
      p.ct[CT_FLIP] = ((b1 < b0 ? 1u : 0u) ^ ((xcc >> 2) & 1u)) + acc;
    }
    return;
  }
  bid -= 1;
  bid = bid < PRE_KV + PRE_PW ? bid + PRE_BG : (bid < PRE_KV + PRE_PW + PRE_BG ? bid - (PRE_KV + PRE_PW) : bid);
  if (bid < PRE_BG) {
    const int r_ = bid >> 3, h = (bid & 7) + 8 * (r_ / 12), cq = (r_ % 12) >> 1, k0 = (r_ & 1) * 9;
    const int lane = tid & 63, w = __builtin_amdgcn_readfirstlane(tid >> 6), fr = lane & 15, g = lane >> 4;
    v4u af[3][2][4];
    {
      v4u ld[6];
#pragma unroll
      for (int i = 0; i < 6; ++i) { int c = tid + 256 * i, r = c / 12, q = c % 12; ld[i] = *(const v4u*)&p.Wo[(long)(h * DH + r) * DM + 96 * cq + 8 * q]; }
#pragma unroll
      for (int u = 0; u < 3; ++u)
#pragma unroll
        for (int T = 0; T < 2; ++T)
#pragma unroll
          for (int i = 0; i < 4; ++i)
            if (u < 2 || w == 0) af[u][T][i] = *(const v4u*)&p.Wv[(long)(32 * (k0 + w + 4 * u) + 8 * i + g + 4 * T) * HD + h * DH + 8 * fr];
#pragma unroll
      for (int i = 0; i < 6; ++i) { int c = tid + 256 * i, r = c / 12, q = c % 12; *(v4u*)&sh[r * 104 + 8 * q] = ld[i]; }
      u16* st = sh + 128 * 104 + w * (16 * 136);
#pragma unroll
      for (int u = 0; u < 3; ++u)
#pragma unroll
        for (int T = 0; T < 2; ++T)
          if (u < 2 || w == 0) {
#pragma unroll
            for (int i = 0; i < 4; ++i) *(v4u*)&st[(4 * i + g) * 136 + 8 * fr] = af[u][T][i];
            asm volatile("" ::: "memory");
#pragma unroll
            for (int es = 0; es < 4; ++es) af[u][T][es] = *(const v4u*)&st[fr * 136 + 32 * es + 8 * g];
            asm volatile("" ::: "memory");
          }
    }
    __syncthreads();
#pragma nounroll
    for (int ct = 0; ct < 6; ++ct) {
      v4u bf[4];
#pragma unroll
      for (int es = 0; es < 4; ++es) {
        const u16* a0 = &sh[(32 * es + 8 * g + (fr >> 2)) * 104 + 16 * ct + 4 * (fr & 3)];
        v2u x = __builtin_bit_cast(v2u, __builtin_amdgcn_ds_read_tr16_b64_v4i16((__attribute__((address_space(3))) s16x4*)a0));
        v2u y = __builtin_bit_cast(v2u, __builtin_amdgcn_ds_read_tr16_b64_v4i16((__attribute__((address_space(3))) s16x4*)(a0 + 4 * 104)));
        bf[es] = (v4u){x.x, x.y, y.x, y.y};
      }
      const int j = 12 * cq + 2 * ct + ((fr >> 1) & 1);
#pragma unroll
      for (int u = 0; u < 3; ++u) {
        if (u < 2 || w == 0) {
          f32x4 a = {0, 0, 0, 0}, b = {0, 0, 0, 0};
#pragma unroll
          for (int es = 0; es < 4; ++es) { a = MFMA32(af[u][0][es], bf[es], a); b = MFMA32(af[u][1][es], bf[es], b); }
          __builtin_amdgcn_raw_buffer_store_b128((v4u){pk2(a[0], a[1]), pk2(a[2], a[3]), pk2(b[0], b[1]), pk2(b[2], b[3])}, mkr(p.BP, NH * DM * DM * 2u),
              (unsigned)(((j * 16 + (h >> 1)) * 18 + k0 + w + 4 * u) * 64 + g * 16 + (h & 1) * 8 + 2 * (fr >> 2) + (fr & 1)) * 16u, 0, BAUX);
        }
      }
    }
    return;
  }
  bid -= PRE_BG;
  if (bid < PRE_KV) {
    const int d0 = (bid % 9) * 64, s0 = (bid / 9) * 128;
    const int dq = tid & 7, sr = tid >> 3;
    const int d = d0 + dq * 8, s = s0 + 4 * sr;
    v4u kr[7], vr[4], sw[4];
#pragma unroll
    for (int a = 0; a < 7; ++a) kr[a] = (s - 3 + a >= 0) ? *(const v4u*)&p.kk[(long)(s - 3 + a) * DM + d] : (v4u){0, 0, 0, 0};
#pragma unroll
    for (int a = 0; a < 4; ++a) { vr[a] = *(const v4u*)&p.vv[(long)(s + a) * DM + d]; sw[a] = *(const v4u*)&p.shift_k[d * 4 + 8 * a]; }
#pragma unroll
    for (int jp = 0; jp < 4; ++jp) {
      const float w0[4] = {lo2f(sw[jp].x), hi2f(sw[jp].x), lo2f(sw[jp].y), hi2f(sw[jp].y)}, w1[4] = {lo2f(sw[jp].z), hi2f(sw[jp].z), lo2f(sw[jp].w), hi2f(sw[jp].w)};
      unsigned o[4];
#pragma unroll
      for (int r = 0; r < 4; ++r) {
        unsigned acc = pk2(lo2f(kr[r][jp]) * w0[3], hi2f(kr[r][jp]) * w1[3]);
#pragma unroll
        for (int a = 1; a < 4; ++a) {
          const unsigned m = pk2(lo2f(kr[r + a][jp]) * w0[3 - a], hi2f(kr[r + a][jp]) * w1[3 - a]);
          acc = pk2(lo2f(acc) + lo2f(m), hi2f(acc) + hi2f(m));
        }
        o[r] = pk2(lo2f(acc) * lo2f(vr[r][jp]), hi2f(acc) * hi2f(vr[r][jp]));
      }
      *(v2u*)&sh[(dq * 8 + 2 * jp) * 136 + 4 * sr] = (v2u){(o[0] & 0xFFFFu) | (o[1] << 16), (o[2] & 0xFFFFu) | (o[3] << 16)};
      *(v2u*)&sh[(dq * 8 + 2 * jp + 1) * 136 + 4 * sr] = (v2u){(o[0] >> 16) | (o[1] & 0xFFFF0000u), (o[2] >> 16) | (o[3] & 0xFFFF0000u)};
    }
    __syncthreads();
#pragma unroll
    for (int i = 0; i < 4; ++i) {
      const int c = tid + 256 * i, dd = c >> 4, q = c & 15;
      STW(p.kvT, DM * S_LEN * 2u, (d0 + dd) * (S_LEN / 8) + s0 / 8 + q, *(const v4u*)&sh[dd * 136 + 8 * q]);
    }
    return;
  }
  bid -= PRE_KV;
  if (bid < PRE_PW) {
    u16 (*t)[136] = (u16 (*)[136])sh;
    const int kq = bid % 6, h = bid / 6;
    v4u ld[2][6];
#pragma unroll
    for (int i = 0; i < 6; ++i) {
      int c = tid + 256 * i, r = c >> 4, q = c & 15;
      ld[0][i] = *reinterpret_cast<const v4u*>(&p.Wq[(long)(96 * kq + r) * HD + h * DH + q * 8]);
      ld[1][i] = *reinterpret_cast<const v4u*>(&p.Wk[(long)(96 * kq + r) * HD + h * DH + q * 8]);
    }
#pragma unroll
    for (int which = 0; which < 2; ++which) {
      if (which) __syncthreads();
#pragma unroll
      for (int i = 0; i < 6; ++i) {
        int c = tid + 256 * i, r = c >> 4, q = c & 15;
        *reinterpret_cast<v4u*>(&t[r][q * 8]) = ld[which][i];
      }
      __syncthreads();
#pragma unroll
      for (int i = 0; i < 6; ++i) {
        int c = tid + 256 * i, k3 = c >> 9, tile = (c >> 6) & 7, l = c & 63, fr = l & 15, g = l >> 4;
        const u16* a0 = &t[32 * k3 + 8 * g + (fr >> 2)][16 * tile + 4 * (fr & 3)];
        v2u x = __builtin_bit_cast(v2u, __builtin_amdgcn_ds_read_tr16_b64_v4i16((__attribute__((address_space(3))) s16x4*)a0));
        v2u y = __builtin_bit_cast(v2u, __builtin_amdgcn_ds_read_tr16_b64_v4i16((__attribute__((address_space(3))) s16x4*)(a0 + 4 * 136)));
        __builtin_amdgcn_raw_buffer_store_b128((v4u){x.x, x.y, y.x, y.y}, mkr(p.WP, 2u * HD * DM * 2u), (unsigned)(((h * 16 + (tile >> 1) * 4 + which * 2 + (tile & 1)) * 18 + 3 * kq + k3) * 64 + l) * 16u, 0, WAUX);
      }
    }
    return;
  }
  bid -= PRE_PW;
  {
    const v4u ones = {0xFFFFFFFFu, 0xFFFFFFFFu, 0xFFFFFFFFu, 0xFFFFFFFFu};
    for (unsigned i = bid * 256 + tid; i < WIN_USED_CH * CH / 16u; i += PRE_FILL * 256) {
      if (i >= PT_CH * CH / 16u && (i & (8u * CH / 16u))) continue;
      v4u v = ones;
      const unsigned c = ((i >> 10) & 1u) * 512u + (i & 511u);
      if (i < 2u * NREP * CH / 16u && c < 576u) {
        const unsigned* s = (const unsigned*)&p.mem0[(2 * ((c >> 2) & 3) + (c & 1)) * DM + (c >> 4) * 16 + (c & 2)];
        v = (v4u){nosent(s[0]), nosent(s[2]), nosent(s[4]), nosent(s[6])};
      }
      STW(p.win, WIN_BYTES, i, v);
    }
    if (bid < 2) STW(p.win, WIN_BYTES, CTL_CH * CH / 16u + bid * 512 + tid, ((v4u){0u, 0u, 0u, 0u}));
    for (unsigned i = bid * 256 + tid; i < DM * 128; i += PRE_FILL * 256) STW(p.Y, DM * YS, (i >> 7) * (YS / 16) + (i & 127), ones);
    for (unsigned i = bid * 256 + tid; i < 12 * SEGB / 16; i += PRE_FILL * 256) STW(p.H, 2u * NSEG * SEGB, i, ones);
    if (bid * 256 + tid < 2 * SEGB / 16) STW(p.H, 2u * NSEG * SEGB, NSEG * SEGB / 16 + bid * 256 + tid, ones);
  }
}

extern "C" __device__ __attribute__((used, noinline)) void code_end() {}

extern "C" void kernel_launch(void* const* d_in, const int* in_sizes, int n_in, void* d_out, int out_size, void* d_ws,
                              size_t ws_size, hipStream_t stream) {
  const u16* A_q = (const u16*)d_in[0];
  const u16* A_k = (const u16*)d_in[1];
  const u16* A_v = (const u16*)d_in[2];
  const u16* A_shift = (const u16*)d_in[3];
  const u16* A_ssmk = (const u16*)d_in[4];
  const u16* A_ssmD = (const u16*)d_in[5];
  const u16* B_mem0 = (const u16*)d_in[7];
  const u16* B_Wq = (const u16*)d_in[8];
  const u16* B_Wk = (const u16*)d_in[9];
  const u16* B_Wv = (const u16*)d_in[10];
  const u16* B_Wo = (const u16*)d_in[11];

  char* ws = (char*)(((uintptr_t)d_ws + (WIN_BYTES - 1)) & ~(uintptr_t)(WIN_BYTES - 1));
  size_t off = 0;
  auto alloc = [&](size_t bytes) { char* q = ws + off; off += (bytes + 4095) & ~(size_t)4095; return q; };
  unsigned* win = (unsigned*)alloc(WIN_BYTES);
  unsigned* ct = (unsigned*)alloc(CT_TOTAL * 4);
  u16* Y = (u16*)alloc((size_t)DM * YS);
  u16* H = (u16*)alloc((size_t)2 * NSEG * SEGB);
  u16* OT = (u16*)alloc((size_t)2 * NSEG * SEGB);
  u16* WP = (u16*)alloc((size_t)2 * HD * DM * 2);
  u16* BP = (u16*)alloc((size_t)NH * DM * DM * 2);
  u16* kvT = (u16*)alloc((size_t)DM * S_LEN * 2);

  PreP pp;
  pp.kk = A_k; pp.vv = A_v; pp.shift_k = A_shift; pp.kvT = kvT;
  pp.Wq = B_Wq; pp.Wk = B_Wk; pp.Wv = B_Wv; pp.Wo = B_Wo; pp.WP = WP; pp.BP = BP;
  pp.mem0 = B_mem0; pp.win = (v4u*)win; pp.Y = (v4u*)Y; pp.H = (v4u*)H; pp.ct = ct;
  k_pre<<<1 + PRE_BG + PRE_KV + PRE_PW + PRE_FILL, 256, 0, stream>>>(pp);
  MP mp;
  mp.WP = WP; mp.BP = BP; mp.x0 = A_q; mp.kvT = kvT; mp.ssm_k = A_ssmk; mp.ssm_D = A_ssmD; mp.mem0 = B_mem0;
  mp.out = (u16*)d_out; mp.win = win; mp.ct = ct; mp.Y = Y; mp.H = H; mp.OT = OT;
  const int smem_bytes = L_TOTAL;
  (void)hipFuncSetAttribute((const void*)k_main, hipFuncAttributeMaxDynamicSharedMemorySize, smem_bytes);
  k_main<<<256, 256, smem_bytes, stream>>>(mp);
}
```

```cpp
#include <hip/hip_runtime.h>
#include <hip/hip_bf16.h>
#include <stdint.h>

typedef unsigned short u16;
using bf16x8 = __attribute__((ext_vector_type(8))) short;
using s16x4 = __attribute__((ext_vector_type(4))) short;
using f32x4 = __attribute__((ext_vector_type(4))) float;
using f32x2 = __attribute__((ext_vector_type(2))) float;
using f32x16 = __attribute__((ext_vector_type(16))) float;
using bfx2 = __attribute__((ext_vector_type(2))) __bf16;
typedef unsigned v4u __attribute__((ext_vector_type(4)));
typedef unsigned v2u __attribute__((ext_vector_type(2)));

#define S_LEN 2048
#define DM 576
#define NH 32
#define DH 128
#define HD (NH * DH)
#define SEG 32
#define NSEG 64
#define RGB 9216u
#define SEGB (4u * RGB)

__device__ __forceinline__ unsigned pk2(float a, float b) { f32x2 v = {a, b}; bfx2 r = __builtin_convertvector(v, bfx2); return __builtin_bit_cast(unsigned, r); }
__device__ __forceinline__ float lo2f(unsigned u) { return __uint_as_float(u << 16); }
__device__ __forceinline__ float hi2f(unsigned u) { return __uint_as_float(u & 0xFFFF0000u); }
__device__ __forceinline__ unsigned nosent(unsigned u) { return u < 0xFFFFFFFEu ? u : 0xFFFFFFFEu; }
__device__ __forceinline__ bf16x8 asb8(v4u v) { return __builtin_bit_cast(bf16x8, v); }
#define MFMA32(a, b, c) __builtin_amdgcn_mfma_f32_16x16x32_bf16(asb8(a), asb8(b), c, 0, 0, 0)

typedef __amdgpu_buffer_rsrc_t rsrc_t;
__device__ __forceinline__ rsrc_t mkr(const void* p, unsigned bytes) { return __builtin_amdgcn_make_buffer_rsrc((void*)p, 0, bytes, 0x00020000); }
__device__ __forceinline__ void st16(rsrc_t r, unsigned vo, unsigned so, v4u v) {
  asm volatile("" : "+v"(v));
  __builtin_amdgcn_raw_buffer_store_b128(v, r, vo, so, 16);
  asm volatile("s_nop 1" :: "v"(v));
}
__device__ __forceinline__ void st4(rsrc_t r, unsigned vo, unsigned so, unsigned v) { __builtin_amdgcn_raw_buffer_store_b32(v, r, vo, so, 16); }
__device__ __forceinline__ v4u ld16(rsrc_t r, unsigned vo, unsigned so) { return __builtin_amdgcn_raw_buffer_load_b128(r, vo, so, 16); }
__device__ __forceinline__ unsigned ld4(rsrc_t r, unsigned vo, unsigned so) { return __builtin_amdgcn_raw_buffer_load_b32(r, vo, so, 16); }

#define WIN_BYTES (36u << 20)
#define CH 16384u
#define NREP 4
#define NPR 4
#define MEM_CH 0
#define PM_CH 32
#define PT_CH 128
#define VM_CH 144
#define VT_CH 256
#define CTL_CH 64
#define CAL_CH 127
#define CT_TICK 0
#define CT_TMO 256
#define CT_FLIP 352
#define CT_YC 384
#define CT_TOTAL 640
#define YS 4352u
#define SENT 0xFFFFFFFFu
#define SPIN_CAP 400000u

struct MP {
  const u16* WP; const u16* VP; const u16* x0; const u16* kvT; const u16* ssm_k; const u16* ssm_D; const u16* mem0;
  u16* out; unsigned* win; unsigned* ct; u16* Y; u16* H; u16* OT;
};

#define BAR() do { asm volatile("s_waitcnt lgkmcnt(0)\n\ts_barrier" ::: "memory"); } while (0)
#define VM0() __builtin_amdgcn_s_waitcnt(0x0F70)
#define PIN() __builtin_amdgcn_sched_barrier(0)
__device__ __forceinline__ unsigned umax3(unsigned a, unsigned b, unsigned c) { return __builtin_elementwise_max(__builtin_elementwise_max(a, b), c); }
__device__ __forceinline__ unsigned mx4(unsigned m, v4u v) { return umax3(umax3(m, v.x, v.y), v.z, v.w); }
__device__ __forceinline__ unsigned phys(unsigned o) { return (o >> 13) * CH + (o & 8191u); }
#define AOWN() asm volatile("" ::: "a0", "a1", "a2", "a3", "a4", "a5", "a6", "a7", "a8", "a9", "a10", "a11", "a12", "a13", "a14", "a15", "a16", "a17", "a18", "a19", "a20", "a21", "a22", "a23", "a24", "a25", "a26", "a27", "a28", "a29", "a30", "a31", "a32", "a33", "a34", "a35", "a36", "a37", "a38", "a39", "a40", "a41", "a42", "a43", "a44", "a45", "a46", "a47", "a48", "a49", "a50", "a51", "a52", "a53", "a54", "a55", "a56", "a57", "a58", "a59", "a60", "a61", "a62", "a63", "a64", "a65", "a66", "a67", "a68", "a69", "a70", "a71", "a72", "a73", "a74", "a75", "a76", "a77", "a78", "a79", "a80", "a81", "a82", "a83", "a84", "a85", "a86", "a87", "a88", "a89", "a90", "a91", "a92", "a93", "a94", "a95", "a96", "a97", "a98", "a99", "a100", "a101", "a102", "a103", "a104", "a105", "a106", "a107", "a108", "a109", "a110", "a111", "a112", "a113", "a114", "a115", "a116", "a117", "a118", "a119", "a120", "a121", "a122", "a123", "a124", "a125", "a126", "a127", "a128", "a129", "a130", "a131", "a132", "a133", "a134", "a135", "a136", "a137", "a138", "a139", "a140", "a141", "a142", "a143", "a144", "a145", "a146", "a147", "a148", "a149", "a150", "a151", "a152", "a153", "a154", "a155", "a156", "a157", "a158", "a159", "a160", "a161", "a162", "a163", "a164", "a165", "a166", "a167", "a168", "a169", "a170", "a171", "a172", "a173", "a174", "a175", "a176", "a177", "a178", "a179", "a180", "a181", "a182", "a183", "a184", "a185", "a186", "a187", "a188", "a189", "a190", "a191", "a192", "a193", "a194", "a195", "a196", "a197", "a198", "a199", "a200", "a201", "a202", "a203", "a204", "a205", "a206", "a207", "a208", "a209", "a210", "a211", "a212", "a213", "a214", "a215", "a216", "a217", "a218", "a219", "a220", "a221", "a222", "a223", "a224", "a225", "a226", "a227", "a228", "a229", "a230", "a231", "a232", "a233", "a234", "a235", "a236", "a237", "a238", "a239", "a240", "a241", "a242", "a243", "a244", "a245", "a246", "a247", "a248", "a249", "a250", "a251", "a252", "a253", "a254", "a255")
template <int N> struct IC { static constexpr int v = N; };
template <int B, int E, typename F> __device__ __forceinline__ void sfor(F&& f) { if constexpr (B < E) { f(IC<B>{}); sfor<B + 1, E>(f); } }

__device__ __forceinline__ bool spin_fail(unsigned& spins, rsrc_t F) {
  if (__builtin_expect((++spins & 1023u) != 0u, 1)) return false;
  if (spins > SPIN_CAP) st4(F, CT_TMO * 4u, 0, 1u);
  VM0();
  return (__builtin_amdgcn_readfirstlane(ld4(F, CT_TMO * 4u, 0)) != 0) | (spins > SPIN_CAP);
}

#define RED8(name, ins) __device__ __forceinline__ float name(float a) { float d, t; asm volatile("s_nop 1\n\t" ins "_dpp %0, %2, %2 row_ror:8 row_mask:0xf bank_mask:0xf\n\t" \
    "v_mov_b32 %1, %0\n\ts_nop 1\n\tv_permlane16_swap_b32 %0, %1\n\t" ins " %0, %0, %1\n\tv_mov_b32 %1, %0\n\ts_nop 1\n\tv_permlane32_swap_b32 %0, %1\n\t" ins " %0, %0, %1" \
    : "=&v"(d), "=&v"(t) : "v"(a)); return d; }
RED8(i_red8add, "v_add_f32")

__device__ __forceinline__ void wait_time(const rsrc_t X, unsigned o, int c) {
  unsigned v = 0;
#pragma nounroll
  for (int i = 0; i < 2000; ++i) {
    asm volatile("" ::: "memory");
    v = __builtin_amdgcn_readfirstlane(ld4(X, o, 0));
    if (v) break;
    __builtin_amdgcn_s_sleep(12);
  }
  v += (unsigned)c;
#pragma nounroll
  while ((unsigned)__builtin_amdgcn_s_memrealtime() - v > 0xFFFFF000u) __builtin_amdgcn_s_sleep(1);
}
__device__ __forceinline__ v4u mkd(const void* p, unsigned bytes) { return (v4u){(unsigned)(uintptr_t)p, (unsigned)((uintptr_t)p >> 32) & 0xFFFFu, bytes, 0x00020000u}; }
typedef __attribute__((address_space(3))) char* lds_ptr;
#define DROP 0x40000000u
#define US(x) __builtin_amdgcn_readfirstlane((int)(x))

#define L_QI 36864
#define L_KT 102400
#define L_VI 36864
#define LO_MEM 0
#define LO_A 12288
#define LO_RM 16384
#define L_ROLE 137728
#define L_TOTAL 137792
__device__ __forceinline__ v2u trd(const char* a) { return __builtin_bit_cast(v2u, __builtin_amdgcn_ds_read_tr16_b64_v4i16((__attribute__((address_space(3))) s16x4*)a)); }
template <int A0>
__device__ __forceinline__ void ld_w(const char* sb, unsigned vo) {
  asm volatile("global_load_dwordx4 a[%c2:%c3], %0, %1\n\tglobal_load_dwordx4 a[%c4:%c5], %0, %1 offset:1024" :: "v"(vo), "s"(sb), "i"(A0), "i"(A0 + 3), "i"(A0 + 4), "i"(A0 + 7) : "memory");
}
template <int G0, int G1>
__device__ __forceinline__ void load_w(const u16* base) {
  unsigned vo = (threadIdx.x >> 6) * (72 * 1024) + (threadIdx.x & 63) * 16;
  asm volatile("" : "+v"(vo));
  AOWN();
  sfor<G0, G1>([&](auto G) { ld_w<G.v * 8>((const char*)base + 2048 * G.v, vo); });
}

#define QK_ASM \
  "s_nop 4\n" \
  "s_cmp_eq_u32 %[grp], 0\n" \
  "s_cbranch_scc1 .Lqk_w0\n" \
  "v_add_u32 v229, 65536, %[wo]\n" \
  "global_load_dwordx4 v[32:35], v229, %[wp] offset:0\n" \
  "global_load_dwordx4 v[36:39], v229, %[wp] offset:1024\n" \
  "global_load_dwordx4 v[40:43], v229, %[wp] offset:2048\n" \
  "global_load_dwordx4 v[44:47], v229, %[wp] offset:3072\n" \
  "v_add_u32 v229, 4096, v229\n" \
  "global_load_dwordx4 v[48:51], v229, %[wp] offset:0\n" \
  "global_load_dwordx4 v[52:55], v229, %[wp] offset:1024\n" \
  "global_load_dwordx4 v[56:59], v229, %[wp] offset:2048\n" \
  "global_load_dwordx4 v[60:63], v229, %[wp] offset:3072\n" \
  "v_add_u32 v229, 4096, v229\n" \
  "s_branch .Lqk_w1\n" \
  ".Lqk_w0:\n" \
  "v_add_u32 v229, 0, %[wo]\n" \
  "global_load_dwordx4 a[0:3], v229, %[wp] offset:0\n" \
  "global_load_dwordx4 a[4:7], v229, %[wp] offset:1024\n" \
  "global_load_dwordx4 a[8:11], v229, %[wp] offset:2048\n" \
  "global_load_dwordx4 a[12:15], v229, %[wp] offset:3072\n" \
  "v_add_u32 v229, 4096, v229\n" \
  "global_load_dwordx4 a[16:19], v229, %[wp] offset:0\n" \
  "global_load_dwordx4 a[20:23], v229, %[wp] offset:1024\n" \
  "global_load_dwordx4 a[24:27], v229, %[wp] offset:2048\n" \
  "global_load_dwordx4 a[28:31], v229, %[wp] offset:3072\n" \
  "v_add_u32 v229, 4096, v229\n" \
  "global_load_dwordx4 a[32:35], v229, %[wp] offset:0\n" \
  "global_load_dwordx4 a[36:39], v229, %[wp] offset:1024\n" \
  "global_load_dwordx4 a[40:43], v229, %[wp] offset:2048\n" \
  "global_load_dwordx4 a[44:47], v229, %[wp] offset:3072\n" \
  "v_add_u32 v229, 4096, v229\n" \
  "global_load_dwordx4 a[48:51], v229, %[wp] offset:0\n" \
  "global_load_dwordx4 a[52:55], v229, %[wp] offset:1024\n" \
  "global_load_dwordx4 a[56:59], v229, %[wp] offset:2048\n" \
  "global_load_dwordx4 a[60:63], v229, %[wp] offset:3072\n" \
  "v_add_u32 v229, 4096, v229\n" \
  "global_load_dwordx4 a[64:67], v229, %[wp] offset:0\n" \
  "global_load_dwordx4 a[68:71], v229, %[wp] offset:1024\n" \
  "global_load_dwordx4 a[72:75], v229, %[wp] offset:2048\n" \
  "global_load_dwordx4 a[76:79], v229, %[wp] offset:3072\n" \
  "v_add_u32 v229, 4096, v229\n" \
  "global_load_dwordx4 a[80:83], v229, %[wp] offset:0\n" \
  "global_load_dwordx4 a[84:87], v229, %[wp] offset:1024\n" \
  "global_load_dwordx4 a[88:91], v229, %[wp] offset:2048\n" \
  "global_load_dwordx4 a[92:95], v229, %[wp] offset:3072\n" \
  "v_add_u32 v229, 4096, v229\n" \
  "global_load_dwordx4 a[96:99], v229, %[wp] offset:0\n" \
  "global_load_dwordx4 a[100:103], v229, %[wp] offset:1024\n" \
  "global_load_dwordx4 a[104:107], v229, %[wp] offset:2048\n" \
  "global_load_dwordx4 a[108:111], v229, %[wp] offset:3072\n" \
  "v_add_u32 v229, 4096, v229\n" \
  "global_load_dwordx4 a[112:115], v229, %[wp] offset:0\n" \
  "global_load_dwordx4 a[116:119], v229, %[wp] offset:1024\n" \
  "global_load_dwordx4 a[120:123], v229, %[wp] offset:2048\n" \
  "global_load_dwordx4 a[124:127], v229, %[wp] offset:3072\n" \
  "v_add_u32 v229, 4096, v229\n" \
  "global_load_dwordx4 a[128:131], v229, %[wp] offset:0\n" \
  "global_load_dwordx4 a[132:135], v229, %[wp] offset:1024\n" \
  "global_load_dwordx4 a[136:139], v229, %[wp] offset:2048\n" \
  "global_load_dwordx4 a[140:143], v229, %[wp] offset:3072\n" \
  "v_add_u32 v229, 4096, v229\n" \
  "global_load_dwordx4 a[144:147], v229, %[wp] offset:0\n" \
  "global_load_dwordx4 a[148:151], v229, %[wp] offset:1024\n" \
  "global_load_dwordx4 a[152:155], v229, %[wp] offset:2048\n" \
  "global_load_dwordx4 a[156:159], v229, %[wp] offset:3072\n" \
  "v_add_u32 v229, 4096, v229\n" \
  "global_load_dwordx4 a[160:163], v229, %[wp] offset:0\n" \
  "global_load_dwordx4 a[164:167], v229, %[wp] offset:1024\n" \
  "global_load_dwordx4 a[168:171], v229, %[wp] offset:2048\n" \
  "global_load_dwordx4 a[172:175], v229, %[wp] offset:3072\n" \
  "v_add_u32 v229, 4096, v229\n" \
  "global_load_dwordx4 a[176:179], v229, %[wp] offset:0\n" \
  "global_load_dwordx4 a[180:183], v229, %[wp] offset:1024\n" \
  "global_load_dwordx4 a[184:187], v229, %[wp] offset:2048\n" \
  "global_load_dwordx4 a[188:191], v229, %[wp] offset:3072\n" \
  "v_add_u32 v229, 4096, v229\n" \
  "global_load_dwordx4 a[192:195], v229, %[wp] offset:0\n" \
  "global_load_dwordx4 a[196:199], v229, %[wp] offset:1024\n" \
  "global_load_dwordx4 a[200:203], v229, %[wp] offset:2048\n" \
  "global_load_dwordx4 a[204:207], v229, %[wp] offset:3072\n" \
  "v_add_u32 v229, 4096, v229\n" \
  "global_load_dwordx4 a[208:211], v229, %[wp] offset:0\n" \
  "global_load_dwordx4 a[212:215], v229, %[wp] offset:1024\n" \
  "global_load_dwordx4 a[216:219], v229, %[wp] offset:2048\n" \
  "global_load_dwordx4 a[220:223], v229, %[wp] offset:3072\n" \
  "v_add_u32 v229, 4096, v229\n" \
  "global_load_dwordx4 a[224:227], v229, %[wp] offset:0\n" \
  "global_load_dwordx4 a[228:231], v229, %[wp] offset:1024\n" \
  "global_load_dwordx4 a[232:235], v229, %[wp] offset:2048\n" \
  "global_load_dwordx4 a[236:239], v229, %[wp] offset:3072\n" \
  "v_add_u32 v229, 4096, v229\n" \
  "global_load_dwordx4 a[240:243], v229, %[wp] offset:0\n" \
  "global_load_dwordx4 a[244:247], v229, %[wp] offset:1024\n" \
  "global_load_dwordx4 a[248:251], v229, %[wp] offset:2048\n" \
  "global_load_dwordx4 a[252:255], v229, %[wp] offset:3072\n" \
  "v_add_u32 v229, 4096, v229\n" \
  "global_load_dwordx4 v[32:35], v229, %[wp] offset:0\n" \
  "global_load_dwordx4 v[36:39], v229, %[wp] offset:1024\n" \
  "global_load_dwordx4 v[40:43], v229, %[wp] offset:2048\n" \
  "global_load_dwordx4 v[44:47], v229, %[wp] offset:3072\n" \
  "v_add_u32 v229, 4096, v229\n" \
  "global_load_dwordx4 v[48:51], v229, %[wp] offset:0\n" \
  "global_load_dwordx4 v[52:55], v229, %[wp] offset:1024\n" \
  "global_load_dwordx4 v[56:59], v229, %[wp] offset:2048\n" \
  "global_load_dwordx4 v[60:63], v229, %[wp] offset:3072\n" \
  "v_add_u32 v229, 4096, v229\n" \
  ".Lqk_w1:\n" \
  "s_mov_b32 s60, -2\n" \
  "s_mov_b32 s70, 0x3db504f3\n" \
  "s_mov_b32 s71, 0x3e0293ee\n" \
  "v_add_u32 v228, 0x1000, %[mo]\n" \
  "v_mov_b32 v230, 0x7fffffff\n" \
  "v_mov_b32 v232, 0xff800000\n" \
  "s_mov_b32 s78, 0\n" \
  "s_mov_b32 s79, 0\n" \
  "s_mov_b32 s80, 0\n" \
  "s_mov_b32 s65, 2.0\n" \
  "s_mov_b32 s81, -1\n" \
  "s_mov_b32 s87, 2.0\n" \
  "s_mov_b32 s93, 0\n" \
  "s_mov_b32 s94, 0\n" \
  "s_lshl_b32 s96, s60, 4\n" \
  "s_add_i32 s96, s96, %[bas]\n" \
  "v_cmp_lt_i32_e64 vcc, %[lim], 0\n" \
  "s_nop 1\n" \
  "v_cndmask_b32 v212, 0, v232, vcc\n" \
  "v_cmp_lt_i32_e64 vcc, %[lim], 1\n" \
  "s_nop 1\n" \
  "v_cndmask_b32 v213, 0, v232, vcc\n" \
  "v_cmp_lt_i32_e64 vcc, %[lim], 2\n" \
  "s_nop 1\n" \
  "v_cndmask_b32 v214, 0, v232, vcc\n" \
  "v_cmp_lt_i32_e64 vcc, %[lim], 3\n" \
  "s_nop 1\n" \
  "v_cndmask_b32 v215, 0, v232, vcc\n" \
  "v_cmp_lt_i32_e64 vcc, %[lim], 16\n" \
  "s_nop 1\n" \
  "v_cndmask_b32 v216, 0, v232, vcc\n" \
  "v_cmp_lt_i32_e64 vcc, %[lim], 17\n" \
  "s_nop 1\n" \
  "v_cndmask_b32 v217, 0, v232, vcc\n" \
  "v_cmp_lt_i32_e64 vcc, %[lim], 18\n" \
  "s_nop 1\n" \
  "v_cndmask_b32 v218, 0, v232, vcc\n" \
  "v_cmp_lt_i32_e64 vcc, %[lim], 19\n" \
  "s_nop 1\n" \
  "v_cndmask_b32 v219, 0, v232, vcc\n" \
  "s_mov_b32 s90, 0\n" \
  "s_waitcnt vmcnt(0)\n" \
  ".Lqk_top:\n" \
  "v_mfma_f32_16x16x32_bf16 v[116:119], v[164:167], a[32:35], v[116:119]\n" \
  "ds_read_b128 v[148:151], %[hA] offset:2048\n" \
  "buffer_store_dwordx2 v[232:233], %[po3], %[X], s65 offen sc1\n" \
  "v_mfma_f32_16x16x32_bf16 v[132:135], v[168:171], a[32:35], v[132:135]\n" \
  "ds_read_b128 v[152:155], %[hA] offset:20480\n" \
  "v_mfma_f32_16x16x32_bf16 v[120:123], v[164:167], a[40:43], v[120:123]\n" \
  "buffer_load_dword v251, %[cpo], %[X], s94 offen sc1\n" \
  "s_xor_b32 s76, s96, 0x2000\n" \
  "v_mfma_f32_16x16x32_bf16 v[136:139], v[168:171], a[40:43], v[136:139]\n" \
  "buffer_load_dword v255, %[cpa], %[X], s76 offen sc1\n" \
  "v_mfma_f32_16x16x32_bf16 v[124:127], a[48:51], v[164:167], v[124:127]\n" \
  "s_add_i32 s67, s60, 2\n" \
  "s_min_i32 s67, s67, 63\n" \
  "s_mul_i32 s61, s67, 36864\n" \
  "v_mfma_f32_16x16x32_bf16 v[140:143], a[48:51], v[168:171], v[140:143]\n" \
  "s_lshl_b32 s67, s60, 16\n" \
  "s_and_b32 s67, s67, 0x10000\n" \
  "s_add_i32 s62, %[hl0], s67\n" \
  "v_mfma_f32_16x16x32_bf16 v[128:131], a[56:59], v[164:167], v[128:131]\n" \
  "s_add_i32 m0, s62, 0\n" \
  "v_mfma_f32_16x16x32_bf16 v[144:147], a[56:59], v[168:171], v[144:147]\n" \
  "s_add_i32 s68, s60, 1\n" \
  "s_lshl_b32 s73, s68, 17\n" \
  "s_and_b32 s73, s73, 0x60000\n" \
  "v_mfma_f32_16x16x32_bf16 v[116:119], v[172:175], a[36:39], v[116:119]\n" \
  "ds_read_b128 v[156:159], %[hA] offset:2560\n" \
  "buffer_load_dword v250, off, %[X], s96 offset:16 sc1\n" \
  "v_mfma_f32_16x16x32_bf16 v[132:135], v[176:179], a[36:39], v[132:135]\n" \
  "s_add_i32 s63, s87, s73\n" \
  "s_add_i32 s64, s63, 0x4000\n" \
  "ds_read_b128 v[160:163], %[hA] offset:20992\n" \
  "v_mfma_f32_16x16x32_bf16 v[120:123], v[172:175], a[44:47], v[120:123]\n" \
  "buffer_load_dwordx4 %[hvo], %[HS], s61 offen offset:0 sc1 lds\n" \
  "v_mov_b64 v[100:101], 0\n" \
  "v_mfma_f32_16x16x32_bf16 v[136:139], v[176:179], a[44:47], v[136:139]\n" \
  "s_and_b32 s73, s73, 0x20000\n" \
  "s_add_i32 s65, %[pm0], s73\n" \
  "s_lshl_b32 s73, s68, 30\n" \
  "v_mfma_f32_16x16x32_bf16 v[124:127], a[52:55], v[172:175], v[124:127]\n" \
  "s_and_b32 s66, s73, 0x80000000\n" \
  "s_cmp_lt_u32 s68, 64\n" \
  "s_cselect_b32 s65, s65, 2.0\n" \
  "v_mfma_f32_16x16x32_bf16 v[140:143], a[52:55], v[176:179], v[140:143]\n" \
  "s_ashr_i32 s67, s66, 31\n" \
  "v_not_b32 v232, s67\n" \
  "v_mov_b32 v233, s90\n" \
  "v_mfma_f32_16x16x32_bf16 v[128:131], a[60:63], v[172:175], v[128:131]\n" \
  "buffer_load_dwordx4 %[hvo], %[HS], s61 offen offset:1024 sc1 lds\n" \
  "v_mov_b64 v[102:103], 0\n" \
  "v_mfma_f32_16x16x32_bf16 v[144:147], a[60:63], v[176:179], v[144:147]\n" \
  "v_mov_b64 v[104:105], 0\n" \
  "s_waitcnt lgkmcnt(2)\n" \
  "v_mfma_f32_16x16x32_bf16 v[116:119], v[148:151], a[64:67], v[116:119]\n" \
  "ds_read_b128 v[164:167], %[hA] offset:3072\n" \
  "v_mov_b64 v[106:107], 0\n" \
  "v_mfma_f32_16x16x32_bf16 v[132:135], v[152:155], a[64:67], v[132:135]\n" \
  "ds_read_b128 v[168:171], %[hA] offset:21504\n" \
  "v_mov_b64 v[108:109], 0\n" \
  "v_mfma_f32_16x16x32_bf16 v[120:123], v[148:151], a[72:75], v[120:123]\n" \
  "buffer_load_dwordx4 %[hvo], %[HS], s61 offen offset:2048 sc1 lds\n" \
  "v_mov_b64 v[110:111], 0\n" \
  "v_mfma_f32_16x16x32_bf16 v[136:139], v[152:155], a[72:75], v[136:139]\n" \
  "v_mov_b64 v[112:113], 0\n" \
  "v_mov_b64 v[114:115], 0\n" \
  "v_mfma_f32_16x16x32_bf16 v[124:127], a[80:83], v[148:151], v[124:127]\n" \
  "v_mfma_f32_16x16x32_bf16 v[140:143], a[80:83], v[152:155], v[140:143]\n" \
  "v_mfma_f32_16x16x32_bf16 v[128:131], a[88:91], v[148:151], v[128:131]\n" \
  "buffer_load_dwordx4 %[hvo], %[HS], s61 offen offset:3072 sc1 lds\n" \
  "v_mfma_f32_16x16x32_bf16 v[144:147], a[88:91], v[152:155], v[144:147]\n" \
  "s_add_i32 s61, s61, 0x1000\n" \
  "s_add_i32 m0, s62, 4096\n" \
  "s_waitcnt lgkmcnt(2)\n" \
  "v_mfma_f32_16x16x32_bf16 v[116:119], v[156:159], a[68:71], v[116:119]\n" \
  "ds_read_b128 v[172:175], %[hA] offset:3584\n" \
  "v_mfma_f32_16x16x32_bf16 v[132:135], v[160:163], a[68:71], v[132:135]\n" \
  "ds_read_b128 v[176:179], %[hA] offset:22016\n" \
  "v_mfma_f32_16x16x32_bf16 v[120:123], v[156:159], a[76:79], v[120:123]\n" \
  "buffer_load_dwordx4 %[hvo], %[HS], s61 offen offset:0 sc1 lds\n" \
  "v_mfma_f32_16x16x32_bf16 v[136:139], v[160:163], a[76:79], v[136:139]\n" \
  "v_mfma_f32_16x16x32_bf16 v[124:127], a[84:87], v[156:159], v[124:127]\n" \
  "v_mfma_f32_16x16x32_bf16 v[140:143], a[84:87], v[160:163], v[140:143]\n" \
  "v_mfma_f32_16x16x32_bf16 v[128:131], a[92:95], v[156:159], v[128:131]\n" \
  "buffer_load_dwordx4 %[hvo], %[HS], s61 offen offset:1024 sc1 lds\n" \
  "v_mfma_f32_16x16x32_bf16 v[144:147], a[92:95], v[160:163], v[144:147]\n" \
  "s_waitcnt lgkmcnt(2)\n" \
  "v_mfma_f32_16x16x32_bf16 v[116:119], v[164:167], a[96:99], v[116:119]\n" \
  "ds_read_b128 v[148:151], %[hA] offset:4096\n" \
  "v_mfma_f32_16x16x32_bf16 v[132:135], v[168:171], a[96:99], v[132:135]\n" \
  "ds_read_b128 v[152:155], %[hA] offset:22528\n" \
  "v_mfma_f32_16x16x32_bf16 v[120:123], v[164:167], a[104:107], v[120:123]\n" \
  "buffer_load_dwordx4 %[hvo], %[HS], s61 offen offset:2048 sc1 lds\n" \
  "v_mfma_f32_16x16x32_bf16 v[136:139], v[168:171], a[104:107], v[136:139]\n" \
  "v_mfma_f32_16x16x32_bf16 v[124:127], a[112:115], v[164:167], v[124:127]\n" \
  "v_mfma_f32_16x16x32_bf16 v[140:143], a[112:115], v[168:171], v[140:143]\n" \
  "v_mfma_f32_16x16x32_bf16 v[128:131], a[120:123], v[164:167], v[128:131]\n" \
  "buffer_load_dwordx4 %[hvo], %[HS], s61 offen offset:3072 sc1 lds\n" \
  "v_mfma_f32_16x16x32_bf16 v[144:147], a[120:123], v[168:171], v[144:147]\n" \
  "s_add_i32 s61, s61, 0x1000\n" \
  "s_add_i32 m0, s62, 8192\n" \
  "s_waitcnt lgkmcnt(2)\n" \
  "v_mfma_f32_16x16x32_bf16 v[116:119], v[172:175], a[100:103], v[116:119]\n" \
  "ds_read_b128 v[156:159], %[hA] offset:4608\n" \
  "v_mfma_f32_16x16x32_bf16 v[132:135], v[176:179], a[100:103], v[132:135]\n" \
  "ds_read_b128 v[160:163], %[hA] offset:23040\n" \
  "v_mfma_f32_16x16x32_bf16 v[120:123], v[172:175], a[108:111], v[120:123]\n" \
  "buffer_load_dwordx4 %[hvo], %[HS], s61 offen offset:0 sc1 lds\n" \
  "v_mfma_f32_16x16x32_bf16 v[136:139], v[176:179], a[108:111], v[136:139]\n" \
  "v_mfma_f32_16x16x32_bf16 v[124:127], a[116:119], v[172:175], v[124:127]\n" \
  "v_mfma_f32_16x16x32_bf16 v[140:143], a[116:119], v[176:179], v[140:143]\n" \
  "buffer_store_dword v232, %[pcl], %[X], s65 offen sc1\n" \
  "v_mfma_f32_16x16x32_bf16 v[128:131], a[124:127], v[172:175], v[128:131]\n" \
  "v_mfma_f32_16x16x32_bf16 v[144:147], a[124:127], v[176:179], v[144:147]\n" \
  "buffer_atomic_umax v233, %[rpo], %[X], s93 offen sc1\n" \
  "s_waitcnt lgkmcnt(2)\n" \
  "v_mfma_f32_16x16x32_bf16 v[116:119], v[148:151], a[128:131], v[116:119]\n" \
  "ds_read_b128 v[164:167], %[hA] offset:5120\n" \
  "v_mfma_f32_16x16x32_bf16 v[132:135], v[152:155], a[128:131], v[132:135]\n" \
  "ds_read_b128 v[168:171], %[hA] offset:23552\n" \
  "v_mfma_f32_16x16x32_bf16 v[120:123], v[148:151], a[136:139], v[120:123]\n" \
  "v_mfma_f32_16x16x32_bf16 v[136:139], v[152:155], a[136:139], v[136:139]\n" \
  "v_mfma_f32_16x16x32_bf16 v[124:127], a[144:147], v[148:151], v[124:127]\n" \
  "v_mfma_f32_16x16x32_bf16 v[140:143], a[144:147], v[152:155], v[140:143]\n" \
  "v_mfma_f32_16x16x32_bf16 v[128:131], a[152:155], v[148:151], v[128:131]\n" \
  "v_mfma_f32_16x16x32_bf16 v[144:147], a[152:155], v[152:155], v[144:147]\n" \
  "s_waitcnt lgkmcnt(2)\n" \
  "v_mfma_f32_16x16x32_bf16 v[116:119], v[156:159], a[132:135], v[116:119]\n" \
  "ds_read_b128 v[172:175], %[hA] offset:5632\n" \
  "v_mfma_f32_16x16x32_bf16 v[132:135], v[160:163], a[132:135], v[132:135]\n" \
  "ds_read_b128 v[176:179], %[hA] offset:24064\n" \
  "v_mfma_f32_16x16x32_bf16 v[120:123], v[156:159], a[140:143], v[120:123]\n" \
  "v_mfma_f32_16x16x32_bf16 v[136:139], v[160:163], a[140:143], v[136:139]\n" \
  "v_mfma_f32_16x16x32_bf16 v[124:127], a[148:151], v[156:159], v[124:127]\n" \
  "s_waitcnt vmcnt(11)\n" \
  "v_readfirstlane_b32 s72, v250\n" \
  "v_mfma_f32_16x16x32_bf16 v[140:143], a[148:151], v[160:163], v[140:143]\n" \
  "v_max_u32_dpp v251, v251, v251 quad_perm:[1,0,3,2] row_mask:0xf bank_mask:0xf\n" \
  "v_mfma_f32_16x16x32_bf16 v[128:131], a[156:159], v[156:159], v[128:131]\n" \
  "v_mfma_f32_16x16x32_bf16 v[144:147], a[156:159], v[160:163], v[144:147]\n" \
  "v_max_u32_dpp v251, v251, v251 quad_perm:[2,3,0,1] row_mask:0xf bank_mask:0xf\n" \
  "s_waitcnt lgkmcnt(2)\n" \
  "v_mfma_f32_16x16x32_bf16 v[116:119], v[164:167], a[160:163], v[116:119]\n" \
  "ds_read_b128 v[148:151], %[hA] offset:6144\n" \
  "v_mfma_f32_16x16x32_bf16 v[132:135], v[168:171], a[160:163], v[132:135]\n" \
  "ds_read_b128 v[152:155], %[hA] offset:24576\n" \
  "v_max_u32_dpp v251, v251, v251 row_half_mirror row_mask:0xf bank_mask:0xf\n" \
  "v_mfma_f32_16x16x32_bf16 v[120:123], v[164:167], a[168:171], v[120:123]\n" \
  "v_mfma_f32_16x16x32_bf16 v[136:139], v[168:171], a[168:171], v[136:139]\n" \
  "v_readfirstlane_b32 s92, v251\n" \
  "v_readfirstlane_b32 s73, v255\n" \
  "v_mfma_f32_16x16x32_bf16 v[124:127], a[176:179], v[164:167], v[124:127]\n" \
  "s_sub_i32 s92, s92, s95\n" \
  "s_add_i32 s94, s92, 1\n" \
  "s_ashr_i32 s94, s94, 2\n" \
  "v_mfma_f32_16x16x32_bf16 v[140:143], a[176:179], v[168:171], v[140:143]\n" \
  "s_max_i32 s92, s92, s94\n" \
  "s_max_i32 s92, s92, %[dn]\n" \
  "s_min_i32 s92, s92, %[up]\n" \
  "v_mfma_f32_16x16x32_bf16 v[128:131], a[184:187], v[164:167], v[128:131]\n" \
  "s_cmp_lt_i32 s60, s88\n" \
  "s_cselect_b32 s92, 0, s92\n" \
  "s_add_i32 s95, s95, s92\n" \
  "v_mfma_f32_16x16x32_bf16 v[144:147], a[184:187], v[168:171], v[144:147]\n" \
  "s_waitcnt lgkmcnt(2)\n" \
  "v_mfma_f32_16x16x32_bf16 v[116:119], v[172:175], a[164:167], v[116:119]\n" \
  "ds_read_b128 v[156:159], %[hA] offset:6656\n" \
  "v_mfma_f32_16x16x32_bf16 v[132:135], v[176:179], a[164:167], v[132:135]\n" \
  "ds_read_b128 v[160:163], %[hA] offset:25088\n" \
  "v_mfma_f32_16x16x32_bf16 v[120:123], v[172:175], a[172:175], v[120:123]\n" \
  "s_mul_i32 s76, s95, 209\n" \
  "s_lshr_b32 s76, s76, 8\n" \
  "s_add_i32 s76, s76, %[c2]\n" \
  "v_mfma_f32_16x16x32_bf16 v[136:139], v[176:179], a[172:175], v[136:139]\n" \
  "s_add_i32 s92, s95, %[mrg]\n" \
  "s_max_i32 s92, s92, s76\n" \
  "s_add_i32 s94, s72, s92\n" \
  "v_mfma_f32_16x16x32_bf16 v[124:127], a[180:183], v[172:175], v[124:127]\n" \
  "s_sub_i32 s76, s92, s76\n" \
  "s_lshr_b32 s76, s76, 1\n" \
  "s_add_i32 s76, s76, %[c4]\n" \
  "v_mfma_f32_16x16x32_bf16 v[140:143], a[180:183], v[176:179], v[140:143]\n" \
  "s_mul_i32 s92, s95, 135\n" \
  "s_lshr_b32 s92, s92, 8\n" \
  "s_add_i32 s76, s76, s92\n" \
  "v_mfma_f32_16x16x32_bf16 v[128:131], a[188:191], v[172:175], v[128:131]\n" \
  "s_add_i32 s92, s73, %[lag]\n" \
  "s_sub_i32 s92, s92, s94\n" \
  "s_max_i32 s92, s92, 0\n" \
  "v_mfma_f32_16x16x32_bf16 v[144:147], a[188:191], v[176:179], v[144:147]\n" \
  "s_cmp_lt_i32 s60, 59\n" \
  "s_cselect_b32 s77, %[gg], 0\n" \
  "s_waitcnt lgkmcnt(2)\n" \
  "v_mfma_f32_16x16x32_bf16 v[116:119], v[148:151], a[192:195], v[116:119]\n" \
  "ds_read_b128 v[164:167], %[hA] offset:7168\n" \
  "v_mfma_f32_16x16x32_bf16 v[132:135], v[152:155], a[192:195], v[132:135]\n" \
  "ds_read_b128 v[168:171], %[hA] offset:25600\n" \
  "v_mfma_f32_16x16x32_bf16 v[120:123], v[148:151], a[200:203], v[120:123]\n" \
  "s_cmp_lg_u32 s73, 0\n" \
  "s_cselect_b32 s92, s92, s77\n" \
  "s_add_i32 s86, s94, s92\n" \
  "v_mfma_f32_16x16x32_bf16 v[136:139], v[152:155], a[200:203], v[136:139]\n" \
  "s_add_i32 s76, s76, s86\n" \
  "v_or_b32_e64 v252, s86, 1\n" \
  "v_mov_b32 v253, s76\n" \
  "v_mfma_f32_16x16x32_bf16 v[124:127], a[208:211], v[148:151], v[124:127]\n" \
  "s_cmp_lt_i32 s60, 0\n" \
  "s_cselect_b32 s73, 2.0, s96\n" \
  "v_mfma_f32_16x16x32_bf16 v[140:143], a[208:211], v[152:155], v[140:143]\n" \
  "v_mfma_f32_16x16x32_bf16 v[128:131], a[216:219], v[148:151], v[128:131]\n" \
  "v_mfma_f32_16x16x32_bf16 v[144:147], a[216:219], v[152:155], v[144:147]\n" \
  "s_waitcnt lgkmcnt(2)\n" \
  "v_mfma_f32_16x16x32_bf16 v[116:119], v[156:159], a[196:199], v[116:119]\n" \
  "ds_read_b128 v[172:175], %[hA] offset:7680\n" \
  "v_mfma_f32_16x16x32_bf16 v[132:135], v[160:163], a[196:199], v[132:135]\n" \
  "ds_read_b128 v[176:179], %[hA] offset:26112\n" \
  "v_mfma_f32_16x16x32_bf16 v[120:123], v[156:159], a[204:207], v[120:123]\n" \
  "ds_read_b128 v[148:151], %[hA] offset:8192\n" \
  "v_mfma_f32_16x16x32_bf16 v[136:139], v[160:163], a[204:207], v[136:139]\n" \
  "ds_read_b128 v[152:155], %[hA] offset:26624\n" \
  "v_mfma_f32_16x16x32_bf16 v[124:127], a[212:215], v[156:159], v[124:127]\n" \
  "v_mfma_f32_16x16x32_bf16 v[140:143], a[212:215], v[160:163], v[140:143]\n" \
  "v_mfma_f32_16x16x32_bf16 v[128:131], a[220:223], v[156:159], v[128:131]\n" \
  "v_mfma_f32_16x16x32_bf16 v[144:147], a[220:223], v[160:163], v[144:147]\n" \
  "s_memrealtime s[74:75]\n" \
  "s_waitcnt lgkmcnt(4)\n" \
  "v_mfma_f32_16x16x32_bf16 v[124:127], a[240:243], v[164:167], v[124:127]\n" \
  "ds_read_b128 v[156:159], %[hA] offset:8704\n" \
  "v_mfma_f32_16x16x32_bf16 v[140:143], a[240:243], v[168:171], v[140:143]\n" \
  "ds_read_b128 v[160:163], %[hA] offset:27136\n" \
  "v_mfma_f32_16x16x32_bf16 v[128:131], a[248:251], v[164:167], v[128:131]\n" \
  "v_mfma_f32_16x16x32_bf16 v[144:147], a[248:251], v[168:171], v[144:147]\n" \
  "s_waitcnt lgkmcnt(0)\n" \
  "s_sub_i32 s91, s74, s72\n" \
  "s_cmp_gt_u32 s91, 14\n" \
  "v_mfma_f32_16x16x32_bf16 v[124:127], a[244:247], v[172:175], v[124:127]\n" \
  "s_cbranch_scc1 .Lqk_late\n" \
  ".Lqk_al_ok:\n" \
  "v_mfma_f32_16x16x32_bf16 v[140:143], a[244:247], v[176:179], v[140:143]\n" \
  "buffer_load_dwordx4 v[64:67], %[mo], %[X], s63 offen offset:0 sc1\n" \
  "v_mfma_f32_16x16x32_bf16 v[128:131], a[252:255], v[172:175], v[128:131]\n" \
  "v_mfma_f32_16x16x32_bf16 v[144:147], a[252:255], v[176:179], v[144:147]\n" \
  "v_mfma_f32_16x16x32_bf16 v[124:127], v[48:51], v[148:151], v[124:127]\n" \
  "buffer_load_dwordx4 v[68:71], %[mo], %[X], s63 offen offset:1024 sc1\n" \
  "v_mfma_f32_16x16x32_bf16 v[140:143], v[48:51], v[152:155], v[140:143]\n" \
  "v_mfma_f32_16x16x32_bf16 v[128:131], v[56:59], v[148:151], v[128:131]\n" \
  "v_mfma_f32_16x16x32_bf16 v[144:147], v[56:59], v[152:155], v[144:147]\n" \
  "buffer_load_dwordx4 v[72:75], %[mo], %[X], s63 offen offset:2048 sc1\n" \
  "v_mfma_f32_16x16x32_bf16 v[124:127], v[52:55], v[156:159], v[124:127]\n" \
  "v_mfma_f32_16x16x32_bf16 v[140:143], v[52:55], v[160:163], v[140:143]\n" \
  "v_mfma_f32_16x16x32_bf16 v[128:131], v[60:63], v[156:159], v[128:131]\n" \
  "buffer_load_dwordx4 v[76:79], %[mo], %[X], s63 offen offset:3072 sc1\n" \
  "v_mfma_f32_16x16x32_bf16 v[144:147], v[60:63], v[160:163], v[144:147]\n" \
  "v_mfma_f32_16x16x32_bf16 v[116:119], v[164:167], a[224:227], v[116:119]\n" \
  "v_mfma_f32_16x16x32_bf16 v[132:135], v[168:171], a[224:227], v[132:135]\n" \
  "buffer_load_dwordx4 v[80:83], v228, %[X], s63 offen offset:0 sc1\n" \
  "v_mfma_f32_16x16x32_bf16 v[120:123], v[164:167], a[232:235], v[120:123]\n" \
  "v_cvt_pk_bf16_f32 v220, v124, v125\n" \
  "v_cvt_pk_bf16_f32 v221, v126, v127\n" \
  "v_mfma_f32_16x16x32_bf16 v[136:139], v[168:171], a[232:235], v[136:139]\n" \
  "v_cvt_pk_bf16_f32 v222, v128, v129\n" \
  "v_cvt_pk_bf16_f32 v223, v130, v131\n" \
  "v_mfma_f32_16x16x32_bf16 v[116:119], v[172:175], a[228:231], v[116:119]\n" \
  "buffer_load_dwordx4 v[84:87], v228, %[X], s63 offen offset:1024 sc1\n" \
  "v_cvt_pk_bf16_f32 v224, v140, v141\n" \
  "v_mfma_f32_16x16x32_bf16 v[132:135], v[176:179], a[228:231], v[132:135]\n" \
  "ds_write_b128 %[ktW], v[220:223] offset:0\n" \
  "v_mfma_f32_16x16x32_bf16 v[120:123], v[172:175], a[236:239], v[120:123]\n" \
  "v_cvt_pk_bf16_f32 v225, v142, v143\n" \
  "v_cvt_pk_bf16_f32 v226, v144, v145\n" \
  "v_mfma_f32_16x16x32_bf16 v[136:139], v[176:179], a[236:239], v[136:139]\n" \
  "buffer_load_dwordx4 v[88:91], v228, %[X], s63 offen offset:2048 sc1\n" \
  "v_cvt_pk_bf16_f32 v227, v146, v147\n" \
  "v_mfma_f32_16x16x32_bf16 v[116:119], v[148:151], v[32:35], v[116:119]\n" \
  "ds_write_b128 %[ktW], v[224:227] offset:4096\n" \
  "v_mfma_f32_16x16x32_bf16 v[132:135], v[152:155], v[32:35], v[132:135]\n" \
  "v_add_f32 v231, v124, v140\n" \
  "v_xor_b32 %[hA], 0x10000, %[hA]\n" \
  "v_mfma_f32_16x16x32_bf16 v[120:123], v[148:151], v[40:43], v[120:123]\n" \
  "buffer_load_dwordx4 v[92:95], v228, %[X], s63 offen offset:3072 sc1\n" \
  "v_mfma_f32_16x16x32_bf16 v[136:139], v[152:155], v[40:43], v[136:139]\n" \
  "v_mfma_f32_16x16x32_bf16 v[116:119], v[156:159], v[36:39], v[116:119]\n" \
  "s_waitcnt vmcnt(10)\n" \
  "s_waitcnt lgkmcnt(0)\n" \
  "s_barrier\n" \
  "v_mfma_f32_16x16x32_bf16 v[132:135], v[160:163], v[36:39], v[132:135]\n" \
  "buffer_load_dwordx4 v[96:99], %[mo], %[X], s64 offen offset:0 sc1\n" \
  "ds_read_b128 v[180:183], %[ktR] offset:0\n" \
  "v_mfma_f32_16x16x32_bf16 v[120:123], v[156:159], v[44:47], v[120:123]\n" \
  "ds_read_b128 v[184:187], %[ktR] offset:1024\n" \
  "v_mfma_f32_16x16x32_bf16 v[136:139], v[160:163], v[44:47], v[136:139]\n" \
  "ds_read_b128 v[188:191], %[ktR] offset:2048\n" \
  ".Lqk_m:\n" \
  "s_waitcnt vmcnt(8)\n" \
  "v_smfmac_f32_16x16x64_bf16 v[100:103], v[64:67], a[0:7], %[idx]\n" \
  "ds_read_b128 v[192:195], %[ktR] offset:3072\n" \
  "v_smfmac_f32_16x16x64_bf16 v[104:107], v[64:67], a[8:15], %[idx]\n" \
  "ds_read_b128 v[196:199], %[ktR] offset:4096\n" \
  "v_smfmac_f32_16x16x64_bf16 v[108:111], v[64:67], a[16:23], %[idx]\n" \
  "ds_read_b128 v[200:203], %[ktR] offset:5120\n" \
  "v_cvt_pk_bf16_f32 v220, v116, v117\n" \
  "v_smfmac_f32_16x16x64_bf16 v[112:115], v[64:67], a[24:31], %[idx]\n" \
  "ds_read_b128 v[204:207], %[ktR] offset:6144\n" \
  "s_waitcnt vmcnt(7)\n" \
  "v_smfmac_f32_16x16x64_bf16 v[100:103], v[68:71], a[32:39], %[idx]\n" \
  "ds_read_b128 v[208:211], %[ktR] offset:7168\n" \
  "v_cvt_pk_bf16_f32 v221, v118, v119\n" \
  "v_smfmac_f32_16x16x64_bf16 v[104:107], v[68:71], a[40:47], %[idx]\n" \
  "ds_read_b128 v[164:167], %[hA] offset:1024\n" \
  "v_cvt_pk_bf16_f32 v222, v120, v121\n" \
  "v_smfmac_f32_16x16x64_bf16 v[108:111], v[68:71], a[48:55], %[idx]\n" \
  "ds_read_b128 v[168:171], %[hA] offset:19456\n" \
  "v_cvt_pk_bf16_f32 v223, v122, v123\n" \
  "v_smfmac_f32_16x16x64_bf16 v[112:115], v[68:71], a[56:63], %[idx]\n" \
  "ds_read_b128 v[172:175], %[hA] offset:1536\n" \
  "s_waitcnt vmcnt(6)\n" \
  "v_smfmac_f32_16x16x64_bf16 v[100:103], v[72:75], a[64:71], %[idx]\n" \
  "ds_read_b128 v[176:179], %[hA] offset:19968\n" \
  "v_cvt_pk_bf16_f32 v224, v132, v133\n" \
  "v_smfmac_f32_16x16x64_bf16 v[104:107], v[72:75], a[72:79], %[idx]\n" \
  "ds_read_b128 v[148:151], %[hA] offset:0\n" \
  "v_cvt_pk_bf16_f32 v225, v134, v135\n" \
  "v_smfmac_f32_16x16x64_bf16 v[108:111], v[72:75], a[80:87], %[idx]\n" \
  "ds_read_b128 v[152:155], %[hA] offset:18432\n" \
  "v_cvt_pk_bf16_f32 v226, v136, v137\n" \
  "v_smfmac_f32_16x16x64_bf16 v[112:115], v[72:75], a[88:95], %[idx]\n" \
  "ds_read_b128 v[156:159], %[hA] offset:512\n" \
  "s_waitcnt vmcnt(5)\n" \
  "v_smfmac_f32_16x16x64_bf16 v[100:103], v[76:79], a[96:103], %[idx]\n" \
  "ds_read_b128 v[160:163], %[hA] offset:18944\n" \
  "v_cvt_pk_bf16_f32 v227, v138, v139\n" \
  "v_smfmac_f32_16x16x64_bf16 v[104:107], v[76:79], a[104:111], %[idx]\n" \
  "ds_write_b64 %[qtW], v[220:221] offset:0\n" \
  "v_smfmac_f32_16x16x64_bf16 v[108:111], v[76:79], a[112:119], %[idx]\n" \
  "ds_write_b64 %[qtW], v[222:223] offset:2816\n" \
  "v_smfmac_f32_16x16x64_bf16 v[112:115], v[76:79], a[120:127], %[idx]\n" \
  "s_waitcnt vmcnt(4)\n" \
  "v_smfmac_f32_16x16x64_bf16 v[100:103], v[80:83], a[128:135], %[idx]\n" \
  "ds_write_b64 %[qtW], v[224:225] offset:64\n" \
  "v_smfmac_f32_16x16x64_bf16 v[104:107], v[80:83], a[136:143], %[idx]\n" \
  "ds_write_b64 %[qtW], v[226:227] offset:2880\n" \
  "v_smfmac_f32_16x16x64_bf16 v[108:111], v[80:83], a[144:151], %[idx]\n" \
  "s_add_i32 s60, s60, 1\n" \
  "s_add_i32 s96, s96, 16\n" \
  "s_mov_b32 s94, s93\n" \
  "v_smfmac_f32_16x16x64_bf16 v[112:115], v[80:83], a[152:159], %[idx]\n" \
  "s_waitcnt vmcnt(3)\n" \
  "v_smfmac_f32_16x16x64_bf16 v[100:103], v[84:87], a[160:167], %[idx]\n" \
  "s_sub_i32 s90, s74, s89\n" \
  "s_max_i32 s92, s91, -2\n" \
  "s_add_i32 s89, s72, s92\n" \
  "v_smfmac_f32_16x16x64_bf16 v[104:107], v[84:87], a[168:175], %[idx]\n" \
  "s_ashr_i32 s92, s92, 1\n" \
  "s_add_i32 s90, s90, s92\n" \
  "v_smfmac_f32_16x16x64_bf16 v[108:111], v[84:87], a[176:183], %[idx]\n" \
  "s_mul_i32 s93, s60, 130\n" \
  "s_and_b32 s93, s93, 0xfc\n" \
  "v_smfmac_f32_16x16x64_bf16 v[112:115], v[84:87], a[184:191], %[idx]\n" \
  "s_waitcnt vmcnt(2)\n" \
  "v_smfmac_f32_16x16x64_bf16 v[100:103], v[88:91], a[192:199], %[idx]\n" \
  "v_smfmac_f32_16x16x64_bf16 v[104:107], v[88:91], a[200:207], %[idx]\n" \
  "v_smfmac_f32_16x16x64_bf16 v[108:111], v[88:91], a[208:215], %[idx]\n" \
  "v_smfmac_f32_16x16x64_bf16 v[112:115], v[88:91], a[216:223], %[idx]\n" \
  "s_waitcnt vmcnt(1)\n" \
  "v_smfmac_f32_16x16x64_bf16 v[100:103], v[92:95], a[224:231], %[idx]\n" \
  "v_smfmac_f32_16x16x64_bf16 v[104:107], v[92:95], a[232:239], %[idx]\n" \
  "s_waitcnt vmcnt(0)\n" \
  "v_smfmac_f32_16x16x64_bf16 v[100:103], v[96:99], v[32:39], %[idx]\n" \
  "v_smfmac_f32_16x16x64_bf16 v[104:107], v[96:99], v[40:47], %[idx]\n" \
  "s_nop 1\n" \
  "v_smfmac_f32_16x16x64_bf16 v[108:111], v[92:95], a[240:247], %[idx]\n" \
  "s_nop 1\n" \
  "v_smfmac_f32_16x16x64_bf16 v[112:115], v[92:95], a[248:255], %[idx]\n" \
  "s_waitcnt lgkmcnt(4)\n" \
  "s_nop 0\n" \
  "v_add_f32 v232, v100, v102\n" \
  "v_smfmac_f32_16x16x64_bf16 v[108:111], v[96:99], v[48:55], %[idx]\n" \
  "v_add_f32 v233, v101, v103\n" \
  "v_smfmac_f32_16x16x64_bf16 v[112:115], v[96:99], v[56:63], %[idx]\n" \
  "v_add_f32 v234, v104, v106\n" \
  "v_add_f32 v235, v105, v107\n" \
  "v_cvt_pk_bf16_f32 v236, v232, v233\n" \
  "v_cvt_pk_bf16_f32 v237, v234, v235\n" \
  "ds_write2st64_b32 %[qW], v236, v237 offset1:11\n" \
  "s_nop 0\n" \
  "v_pk_add_f32 v[232:233], v[108:109], v[110:111]\n" \
  "s_nop 0\n" \
  "v_pk_add_f32 v[234:235], v[112:113], v[114:115]\n" \
  "v_cvt_pk_bf16_f32 v236, v232, v233\n" \
  "v_cvt_pk_bf16_f32 v237, v234, v235\n" \
  "ds_write2st64_b32 %[kW], v236, v237 offset1:11\n" \
  "v_fma_f32 v232, v232, v233, v231\n" \
  "v_cmp_u_f32 vcc, v232, v232\n" \
  "v_mfma_f32_16x16x32_bf16 v[116:119], v[148:151], a[0:3], 0\n" \
  "v_mfma_f32_16x16x32_bf16 v[132:135], v[152:155], a[0:3], 0\n" \
  "s_cbranch_vccnz .Lqk_sm\n" \
  ".Lqk_sm_back:\n" \
  "s_waitcnt lgkmcnt(0)\n" \
  "s_barrier\n" \
  "ds_read_b64_tr_b16 v[64:65], %[qR] offset:0\n" \
  "ds_read_b64_tr_b16 v[66:67], %[qR] offset:2816\n" \
  "v_mfma_f32_16x16x32_bf16 v[120:123], v[148:151], a[8:11], 0\n" \
  "ds_read_b64_tr_b16 v[68:69], %[qR] offset:5632\n" \
  "ds_read_b64_tr_b16 v[70:71], %[qR] offset:8448\n" \
  "v_mfma_f32_16x16x32_bf16 v[136:139], v[152:155], a[8:11], 0\n" \
  "ds_read_b64_tr_b16 v[72:73], %[qR] offset:11264\n" \
  "ds_read_b64_tr_b16 v[74:75], %[qR] offset:14080\n" \
  "v_mfma_f32_16x16x32_bf16 v[124:127], a[16:19], v[148:151], 0\n" \
  "ds_read_b64_tr_b16 v[76:77], %[qR] offset:16896\n" \
  "ds_read_b64_tr_b16 v[78:79], %[qR] offset:19712\n" \
  "s_waitcnt lgkmcnt(6)\n" \
  "v_mfma_f32_16x16x32_bf16 v[100:103], v[180:183], v[64:67], v[212:215]\n" \
  "ds_read_b64_tr_b16 v[80:81], %[kR] offset:0\n" \
  "ds_read_b64_tr_b16 v[82:83], %[kR] offset:2816\n" \
  "s_waitcnt lgkmcnt(6)\n" \
  "v_mfma_f32_16x16x32_bf16 v[100:103], v[184:187], v[68:71], v[100:103]\n" \
  "ds_read_b64_tr_b16 v[84:85], %[kR] offset:5632\n" \
  "ds_read_b64_tr_b16 v[86:87], %[kR] offset:8448\n" \
  "s_waitcnt lgkmcnt(6)\n" \
  "v_mfma_f32_16x16x32_bf16 v[100:103], v[188:191], v[72:75], v[100:103]\n" \
  "ds_read_b64_tr_b16 v[88:89], %[kR] offset:11264\n" \
  "ds_read_b64_tr_b16 v[90:91], %[kR] offset:14080\n" \
  "s_waitcnt lgkmcnt(6)\n" \
  "v_mfma_f32_16x16x32_bf16 v[100:103], v[192:195], v[76:79], v[100:103]\n" \
  "ds_read_b64_tr_b16 v[92:93], %[kR] offset:16896\n" \
  "ds_read_b64_tr_b16 v[94:95], %[kR] offset:19712\n" \
  "v_mfma_f32_16x16x32_bf16 v[104:107], v[196:199], v[64:67], v[216:219]\n" \
  "buffer_store_dwordx2 v[252:253], %[cpw], %[X], s73 offen offset:32 sc1\n" \
  "v_mfma_f32_16x16x32_bf16 v[104:107], v[200:203], v[68:71], v[104:107]\n" \
  "v_mfma_f32_16x16x32_bf16 v[104:107], v[204:207], v[72:75], v[104:107]\n" \
  "v_mfma_f32_16x16x32_bf16 v[104:107], v[208:211], v[76:79], v[104:107]\n" \
  "s_waitcnt lgkmcnt(6)\n" \
  "v_mfma_f32_16x16x32_bf16 v[108:111], v[80:83], v[64:67], 0\n" \
  "s_waitcnt lgkmcnt(4)\n" \
  "v_mfma_f32_16x16x32_bf16 v[108:111], v[84:87], v[68:71], v[108:111]\n" \
  "v_max3_f32 v248, v100, v101, v102\n" \
  "s_waitcnt lgkmcnt(2)\n" \
  "v_mfma_f32_16x16x32_bf16 v[108:111], v[88:91], v[72:75], v[108:111]\n" \
  "s_waitcnt lgkmcnt(0)\n" \
  "v_mfma_f32_16x16x32_bf16 v[108:111], v[92:95], v[76:79], v[108:111]\n" \
  "v_max3_f32 v249, v103, v104, v105\n" \
  "v_max3_f32 v248, v248, v106, v107\n" \
  "v_mfma_f32_16x16x32_bf16 v[140:143], a[16:19], v[152:155], 0\n" \
  "s_nop 4\n" \
  "v_max3_f32 v249, v249, v108, v109\n" \
  "v_max_f32 v232, v248, v249\n" \
  "v_max_f32 v233, v248, v249\n" \
  "v_mfma_f32_16x16x32_bf16 v[128:131], a[24:27], v[148:151], 0\n" \
  "s_nop 0\n" \
  "v_permlane16_swap_b32 v232, v233\n" \
  "v_max_f32 v248, v232, v233\n" \
  "v_max_f32 v249, v232, v233\n" \
  "v_mfma_f32_16x16x32_bf16 v[144:147], a[24:27], v[152:155], 0\n" \
  "s_nop 0\n" \
  "v_permlane32_swap_b32 v248, v249\n" \
  "v_max_f32 v248, v248, v249\n" \
  "v_mul_f32_e64 v248, -s71, v248\n" \
  "v_pk_fma_f32 v[100:101], v[100:101], s[70:71], v[248:249] op_sel:[0,1,0] op_sel_hi:[1,1,0]\n" \
  "v_pk_fma_f32 v[102:103], v[102:103], s[70:71], v[248:249] op_sel:[0,1,0] op_sel_hi:[1,1,0]\n" \
  "v_pk_fma_f32 v[104:105], v[104:105], s[70:71], v[248:249] op_sel:[0,1,0] op_sel_hi:[1,1,0]\n" \
  "v_pk_fma_f32 v[106:107], v[106:107], s[70:71], v[248:249] op_sel:[0,1,0] op_sel_hi:[1,1,0]\n" \
  "v_pk_fma_f32 v[108:109], v[108:109], s[70:71], v[248:249] op_sel:[0,1,0] op_sel_hi:[1,1,0]\n" \
  "v_exp_f32 v100, v100\n" \
  "v_exp_f32 v101, v101\n" \
  "v_exp_f32 v102, v102\n" \
  "v_exp_f32 v103, v103\n" \
  "v_exp_f32 v104, v104\n" \
  "v_exp_f32 v105, v105\n" \
  "v_exp_f32 v106, v106\n" \
  "v_exp_f32 v107, v107\n" \
  "v_exp_f32 v108, v108\n" \
  "v_exp_f32 v109, v109\n" \
  "v_pk_add_f32 v[248:249], v[100:101], v[102:103]\n" \
  "v_pk_add_f32 v[232:233], v[104:105], v[106:107]\n" \
  "v_pk_add_f32 v[248:249], v[248:249], v[108:109]\n" \
  "v_pk_add_f32 v[248:249], v[248:249], v[232:233]\n" \
  "v_add_f32 v232, v248, v249\n" \
  "v_add_f32 v233, v248, v249\n" \
  "v_mfma_f32_16x16x32_bf16 v[116:119], v[156:159], a[4:7], v[116:119]\n" \
  "s_nop 0\n" \
  "v_permlane16_swap_b32 v232, v233\n" \
  "v_add_f32 v248, v232, v233\n" \
  "v_add_f32 v249, v232, v233\n" \
  "v_mfma_f32_16x16x32_bf16 v[132:135], v[160:163], a[4:7], v[132:135]\n" \
  "s_nop 0\n" \
  "v_permlane32_swap_b32 v248, v249\n" \
  "v_add_f32 v248, v248, v249\n" \
  "v_rcp_f32 v248, v248\n" \
  "s_cmp_lg_u32 s60, s81\n" \
  "v_bfi_b32 v248, v230, v248, s66\n" \
  "v_pk_mul_f32 v[100:101], v[248:249], v[100:101] op_sel_hi:[0,1]\n" \
  "v_pk_mul_f32 v[102:103], v[248:249], v[102:103] op_sel_hi:[0,1]\n" \
  "v_pk_mul_f32 v[104:105], v[248:249], v[104:105] op_sel_hi:[0,1]\n" \
  "v_pk_mul_f32 v[106:107], v[248:249], v[106:107] op_sel_hi:[0,1]\n" \
  "v_pk_mul_f32 v[108:109], v[248:249], v[108:109] op_sel_hi:[0,1]\n" \
  "v_cvt_pk_bf16_f32 v232, v100, v101\n" \
  "v_cvt_pk_bf16_f32 v233, v102, v103\n" \
  "v_cvt_pk_bf16_f32 v234, v104, v105\n" \
  "v_cvt_pk_bf16_f32 v235, v106, v107\n" \
  "v_cvt_pk_bf16_f32 v236, v108, v109\n" \
  "buffer_store_dwordx4 v[232:235], %[po1], %[X], s65 offen sc1\n" \
  "v_mfma_f32_16x16x32_bf16 v[120:123], v[156:159], a[12:15], v[120:123]\n" \
  "v_mfma_f32_16x16x32_bf16 v[136:139], v[160:163], a[12:15], v[136:139]\n" \
  "buffer_store_dword v236, %[po2], %[X], s65 offen sc1\n" \
  "v_mfma_f32_16x16x32_bf16 v[124:127], a[20:23], v[156:159], v[124:127]\n" \
  "v_mfma_f32_16x16x32_bf16 v[140:143], a[20:23], v[160:163], v[140:143]\n" \
  "v_mfma_f32_16x16x32_bf16 v[128:131], a[28:31], v[156:159], v[128:131]\n" \
  "v_mfma_f32_16x16x32_bf16 v[144:147], a[28:31], v[160:163], v[144:147]\n" \
  "s_cbranch_scc1 .Lqk_top\n" \
  "s_cmp_eq_u32 s60, -1\n" \
  "s_cbranch_scc1 .Lqk_entry\n" \
  "buffer_store_dwordx2 v[232:233], %[po3], %[X], s65 offen sc1\n" \
  "s_branch .Lqk_end\n" \
  ".Lqk_sm:\n" \
  "s_cmp_lt_i32 s60, 0\n" \
  "s_cbranch_scc1 .Lqk_sm_back\n" \
  "v_cmp_u_f32 vcc, v231, v231\n" \
  "s_cbranch_vccnz .Lqk_sh\n" \
  "s_mov_b32 s69, 0\n" \
  ".Lqk_smw:\n" \
  "buffer_load_dwordx4 v[64:67], %[mo], %[X], s63 offen offset:0 sc1\n" \
  "buffer_load_dwordx4 v[68:71], %[mo], %[X], s63 offen offset:1024 sc1\n" \
  "buffer_load_dwordx4 v[72:75], %[mo], %[X], s63 offen offset:2048 sc1\n" \
  "buffer_load_dwordx4 v[76:79], %[mo], %[X], s63 offen offset:3072 sc1\n" \
  "buffer_load_dwordx4 v[80:83], v228, %[X], s63 offen offset:0 sc1\n" \
  "buffer_load_dwordx4 v[84:87], v228, %[X], s63 offen offset:1024 sc1\n" \
  "buffer_load_dwordx4 v[88:91], v228, %[X], s63 offen offset:2048 sc1\n" \
  "buffer_load_dwordx4 v[92:95], v228, %[X], s63 offen offset:3072 sc1\n" \
  "buffer_load_dwordx4 v[96:99], %[mo], %[X], s64 offen offset:0 sc1\n" \
  "s_waitcnt vmcnt(0)\n" \
  "v_max3_u32 v229, v64, v65, v66\n" \
  "v_max3_u32 v229, v229, v67, v68\n" \
  "v_max3_u32 v229, v229, v69, v70\n" \
  "v_max3_u32 v229, v229, v71, v72\n" \
  "v_max3_u32 v229, v229, v73, v74\n" \
  "v_max3_u32 v229, v229, v75, v76\n" \
  "v_max3_u32 v229, v229, v77, v78\n" \
  "v_max3_u32 v229, v229, v79, v80\n" \
  "v_max3_u32 v229, v229, v81, v82\n" \
  "v_max3_u32 v229, v229, v83, v84\n" \
  "v_max3_u32 v229, v229, v85, v86\n" \
  "v_max3_u32 v229, v229, v87, v88\n" \
  "v_max3_u32 v229, v229, v89, v90\n" \
  "v_max3_u32 v229, v229, v91, v92\n" \
  "v_max3_u32 v229, v229, v93, v94\n" \
  "v_max3_u32 v229, v229, v95, v96\n" \
  "v_max3_u32 v229, v229, v97, v98\n" \
  "v_max_u32 v229, v229, v99\n" \
  "v_cmp_eq_u32 vcc, -1, v229\n" \
  "s_cbranch_vccz .Lqk_smw_ok\n" \
  "s_add_i32 s69, s69, 1\n" \
  "s_and_b32 s67, s69, 1023\n" \
  "s_cbranch_scc1 .Lqk_smw\n" \
  "buffer_load_dword v229, off, %[F], 0 offset:1024 sc1\n" \
  "s_waitcnt vmcnt(0)\n" \
  "v_readfirstlane_b32 s67, v229\n" \
  "s_cmp_lg_u32 s67, 0\n" \
  "s_cbranch_scc1 .Lqk_smw_ok\n" \
  "s_cmp_lt_u32 s69, 400000\n" \
  "s_cbranch_scc1 .Lqk_smw\n" \
  "v_mov_b32 v229, 1\n" \
  "buffer_store_dword v229, off, %[F], 0 offset:1024 sc1\n" \
  ".Lqk_smw_ok:\n" \
  "s_memrealtime s[76:77]\n" \
  "s_waitcnt lgkmcnt(0)\n" \
  "s_add_i32 s76, s76, s95\n" \
  "s_add_i32 s76, s76, %[sh1]\n" \
  "s_sub_i32 s76, s76, s86\n" \
  "s_cmp_gt_u32 s76, -3000\n" \
  "s_cselect_b32 s76, 0, s76\n" \
  "s_add_i32 s86, s86, s76\n" \
  "s_add_i32 s88, s60, 3\n" \
  "v_or_b32_e64 v252, s86, 1\n" \
  "v_add_u32 v253, s76, v253\n" \
  "v_mov_b64 v[100:101], 0\n" \
  "v_mov_b64 v[102:103], 0\n" \
  "v_mov_b64 v[104:105], 0\n" \
  "v_mov_b64 v[106:107], 0\n" \
  "v_mov_b64 v[108:109], 0\n" \
  "v_mov_b64 v[110:111], 0\n" \
  "v_mov_b64 v[112:113], 0\n" \
  "v_mov_b64 v[114:115], 0\n" \
  "s_nop 1\n" \
  "v_smfmac_f32_16x16x64_bf16 v[100:103], v[64:67], a[0:7], %[idx]\n" \
  "v_smfmac_f32_16x16x64_bf16 v[104:107], v[64:67], a[8:15], %[idx]\n" \
  "v_smfmac_f32_16x16x64_bf16 v[108:111], v[64:67], a[16:23], %[idx]\n" \
  "v_smfmac_f32_16x16x64_bf16 v[112:115], v[64:67], a[24:31], %[idx]\n" \
  "v_smfmac_f32_16x16x64_bf16 v[100:103], v[68:71], a[32:39], %[idx]\n" \
  "v_smfmac_f32_16x16x64_bf16 v[104:107], v[68:71], a[40:47], %[idx]\n" \
  "v_smfmac_f32_16x16x64_bf16 v[108:111], v[68:71], a[48:55], %[idx]\n" \
  "v_smfmac_f32_16x16x64_bf16 v[112:115], v[68:71], a[56:63], %[idx]\n" \
  "v_smfmac_f32_16x16x64_bf16 v[100:103], v[72:75], a[64:71], %[idx]\n" \
  "v_smfmac_f32_16x16x64_bf16 v[104:107], v[72:75], a[72:79], %[idx]\n" \
  "v_smfmac_f32_16x16x64_bf16 v[108:111], v[72:75], a[80:87], %[idx]\n" \
  "v_smfmac_f32_16x16x64_bf16 v[112:115], v[72:75], a[88:95], %[idx]\n" \
  "v_smfmac_f32_16x16x64_bf16 v[100:103], v[76:79], a[96:103], %[idx]\n" \
  "v_smfmac_f32_16x16x64_bf16 v[104:107], v[76:79], a[104:111], %[idx]\n" \
  "v_smfmac_f32_16x16x64_bf16 v[108:111], v[76:79], a[112:119], %[idx]\n" \
  "v_smfmac_f32_16x16x64_bf16 v[112:115], v[76:79], a[120:127], %[idx]\n" \
  "v_smfmac_f32_16x16x64_bf16 v[100:103], v[80:83], a[128:135], %[idx]\n" \
  "v_smfmac_f32_16x16x64_bf16 v[104:107], v[80:83], a[136:143], %[idx]\n" \
  "v_smfmac_f32_16x16x64_bf16 v[108:111], v[80:83], a[144:151], %[idx]\n" \
  "v_smfmac_f32_16x16x64_bf16 v[112:115], v[80:83], a[152:159], %[idx]\n" \
  "v_smfmac_f32_16x16x64_bf16 v[100:103], v[84:87], a[160:167], %[idx]\n" \
  "v_smfmac_f32_16x16x64_bf16 v[104:107], v[84:87], a[168:175], %[idx]\n" \
  "v_smfmac_f32_16x16x64_bf16 v[108:111], v[84:87], a[176:183], %[idx]\n" \
  "v_smfmac_f32_16x16x64_bf16 v[112:115], v[84:87], a[184:191], %[idx]\n" \
  "v_smfmac_f32_16x16x64_bf16 v[100:103], v[88:91], a[192:199], %[idx]\n" \
  "v_smfmac_f32_16x16x64_bf16 v[104:107], v[88:91], a[200:207], %[idx]\n" \
  "v_smfmac_f32_16x16x64_bf16 v[108:111], v[88:91], a[208:215], %[idx]\n" \
  "v_smfmac_f32_16x16x64_bf16 v[112:115], v[88:91], a[216:223], %[idx]\n" \
  "v_smfmac_f32_16x16x64_bf16 v[100:103], v[92:95], a[224:231], %[idx]\n" \
  "v_smfmac_f32_16x16x64_bf16 v[104:107], v[92:95], a[232:239], %[idx]\n" \
  "v_smfmac_f32_16x16x64_bf16 v[108:111], v[92:95], a[240:247], %[idx]\n" \
  "v_smfmac_f32_16x16x64_bf16 v[112:115], v[92:95], a[248:255], %[idx]\n" \
  "v_smfmac_f32_16x16x64_bf16 v[100:103], v[96:99], v[32:39], %[idx]\n" \
  "v_smfmac_f32_16x16x64_bf16 v[104:107], v[96:99], v[40:47], %[idx]\n" \
  "v_smfmac_f32_16x16x64_bf16 v[108:111], v[96:99], v[48:55], %[idx]\n" \
  "v_smfmac_f32_16x16x64_bf16 v[112:115], v[96:99], v[56:63], %[idx]\n" \
  "s_nop 7\n" \
  "v_pk_add_f32 v[232:233], v[100:101], v[102:103]\n" \
  "v_pk_add_f32 v[234:235], v[104:105], v[106:107]\n" \
  "v_cvt_pk_bf16_f32 v236, v232, v233\n" \
  "v_cvt_pk_bf16_f32 v237, v234, v235\n" \
  "ds_write2st64_b32 %[qW], v236, v237 offset1:11\n" \
  "v_pk_add_f32 v[232:233], v[108:109], v[110:111]\n" \
  "v_pk_add_f32 v[234:235], v[112:113], v[114:115]\n" \
  "v_cvt_pk_bf16_f32 v236, v232, v233\n" \
  "v_cvt_pk_bf16_f32 v237, v234, v235\n" \
  "ds_write2st64_b32 %[kW], v236, v237 offset1:11\n" \
  "s_branch .Lqk_sm_back\n" \
  ".Lqk_late:\n" \
  "s_cmp_lt_i32 s60, 0\n" \
  "s_cbranch_scc1 .Lqk_al_ok\n" \
  "s_cmp_gt_u32 s91, -3000\n" \
  "s_cbranch_scc0 .Lqk_late2\n" \
  ".Lqk_wt:\n" \
  "s_memrealtime s[76:77]\n" \
  "s_waitcnt lgkmcnt(0)\n" \
  "s_sub_i32 s76, s76, s72\n" \
  "s_cmp_lt_i32 s76, 0\n" \
  "s_cbranch_scc1 .Lqk_wt\n" \
  "s_branch .Lqk_al_ok\n" \
  ".Lqk_late2:\n" \
  "s_cmp_lg_u32 s72, 0\n" \
  "s_cbranch_scc1 .Lqk_late3\n" \
  "s_mov_b32 s69, 0\n" \
  ".Lqk_tw:\n" \
  "buffer_load_dword v229, off, %[X], s96 offset:16 sc1\n" \
  "s_waitcnt vmcnt(0)\n" \
  "v_readfirstlane_b32 s72, v229\n" \
  "s_cmp_lg_u32 s72, 0\n" \
  "s_cbranch_scc1 .Lqk_tw_ok\n" \
  "s_sleep 1\n" \
  "s_add_i32 s69, s69, 1\n" \
  "s_cmp_lt_u32 s69, 200\n" \
  "s_cbranch_scc1 .Lqk_tw\n" \
  ".Lqk_tw_ok:\n" \
  "s_add_i32 s76, s72, s95\n" \
  "s_add_i32 s76, s76, 8\n" \
  "s_sub_i32 s76, s76, s86\n" \
  "s_cmp_gt_u32 s76, -3000\n" \
  "s_cselect_b32 s76, 0, s76\n" \
  "s_add_i32 s86, s86, s76\n" \
  "s_add_i32 s88, s60, 3\n" \
  "v_or_b32_e64 v252, s86, 1\n" \
  "v_add_u32 v253, s76, v253\n" \
  "s_sub_i32 s91, s74, s72\n" \
  "s_cmp_gt_u32 s91, -3000\n" \
  "s_cbranch_scc1 .Lqk_wt\n" \
  ".Lqk_late3:\n" \
  "s_memrealtime s[76:77]\n" \
  "s_waitcnt lgkmcnt(0)\n" \
  "s_add_i32 s76, s76, s95\n" \
  "s_add_i32 s76, s76, 90\n" \
  "s_sub_i32 s76, s76, s86\n" \
  "s_cmp_gt_u32 s76, -3000\n" \
  "s_cselect_b32 s76, 0, s76\n" \
  "s_add_i32 s86, s86, s76\n" \
  "s_add_i32 s88, s60, 3\n" \
  "v_or_b32_e64 v252, s86, 1\n" \
  "v_add_u32 v253, s76, v253\n" \
  "s_branch .Lqk_al_ok\n" \
  ".Lqk_sh:\n" \
  "s_mul_i32 s75, s60, 36864\n" \
  "s_add_i32 s76, s75, 0x1000\n" \
  "s_add_i32 s77, s75, 0x2000\n" \
  "s_add_i32 s60, s60, -1\n" \
  "s_add_i32 s96, s96, -16\n" \
  "s_mov_b32 s93, s94\n" \
  "v_xor_b32 %[hA], 0x10000, %[hA]\n" \
  "s_waitcnt vmcnt(0) lgkmcnt(0)\n" \
  "s_barrier\n" \
  "s_mov_b32 s69, 0\n" \
  ".Lqk_shw:\n" \
  "buffer_load_dwordx4 v[64:67], %[hvo], %[HS], s75 offen offset:0 sc1\n" \
  "buffer_load_dwordx4 v[68:71], %[hvo], %[HS], s75 offen offset:1024 sc1\n" \
  "buffer_load_dwordx4 v[72:75], %[hvo], %[HS], s75 offen offset:2048 sc1\n" \
  "buffer_load_dwordx4 v[76:79], %[hvo], %[HS], s75 offen offset:3072 sc1\n" \
  "buffer_load_dwordx4 v[80:83], %[hvo], %[HS], s76 offen offset:0 sc1\n" \
  "buffer_load_dwordx4 v[84:87], %[hvo], %[HS], s76 offen offset:1024 sc1\n" \
  "buffer_load_dwordx4 v[88:91], %[hvo], %[HS], s76 offen offset:2048 sc1\n" \
  "buffer_load_dwordx4 v[92:95], %[hvo], %[HS], s76 offen offset:3072 sc1\n" \
  "buffer_load_dwordx4 v[96:99], %[hvo], %[HS], s77 offen offset:0 sc1\n" \
  "s_waitcnt vmcnt(0)\n" \
  "v_max3_u32 v229, v64, v65, v66\n" \
  "v_max3_u32 v229, v229, v67, v68\n" \
  "v_max3_u32 v229, v229, v69, v70\n" \
  "v_max3_u32 v229, v229, v71, v72\n" \
  "v_max3_u32 v229, v229, v73, v74\n" \
  "v_max3_u32 v229, v229, v75, v76\n" \
  "v_max3_u32 v229, v229, v77, v78\n" \
  "v_max3_u32 v229, v229, v79, v80\n" \
  "v_max3_u32 v229, v229, v81, v82\n" \
  "v_max3_u32 v229, v229, v83, v84\n" \
  "v_max3_u32 v229, v229, v85, v86\n" \
  "v_max3_u32 v229, v229, v87, v88\n" \
  "v_max3_u32 v229, v229, v89, v90\n" \
  "v_max3_u32 v229, v229, v91, v92\n" \
  "v_max3_u32 v229, v229, v93, v94\n" \
  "v_max3_u32 v229, v229, v95, v96\n" \
  "v_max3_u32 v229, v229, v97, v98\n" \
  "v_max_u32 v229, v229, v99\n" \
  "v_cmp_eq_u32 vcc, -1, v229\n" \
  "s_cbranch_vccz .Lqk_shw_ok\n" \
  "s_sleep 4\n" \
  "s_add_i32 s69, s69, 1\n" \
  "s_and_b32 s67, s69, 1023\n" \
  "s_cbranch_scc1 .Lqk_shw\n" \
  "buffer_load_dword v229, off, %[F], 0 offset:1024 sc1\n" \
  "s_waitcnt vmcnt(0)\n" \
  "v_readfirstlane_b32 s67, v229\n" \
  "s_cmp_lg_u32 s67, 0\n" \
  "s_cbranch_scc1 .Lqk_shw_ok\n" \
  "s_cmp_lt_u32 s69, 400000\n" \
  "s_cbranch_scc1 .Lqk_shw\n" \
  "v_mov_b32 v229, 1\n" \
  "buffer_store_dword v229, off, %[F], 0 offset:1024 sc1\n" \
  ".Lqk_shw_ok:\n" \
  "s_xor_b32 s67, s62, 0x10000\n" \
  "v_add_u32 v229, s67, %[mo]\n" \
  "ds_write_b128 v229, v[64:67] offset:0\n" \
  "ds_write_b128 v229, v[68:71] offset:1024\n" \
  "ds_write_b128 v229, v[72:75] offset:2048\n" \
  "ds_write_b128 v229, v[76:79] offset:3072\n" \
  "ds_write_b128 v229, v[80:83] offset:4096\n" \
  "ds_write_b128 v229, v[84:87] offset:5120\n" \
  "ds_write_b128 v229, v[88:91] offset:6144\n" \
  "ds_write_b128 v229, v[92:95] offset:7168\n" \
  "ds_write_b128 v229, v[96:99] offset:8192\n" \
  "s_waitcnt lgkmcnt(0)\n" \
  "s_barrier\n" \
  "ds_read_b128 v[148:151], %[hA] offset:0\n" \
  "ds_read_b128 v[152:155], %[hA] offset:18432\n" \
  "ds_read_b128 v[156:159], %[hA] offset:512\n" \
  "ds_read_b128 v[160:163], %[hA] offset:18944\n" \
  "ds_read_b128 v[164:167], %[hA] offset:1024\n" \
  "ds_read_b128 v[168:171], %[hA] offset:19456\n" \
  "ds_read_b128 v[172:175], %[hA] offset:1536\n" \
  "ds_read_b128 v[176:179], %[hA] offset:19968\n" \
  "s_waitcnt lgkmcnt(0)\n" \
  "v_mfma_f32_16x16x32_bf16 v[116:119], v[148:151], a[0:3], 0\n" \
  "v_mfma_f32_16x16x32_bf16 v[132:135], v[152:155], a[0:3], 0\n" \
  "v_mfma_f32_16x16x32_bf16 v[120:123], v[148:151], a[8:11], 0\n" \
  "v_mfma_f32_16x16x32_bf16 v[136:139], v[152:155], a[8:11], 0\n" \
  "v_mfma_f32_16x16x32_bf16 v[124:127], a[16:19], v[148:151], 0\n" \
  "v_mfma_f32_16x16x32_bf16 v[140:143], a[16:19], v[152:155], 0\n" \
  "v_mfma_f32_16x16x32_bf16 v[128:131], a[24:27], v[148:151], 0\n" \
  "v_mfma_f32_16x16x32_bf16 v[144:147], a[24:27], v[152:155], 0\n" \
  "v_mfma_f32_16x16x32_bf16 v[116:119], v[156:159], a[4:7], v[116:119]\n" \
  "v_mfma_f32_16x16x32_bf16 v[132:135], v[160:163], a[4:7], v[132:135]\n" \
  "v_mfma_f32_16x16x32_bf16 v[120:123], v[156:159], a[12:15], v[120:123]\n" \
  "v_mfma_f32_16x16x32_bf16 v[136:139], v[160:163], a[12:15], v[136:139]\n" \
  "v_mfma_f32_16x16x32_bf16 v[124:127], a[20:23], v[156:159], v[124:127]\n" \
  "v_mfma_f32_16x16x32_bf16 v[140:143], a[20:23], v[160:163], v[140:143]\n" \
  "v_mfma_f32_16x16x32_bf16 v[128:131], a[28:31], v[156:159], v[128:131]\n" \
  "v_mfma_f32_16x16x32_bf16 v[144:147], a[28:31], v[160:163], v[144:147]\n" \
  "v_mfma_f32_16x16x32_bf16 v[116:119], v[164:167], a[32:35], v[116:119]\n" \
  "ds_read_b128 v[148:151], %[hA] offset:2048\n" \
  "v_mfma_f32_16x16x32_bf16 v[132:135], v[168:171], a[32:35], v[132:135]\n" \
  "ds_read_b128 v[152:155], %[hA] offset:20480\n" \
  "v_mfma_f32_16x16x32_bf16 v[120:123], v[164:167], a[40:43], v[120:123]\n" \
  "v_mfma_f32_16x16x32_bf16 v[136:139], v[168:171], a[40:43], v[136:139]\n" \
  "v_mfma_f32_16x16x32_bf16 v[124:127], a[48:51], v[164:167], v[124:127]\n" \
  "v_mfma_f32_16x16x32_bf16 v[140:143], a[48:51], v[168:171], v[140:143]\n" \
  "v_mfma_f32_16x16x32_bf16 v[128:131], a[56:59], v[164:167], v[128:131]\n" \
  "v_mfma_f32_16x16x32_bf16 v[144:147], a[56:59], v[168:171], v[144:147]\n" \
  "v_mfma_f32_16x16x32_bf16 v[116:119], v[172:175], a[36:39], v[116:119]\n" \
  "ds_read_b128 v[156:159], %[hA] offset:2560\n" \
  "v_mfma_f32_16x16x32_bf16 v[132:135], v[176:179], a[36:39], v[132:135]\n" \
  "ds_read_b128 v[160:163], %[hA] offset:20992\n" \
  "v_mfma_f32_16x16x32_bf16 v[120:123], v[172:175], a[44:47], v[120:123]\n" \
  "v_mfma_f32_16x16x32_bf16 v[136:139], v[176:179], a[44:47], v[136:139]\n" \
  "v_mfma_f32_16x16x32_bf16 v[124:127], a[52:55], v[172:175], v[124:127]\n" \
  "v_mfma_f32_16x16x32_bf16 v[140:143], a[52:55], v[176:179], v[140:143]\n" \
  "v_mfma_f32_16x16x32_bf16 v[128:131], a[60:63], v[172:175], v[128:131]\n" \
  "v_mfma_f32_16x16x32_bf16 v[144:147], a[60:63], v[176:179], v[144:147]\n" \
  "s_waitcnt lgkmcnt(2)\n" \
  "v_mfma_f32_16x16x32_bf16 v[116:119], v[148:151], a[64:67], v[116:119]\n" \
  "ds_read_b128 v[164:167], %[hA] offset:3072\n" \
  "v_mfma_f32_16x16x32_bf16 v[132:135], v[152:155], a[64:67], v[132:135]\n" \
  "ds_read_b128 v[168:171], %[hA] offset:21504\n" \
  "v_mfma_f32_16x16x32_bf16 v[120:123], v[148:151], a[72:75], v[120:123]\n" \
  "v_mfma_f32_16x16x32_bf16 v[136:139], v[152:155], a[72:75], v[136:139]\n" \
  "v_mfma_f32_16x16x32_bf16 v[124:127], a[80:83], v[148:151], v[124:127]\n" \
  "v_mfma_f32_16x16x32_bf16 v[140:143], a[80:83], v[152:155], v[140:143]\n" \
  "v_mfma_f32_16x16x32_bf16 v[128:131], a[88:91], v[148:151], v[128:131]\n" \
  "v_mfma_f32_16x16x32_bf16 v[144:147], a[88:91], v[152:155], v[144:147]\n" \
  "s_waitcnt lgkmcnt(2)\n" \
  "v_mfma_f32_16x16x32_bf16 v[116:119], v[156:159], a[68:71], v[116:119]\n" \
  "ds_read_b128 v[172:175], %[hA] offset:3584\n" \
  "v_mfma_f32_16x16x32_bf16 v[132:135], v[160:163], a[68:71], v[132:135]\n" \
  "ds_read_b128 v[176:179], %[hA] offset:22016\n" \
  "v_mfma_f32_16x16x32_bf16 v[120:123], v[156:159], a[76:79], v[120:123]\n" \
  "v_mfma_f32_16x16x32_bf16 v[136:139], v[160:163], a[76:79], v[136:139]\n" \
  "v_mfma_f32_16x16x32_bf16 v[124:127], a[84:87], v[156:159], v[124:127]\n" \
  "v_mfma_f32_16x16x32_bf16 v[140:143], a[84:87], v[160:163], v[140:143]\n" \
  "v_mfma_f32_16x16x32_bf16 v[128:131], a[92:95], v[156:159], v[128:131]\n" \
  "v_mfma_f32_16x16x32_bf16 v[144:147], a[92:95], v[160:163], v[144:147]\n" \
  "s_waitcnt lgkmcnt(2)\n" \
  "v_mfma_f32_16x16x32_bf16 v[116:119], v[164:167], a[96:99], v[116:119]\n" \
  "ds_read_b128 v[148:151], %[hA] offset:4096\n" \
  "v_mfma_f32_16x16x32_bf16 v[132:135], v[168:171], a[96:99], v[132:135]\n" \
  "ds_read_b128 v[152:155], %[hA] offset:22528\n" \
  "v_mfma_f32_16x16x32_bf16 v[120:123], v[164:167], a[104:107], v[120:123]\n" \
  "v_mfma_f32_16x16x32_bf16 v[136:139], v[168:171], a[104:107], v[136:139]\n" \
  "v_mfma_f32_16x16x32_bf16 v[124:127], a[112:115], v[164:167], v[124:127]\n" \
  "v_mfma_f32_16x16x32_bf16 v[140:143], a[112:115], v[168:171], v[140:143]\n" \
  "v_mfma_f32_16x16x32_bf16 v[128:131], a[120:123], v[164:167], v[128:131]\n" \
  "v_mfma_f32_16x16x32_bf16 v[144:147], a[120:123], v[168:171], v[144:147]\n" \
  "s_waitcnt lgkmcnt(2)\n" \
  "v_mfma_f32_16x16x32_bf16 v[116:119], v[172:175], a[100:103], v[116:119]\n" \
  "ds_read_b128 v[156:159], %[hA] offset:4608\n" \
  "v_mfma_f32_16x16x32_bf16 v[132:135], v[176:179], a[100:103], v[132:135]\n" \
  "ds_read_b128 v[160:163], %[hA] offset:23040\n" \
  "v_mfma_f32_16x16x32_bf16 v[120:123], v[172:175], a[108:111], v[120:123]\n" \
  "v_mfma_f32_16x16x32_bf16 v[136:139], v[176:179], a[108:111], v[136:139]\n" \
  "v_mfma_f32_16x16x32_bf16 v[124:127], a[116:119], v[172:175], v[124:127]\n" \
  "v_mfma_f32_16x16x32_bf16 v[140:143], a[116:119], v[176:179], v[140:143]\n" \
  "v_mfma_f32_16x16x32_bf16 v[128:131], a[124:127], v[172:175], v[128:131]\n" \
  "v_mfma_f32_16x16x32_bf16 v[144:147], a[124:127], v[176:179], v[144:147]\n" \
  "s_waitcnt lgkmcnt(2)\n" \
  "v_mfma_f32_16x16x32_bf16 v[116:119], v[148:151], a[128:131], v[116:119]\n" \
  "ds_read_b128 v[164:167], %[hA] offset:5120\n" \
  "v_mfma_f32_16x16x32_bf16 v[132:135], v[152:155], a[128:131], v[132:135]\n" \
  "ds_read_b128 v[168:171], %[hA] offset:23552\n" \
  "v_mfma_f32_16x16x32_bf16 v[120:123], v[148:151], a[136:139], v[120:123]\n" \
  "v_mfma_f32_16x16x32_bf16 v[136:139], v[152:155], a[136:139], v[136:139]\n" \
  "v_mfma_f32_16x16x32_bf16 v[124:127], a[144:147], v[148:151], v[124:127]\n" \
  "v_mfma_f32_16x16x32_bf16 v[140:143], a[144:147], v[152:155], v[140:143]\n" \
  "v_mfma_f32_16x16x32_bf16 v[128:131], a[152:155], v[148:151], v[128:131]\n" \
  "v_mfma_f32_16x16x32_bf16 v[144:147], a[152:155], v[152:155], v[144:147]\n" \
  "s_waitcnt lgkmcnt(2)\n" \
  "v_mfma_f32_16x16x32_bf16 v[116:119], v[156:159], a[132:135], v[116:119]\n" \
  "ds_read_b128 v[172:175], %[hA] offset:5632\n" \
  "v_mfma_f32_16x16x32_bf16 v[132:135], v[160:163], a[132:135], v[132:135]\n" \
  "ds_read_b128 v[176:179], %[hA] offset:24064\n" \
  "v_mfma_f32_16x16x32_bf16 v[120:123], v[156:159], a[140:143], v[120:123]\n" \
  "v_mfma_f32_16x16x32_bf16 v[136:139], v[160:163], a[140:143], v[136:139]\n" \
  "v_mfma_f32_16x16x32_bf16 v[124:127], a[148:151], v[156:159], v[124:127]\n" \
  "v_mfma_f32_16x16x32_bf16 v[140:143], a[148:151], v[160:163], v[140:143]\n" \
  "v_mfma_f32_16x16x32_bf16 v[128:131], a[156:159], v[156:159], v[128:131]\n" \
  "v_mfma_f32_16x16x32_bf16 v[144:147], a[156:159], v[160:163], v[144:147]\n" \
  "s_waitcnt lgkmcnt(2)\n" \
  "v_mfma_f32_16x16x32_bf16 v[116:119], v[164:167], a[160:163], v[116:119]\n" \
  "ds_read_b128 v[148:151], %[hA] offset:6144\n" \
  "v_mfma_f32_16x16x32_bf16 v[132:135], v[168:171], a[160:163], v[132:135]\n" \
  "ds_read_b128 v[152:155], %[hA] offset:24576\n" \
  "v_mfma_f32_16x16x32_bf16 v[120:123], v[164:167], a[168:171], v[120:123]\n" \
  "v_mfma_f32_16x16x32_bf16 v[136:139], v[168:171], a[168:171], v[136:139]\n" \
  "v_mfma_f32_16x16x32_bf16 v[124:127], a[176:179], v[164:167], v[124:127]\n" \
  "v_mfma_f32_16x16x32_bf16 v[140:143], a[176:179], v[168:171], v[140:143]\n" \
  "v_mfma_f32_16x16x32_bf16 v[128:131], a[184:187], v[164:167], v[128:131]\n" \
  "v_mfma_f32_16x16x32_bf16 v[144:147], a[184:187], v[168:171], v[144:147]\n" \
  "s_waitcnt lgkmcnt(2)\n" \
  "v_mfma_f32_16x16x32_bf16 v[116:119], v[172:175], a[164:167], v[116:119]\n" \
  "ds_read_b128 v[156:159], %[hA] offset:6656\n" \
  "v_mfma_f32_16x16x32_bf16 v[132:135], v[176:179], a[164:167], v[132:135]\n" \
  "ds_read_b128 v[160:163], %[hA] offset:25088\n" \
  "v_mfma_f32_16x16x32_bf16 v[120:123], v[172:175], a[172:175], v[120:123]\n" \
  "v_mfma_f32_16x16x32_bf16 v[136:139], v[176:179], a[172:175], v[136:139]\n" \
  "v_mfma_f32_16x16x32_bf16 v[124:127], a[180:183], v[172:175], v[124:127]\n" \
  "v_mfma_f32_16x16x32_bf16 v[140:143], a[180:183], v[176:179], v[140:143]\n" \
  "v_mfma_f32_16x16x32_bf16 v[128:131], a[188:191], v[172:175], v[128:131]\n" \
  "v_mfma_f32_16x16x32_bf16 v[144:147], a[188:191], v[176:179], v[144:147]\n" \
  "s_waitcnt lgkmcnt(2)\n" \
  "v_mfma_f32_16x16x32_bf16 v[116:119], v[148:151], a[192:195], v[116:119]\n" \
  "ds_read_b128 v[164:167], %[hA] offset:7168\n" \
  "v_mfma_f32_16x16x32_bf16 v[132:135], v[152:155], a[192:195], v[132:135]\n" \
  "ds_read_b128 v[168:171], %[hA] offset:25600\n" \
  "v_mfma_f32_16x16x32_bf16 v[120:123], v[148:151], a[200:203], v[120:123]\n" \
  "v_mfma_f32_16x16x32_bf16 v[136:139], v[152:155], a[200:203], v[136:139]\n" \
  "v_mfma_f32_16x16x32_bf16 v[124:127], a[208:211], v[148:151], v[124:127]\n" \
  "v_mfma_f32_16x16x32_bf16 v[140:143], a[208:211], v[152:155], v[140:143]\n" \
  "v_mfma_f32_16x16x32_bf16 v[128:131], a[216:219], v[148:151], v[128:131]\n" \
  "v_mfma_f32_16x16x32_bf16 v[144:147], a[216:219], v[152:155], v[144:147]\n" \
  "s_waitcnt lgkmcnt(2)\n" \
  "v_mfma_f32_16x16x32_bf16 v[116:119], v[156:159], a[196:199], v[116:119]\n" \
  "ds_read_b128 v[172:175], %[hA] offset:7680\n" \
  "v_mfma_f32_16x16x32_bf16 v[132:135], v[160:163], a[196:199], v[132:135]\n" \
  "ds_read_b128 v[176:179], %[hA] offset:26112\n" \
  "v_mfma_f32_16x16x32_bf16 v[120:123], v[156:159], a[204:207], v[120:123]\n" \
  "ds_read_b128 v[148:151], %[hA] offset:8192\n" \
  "v_mfma_f32_16x16x32_bf16 v[136:139], v[160:163], a[204:207], v[136:139]\n" \
  "ds_read_b128 v[152:155], %[hA] offset:26624\n" \
  "v_mfma_f32_16x16x32_bf16 v[124:127], a[212:215], v[156:159], v[124:127]\n" \
  "v_mfma_f32_16x16x32_bf16 v[140:143], a[212:215], v[160:163], v[140:143]\n" \
  "v_mfma_f32_16x16x32_bf16 v[128:131], a[220:223], v[156:159], v[128:131]\n" \
  "v_mfma_f32_16x16x32_bf16 v[144:147], a[220:223], v[160:163], v[144:147]\n" \
  "s_waitcnt lgkmcnt(4)\n" \
  "v_mfma_f32_16x16x32_bf16 v[124:127], a[240:243], v[164:167], v[124:127]\n" \
  "ds_read_b128 v[156:159], %[hA] offset:8704\n" \
  "v_mfma_f32_16x16x32_bf16 v[140:143], a[240:243], v[168:171], v[140:143]\n" \
  "ds_read_b128 v[160:163], %[hA] offset:27136\n" \
  "v_mfma_f32_16x16x32_bf16 v[128:131], a[248:251], v[164:167], v[128:131]\n" \
  "v_mfma_f32_16x16x32_bf16 v[144:147], a[248:251], v[168:171], v[144:147]\n" \
  "s_waitcnt lgkmcnt(4)\n" \
  "v_mfma_f32_16x16x32_bf16 v[124:127], a[244:247], v[172:175], v[124:127]\n" \
  "v_mfma_f32_16x16x32_bf16 v[140:143], a[244:247], v[176:179], v[140:143]\n" \
  "v_mfma_f32_16x16x32_bf16 v[128:131], a[252:255], v[172:175], v[128:131]\n" \
  "v_mfma_f32_16x16x32_bf16 v[144:147], a[252:255], v[176:179], v[144:147]\n" \
  "s_waitcnt lgkmcnt(2)\n" \
  "v_mfma_f32_16x16x32_bf16 v[124:127], v[48:51], v[148:151], v[124:127]\n" \
  "v_mfma_f32_16x16x32_bf16 v[140:143], v[48:51], v[152:155], v[140:143]\n" \
  "v_mfma_f32_16x16x32_bf16 v[128:131], v[56:59], v[148:151], v[128:131]\n" \
  "v_mfma_f32_16x16x32_bf16 v[144:147], v[56:59], v[152:155], v[144:147]\n" \
  "s_waitcnt lgkmcnt(0)\n" \
  "v_mfma_f32_16x16x32_bf16 v[124:127], v[52:55], v[156:159], v[124:127]\n" \
  "v_mfma_f32_16x16x32_bf16 v[140:143], v[52:55], v[160:163], v[140:143]\n" \
  "v_mfma_f32_16x16x32_bf16 v[128:131], v[60:63], v[156:159], v[128:131]\n" \
  "v_mfma_f32_16x16x32_bf16 v[144:147], v[60:63], v[160:163], v[144:147]\n" \
  "v_mfma_f32_16x16x32_bf16 v[116:119], v[164:167], a[224:227], v[116:119]\n" \
  "v_mfma_f32_16x16x32_bf16 v[132:135], v[168:171], a[224:227], v[132:135]\n" \
  "v_mfma_f32_16x16x32_bf16 v[120:123], v[164:167], a[232:235], v[120:123]\n" \
  "v_mfma_f32_16x16x32_bf16 v[136:139], v[168:171], a[232:235], v[136:139]\n" \
  "v_mfma_f32_16x16x32_bf16 v[116:119], v[172:175], a[228:231], v[116:119]\n" \
  "v_mfma_f32_16x16x32_bf16 v[132:135], v[176:179], a[228:231], v[132:135]\n" \
  "v_mfma_f32_16x16x32_bf16 v[120:123], v[172:175], a[236:239], v[120:123]\n" \
  "v_mfma_f32_16x16x32_bf16 v[136:139], v[176:179], a[236:239], v[136:139]\n" \
  "v_mfma_f32_16x16x32_bf16 v[116:119], v[148:151], v[32:35], v[116:119]\n" \
  "v_mfma_f32_16x16x32_bf16 v[132:135], v[152:155], v[32:35], v[132:135]\n" \
  "v_mfma_f32_16x16x32_bf16 v[120:123], v[148:151], v[40:43], v[120:123]\n" \
  "v_mfma_f32_16x16x32_bf16 v[136:139], v[152:155], v[40:43], v[136:139]\n" \
  "v_mfma_f32_16x16x32_bf16 v[116:119], v[156:159], v[36:39], v[116:119]\n" \
  "v_mfma_f32_16x16x32_bf16 v[132:135], v[160:163], v[36:39], v[132:135]\n" \
  "v_mfma_f32_16x16x32_bf16 v[120:123], v[156:159], v[44:47], v[120:123]\n" \
  "v_mfma_f32_16x16x32_bf16 v[136:139], v[160:163], v[44:47], v[136:139]\n" \
  "s_memrealtime s[76:77]\n" \
  "s_waitcnt lgkmcnt(0)\n" \
  "s_add_i32 s76, s76, s95\n" \
  "s_add_i32 s76, s76, 90\n" \
  "s_sub_i32 s76, s76, s86\n" \
  "s_cmp_gt_u32 s76, -3000\n" \
  "s_cselect_b32 s76, 0, s76\n" \
  "s_add_i32 s86, s86, s76\n" \
  "s_add_i32 s88, s60, 3\n" \
  "v_or_b32_e64 v252, s86, 1\n" \
  "v_add_u32 v253, s76, v253\n" \
  "buffer_load_dwordx4 v[64:67], %[mo], %[X], s63 offen offset:0 sc1\n" \
  "buffer_load_dwordx4 v[68:71], %[mo], %[X], s63 offen offset:1024 sc1\n" \
  "buffer_load_dwordx4 v[72:75], %[mo], %[X], s63 offen offset:2048 sc1\n" \
  "buffer_load_dwordx4 v[76:79], %[mo], %[X], s63 offen offset:3072 sc1\n" \
  "buffer_load_dwordx4 v[80:83], v228, %[X], s63 offen offset:0 sc1\n" \
  "buffer_load_dwordx4 v[84:87], v228, %[X], s63 offen offset:1024 sc1\n" \
  "buffer_load_dwordx4 v[88:91], v228, %[X], s63 offen offset:2048 sc1\n" \
  "buffer_load_dwordx4 v[92:95], v228, %[X], s63 offen offset:3072 sc1\n" \
  "buffer_load_dwordx4 v[96:99], %[mo], %[X], s64 offen offset:0 sc1\n" \
  "v_mov_b64 v[100:101], 0\n" \
  "v_mov_b64 v[102:103], 0\n" \
  "v_mov_b64 v[104:105], 0\n" \
  "v_mov_b64 v[106:107], 0\n" \
  "v_mov_b64 v[108:109], 0\n" \
  "v_mov_b64 v[110:111], 0\n" \
  "v_mov_b64 v[112:113], 0\n" \
  "v_mov_b64 v[114:115], 0\n" \
  "v_cvt_pk_bf16_f32 v220, v124, v125\n" \
  "v_cvt_pk_bf16_f32 v221, v126, v127\n" \
  "v_cvt_pk_bf16_f32 v222, v128, v129\n" \
  "v_cvt_pk_bf16_f32 v223, v130, v131\n" \
  "v_cvt_pk_bf16_f32 v224, v140, v141\n" \
  "v_cvt_pk_bf16_f32 v225, v142, v143\n" \
  "v_cvt_pk_bf16_f32 v226, v144, v145\n" \
  "v_cvt_pk_bf16_f32 v227, v146, v147\n" \
  "ds_write_b128 %[ktW], v[220:223] offset:0\n" \
  "ds_write_b128 %[ktW], v[224:227] offset:4096\n" \
  "v_xor_b32 %[hA], 0x10000, %[hA]\n" \
  "v_mov_b32 v231, 0\n" \
  "s_waitcnt lgkmcnt(0)\n" \
  "s_barrier\n" \
  "ds_read_b128 v[180:183], %[ktR] offset:0\n" \
  "ds_read_b128 v[184:187], %[ktR] offset:1024\n" \
  "ds_read_b128 v[188:191], %[ktR] offset:2048\n" \
  "ds_read_b128 v[192:195], %[ktR] offset:3072\n" \
  "ds_read_b128 v[196:199], %[ktR] offset:4096\n" \
  "ds_read_b128 v[200:203], %[ktR] offset:5120\n" \
  "ds_read_b128 v[204:207], %[ktR] offset:6144\n" \
  "ds_read_b128 v[208:211], %[ktR] offset:7168\n" \
  "s_waitcnt lgkmcnt(0)\n" \
  "s_branch .Lqk_m\n" \
  ".Lqk_entry:\n" \
  "s_mov_b32 s81, 63\n" \
  "s_mov_b32 s87, %[mr0]\n" \
  "s_cmp_eq_u32 %[grp], 0\n" \
  "s_cbranch_scc1 .Lqk_go\n" \
  "s_mov_b32 s69, 0\n" \
  "s_xor_b32 s75, %[bas], 0x2000\n" \
  ".Lqk_g1:\n" \
  "buffer_load_dword v229, off, %[X], s75 offset:48 sc1\n" \
  "s_waitcnt vmcnt(0)\n" \
  "v_readfirstlane_b32 s74, v229\n" \
  "s_cmp_lg_u32 s74, 0\n" \
  "s_cbranch_scc1 .Lqk_g2\n" \
  "s_sleep 8\n" \
  "s_add_i32 s69, s69, 1\n" \
  "s_cmp_lt_u32 s69, 400\n" \
  "s_cbranch_scc1 .Lqk_g1\n" \
  ".Lqk_g2:\n" \
  "s_add_i32 s74, s74, %[g1t]\n" \
  ".Lqk_g3:\n" \
  "s_memrealtime s[76:77]\n" \
  "s_waitcnt lgkmcnt(0)\n" \
  "s_sub_i32 s76, s76, s74\n" \
  "s_cmp_gt_u32 s76, -3000\n" \
  "s_cbranch_scc1 .Lqk_g3\n" \
  ".Lqk_go:\n" \
  "s_waitcnt vmcnt(0) lgkmcnt(0)\n" \
  "s_barrier\n" \
  "s_mov_b32 s75, 0\n" \
  "s_movk_i32 s76, 0x1000\n" \
  "s_movk_i32 s77, 0x2000\n" \
  "v_mov_b32 v236, 1\n" \
  "buffer_atomic_add v236, %[rdy], %[X], %[bas] offen offset:1536 sc0 sc1\n" \
  "s_mov_b32 s69, 0\n" \
  ".Lqk_enw:\n" \
  "buffer_load_dwordx4 v[64:67], %[hvo], %[HS], s75 offen offset:0 sc1\n" \
  "buffer_load_dwordx4 v[68:71], %[hvo], %[HS], s75 offen offset:1024 sc1\n" \
  "buffer_load_dwordx4 v[72:75], %[hvo], %[HS], s75 offen offset:2048 sc1\n" \
  "buffer_load_dwordx4 v[76:79], %[hvo], %[HS], s75 offen offset:3072 sc1\n" \
  "buffer_load_dwordx4 v[80:83], %[hvo], %[HS], s76 offen offset:0 sc1\n" \
  "buffer_load_dwordx4 v[84:87], %[hvo], %[HS], s76 offen offset:1024 sc1\n" \
  "buffer_load_dwordx4 v[88:91], %[hvo], %[HS], s76 offen offset:2048 sc1\n" \
  "buffer_load_dwordx4 v[92:95], %[hvo], %[HS], s76 offen offset:3072 sc1\n" \
  "buffer_load_dwordx4 v[96:99], %[hvo], %[HS], s77 offen offset:0 sc1\n" \
  "s_waitcnt vmcnt(0)\n" \
  "v_max3_u32 v229, v64, v65, v66\n" \
  "v_max3_u32 v229, v229, v67, v68\n" \
  "v_max3_u32 v229, v229, v69, v70\n" \
  "v_max3_u32 v229, v229, v71, v72\n" \
  "v_max3_u32 v229, v229, v73, v74\n" \
  "v_max3_u32 v229, v229, v75, v76\n" \
  "v_max3_u32 v229, v229, v77, v78\n" \
  "v_max3_u32 v229, v229, v79, v80\n" \
  "v_max3_u32 v229, v229, v81, v82\n" \
  "v_max3_u32 v229, v229, v83, v84\n" \
  "v_max3_u32 v229, v229, v85, v86\n" \
  "v_max3_u32 v229, v229, v87, v88\n" \
  "v_max3_u32 v229, v229, v89, v90\n" \
  "v_max3_u32 v229, v229, v91, v92\n" \
  "v_max3_u32 v229, v229, v93, v94\n" \
  "v_max3_u32 v229, v229, v95, v96\n" \
  "v_max3_u32 v229, v229, v97, v98\n" \
  "v_max_u32 v229, v229, v99\n" \
  "v_cmp_eq_u32 vcc, -1, v229\n" \
  "s_cbranch_vccz .Lqk_enw_ok\n" \
  "s_sleep 4\n" \
  "s_add_i32 s69, s69, 1\n" \
  "s_and_b32 s67, s69, 1023\n" \
  "s_cbranch_scc1 .Lqk_enw\n" \
  "buffer_load_dword v229, off, %[F], 0 offset:1024 sc1\n" \
  "s_waitcnt vmcnt(0)\n" \
  "v_readfirstlane_b32 s67, v229\n" \
  "s_cmp_lg_u32 s67, 0\n" \
  "s_cbranch_scc1 .Lqk_enw_ok\n" \
  "s_cmp_lt_u32 s69, 400000\n" \
  "s_cbranch_scc1 .Lqk_enw\n" \
  "v_mov_b32 v229, 1\n" \
  "buffer_store_dword v229, off, %[F], 0 offset:1024 sc1\n" \
  ".Lqk_enw_ok:\n" \
  "ds_write_b128 %[hvo], v[64:67] offset:0\n" \
  "ds_write_b128 %[hvo], v[68:71] offset:1024\n" \
  "ds_write_b128 %[hvo], v[72:75] offset:2048\n" \
  "ds_write_b128 %[hvo], v[76:79] offset:3072\n" \
  "ds_write_b128 %[hvo], v[80:83] offset:4096\n" \
  "ds_write_b128 %[hvo], v[84:87] offset:5120\n" \
  "ds_write_b128 %[hvo], v[88:91] offset:6144\n" \
  "ds_write_b128 %[hvo], v[92:95] offset:7168\n" \
  "ds_write_b128 %[hvo], v[96:99] offset:8192\n" \
  "v_readfirstlane_b32 s92, v236\n" \
  "s_cmp_lg_u32 s92, 63\n" \
  "s_cbranch_scc1 .Lqk_nrdy\n" \
  "s_memrealtime s[74:75]\n" \
  "s_waitcnt lgkmcnt(0)\n" \
  "s_add_i32 s74, s74, %[x0]\n" \
  "s_or_b32 s74, s74, 1\n" \
  "s_mul_i32 s75, %[tau], 162\n" \
  "s_lshr_b32 s75, s75, 8\n" \
  "s_add_i32 s75, s75, s74\n" \
  "v_mov_b32 v236, s74\n" \
  "v_mov_b32 v237, s75\n" \
  "buffer_store_dwordx2 v[236:237], %[rdy], %[X], %[bas] offen offset:0 sc1\n" \
  "s_add_i32 s74, s74, %[tau]\n" \
  "s_or_b32 s74, s74, 1\n" \
  "s_add_i32 s75, s75, %[tau]\n" \
  "v_mov_b32 v236, s74\n" \
  "v_mov_b32 v237, s75\n" \
  "buffer_store_dwordx2 v[236:237], %[rdy], %[X], %[bas] offen offset:16 sc1\n" \
  ".Lqk_nrdy:\n" \
  "s_mov_b32 s95, %[tau]\n" \
  "s_mov_b32 s88, 3\n" \
  "s_mov_b32 s90, 0\n" \
  "s_mov_b32 s78, 0\n" \
  "s_waitcnt lgkmcnt(0)\n" \
  "s_barrier\n" \
  "ds_read_b128 v[148:151], %[hA] offset:0\n" \
  "ds_read_b128 v[152:155], %[hA] offset:18432\n" \
  "ds_read_b128 v[156:159], %[hA] offset:512\n" \
  "ds_read_b128 v[160:163], %[hA] offset:18944\n" \
  "ds_read_b128 v[164:167], %[hA] offset:1024\n" \
  "ds_read_b128 v[168:171], %[hA] offset:19456\n" \
  "ds_read_b128 v[172:175], %[hA] offset:1536\n" \
  "ds_read_b128 v[176:179], %[hA] offset:19968\n" \
  "s_waitcnt lgkmcnt(0)\n" \
  "v_mfma_f32_16x16x32_bf16 v[116:119], v[148:151], a[0:3], 0\n" \
  "v_mfma_f32_16x16x32_bf16 v[132:135], v[152:155], a[0:3], 0\n" \
  "v_mfma_f32_16x16x32_bf16 v[120:123], v[148:151], a[8:11], 0\n" \
  "v_mfma_f32_16x16x32_bf16 v[136:139], v[152:155], a[8:11], 0\n" \
  "v_mfma_f32_16x16x32_bf16 v[124:127], a[16:19], v[148:151], 0\n" \
  "v_mfma_f32_16x16x32_bf16 v[140:143], a[16:19], v[152:155], 0\n" \
  "v_mfma_f32_16x16x32_bf16 v[128:131], a[24:27], v[148:151], 0\n" \
  "v_mfma_f32_16x16x32_bf16 v[144:147], a[24:27], v[152:155], 0\n" \
  "v_mfma_f32_16x16x32_bf16 v[116:119], v[156:159], a[4:7], v[116:119]\n" \
  "v_mfma_f32_16x16x32_bf16 v[132:135], v[160:163], a[4:7], v[132:135]\n" \
  "v_mfma_f32_16x16x32_bf16 v[120:123], v[156:159], a[12:15], v[120:123]\n" \
  "v_mfma_f32_16x16x32_bf16 v[136:139], v[160:163], a[12:15], v[136:139]\n" \
  "v_mfma_f32_16x16x32_bf16 v[124:127], a[20:23], v[156:159], v[124:127]\n" \
  "v_mfma_f32_16x16x32_bf16 v[140:143], a[20:23], v[160:163], v[140:143]\n" \
  "v_mfma_f32_16x16x32_bf16 v[128:131], a[28:31], v[156:159], v[128:131]\n" \
  "v_mfma_f32_16x16x32_bf16 v[144:147], a[28:31], v[160:163], v[144:147]\n" \
  "s_waitcnt lgkmcnt(0)\n" \
  "s_branch .Lqk_top\n" \
  ".Lqk_end:\n"
#define QK_CLOB "v32", "v33", "v34", "v35", "v36", "v37", "v38", "v39", "v40", "v41", "v42", "v43", "v44", "v45", "v46", "v47", "v48", "v49", "v50", "v51", "v52", "v53", "v54", "v55", "v56", "v57", "v58", "v59", "v60", "v61", "v62", "v63", "v64", "v65", "v66", "v67", "v68", "v69", "v70", "v71", "v72", "v73", "v74", "v75", "v76", "v77", "v78", "v79", "v80", "v81", "v82", "v83", "v84", "v85", "v86", "v87", "v88", "v89", "v90", "v91", "v92", "v93", "v94", "v95", "v96", "v97", "v98", "v99", "v100", "v101", "v102", "v103", "v104", "v105", "v106", "v107", "v108", "v109", "v110", "v111", "v112", "v113", "v114", "v115", "v116", "v117", "v118", "v119", "v120", "v121", "v122", "v123", "v124", "v125", "v126", "v127", "v128", "v129", "v130", "v131", "v132", "v133", "v134", "v135", "v136", "v137", "v138", "v139", "v140", "v141", "v142", "v143", "v144", "v145", "v146", "v147", "v148", "v149", "v150", "v151", "v152", "v153", "v154", "v155", "v156", "v157", "v158", "v159", "v160", "v161", "v162", "v163", "v164", "v165", "v166", "v167", "v168", "v169", "v170", "v171", "v172", "v173", "v174", "v175", "v176", "v177", "v178", "v179", "v180", "v181", "v182", "v183", "v184", "v185", "v186", "v187", "v188", "v189", "v190", "v191", "v192", "v193", "v194", "v195", "v196", "v197", "v198", "v199", "v200", "v201", "v202", "v203", "v204", "v205", "v206", "v207", "v208", "v209", "v210", "v211", "v212", "v213", "v214", "v215", "v216", "v217", "v218", "v219", "v220", "v221", "v222", "v223", "v224", "v225", "v226", "v227", "v228", "v229", "v230", "v231", "v232", "v233", "v234", "v235", "v236", "v237", "v238", "v239", "v240", "v241", "v242", "v243", "v244", "v245", "v246", "v247", "v248", "v249", "v250", "v251", "v252", "v253", "v254", "v255", "s60", "s61", "s62", "s63", "s64", "s65", "s66", "s67", "s68", "s69", "s70", "s71", "s72", "s73", "s74", "s75", "s76", "s77", "s78", "s79", "s80", "s81", "s82", "s83", "s84", "s85", "s86", "s87", "s88", "s89", "s90", "s91", "s92", "s93", "s94", "s95", "s96", "a0", "a1", "a2", "a3", "a4", "a5", "a6", "a7", "a8", "a9", "a10", "a11", "a12", "a13", "a14", "a15", "a16", "a17", "a18", "a19", "a20", "a21", "a22", "a23", "a24", "a25", "a26", "a27", "a28", "a29", "a30", "a31", "a32", "a33", "a34", "a35", "a36", "a37", "a38", "a39", "a40", "a41", "a42", "a43", "a44", "a45", "a46", "a47", "a48", "a49", "a50", "a51", "a52", "a53", "a54", "a55", "a56", "a57", "a58", "a59", "a60", "a61", "a62", "a63", "a64", "a65", "a66", "a67", "a68", "a69", "a70", "a71", "a72", "a73", "a74", "a75", "a76", "a77", "a78", "a79", "a80", "a81", "a82", "a83", "a84", "a85", "a86", "a87", "a88", "a89", "a90", "a91", "a92", "a93", "a94", "a95", "a96", "a97", "a98", "a99", "a100", "a101", "a102", "a103", "a104", "a105", "a106", "a107", "a108", "a109", "a110", "a111", "a112", "a113", "a114", "a115", "a116", "a117", "a118", "a119", "a120", "a121", "a122", "a123", "a124", "a125", "a126", "a127", "a128", "a129", "a130", "a131", "a132", "a133", "a134", "a135", "a136", "a137", "a138", "a139", "a140", "a141", "a142", "a143", "a144", "a145", "a146", "a147", "a148", "a149", "a150", "a151", "a152", "a153", "a154", "a155", "a156", "a157", "a158", "a159", "a160", "a161", "a162", "a163", "a164", "a165", "a166", "a167", "a168", "a169", "a170", "a171", "a172", "a173", "a174", "a175", "a176", "a177", "a178", "a179", "a180", "a181", "a182", "a183", "a184", "a185", "a186", "a187", "a188", "a189", "a190", "a191", "a192", "a193", "a194", "a195", "a196", "a197", "a198", "a199", "a200", "a201", "a202", "a203", "a204", "a205", "a206", "a207", "a208", "a209", "a210", "a211", "a212", "a213", "a214", "a215", "a216", "a217", "a218", "a219", "a220", "a221", "a222", "a223", "a224", "a225", "a226", "a227", "a228", "a229", "a230", "a231", "a232", "a233", "a234", "a235", "a236", "a237", "a238", "a239", "a240", "a241", "a242", "a243", "a244", "a245", "a246", "a247", "a248", "a249", "a250", "a251", "a252", "a253", "a254", "a255", "vcc", "scc", "m0", "memory"
#define VT_ASM \
  "s_nop 4\n" \
  "s_cmp_eq_u32 %[grp], 0\n" \
  "s_cbranch_scc1 .Lvt_w0\n" \
  "v_add_u32 v224, 65536, %[wo]\n" \
  "global_load_dwordx4 v[32:35], v224, %[wp] offset:0\n" \
  "global_load_dwordx4 v[36:39], v224, %[wp] offset:1024\n" \
  "global_load_dwordx4 v[40:43], v224, %[wp] offset:2048\n" \
  "global_load_dwordx4 v[44:47], v224, %[wp] offset:3072\n" \
  "v_add_u32 v224, 4096, v224\n" \
  "global_load_dwordx4 v[48:51], v224, %[wp] offset:0\n" \
  "global_load_dwordx4 v[52:55], v224, %[wp] offset:1024\n" \
  "global_load_dwordx4 v[56:59], v224, %[wp] offset:2048\n" \
  "global_load_dwordx4 v[60:63], v224, %[wp] offset:3072\n" \
  "v_add_u32 v224, 4096, v224\n" \
  "s_branch .Lvt_w1\n" \
  ".Lvt_w0:\n" \
  "v_add_u32 v224, 0, %[wo]\n" \
  "global_load_dwordx4 a[0:3], v224, %[wp] offset:0\n" \
  "global_load_dwordx4 a[4:7], v224, %[wp] offset:1024\n" \
  "global_load_dwordx4 a[8:11], v224, %[wp] offset:2048\n" \
  "global_load_dwordx4 a[12:15], v224, %[wp] offset:3072\n" \
  "v_add_u32 v224, 4096, v224\n" \
  "global_load_dwordx4 a[16:19], v224, %[wp] offset:0\n" \
  "global_load_dwordx4 a[20:23], v224, %[wp] offset:1024\n" \
  "global_load_dwordx4 a[24:27], v224, %[wp] offset:2048\n" \
  "global_load_dwordx4 a[28:31], v224, %[wp] offset:3072\n" \
  "v_add_u32 v224, 4096, v224\n" \
  "global_load_dwordx4 a[32:35], v224, %[wp] offset:0\n" \
  "global_load_dwordx4 a[36:39], v224, %[wp] offset:1024\n" \
  "global_load_dwordx4 a[40:43], v224, %[wp] offset:2048\n" \
  "global_load_dwordx4 a[44:47], v224, %[wp] offset:3072\n" \
  "v_add_u32 v224, 4096, v224\n" \
  "global_load_dwordx4 a[48:51], v224, %[wp] offset:0\n" \
  "global_load_dwordx4 a[52:55], v224, %[wp] offset:1024\n" \
  "global_load_dwordx4 a[56:59], v224, %[wp] offset:2048\n" \
  "global_load_dwordx4 a[60:63], v224, %[wp] offset:3072\n" \
  "v_add_u32 v224, 4096, v224\n" \
  "global_load_dwordx4 a[64:67], v224, %[wp] offset:0\n" \
  "global_load_dwordx4 a[68:71], v224, %[wp] offset:1024\n" \
  "global_load_dwordx4 a[72:75], v224, %[wp] offset:2048\n" \
  "global_load_dwordx4 a[76:79], v224, %[wp] offset:3072\n" \
  "v_add_u32 v224, 4096, v224\n" \
  "global_load_dwordx4 a[80:83], v224, %[wp] offset:0\n" \
  "global_load_dwordx4 a[84:87], v224, %[wp] offset:1024\n" \
  "global_load_dwordx4 a[88:91], v224, %[wp] offset:2048\n" \
  "global_load_dwordx4 a[92:95], v224, %[wp] offset:3072\n" \
  "v_add_u32 v224, 4096, v224\n" \
  "global_load_dwordx4 a[96:99], v224, %[wp] offset:0\n" \
  "global_load_dwordx4 a[100:103], v224, %[wp] offset:1024\n" \
  "global_load_dwordx4 a[104:107], v224, %[wp] offset:2048\n" \
  "global_load_dwordx4 a[108:111], v224, %[wp] offset:3072\n" \
  "v_add_u32 v224, 4096, v224\n" \
  "global_load_dwordx4 a[112:115], v224, %[wp] offset:0\n" \
  "global_load_dwordx4 a[116:119], v224, %[wp] offset:1024\n" \
  "global_load_dwordx4 a[120:123], v224, %[wp] offset:2048\n" \
  "global_load_dwordx4 a[124:127], v224, %[wp] offset:3072\n" \
  "v_add_u32 v224, 4096, v224\n" \
  "global_load_dwordx4 a[128:131], v224, %[wp] offset:0\n" \
  "global_load_dwordx4 a[132:135], v224, %[wp] offset:1024\n" \
  "global_load_dwordx4 a[136:139], v224, %[wp] offset:2048\n" \
  "global_load_dwordx4 a[140:143], v224, %[wp] offset:3072\n" \
  "v_add_u32 v224, 4096, v224\n" \
  "global_load_dwordx4 a[144:147], v224, %[wp] offset:0\n" \
  "global_load_dwordx4 a[148:151], v224, %[wp] offset:1024\n" \
  "global_load_dwordx4 a[152:155], v224, %[wp] offset:2048\n" \
  "global_load_dwordx4 a[156:159], v224, %[wp] offset:3072\n" \
  "v_add_u32 v224, 4096, v224\n" \
  "global_load_dwordx4 a[160:163], v224, %[wp] offset:0\n" \
  "global_load_dwordx4 a[164:167], v224, %[wp] offset:1024\n" \
  "global_load_dwordx4 a[168:171], v224, %[wp] offset:2048\n" \
  "global_load_dwordx4 a[172:175], v224, %[wp] offset:3072\n" \
  "v_add_u32 v224, 4096, v224\n" \
  "global_load_dwordx4 a[176:179], v224, %[wp] offset:0\n" \
  "global_load_dwordx4 a[180:183], v224, %[wp] offset:1024\n" \
  "global_load_dwordx4 a[184:187], v224, %[wp] offset:2048\n" \
  "global_load_dwordx4 a[188:191], v224, %[wp] offset:3072\n" \
  "v_add_u32 v224, 4096, v224\n" \
  "global_load_dwordx4 a[192:195], v224, %[wp] offset:0\n" \
  "global_load_dwordx4 a[196:199], v224, %[wp] offset:1024\n" \
  "global_load_dwordx4 a[200:203], v224, %[wp] offset:2048\n" \
  "global_load_dwordx4 a[204:207], v224, %[wp] offset:3072\n" \
  "v_add_u32 v224, 4096, v224\n" \
  "global_load_dwordx4 a[208:211], v224, %[wp] offset:0\n" \
  "global_load_dwordx4 a[212:215], v224, %[wp] offset:1024\n" \
  "global_load_dwordx4 a[216:219], v224, %[wp] offset:2048\n" \
  "global_load_dwordx4 a[220:223], v224, %[wp] offset:3072\n" \
  "v_add_u32 v224, 4096, v224\n" \
  "global_load_dwordx4 a[224:227], v224, %[wp] offset:0\n" \
  "global_load_dwordx4 a[228:231], v224, %[wp] offset:1024\n" \
  "global_load_dwordx4 a[232:235], v224, %[wp] offset:2048\n" \
  "global_load_dwordx4 a[236:239], v224, %[wp] offset:3072\n" \
  "v_add_u32 v224, 4096, v224\n" \
  "global_load_dwordx4 a[240:243], v224, %[wp] offset:0\n" \
  "global_load_dwordx4 a[244:247], v224, %[wp] offset:1024\n" \
  "global_load_dwordx4 a[248:251], v224, %[wp] offset:2048\n" \
  "global_load_dwordx4 a[252:255], v224, %[wp] offset:3072\n" \
  "v_add_u32 v224, 4096, v224\n" \
  "global_load_dwordx4 v[32:35], v224, %[wp] offset:0\n" \
  "global_load_dwordx4 v[36:39], v224, %[wp] offset:1024\n" \
  "global_load_dwordx4 v[40:43], v224, %[wp] offset:2048\n" \
  "global_load_dwordx4 v[44:47], v224, %[wp] offset:3072\n" \
  "v_add_u32 v224, 4096, v224\n" \
  "global_load_dwordx4 v[48:51], v224, %[wp] offset:0\n" \
  "global_load_dwordx4 v[52:55], v224, %[wp] offset:1024\n" \
  "global_load_dwordx4 v[56:59], v224, %[wp] offset:2048\n" \
  "global_load_dwordx4 v[60:63], v224, %[wp] offset:3072\n" \
  "v_add_u32 v224, 4096, v224\n" \
  ".Lvt_w1:\n" \
  "s_mov_b32 s60, -1\n" \
  "s_mov_b32 s81, 0\n" \
  "s_mov_b32 s87, 2.0\n" \
  "s_mov_b32 s78, 0\n" \
  "s_mov_b32 s79, 0\n" \
  "s_mov_b32 s80, 0\n" \
  "s_add_i32 s67, %[bas], -48\n" \
  "s_mov_b32 s66, 0\n" \
  ".Lvt_top:\n" \
  "s_memrealtime s[74:75]\n" \
  "s_waitcnt lgkmcnt(0)\n" \
  "s_sub_i32 s91, s74, s66\n" \
  "s_cmp_gt_u32 s91, 14\n" \
  "s_cbranch_scc1 .Lvt_late\n" \
  ".Lvt_al_ok:\n" \
  "v_mfma_f32_16x16x32_bf16 v[64:67], a[0:3], v[80:83], 0\n" \
  "ds_read_b128 v[104:107], %[hA] offset:1536\n" \
  "v_mfma_f32_16x16x32_bf16 v[72:75], a[0:3], v[84:87], 0\n" \
  "ds_read_b128 v[108:111], %[hA] offset:19968\n" \
  "v_mfma_f32_16x16x32_bf16 v[68:71], a[8:11], v[80:83], 0\n" \
  "v_mov_b32 v225, s60\n" \
  "s_add_i32 s67, s67, 16\n" \
  "v_mfma_f32_16x16x32_bf16 v[76:79], a[8:11], v[84:87], 0\n" \
  "v_mfma_f32_16x16x32_bf16 v[64:67], a[4:7], v[88:91], v[64:67]\n" \
  "ds_read_b128 v[80:83], %[hA] offset:2048\n" \
  "v_mfma_f32_16x16x32_bf16 v[72:75], a[4:7], v[92:95], v[72:75]\n" \
  "ds_read_b128 v[84:87], %[hA] offset:20480\n" \
  "v_mfma_f32_16x16x32_bf16 v[68:71], a[12:15], v[88:91], v[68:71]\n" \
  "s_sub_i32 s90, s74, s89\n" \
  "s_max_i32 s92, s91, -2\n" \
  "s_add_i32 s89, s66, s92\n" \
  "v_mfma_f32_16x16x32_bf16 v[76:79], a[12:15], v[92:95], v[76:79]\n" \
  "s_ashr_i32 s92, s92, 1\n" \
  "s_add_i32 s90, s90, s92\n" \
  "v_mfma_f32_16x16x32_bf16 v[64:67], a[16:19], v[96:99], v[64:67]\n" \
  "s_mul_i32 s93, s60, 130\n" \
  "s_and_b32 s93, s93, 0xfc\n" \
  "ds_read_b128 v[88:91], %[hA] offset:2560\n" \
  "v_mfma_f32_16x16x32_bf16 v[72:75], a[16:19], v[100:103], v[72:75]\n" \
  "ds_read_b128 v[92:95], %[hA] offset:20992\n" \
  "v_mov_b32 v227, s90\n" \
  "v_mfma_f32_16x16x32_bf16 v[68:71], a[24:27], v[96:99], v[68:71]\n" \
  "v_mfma_f32_16x16x32_bf16 v[76:79], a[24:27], v[100:103], v[76:79]\n" \
  "s_waitcnt lgkmcnt(4)\n" \
  "v_mfma_f32_16x16x32_bf16 v[64:67], a[20:23], v[104:107], v[64:67]\n" \
  "ds_read_b128 v[96:99], %[hA] offset:3072\n" \
  "v_mfma_f32_16x16x32_bf16 v[72:75], a[20:23], v[108:111], v[72:75]\n" \
  "ds_read_b128 v[100:103], %[hA] offset:21504\n" \
  "v_mfma_f32_16x16x32_bf16 v[68:71], a[28:31], v[104:107], v[68:71]\n" \
  "v_mfma_f32_16x16x32_bf16 v[76:79], a[28:31], v[108:111], v[76:79]\n" \
  "s_waitcnt lgkmcnt(4)\n" \
  "v_mfma_f32_16x16x32_bf16 v[64:67], a[32:35], v[80:83], v[64:67]\n" \
  "ds_read_b128 v[104:107], %[hA] offset:3584\n" \
  "v_mfma_f32_16x16x32_bf16 v[72:75], a[32:35], v[84:87], v[72:75]\n" \
  "ds_read_b128 v[108:111], %[hA] offset:22016\n" \
  "v_mfma_f32_16x16x32_bf16 v[68:71], a[40:43], v[80:83], v[68:71]\n" \
  "v_mfma_f32_16x16x32_bf16 v[76:79], a[40:43], v[84:87], v[76:79]\n" \
  "s_waitcnt lgkmcnt(4)\n" \
  "v_mfma_f32_16x16x32_bf16 v[64:67], a[36:39], v[88:91], v[64:67]\n" \
  "ds_read_b128 v[80:83], %[hA] offset:4096\n" \
  "v_mfma_f32_16x16x32_bf16 v[72:75], a[36:39], v[92:95], v[72:75]\n" \
  "ds_read_b128 v[84:87], %[hA] offset:22528\n" \
  "v_mfma_f32_16x16x32_bf16 v[68:71], a[44:47], v[88:91], v[68:71]\n" \
  "s_waitcnt vmcnt(0)\n" \
  "s_barrier\n" \
  "buffer_store_dword v225, %[flo], %[X], s87 offen sc1\n" \
  "v_mfma_f32_16x16x32_bf16 v[76:79], a[44:47], v[92:95], v[76:79]\n" \
  "s_waitcnt lgkmcnt(4)\n" \
  "v_mfma_f32_16x16x32_bf16 v[64:67], a[48:51], v[96:99], v[64:67]\n" \
  "ds_read_b128 v[88:91], %[hA] offset:4608\n" \
  "v_mfma_f32_16x16x32_bf16 v[72:75], a[48:51], v[100:103], v[72:75]\n" \
  "ds_read_b128 v[92:95], %[hA] offset:23040\n" \
  "v_mfma_f32_16x16x32_bf16 v[68:71], a[56:59], v[96:99], v[68:71]\n" \
  "buffer_load_dwordx2 v[228:229], off, %[X], s67 sc1\n" \
  "v_mfma_f32_16x16x32_bf16 v[76:79], a[56:59], v[100:103], v[76:79]\n" \
  "s_waitcnt lgkmcnt(4)\n" \
  "v_mfma_f32_16x16x32_bf16 v[64:67], a[52:55], v[104:107], v[64:67]\n" \
  "ds_read_b128 v[96:99], %[hA] offset:5120\n" \
  "v_mfma_f32_16x16x32_bf16 v[72:75], a[52:55], v[108:111], v[72:75]\n" \
  "ds_read_b128 v[100:103], %[hA] offset:23552\n" \
  "v_mfma_f32_16x16x32_bf16 v[68:71], a[60:63], v[104:107], v[68:71]\n" \
  "buffer_atomic_umax v227, %[rpo], %[X], s93 offen sc1\n" \
  "v_mfma_f32_16x16x32_bf16 v[76:79], a[60:63], v[108:111], v[76:79]\n" \
  "s_waitcnt lgkmcnt(4)\n" \
  "v_mfma_f32_16x16x32_bf16 v[64:67], a[64:67], v[80:83], v[64:67]\n" \
  "ds_read_b128 v[104:107], %[hA] offset:5632\n" \
  "v_mfma_f32_16x16x32_bf16 v[72:75], a[64:67], v[84:87], v[72:75]\n" \
  "ds_read_b128 v[108:111], %[hA] offset:24064\n" \
  "v_mfma_f32_16x16x32_bf16 v[68:71], a[72:75], v[80:83], v[68:71]\n" \
  "s_add_i32 s64, s60, 1\n" \
  "s_min_i32 s61, s64, 63\n" \
  "s_mul_i32 s61, s61, 36864\n" \
  "v_mfma_f32_16x16x32_bf16 v[76:79], a[72:75], v[84:87], v[76:79]\n" \
  "s_waitcnt lgkmcnt(4)\n" \
  "v_mfma_f32_16x16x32_bf16 v[64:67], a[68:71], v[88:91], v[64:67]\n" \
  "ds_read_b128 v[80:83], %[hA] offset:6144\n" \
  "v_mfma_f32_16x16x32_bf16 v[72:75], a[68:71], v[92:95], v[72:75]\n" \
  "ds_read_b128 v[84:87], %[hA] offset:24576\n" \
  "v_mfma_f32_16x16x32_bf16 v[68:71], a[76:79], v[88:91], v[68:71]\n" \
  "s_lshl_b32 s62, s64, 16\n" \
  "s_and_b32 s62, s62, 0x10000\n" \
  "s_add_i32 s62, %[hl0], s62\n" \
  "v_mfma_f32_16x16x32_bf16 v[76:79], a[76:79], v[92:95], v[76:79]\n" \
  "s_waitcnt lgkmcnt(4)\n" \
  "v_mfma_f32_16x16x32_bf16 v[64:67], a[80:83], v[96:99], v[64:67]\n" \
  "ds_read_b128 v[88:91], %[hA] offset:6656\n" \
  "v_mfma_f32_16x16x32_bf16 v[72:75], a[80:83], v[100:103], v[72:75]\n" \
  "ds_read_b128 v[92:95], %[hA] offset:25088\n" \
  "v_mfma_f32_16x16x32_bf16 v[68:71], a[88:91], v[96:99], v[68:71]\n" \
  "s_and_b32 s63, s60, 7\n" \
  "s_lshl_b32 s63, s63, 22\n" \
  "s_add_i32 m0, s62, 0\n" \
  "v_mfma_f32_16x16x32_bf16 v[76:79], a[88:91], v[100:103], v[76:79]\n" \
  "s_waitcnt lgkmcnt(4)\n" \
  "v_mfma_f32_16x16x32_bf16 v[64:67], a[84:87], v[104:107], v[64:67]\n" \
  "ds_read_b128 v[96:99], %[hA] offset:7168\n" \
  "v_mfma_f32_16x16x32_bf16 v[72:75], a[84:87], v[108:111], v[72:75]\n" \
  "ds_read_b128 v[100:103], %[hA] offset:25600\n" \
  "v_mfma_f32_16x16x32_bf16 v[68:71], a[92:95], v[104:107], v[68:71]\n" \
  "s_cmp_lt_i32 s60, 0\n" \
  "s_cselect_b32 s63, 2.0, s63\n" \
  "s_cselect_b32 s61, 2.0, s61\n" \
  "v_mfma_f32_16x16x32_bf16 v[76:79], a[92:95], v[108:111], v[76:79]\n" \
  "s_waitcnt lgkmcnt(4)\n" \
  "v_mfma_f32_16x16x32_bf16 v[64:67], a[96:99], v[80:83], v[64:67]\n" \
  "ds_read_b128 v[104:107], %[hA] offset:7680\n" \
  "v_mfma_f32_16x16x32_bf16 v[72:75], a[96:99], v[84:87], v[72:75]\n" \
  "ds_read_b128 v[108:111], %[hA] offset:26112\n" \
  "v_mfma_f32_16x16x32_bf16 v[68:71], a[104:107], v[80:83], v[68:71]\n" \
  "v_mfma_f32_16x16x32_bf16 v[76:79], a[104:107], v[84:87], v[76:79]\n" \
  "s_waitcnt lgkmcnt(4)\n" \
  "v_mfma_f32_16x16x32_bf16 v[64:67], a[100:103], v[88:91], v[64:67]\n" \
  "ds_read_b128 v[80:83], %[hA] offset:8192\n" \
  "v_mfma_f32_16x16x32_bf16 v[72:75], a[100:103], v[92:95], v[72:75]\n" \
  "ds_read_b128 v[84:87], %[hA] offset:26624\n" \
  "v_mfma_f32_16x16x32_bf16 v[68:71], a[108:111], v[88:91], v[68:71]\n" \
  "v_mfma_f32_16x16x32_bf16 v[76:79], a[108:111], v[92:95], v[76:79]\n" \
  "s_waitcnt lgkmcnt(4)\n" \
  "v_mfma_f32_16x16x32_bf16 v[64:67], a[112:115], v[96:99], v[64:67]\n" \
  "ds_read_b128 v[88:91], %[hA] offset:8704\n" \
  "v_mfma_f32_16x16x32_bf16 v[72:75], a[112:115], v[100:103], v[72:75]\n" \
  "ds_read_b128 v[92:95], %[hA] offset:27136\n" \
  "v_mfma_f32_16x16x32_bf16 v[68:71], a[120:123], v[96:99], v[68:71]\n" \
  "v_mfma_f32_16x16x32_bf16 v[76:79], a[120:123], v[100:103], v[76:79]\n" \
  "s_waitcnt lgkmcnt(4)\n" \
  "v_mfma_f32_16x16x32_bf16 v[64:67], a[116:119], v[104:107], v[64:67]\n" \
  "v_mfma_f32_16x16x32_bf16 v[72:75], a[116:119], v[108:111], v[72:75]\n" \
  "v_mfma_f32_16x16x32_bf16 v[68:71], a[124:127], v[104:107], v[68:71]\n" \
  "v_mfma_f32_16x16x32_bf16 v[76:79], a[124:127], v[108:111], v[76:79]\n" \
  "s_waitcnt lgkmcnt(2)\n" \
  "v_mfma_f32_16x16x32_bf16 v[64:67], a[128:131], v[80:83], v[64:67]\n" \
  "buffer_load_dwordx4 %[hvo], %[HS], s61 offen offset:0 sc1 lds\n" \
  "v_mfma_f32_16x16x32_bf16 v[72:75], a[128:131], v[84:87], v[72:75]\n" \
  "v_mfma_f32_16x16x32_bf16 v[68:71], a[136:139], v[80:83], v[68:71]\n" \
  "v_mfma_f32_16x16x32_bf16 v[76:79], a[136:139], v[84:87], v[76:79]\n" \
  "s_waitcnt lgkmcnt(0)\n" \
  "v_mfma_f32_16x16x32_bf16 v[64:67], a[132:135], v[88:91], v[64:67]\n" \
  "buffer_load_dwordx4 %[hvo], %[HS], s61 offen offset:1024 sc1 lds\n" \
  "v_mfma_f32_16x16x32_bf16 v[72:75], a[132:135], v[92:95], v[72:75]\n" \
  "v_mfma_f32_16x16x32_bf16 v[68:71], a[140:143], v[88:91], v[68:71]\n" \
  "s_nop 2\n" \
  "v_mfma_f32_16x16x32_bf16 v[76:79], a[140:143], v[92:95], v[76:79]\n" \
  "buffer_load_dwordx4 %[hvo], %[HS], s61 offen offset:2048 sc1 lds\n" \
  "v_cvt_pk_bf16_f32 v112, v64, v65\n" \
  "v_cvt_pk_bf16_f32 v113, v66, v67\n" \
  "s_nop 0\n" \
  "v_cvt_pk_bf16_f32 v114, v68, v69\n" \
  "v_cvt_pk_bf16_f32 v115, v70, v71\n" \
  "ds_write_b128 %[vW], v[112:115]\n" \
  "v_cvt_pk_bf16_f32 v116, v72, v73\n" \
  "v_cvt_pk_bf16_f32 v117, v74, v75\n" \
  "v_cvt_pk_bf16_f32 v118, v76, v77\n" \
  "v_cvt_pk_bf16_f32 v119, v78, v79\n" \
  "ds_write_b128 %[vW], v[116:119] offset:4096\n" \
  "buffer_load_dwordx4 %[hvo], %[HS], s61 offen offset:3072 sc1 lds\n" \
  "v_cmp_u_f32 vcc, v64, v72\n" \
  "s_cbranch_vccnz .Lvt_sh\n" \
  ".Lvt_sh_back:\n" \
  "s_waitcnt lgkmcnt(0)\n" \
  "s_barrier\n" \
  "s_add_i32 s61, s61, 0x1000\n" \
  "s_add_i32 m0, s62, 4096\n" \
  "s_nop 0\n" \
  "buffer_load_dwordx4 %[hvo], %[HS], s61 offen offset:0 sc1 lds\n" \
  "ds_read_b128 v[120:123], %[vR] offset:0\n" \
  "ds_read_b128 v[136:139], %[vR] offset:4096\n" \
  "ds_read_b128 v[124:127], %[vR] offset:1024\n" \
  "ds_read_b128 v[140:143], %[vR] offset:5120\n" \
  "ds_read_b128 v[128:131], %[vR] offset:2048\n" \
  "ds_read_b128 v[144:147], %[vR] offset:6144\n" \
  "ds_read_b128 v[132:135], %[vR] offset:3072\n" \
  "ds_read_b128 v[148:151], %[vR] offset:7168\n" \
  "buffer_load_dwordx4 %[hvo], %[HS], s61 offen offset:1024 sc1 lds\n" \
  "s_waitcnt lgkmcnt(6)\n" \
  "v_mfma_f32_16x16x32_bf16 v[152:155], v[120:123], a[144:147], 0\n" \
  "buffer_load_dwordx4 %[hvo], %[HS], s61 offen offset:2048 sc1 lds\n" \
  "v_mfma_f32_16x16x32_bf16 v[156:159], v[136:139], a[144:147], 0\n" \
  "s_waitcnt lgkmcnt(4)\n" \
  "v_mfma_f32_16x16x32_bf16 v[152:155], v[124:127], a[148:151], v[152:155]\n" \
  "v_mfma_f32_16x16x32_bf16 v[156:159], v[140:143], a[148:151], v[156:159]\n" \
  "s_waitcnt lgkmcnt(2)\n" \
  "v_mfma_f32_16x16x32_bf16 v[152:155], v[128:131], a[152:155], v[152:155]\n" \
  "buffer_load_dwordx4 %[hvo], %[HS], s61 offen offset:3072 sc1 lds\n" \
  "v_mfma_f32_16x16x32_bf16 v[156:159], v[144:147], a[152:155], v[156:159]\n" \
  "s_waitcnt lgkmcnt(0)\n" \
  "v_mfma_f32_16x16x32_bf16 v[152:155], v[132:135], a[156:159], v[152:155]\n" \
  "v_mfma_f32_16x16x32_bf16 v[156:159], v[148:151], a[156:159], v[156:159]\n" \
  "v_mfma_f32_16x16x32_bf16 v[160:163], v[120:123], a[160:163], 0\n" \
  "s_add_i32 s61, s61, 0x1000\n" \
  "s_add_i32 m0, s62, 8192\n" \
  "s_nop 0\n" \
  "buffer_load_dwordx4 %[hvo], %[HS], s61 offen offset:0 sc1 lds\n" \
  "v_mfma_f32_16x16x32_bf16 v[164:167], v[136:139], a[160:163], 0\n" \
  "v_mfma_f32_16x16x32_bf16 v[160:163], v[124:127], a[164:167], v[160:163]\n" \
  "v_cvt_pk_bf16_f32 v176, v152, v153\n" \
  "v_cvt_pk_bf16_f32 v177, v154, v155\n" \
  "v_mfma_f32_16x16x32_bf16 v[164:167], v[140:143], a[164:167], v[164:167]\n" \
  "v_cvt_pk_bf16_f32 v178, v156, v157\n" \
  "v_cvt_pk_bf16_f32 v179, v158, v159\n" \
  "v_mfma_f32_16x16x32_bf16 v[160:163], v[128:131], a[168:171], v[160:163]\n" \
  "buffer_store_dwordx4 v[176:179], %[sto], %[X], s63 offen sc1\n" \
  "s_add_i32 s63, s63, 0x10000\n" \
  "v_mfma_f32_16x16x32_bf16 v[164:167], v[144:147], a[168:171], v[164:167]\n" \
  "v_mfma_f32_16x16x32_bf16 v[160:163], v[132:135], a[172:175], v[160:163]\n" \
  "v_mfma_f32_16x16x32_bf16 v[164:167], v[148:151], a[172:175], v[164:167]\n" \
  "v_mfma_f32_16x16x32_bf16 v[168:171], v[120:123], a[176:179], 0\n" \
  "s_nop 0\n" \
  "v_mfma_f32_16x16x32_bf16 v[172:175], v[136:139], a[176:179], 0\n" \
  "s_nop 2\n" \
  "v_mfma_f32_16x16x32_bf16 v[168:171], v[124:127], a[180:183], v[168:171]\n" \
  "v_cvt_pk_bf16_f32 v180, v160, v161\n" \
  "v_cvt_pk_bf16_f32 v181, v162, v163\n" \
  "v_mfma_f32_16x16x32_bf16 v[172:175], v[140:143], a[180:183], v[172:175]\n" \
  "v_cvt_pk_bf16_f32 v182, v164, v165\n" \
  "v_cvt_pk_bf16_f32 v183, v166, v167\n" \
  "v_mfma_f32_16x16x32_bf16 v[168:171], v[128:131], a[184:187], v[168:171]\n" \
  "buffer_store_dwordx4 v[180:183], %[sto], %[X], s63 offen sc1\n" \
  "s_add_i32 s63, s63, 0x10000\n" \
  "v_mfma_f32_16x16x32_bf16 v[172:175], v[144:147], a[184:187], v[172:175]\n" \
  "v_mfma_f32_16x16x32_bf16 v[168:171], v[132:135], a[188:191], v[168:171]\n" \
  "v_mfma_f32_16x16x32_bf16 v[172:175], v[148:151], a[188:191], v[172:175]\n" \
  "v_mfma_f32_16x16x32_bf16 v[152:155], v[120:123], a[192:195], 0\n" \
  "s_nop 0\n" \
  "v_mfma_f32_16x16x32_bf16 v[156:159], v[136:139], a[192:195], 0\n" \
  "s_nop 2\n" \
  "v_mfma_f32_16x16x32_bf16 v[152:155], v[124:127], a[196:199], v[152:155]\n" \
  "v_cvt_pk_bf16_f32 v184, v168, v169\n" \
  "v_cvt_pk_bf16_f32 v185, v170, v171\n" \
  "v_mfma_f32_16x16x32_bf16 v[156:159], v[140:143], a[196:199], v[156:159]\n" \
  "v_cvt_pk_bf16_f32 v186, v172, v173\n" \
  "v_cvt_pk_bf16_f32 v187, v174, v175\n" \
  "v_mfma_f32_16x16x32_bf16 v[152:155], v[128:131], a[200:203], v[152:155]\n" \
  "buffer_store_dwordx4 v[184:187], %[sto], %[X], s63 offen sc1\n" \
  "s_add_i32 s63, s63, 0x10000\n" \
  "v_mfma_f32_16x16x32_bf16 v[156:159], v[144:147], a[200:203], v[156:159]\n" \
  "v_mfma_f32_16x16x32_bf16 v[152:155], v[132:135], a[204:207], v[152:155]\n" \
  "v_mfma_f32_16x16x32_bf16 v[156:159], v[148:151], a[204:207], v[156:159]\n" \
  "v_mfma_f32_16x16x32_bf16 v[160:163], v[120:123], a[208:211], 0\n" \
  "s_nop 0\n" \
  "v_mfma_f32_16x16x32_bf16 v[164:167], v[136:139], a[208:211], 0\n" \
  "s_nop 2\n" \
  "v_mfma_f32_16x16x32_bf16 v[160:163], v[124:127], a[212:215], v[160:163]\n" \
  "v_cvt_pk_bf16_f32 v176, v152, v153\n" \
  "v_cvt_pk_bf16_f32 v177, v154, v155\n" \
  "v_mfma_f32_16x16x32_bf16 v[164:167], v[140:143], a[212:215], v[164:167]\n" \
  "v_cvt_pk_bf16_f32 v178, v156, v157\n" \
  "v_cvt_pk_bf16_f32 v179, v158, v159\n" \
  "v_mfma_f32_16x16x32_bf16 v[160:163], v[128:131], a[216:219], v[160:163]\n" \
  "buffer_store_dwordx4 v[176:179], %[sto], %[X], s63 offen sc1\n" \
  "s_add_i32 s63, s63, 0x10000\n" \
  "v_mfma_f32_16x16x32_bf16 v[164:167], v[144:147], a[216:219], v[164:167]\n" \
  "v_mfma_f32_16x16x32_bf16 v[160:163], v[132:135], a[220:223], v[160:163]\n" \
  "v_mfma_f32_16x16x32_bf16 v[164:167], v[148:151], a[220:223], v[164:167]\n" \
  "v_mfma_f32_16x16x32_bf16 v[168:171], v[120:123], a[224:227], 0\n" \
  "s_nop 0\n" \
  "v_mfma_f32_16x16x32_bf16 v[172:175], v[136:139], a[224:227], 0\n" \
  "s_nop 2\n" \
  "v_mfma_f32_16x16x32_bf16 v[168:171], v[124:127], a[228:231], v[168:171]\n" \
  "v_cvt_pk_bf16_f32 v180, v160, v161\n" \
  "v_cvt_pk_bf16_f32 v181, v162, v163\n" \
  "v_mfma_f32_16x16x32_bf16 v[172:175], v[140:143], a[228:231], v[172:175]\n" \
  "v_cvt_pk_bf16_f32 v182, v164, v165\n" \
  "v_cvt_pk_bf16_f32 v183, v166, v167\n" \
  "v_mfma_f32_16x16x32_bf16 v[168:171], v[128:131], a[232:235], v[168:171]\n" \
  "buffer_store_dwordx4 v[180:183], %[sto], %[X], s63 offen sc1\n" \
  "s_add_i32 s63, s63, %[px]\n" \
  "v_mfma_f32_16x16x32_bf16 v[172:175], v[144:147], a[232:235], v[172:175]\n" \
  "v_mfma_f32_16x16x32_bf16 v[168:171], v[132:135], a[236:239], v[168:171]\n" \
  "v_mfma_f32_16x16x32_bf16 v[172:175], v[148:151], a[236:239], v[172:175]\n" \
  "v_mfma_f32_16x16x32_bf16 v[152:155], v[120:123], a[240:243], 0\n" \
  "s_nop 0\n" \
  "v_mfma_f32_16x16x32_bf16 v[156:159], v[136:139], a[240:243], 0\n" \
  "s_nop 2\n" \
  "v_mfma_f32_16x16x32_bf16 v[152:155], v[124:127], a[244:247], v[152:155]\n" \
  "v_cvt_pk_bf16_f32 v184, v168, v169\n" \
  "v_cvt_pk_bf16_f32 v185, v170, v171\n" \
  "v_mfma_f32_16x16x32_bf16 v[156:159], v[140:143], a[244:247], v[156:159]\n" \
  "v_cvt_pk_bf16_f32 v186, v172, v173\n" \
  "v_cvt_pk_bf16_f32 v187, v174, v175\n" \
  "v_mfma_f32_16x16x32_bf16 v[152:155], v[128:131], a[248:251], v[152:155]\n" \
  "buffer_store_dwordx4 v[184:187], %[sto], %[X], s63 offen sc1\n" \
  "s_add_i32 s63, s63, 0x10000\n" \
  "v_mfma_f32_16x16x32_bf16 v[156:159], v[144:147], a[248:251], v[156:159]\n" \
  "v_mfma_f32_16x16x32_bf16 v[152:155], v[132:135], a[252:255], v[152:155]\n" \
  "v_mfma_f32_16x16x32_bf16 v[156:159], v[148:151], a[252:255], v[156:159]\n" \
  "v_mfma_f32_16x16x32_bf16 v[160:163], v[120:123], v[32:35], 0\n" \
  "s_nop 0\n" \
  "v_mfma_f32_16x16x32_bf16 v[164:167], v[136:139], v[32:35], 0\n" \
  "s_nop 2\n" \
  "v_mfma_f32_16x16x32_bf16 v[160:163], v[124:127], v[36:39], v[160:163]\n" \
  "v_cvt_pk_bf16_f32 v176, v152, v153\n" \
  "v_cvt_pk_bf16_f32 v177, v154, v155\n" \
  "v_mfma_f32_16x16x32_bf16 v[164:167], v[140:143], v[36:39], v[164:167]\n" \
  "v_cvt_pk_bf16_f32 v178, v156, v157\n" \
  "v_cvt_pk_bf16_f32 v179, v158, v159\n" \
  "v_mfma_f32_16x16x32_bf16 v[160:163], v[128:131], v[40:43], v[160:163]\n" \
  "buffer_store_dwordx4 v[176:179], %[sto], %[X], s63 offen sc1\n" \
  "s_add_i32 s63, s63, 0x10000\n" \
  "v_mfma_f32_16x16x32_bf16 v[164:167], v[144:147], v[40:43], v[164:167]\n" \
  "v_mfma_f32_16x16x32_bf16 v[160:163], v[132:135], v[44:47], v[160:163]\n" \
  "v_mfma_f32_16x16x32_bf16 v[164:167], v[148:151], v[44:47], v[164:167]\n" \
  "v_mfma_f32_16x16x32_bf16 v[168:171], v[120:123], v[48:51], 0\n" \
  "s_nop 0\n" \
  "v_mfma_f32_16x16x32_bf16 v[172:175], v[136:139], v[48:51], 0\n" \
  "s_nop 2\n" \
  "v_mfma_f32_16x16x32_bf16 v[168:171], v[124:127], v[52:55], v[168:171]\n" \
  "v_cvt_pk_bf16_f32 v180, v160, v161\n" \
  "v_cvt_pk_bf16_f32 v181, v162, v163\n" \
  "v_mfma_f32_16x16x32_bf16 v[172:175], v[140:143], v[52:55], v[172:175]\n" \
  "v_cvt_pk_bf16_f32 v182, v164, v165\n" \
  "v_cvt_pk_bf16_f32 v183, v166, v167\n" \
  "v_mfma_f32_16x16x32_bf16 v[168:171], v[128:131], v[56:59], v[168:171]\n" \
  "buffer_store_dwordx4 v[180:183], %[sto], %[X], s63 offen sc1\n" \
  "s_add_i32 s63, s63, 0x10000\n" \
  "v_mfma_f32_16x16x32_bf16 v[172:175], v[144:147], v[56:59], v[172:175]\n" \
  "v_mfma_f32_16x16x32_bf16 v[168:171], v[132:135], v[60:63], v[168:171]\n" \
  "s_nop 2\n" \
  "v_mfma_f32_16x16x32_bf16 v[172:175], v[148:151], v[60:63], v[172:175]\n" \
  "s_nop 3\n" \
  "v_cvt_pk_bf16_f32 v184, v168, v169\n" \
  "v_cvt_pk_bf16_f32 v185, v170, v171\n" \
  "s_nop 1\n" \
  "v_cvt_pk_bf16_f32 v186, v172, v173\n" \
  "v_cvt_pk_bf16_f32 v187, v174, v175\n" \
  "buffer_store_dwordx4 v[184:187], %[sto], %[X], s63 offen sc1\n" \
  "s_add_i32 s63, s63, 0x10000\n" \
  "v_xor_b32 %[hA], 0x10000, %[hA]\n" \
  "s_waitcnt vmcnt(9)\n" \
  "s_barrier\n" \
  "ds_read_b128 v[80:83], %[hA] offset:0\n" \
  "ds_read_b128 v[84:87], %[hA] offset:18432\n" \
  "ds_read_b128 v[88:91], %[hA] offset:512\n" \
  "ds_read_b128 v[92:95], %[hA] offset:18944\n" \
  "ds_read_b128 v[96:99], %[hA] offset:1024\n" \
  "ds_read_b128 v[100:103], %[hA] offset:19456\n" \
  "v_readfirstlane_b32 s64, v228\n" \
  "s_cmp_eq_u32 s64, 0\n" \
  "s_cbranch_scc1 .Lvt_w\n" \
  ".Lvt_w_ok:\n" \
  "v_readfirstlane_b32 s66, v229\n" \
  "s_add_i32 s66, s66, %[vtc]\n" \
  "s_add_i32 s60, s60, 1\n" \
  "s_cmp_lg_u32 s60, s81\n" \
  "s_cbranch_scc1 .Lvt_top\n" \
  "s_cmp_eq_u32 s60, 0\n" \
  "s_cbranch_scc1 .Lvt_entry\n" \
  "v_mov_b32 v225, 64\n" \
  "s_waitcnt vmcnt(0)\n" \
  "s_barrier\n" \
  "buffer_store_dword v225, %[flo], %[X], s87 offen sc1\n" \
  "s_branch .Lvt_end\n" \
  ".Lvt_entry:\n" \
  "s_mov_b32 s81, 64\n" \
  "s_mov_b32 s87, 0\n" \
  "s_cmp_eq_u32 %[grp], 0\n" \
  "s_cbranch_scc1 .Lvt_go\n" \
  "s_mov_b32 s65, 0\n" \
  "s_xor_b32 s75, %[bas], 0x2000\n" \
  ".Lvt_g1:\n" \
  "buffer_load_dword v224, off, %[X], s75 offset:48 sc1\n" \
  "s_waitcnt vmcnt(0)\n" \
  "v_readfirstlane_b32 s74, v224\n" \
  "s_cmp_lg_u32 s74, 0\n" \
  "s_cbranch_scc1 .Lvt_g2\n" \
  "s_sleep 8\n" \
  "s_add_i32 s65, s65, 1\n" \
  "s_cmp_lt_u32 s65, 400\n" \
  "s_cbranch_scc1 .Lvt_g1\n" \
  ".Lvt_g2:\n" \
  "s_add_i32 s74, s74, %[g1t]\n" \
  ".Lvt_g3:\n" \
  "s_memrealtime s[76:77]\n" \
  "s_waitcnt lgkmcnt(0)\n" \
  "s_sub_i32 s76, s76, s74\n" \
  "s_cmp_gt_u32 s76, -3000\n" \
  "s_cbranch_scc1 .Lvt_g3\n" \
  ".Lvt_go:\n" \
  "s_waitcnt vmcnt(0) lgkmcnt(0)\n" \
  "s_barrier\n" \
  "s_mov_b32 s75, 0\n" \
  "s_movk_i32 s76, 0x1000\n" \
  "s_movk_i32 s77, 0x2000\n" \
  "v_mov_b32 v226, 1\n" \
  "buffer_atomic_add v226, %[rdy], %[X], %[bas] offen offset:1536 sc0 sc1\n" \
  "s_mov_b32 s65, 0\n" \
  ".Lvt_enw:\n" \
  "buffer_load_dwordx4 v[188:191], %[hvo], %[HS], s75 offen offset:0 sc1\n" \
  "buffer_load_dwordx4 v[192:195], %[hvo], %[HS], s75 offen offset:1024 sc1\n" \
  "buffer_load_dwordx4 v[196:199], %[hvo], %[HS], s75 offen offset:2048 sc1\n" \
  "buffer_load_dwordx4 v[200:203], %[hvo], %[HS], s75 offen offset:3072 sc1\n" \
  "buffer_load_dwordx4 v[204:207], %[hvo], %[HS], s76 offen offset:0 sc1\n" \
  "buffer_load_dwordx4 v[208:211], %[hvo], %[HS], s76 offen offset:1024 sc1\n" \
  "buffer_load_dwordx4 v[212:215], %[hvo], %[HS], s76 offen offset:2048 sc1\n" \
  "buffer_load_dwordx4 v[216:219], %[hvo], %[HS], s76 offen offset:3072 sc1\n" \
  "buffer_load_dwordx4 v[220:223], %[hvo], %[HS], s77 offen offset:0 sc1\n" \
  "s_waitcnt vmcnt(0)\n" \
  "v_max3_u32 v224, v188, v189, v190\n" \
  "v_max3_u32 v224, v224, v191, v192\n" \
  "v_max3_u32 v224, v224, v193, v194\n" \
  "v_max3_u32 v224, v224, v195, v196\n" \
  "v_max3_u32 v224, v224, v197, v198\n" \
  "v_max3_u32 v224, v224, v199, v200\n" \
  "v_max3_u32 v224, v224, v201, v202\n" \
  "v_max3_u32 v224, v224, v203, v204\n" \
  "v_max3_u32 v224, v224, v205, v206\n" \
  "v_max3_u32 v224, v224, v207, v208\n" \
  "v_max3_u32 v224, v224, v209, v210\n" \
  "v_max3_u32 v224, v224, v211, v212\n" \
  "v_max3_u32 v224, v224, v213, v214\n" \
  "v_max3_u32 v224, v224, v215, v216\n" \
  "v_max3_u32 v224, v224, v217, v218\n" \
  "v_max3_u32 v224, v224, v219, v220\n" \
  "v_max3_u32 v224, v224, v221, v222\n" \
  "v_max_u32 v224, v224, v223\n" \
  "v_cmp_eq_u32 vcc, -1, v224\n" \
  "s_cbranch_vccz .Lvt_enw_ok\n" \
  "s_sleep 4\n" \
  "s_add_i32 s65, s65, 1\n" \
  "s_and_b32 s64, s65, 1023\n" \
  "s_cbranch_scc1 .Lvt_enw\n" \
  "buffer_load_dword v224, off, %[F], 0 offset:1024 sc1\n" \
  "s_waitcnt vmcnt(0)\n" \
  "v_readfirstlane_b32 s64, v224\n" \
  "s_cmp_lg_u32 s64, 0\n" \
  "s_cbranch_scc1 .Lvt_enw_ok\n" \
  "s_cmp_lt_u32 s65, 400000\n" \
  "s_cbranch_scc1 .Lvt_enw\n" \
  "v_mov_b32 v224, 1\n" \
  "buffer_store_dword v224, off, %[F], 0 offset:1024 sc1\n" \
  ".Lvt_enw_ok:\n" \
  "ds_write_b128 %[hvo], v[188:191] offset:0\n" \
  "ds_write_b128 %[hvo], v[192:195] offset:1024\n" \
  "ds_write_b128 %[hvo], v[196:199] offset:2048\n" \
  "ds_write_b128 %[hvo], v[200:203] offset:3072\n" \
  "ds_write_b128 %[hvo], v[204:207] offset:4096\n" \
  "ds_write_b128 %[hvo], v[208:211] offset:5120\n" \
  "ds_write_b128 %[hvo], v[212:215] offset:6144\n" \
  "ds_write_b128 %[hvo], v[216:219] offset:7168\n" \
  "ds_write_b128 %[hvo], v[220:223] offset:8192\n" \
  "v_readfirstlane_b32 s92, v226\n" \
  "s_cmp_lg_u32 s92, 63\n" \
  "s_cbranch_scc1 .Lvt_nrdy\n" \
  "s_memrealtime s[74:75]\n" \
  "s_waitcnt lgkmcnt(0)\n" \
  "s_add_i32 s74, s74, %[x0]\n" \
  "s_or_b32 s74, s74, 1\n" \
  "s_mul_i32 s75, %[tau], 162\n" \
  "s_lshr_b32 s75, s75, 8\n" \
  "s_add_i32 s75, s75, s74\n" \
  "v_mov_b32 v226, s74\n" \
  "v_mov_b32 v227, s75\n" \
  "buffer_store_dwordx2 v[226:227], %[rdy], %[X], %[bas] offen offset:0 sc1\n" \
  "s_add_i32 s74, s74, %[tau]\n" \
  "s_or_b32 s74, s74, 1\n" \
  "s_add_i32 s75, s75, %[tau]\n" \
  "v_mov_b32 v226, s74\n" \
  "v_mov_b32 v227, s75\n" \
  "buffer_store_dwordx2 v[226:227], %[rdy], %[X], %[bas] offen offset:16 sc1\n" \
  ".Lvt_nrdy:\n" \
  "s_waitcnt lgkmcnt(0)\n" \
  "s_barrier\n" \
  "ds_read_b128 v[80:83], %[hA] offset:0\n" \
  "ds_read_b128 v[84:87], %[hA] offset:18432\n" \
  "ds_read_b128 v[88:91], %[hA] offset:512\n" \
  "ds_read_b128 v[92:95], %[hA] offset:18944\n" \
  "ds_read_b128 v[96:99], %[hA] offset:1024\n" \
  "ds_read_b128 v[100:103], %[hA] offset:19456\n" \
  "s_waitcnt lgkmcnt(0)\n" \
  "s_branch .Lvt_al_ok\n" \
  ".Lvt_w:\n" \
  "s_cmp_lt_i32 s60, 1\n" \
  "s_cbranch_scc1 .Lvt_w_ok\n" \
  "s_mov_b32 s65, 0\n" \
  ".Lvt_ww:\n" \
  "buffer_load_dwordx2 v[228:229], off, %[X], s67 sc1\n" \
  "s_waitcnt vmcnt(0)\n" \
  "v_readfirstlane_b32 s64, v228\n" \
  "s_cmp_lg_u32 s64, 0\n" \
  "s_cbranch_scc1 .Lvt_w_ok\n" \
  "s_sleep 2\n" \
  "s_add_i32 s65, s65, 1\n" \
  "s_and_b32 s64, s65, 1023\n" \
  "s_cbranch_scc1 .Lvt_ww\n" \
  "buffer_load_dword v224, off, %[F], 0 offset:1024 sc1\n" \
  "s_waitcnt vmcnt(0)\n" \
  "v_readfirstlane_b32 s64, v224\n" \
  "s_cmp_lg_u32 s64, 0\n" \
  "s_cbranch_scc1 .Lvt_ww_ok\n" \
  "s_cmp_lt_u32 s65, 400000\n" \
  "s_cbranch_scc1 .Lvt_ww\n" \
  "v_mov_b32 v224, 1\n" \
  "buffer_store_dword v224, off, %[F], 0 offset:1024 sc1\n" \
  ".Lvt_ww_ok:\n" \
  "s_branch .Lvt_w_ok\n" \
  ".Lvt_sh:\n" \
  "s_cmp_lt_i32 s60, 0\n" \
  "s_cbranch_scc1 .Lvt_sh_back\n" \
  "s_mul_i32 s75, s60, 36864\n" \
  "s_add_i32 s76, s75, 0x1000\n" \
  "s_add_i32 s77, s75, 0x2000\n" \
  "s_waitcnt lgkmcnt(0)\n" \
  "s_barrier\n" \
  "s_mov_b32 s65, 0\n" \
  ".Lvt_shw:\n" \
  "buffer_load_dwordx4 v[188:191], %[hvo], %[HS], s75 offen offset:0 sc1\n" \
  "buffer_load_dwordx4 v[192:195], %[hvo], %[HS], s75 offen offset:1024 sc1\n" \
  "buffer_load_dwordx4 v[196:199], %[hvo], %[HS], s75 offen offset:2048 sc1\n" \
  "buffer_load_dwordx4 v[200:203], %[hvo], %[HS], s75 offen offset:3072 sc1\n" \
  "buffer_load_dwordx4 v[204:207], %[hvo], %[HS], s76 offen offset:0 sc1\n" \
  "buffer_load_dwordx4 v[208:211], %[hvo], %[HS], s76 offen offset:1024 sc1\n" \
  "buffer_load_dwordx4 v[212:215], %[hvo], %[HS], s76 offen offset:2048 sc1\n" \
  "buffer_load_dwordx4 v[216:219], %[hvo], %[HS], s76 offen offset:3072 sc1\n" \
  "buffer_load_dwordx4 v[220:223], %[hvo], %[HS], s77 offen offset:0 sc1\n" \
  "s_waitcnt vmcnt(0)\n" \
  "v_max3_u32 v224, v188, v189, v190\n" \
  "v_max3_u32 v224, v224, v191, v192\n" \
  "v_max3_u32 v224, v224, v193, v194\n" \
  "v_max3_u32 v224, v224, v195, v196\n" \
  "v_max3_u32 v224, v224, v197, v198\n" \
  "v_max3_u32 v224, v224, v199, v200\n" \
  "v_max3_u32 v224, v224, v201, v202\n" \
  "v_max3_u32 v224, v224, v203, v204\n" \
  "v_max3_u32 v224, v224, v205, v206\n" \
  "v_max3_u32 v224, v224, v207, v208\n" \
  "v_max3_u32 v224, v224, v209, v210\n" \
  "v_max3_u32 v224, v224, v211, v212\n" \
  "v_max3_u32 v224, v224, v213, v214\n" \
  "v_max3_u32 v224, v224, v215, v216\n" \
  "v_max3_u32 v224, v224, v217, v218\n" \
  "v_max3_u32 v224, v224, v219, v220\n" \
  "v_max3_u32 v224, v224, v221, v222\n" \
  "v_max_u32 v224, v224, v223\n" \
  "v_cmp_eq_u32 vcc, -1, v224\n" \
  "s_cbranch_vccz .Lvt_shw_ok\n" \
  "s_sleep 4\n" \
  "s_add_i32 s65, s65, 1\n" \
  "s_and_b32 s64, s65, 1023\n" \
  "s_cbranch_scc1 .Lvt_shw\n" \
  "buffer_load_dword v224, off, %[F], 0 offset:1024 sc1\n" \
  "s_waitcnt vmcnt(0)\n" \
  "v_readfirstlane_b32 s64, v224\n" \
  "s_cmp_lg_u32 s64, 0\n" \
  "s_cbranch_scc1 .Lvt_shw_ok\n" \
  "s_cmp_lt_u32 s65, 400000\n" \
  "s_cbranch_scc1 .Lvt_shw\n" \
  "v_mov_b32 v224, 1\n" \
  "buffer_store_dword v224, off, %[F], 0 offset:1024 sc1\n" \
  ".Lvt_shw_ok:\n" \
  "s_xor_b32 s64, s62, 0x10000\n" \
  "v_add_u32 v224, s64, %[la]\n" \
  "ds_write_b128 v224, v[188:191] offset:0\n" \
  "ds_write_b128 v224, v[192:195] offset:1024\n" \
  "ds_write_b128 v224, v[196:199] offset:2048\n" \
  "ds_write_b128 v224, v[200:203] offset:3072\n" \
  "ds_write_b128 v224, v[204:207] offset:4096\n" \
  "ds_write_b128 v224, v[208:211] offset:5120\n" \
  "ds_write_b128 v224, v[212:215] offset:6144\n" \
  "ds_write_b128 v224, v[216:219] offset:7168\n" \
  "ds_write_b128 v224, v[220:223] offset:8192\n" \
  "s_waitcnt lgkmcnt(0)\n" \
  "s_barrier\n" \
  "ds_read_b128 v[80:83], %[hA] offset:0\n" \
  "ds_read_b128 v[84:87], %[hA] offset:18432\n" \
  "ds_read_b128 v[88:91], %[hA] offset:512\n" \
  "ds_read_b128 v[92:95], %[hA] offset:18944\n" \
  "ds_read_b128 v[96:99], %[hA] offset:1024\n" \
  "ds_read_b128 v[100:103], %[hA] offset:19456\n" \
  "s_waitcnt lgkmcnt(4)\n" \
  "v_mfma_f32_16x16x32_bf16 v[64:67], a[0:3], v[80:83], 0\n" \
  "ds_read_b128 v[104:107], %[hA] offset:1536\n" \
  "v_mfma_f32_16x16x32_bf16 v[72:75], a[0:3], v[84:87], 0\n" \
  "ds_read_b128 v[108:111], %[hA] offset:19968\n" \
  "v_mfma_f32_16x16x32_bf16 v[68:71], a[8:11], v[80:83], 0\n" \
  "v_mfma_f32_16x16x32_bf16 v[76:79], a[8:11], v[84:87], 0\n" \
  "s_waitcnt lgkmcnt(4)\n" \
  "v_mfma_f32_16x16x32_bf16 v[64:67], a[4:7], v[88:91], v[64:67]\n" \
  "ds_read_b128 v[80:83], %[hA] offset:2048\n" \
  "v_mfma_f32_16x16x32_bf16 v[72:75], a[4:7], v[92:95], v[72:75]\n" \
  "ds_read_b128 v[84:87], %[hA] offset:20480\n" \
  "v_mfma_f32_16x16x32_bf16 v[68:71], a[12:15], v[88:91], v[68:71]\n" \
  "v_mfma_f32_16x16x32_bf16 v[76:79], a[12:15], v[92:95], v[76:79]\n" \
  "s_waitcnt lgkmcnt(4)\n" \
  "v_mfma_f32_16x16x32_bf16 v[64:67], a[16:19], v[96:99], v[64:67]\n" \
  "ds_read_b128 v[88:91], %[hA] offset:2560\n" \
  "v_mfma_f32_16x16x32_bf16 v[72:75], a[16:19], v[100:103], v[72:75]\n" \
  "ds_read_b128 v[92:95], %[hA] offset:20992\n" \
  "v_mfma_f32_16x16x32_bf16 v[68:71], a[24:27], v[96:99], v[68:71]\n" \
  "v_mfma_f32_16x16x32_bf16 v[76:79], a[24:27], v[100:103], v[76:79]\n" \
  "s_waitcnt lgkmcnt(4)\n" \
  "v_mfma_f32_16x16x32_bf16 v[64:67], a[20:23], v[104:107], v[64:67]\n" \
  "ds_read_b128 v[96:99], %[hA] offset:3072\n" \
  "v_mfma_f32_16x16x32_bf16 v[72:75], a[20:23], v[108:111], v[72:75]\n" \
  "ds_read_b128 v[100:103], %[hA] offset:21504\n" \
  "v_mfma_f32_16x16x32_bf16 v[68:71], a[28:31], v[104:107], v[68:71]\n" \
  "v_mfma_f32_16x16x32_bf16 v[76:79], a[28:31], v[108:111], v[76:79]\n" \
  "s_waitcnt lgkmcnt(4)\n" \
  "v_mfma_f32_16x16x32_bf16 v[64:67], a[32:35], v[80:83], v[64:67]\n" \
  "ds_read_b128 v[104:107], %[hA] offset:3584\n" \
  "v_mfma_f32_16x16x32_bf16 v[72:75], a[32:35], v[84:87], v[72:75]\n" \
  "ds_read_b128 v[108:111], %[hA] offset:22016\n" \
  "v_mfma_f32_16x16x32_bf16 v[68:71], a[40:43], v[80:83], v[68:71]\n" \
  "v_mfma_f32_16x16x32_bf16 v[76:79], a[40:43], v[84:87], v[76:79]\n" \
  "s_waitcnt lgkmcnt(4)\n" \
  "v_mfma_f32_16x16x32_bf16 v[64:67], a[36:39], v[88:91], v[64:67]\n" \
  "ds_read_b128 v[80:83], %[hA] offset:4096\n" \
  "v_mfma_f32_16x16x32_bf16 v[72:75], a[36:39], v[92:95], v[72:75]\n" \
  "ds_read_b128 v[84:87], %[hA] offset:22528\n" \
  "v_mfma_f32_16x16x32_bf16 v[68:71], a[44:47], v[88:91], v[68:71]\n" \
  "v_mfma_f32_16x16x32_bf16 v[76:79], a[44:47], v[92:95], v[76:79]\n" \
  "s_waitcnt lgkmcnt(4)\n" \
  "v_mfma_f32_16x16x32_bf16 v[64:67], a[48:51], v[96:99], v[64:67]\n" \
  "ds_read_b128 v[88:91], %[hA] offset:4608\n" \
  "v_mfma_f32_16x16x32_bf16 v[72:75], a[48:51], v[100:103], v[72:75]\n" \
  "ds_read_b128 v[92:95], %[hA] offset:23040\n" \
  "v_mfma_f32_16x16x32_bf16 v[68:71], a[56:59], v[96:99], v[68:71]\n" \
  "v_mfma_f32_16x16x32_bf16 v[76:79], a[56:59], v[100:103], v[76:79]\n" \
  "s_waitcnt lgkmcnt(4)\n" \
  "v_mfma_f32_16x16x32_bf16 v[64:67], a[52:55], v[104:107], v[64:67]\n" \
  "ds_read_b128 v[96:99], %[hA] offset:5120\n" \
  "v_mfma_f32_16x16x32_bf16 v[72:75], a[52:55], v[108:111], v[72:75]\n" \
  "ds_read_b128 v[100:103], %[hA] offset:23552\n" \
  "v_mfma_f32_16x16x32_bf16 v[68:71], a[60:63], v[104:107], v[68:71]\n" \
  "v_mfma_f32_16x16x32_bf16 v[76:79], a[60:63], v[108:111], v[76:79]\n" \
  "s_waitcnt lgkmcnt(4)\n" \
  "v_mfma_f32_16x16x32_bf16 v[64:67], a[64:67], v[80:83], v[64:67]\n" \
  "ds_read_b128 v[104:107], %[hA] offset:5632\n" \
  "v_mfma_f32_16x16x32_bf16 v[72:75], a[64:67], v[84:87], v[72:75]\n" \
  "ds_read_b128 v[108:111], %[hA] offset:24064\n" \
  "v_mfma_f32_16x16x32_bf16 v[68:71], a[72:75], v[80:83], v[68:71]\n" \
  "v_mfma_f32_16x16x32_bf16 v[76:79], a[72:75], v[84:87], v[76:79]\n" \
  "s_waitcnt lgkmcnt(4)\n" \
  "v_mfma_f32_16x16x32_bf16 v[64:67], a[68:71], v[88:91], v[64:67]\n" \
  "ds_read_b128 v[80:83], %[hA] offset:6144\n" \
  "v_mfma_f32_16x16x32_bf16 v[72:75], a[68:71], v[92:95], v[72:75]\n" \
  "ds_read_b128 v[84:87], %[hA] offset:24576\n" \
  "v_mfma_f32_16x16x32_bf16 v[68:71], a[76:79], v[88:91], v[68:71]\n" \
  "v_mfma_f32_16x16x32_bf16 v[76:79], a[76:79], v[92:95], v[76:79]\n" \
  "s_waitcnt lgkmcnt(4)\n" \
  "v_mfma_f32_16x16x32_bf16 v[64:67], a[80:83], v[96:99], v[64:67]\n" \
  "ds_read_b128 v[88:91], %[hA] offset:6656\n" \
  "v_mfma_f32_16x16x32_bf16 v[72:75], a[80:83], v[100:103], v[72:75]\n" \
  "ds_read_b128 v[92:95], %[hA] offset:25088\n" \
  "v_mfma_f32_16x16x32_bf16 v[68:71], a[88:91], v[96:99], v[68:71]\n" \
  "v_mfma_f32_16x16x32_bf16 v[76:79], a[88:91], v[100:103], v[76:79]\n" \
  "s_waitcnt lgkmcnt(4)\n" \
  "v_mfma_f32_16x16x32_bf16 v[64:67], a[84:87], v[104:107], v[64:67]\n" \
  "ds_read_b128 v[96:99], %[hA] offset:7168\n" \
  "v_mfma_f32_16x16x32_bf16 v[72:75], a[84:87], v[108:111], v[72:75]\n" \
  "ds_read_b128 v[100:103], %[hA] offset:25600\n" \
  "v_mfma_f32_16x16x32_bf16 v[68:71], a[92:95], v[104:107], v[68:71]\n" \
  "v_mfma_f32_16x16x32_bf16 v[76:79], a[92:95], v[108:111], v[76:79]\n" \
  "s_waitcnt lgkmcnt(4)\n" \
  "v_mfma_f32_16x16x32_bf16 v[64:67], a[96:99], v[80:83], v[64:67]\n" \
  "ds_read_b128 v[104:107], %[hA] offset:7680\n" \
  "v_mfma_f32_16x16x32_bf16 v[72:75], a[96:99], v[84:87], v[72:75]\n" \
  "ds_read_b128 v[108:111], %[hA] offset:26112\n" \
  "v_mfma_f32_16x16x32_bf16 v[68:71], a[104:107], v[80:83], v[68:71]\n" \
  "v_mfma_f32_16x16x32_bf16 v[76:79], a[104:107], v[84:87], v[76:79]\n" \
  "s_waitcnt lgkmcnt(4)\n" \
  "v_mfma_f32_16x16x32_bf16 v[64:67], a[100:103], v[88:91], v[64:67]\n" \
  "ds_read_b128 v[80:83], %[hA] offset:8192\n" \
  "v_mfma_f32_16x16x32_bf16 v[72:75], a[100:103], v[92:95], v[72:75]\n" \
  "ds_read_b128 v[84:87], %[hA] offset:26624\n" \
  "v_mfma_f32_16x16x32_bf16 v[68:71], a[108:111], v[88:91], v[68:71]\n" \
  "v_mfma_f32_16x16x32_bf16 v[76:79], a[108:111], v[92:95], v[76:79]\n" \
  "s_waitcnt lgkmcnt(4)\n" \
  "v_mfma_f32_16x16x32_bf16 v[64:67], a[112:115], v[96:99], v[64:67]\n" \
  "ds_read_b128 v[88:91], %[hA] offset:8704\n" \
  "v_mfma_f32_16x16x32_bf16 v[72:75], a[112:115], v[100:103], v[72:75]\n" \
  "ds_read_b128 v[92:95], %[hA] offset:27136\n" \
  "v_mfma_f32_16x16x32_bf16 v[68:71], a[120:123], v[96:99], v[68:71]\n" \
  "v_mfma_f32_16x16x32_bf16 v[76:79], a[120:123], v[100:103], v[76:79]\n" \
  "s_waitcnt lgkmcnt(4)\n" \
  "v_mfma_f32_16x16x32_bf16 v[64:67], a[116:119], v[104:107], v[64:67]\n" \
  "v_mfma_f32_16x16x32_bf16 v[72:75], a[116:119], v[108:111], v[72:75]\n" \
  "v_mfma_f32_16x16x32_bf16 v[68:71], a[124:127], v[104:107], v[68:71]\n" \
  "v_mfma_f32_16x16x32_bf16 v[76:79], a[124:127], v[108:111], v[76:79]\n" \
  "s_waitcnt lgkmcnt(2)\n" \
  "v_mfma_f32_16x16x32_bf16 v[64:67], a[128:131], v[80:83], v[64:67]\n" \
  "v_mfma_f32_16x16x32_bf16 v[72:75], a[128:131], v[84:87], v[72:75]\n" \
  "v_mfma_f32_16x16x32_bf16 v[68:71], a[136:139], v[80:83], v[68:71]\n" \
  "v_mfma_f32_16x16x32_bf16 v[76:79], a[136:139], v[84:87], v[76:79]\n" \
  "s_waitcnt lgkmcnt(0)\n" \
  "v_mfma_f32_16x16x32_bf16 v[64:67], a[132:135], v[88:91], v[64:67]\n" \
  "v_mfma_f32_16x16x32_bf16 v[72:75], a[132:135], v[92:95], v[72:75]\n" \
  "v_mfma_f32_16x16x32_bf16 v[68:71], a[140:143], v[88:91], v[68:71]\n" \
  "v_mfma_f32_16x16x32_bf16 v[76:79], a[140:143], v[92:95], v[76:79]\n" \
  "s_nop 7\n" \
  "v_cvt_pk_bf16_f32 v112, v64, v65\n" \
  "v_cvt_pk_bf16_f32 v113, v66, v67\n" \
  "v_cvt_pk_bf16_f32 v114, v68, v69\n" \
  "v_cvt_pk_bf16_f32 v115, v70, v71\n" \
  "ds_write_b128 %[vW], v[112:115]\n" \
  "v_cvt_pk_bf16_f32 v116, v72, v73\n" \
  "v_cvt_pk_bf16_f32 v117, v74, v75\n" \
  "v_cvt_pk_bf16_f32 v118, v76, v77\n" \
  "v_cvt_pk_bf16_f32 v119, v78, v79\n" \
  "ds_write_b128 %[vW], v[116:119] offset:4096\n" \
  "s_branch .Lvt_sh_back\n" \
  ".Lvt_late:\n" \
  "s_cmp_lt_i32 s60, 0\n" \
  "s_cbranch_scc1 .Lvt_al_ok\n" \
  "s_cmp_gt_u32 s91, -3000\n" \
  "s_cbranch_scc0 .Lvt_late2\n" \
  ".Lvt_wt:\n" \
  "s_memrealtime s[76:77]\n" \
  "s_waitcnt lgkmcnt(0)\n" \
  "s_sub_i32 s76, s76, s66\n" \
  "s_cmp_lt_i32 s76, 0\n" \
  "s_cbranch_scc1 .Lvt_wt\n" \
  "s_branch .Lvt_al_ok\n" \
  ".Lvt_late2:\n" \
  "s_cmp_lg_u32 s66, 0\n" \
  "s_cbranch_scc1 .Lvt_late3\n" \
  "s_mov_b32 s65, 0\n" \
  ".Lvt_tw:\n" \
  "buffer_load_dword v224, off, %[X], s67 sc1\n" \
  "s_waitcnt vmcnt(0)\n" \
  "v_readfirstlane_b32 s66, v224\n" \
  "s_cmp_lg_u32 s66, 0\n" \
  "s_cbranch_scc1 .Lvt_tw_ok\n" \
  "s_sleep 1\n" \
  "s_add_i32 s65, s65, 1\n" \
  "s_cmp_lt_u32 s65, 200\n" \
  "s_cbranch_scc1 .Lvt_tw\n" \
  ".Lvt_tw_ok:\n" \
  "s_sub_i32 s91, s74, s66\n" \
  "s_cmp_gt_u32 s91, -3000\n" \
  "s_cbranch_scc1 .Lvt_wt\n" \
  ".Lvt_late3:\n" \
  "s_branch .Lvt_al_ok\n" \
  ".Lvt_end:\n"
#define VT_CLOB "v32", "v33", "v34", "v35", "v36", "v37", "v38", "v39", "v40", "v41", "v42", "v43", "v44", "v45", "v46", "v47", "v48", "v49", "v50", "v51", "v52", "v53", "v54", "v55", "v56", "v57", "v58", "v59", "v60", "v61", "v62", "v63", "v64", "v65", "v66", "v67", "v68", "v69", "v70", "v71", "v72", "v73", "v74", "v75", "v76", "v77", "v78", "v79", "v80", "v81", "v82", "v83", "v84", "v85", "v86", "v87", "v88", "v89", "v90", "v91", "v92", "v93", "v94", "v95", "v96", "v97", "v98", "v99", "v100", "v101", "v102", "v103", "v104", "v105", "v106", "v107", "v108", "v109", "v110", "v111", "v112", "v113", "v114", "v115", "v116", "v117", "v118", "v119", "v120", "v121", "v122", "v123", "v124", "v125", "v126", "v127", "v128", "v129", "v130", "v131", "v132", "v133", "v134", "v135", "v136", "v137", "v138", "v139", "v140", "v141", "v142", "v143", "v144", "v145", "v146", "v147", "v148", "v149", "v150", "v151", "v152", "v153", "v154", "v155", "v156", "v157", "v158", "v159", "v160", "v161", "v162", "v163", "v164", "v165", "v166", "v167", "v168", "v169", "v170", "v171", "v172", "v173", "v174", "v175", "v176", "v177", "v178", "v179", "v180", "v181", "v182", "v183", "v184", "v185", "v186", "v187", "v188", "v189", "v190", "v191", "v192", "v193", "v194", "v195", "v196", "v197", "v198", "v199", "v200", "v201", "v202", "v203", "v204", "v205", "v206", "v207", "v208", "v209", "v210", "v211", "v212", "v213", "v214", "v215", "v216", "v217", "v218", "v219", "v220", "v221", "v222", "v223", "v224", "v225", "v226", "v227", "v228", "v229", "v230", "v231", "v232", "v233", "v234", "v235", "v236", "v237", "v238", "v239", "v240", "v241", "v242", "v243", "v244", "v245", "v246", "v247", "v248", "v249", "v250", "v251", "v252", "v253", "v254", "v255", "s60", "s61", "s62", "s63", "s64", "s65", "s66", "s67", "s68", "s69", "s70", "s71", "s72", "s73", "s74", "s75", "s76", "s77", "s78", "s79", "s80", "s81", "s82", "s83", "s84", "s85", "s86", "s87", "s88", "s89", "s90", "s91", "s92", "s93", "a0", "a1", "a2", "a3", "a4", "a5", "a6", "a7", "a8", "a9", "a10", "a11", "a12", "a13", "a14", "a15", "a16", "a17", "a18", "a19", "a20", "a21", "a22", "a23", "a24", "a25", "a26", "a27", "a28", "a29", "a30", "a31", "a32", "a33", "a34", "a35", "a36", "a37", "a38", "a39", "a40", "a41", "a42", "a43", "a44", "a45", "a46", "a47", "a48", "a49", "a50", "a51", "a52", "a53", "a54", "a55", "a56", "a57", "a58", "a59", "a60", "a61", "a62", "a63", "a64", "a65", "a66", "a67", "a68", "a69", "a70", "a71", "a72", "a73", "a74", "a75", "a76", "a77", "a78", "a79", "a80", "a81", "a82", "a83", "a84", "a85", "a86", "a87", "a88", "a89", "a90", "a91", "a92", "a93", "a94", "a95", "a96", "a97", "a98", "a99", "a100", "a101", "a102", "a103", "a104", "a105", "a106", "a107", "a108", "a109", "a110", "a111", "a112", "a113", "a114", "a115", "a116", "a117", "a118", "a119", "a120", "a121", "a122", "a123", "a124", "a125", "a126", "a127", "a128", "a129", "a130", "a131", "a132", "a133", "a134", "a135", "a136", "a137", "a138", "a139", "a140", "a141", "a142", "a143", "a144", "a145", "a146", "a147", "a148", "a149", "a150", "a151", "a152", "a153", "a154", "a155", "a156", "a157", "a158", "a159", "a160", "a161", "a162", "a163", "a164", "a165", "a166", "a167", "a168", "a169", "a170", "a171", "a172", "a173", "a174", "a175", "a176", "a177", "a178", "a179", "a180", "a181", "a182", "a183", "a184", "a185", "a186", "a187", "a188", "a189", "a190", "a191", "a192", "a193", "a194", "a195", "a196", "a197", "a198", "a199", "a200", "a201", "a202", "a203", "a204", "a205", "a206", "a207", "a208", "a209", "a210", "a211", "a212", "a213", "a214", "a215", "a216", "a217", "a218", "a219", "a220", "a221", "a222", "a223", "a224", "a225", "a226", "a227", "a228", "a229", "a230", "a231", "a232", "a233", "a234", "a235", "a236", "a237", "a238", "a239", "a240", "a241", "a242", "a243", "a244", "a245", "a246", "a247", "a248", "a249", "a250", "a251", "a252", "a253", "a254", "a255", "vcc", "scc", "m0", "memory"
#define O_ASM \
  "s_nop 4\n" \
  "s_cmp_eq_u32 %[grp], 0\n" \
  "s_cbranch_scc1 .Lo_w0\n" \
  "v_add_u32 v243, 65536, %[wo]\n" \
  "global_load_dwordx4 v[32:35], v243, %[wp] offset:0\n" \
  "global_load_dwordx4 v[36:39], v243, %[wp] offset:1024\n" \
  "global_load_dwordx4 v[40:43], v243, %[wp] offset:2048\n" \
  "global_load_dwordx4 v[44:47], v243, %[wp] offset:3072\n" \
  "v_add_u32 v243, 4096, v243\n" \
  "global_load_dwordx4 v[48:51], v243, %[wp] offset:0\n" \
  "global_load_dwordx4 v[52:55], v243, %[wp] offset:1024\n" \
  "global_load_dwordx4 v[56:59], v243, %[wp] offset:2048\n" \
  "global_load_dwordx4 v[60:63], v243, %[wp] offset:3072\n" \
  "v_add_u32 v243, 4096, v243\n" \
  "s_waitcnt vmcnt(0)\n" \
  ".Lo_w0:\n" \
  "s_mov_b32 s56, -2\n" \
  "v_add_u32 v242, 0x1000, %[la]\n" \
  "s_mov_b32 s78, 0\n" \
  "s_mov_b32 s79, 0\n" \
  "s_mov_b32 s80, 0\n" \
  "s_mov_b32 s87, 2.0\n" \
  "v_mov_b64 v[246:247], -1\n" \
  "v_mov_b64 v[248:249], -1\n" \
  "s_mov_b32 s68, 2.0\n" \
  "s_mov_b32 s70, 2.0\n" \
  "v_mov_b32 v244, 0\n" \
  "s_mov_b32 s67, 589824\n" \
  "s_add_i32 s94, %[bas], -32\n" \
  ".Lo_top:\n" \
  "s_memrealtime s[74:75]\n" \
  "s_add_i32 s56, s56, 1\n" \
  "s_add_i32 s94, s94, 16\n" \
  "s_lshl_b32 s62, s56, 17\n" \
  "s_and_b32 s63, s62, 0x60000\n" \
  "s_add_i32 s57, s87, s63\n" \
  "s_mov_b32 m0, %[ml0]\n" \
  "v_readfirstlane_b32 s65, v240\n" \
  "v_readfirstlane_b32 s86, v241\n" \
  "s_waitcnt lgkmcnt(0)\n" \
  "s_sub_i32 s91, s74, s65\n" \
  "s_cmp_gt_u32 s91, 14\n" \
  "s_cbranch_scc1 .Loa_late\n" \
  ".Loa_al_ok:\n" \
  "buffer_load_dwordx4 %[c0], %[X], s57 offen sc1 lds\n" \
  "s_add_i32 s57, s57, 0x1000\n" \
  "s_add_i32 m0, %[ml0], 0x1000\n" \
  "s_nop 1\n" \
  "buffer_load_dwordx4 %[c0], %[X], s57 offen sc1 lds\n" \
  "s_add_i32 s57, s57, 0x3000\n" \
  "s_add_i32 m0, %[ml0], 0x2000\n" \
  "s_nop 1\n" \
  "buffer_load_dwordx4 %[c2], %[X], s57 offen sc1 lds\n" \
  "buffer_load_dword v244, %[flo], %[X], 0 offen sc1\n" \
  "s_bfe_i32 s63, s56, 0x10001\n" \
  "s_and_b32 s60, s63, 0x80008000\n" \
  "s_add_i32 s63, s62, 0x40000\n" \
  "s_and_b32 s63, s63, 0x60000\n" \
  "s_add_i32 s58, %[mw0], s63\n" \
  "s_cmp_gt_i32 s56, 1\n" \
  "s_cselect_b32 s58, s58, 2.0\n" \
  "s_mov_b32 s71, s70\n" \
  "s_mov_b32 s70, s68\n" \
  "s_add_i32 s67, s67, 589824\n" \
  "s_cmp_eq_u32 s67, 1769472\n" \
  "s_cselect_b32 s67, 0, s67\n" \
  "s_add_i32 s68, %[vm0], s67\n" \
  "s_add_i32 s63, s62, 0x20000\n" \
  "s_and_b32 s63, s63, 0x60000\n" \
  "s_add_i32 s61, %[mw0], s63\n" \
  "s_cmp_lt_u32 s56, 63\n" \
  "s_cselect_b32 s61, s61, 2.0\n" \
  "s_and_b32 s62, s62, 0x20000\n" \
  "s_add_i32 s59, %[pm0], s62\n" \
  "s_and_b32 s63, s56, 7\n" \
  "s_lshl_b32 s69, s63, 22\n" \
  "s_add_i32 s69, s69, %[vt0]\n" \
  "s_cmp_lt_i32 s56, 0\n" \
  "s_cselect_b32 s66, 2.0, s67\n" \
  "s_cselect_b32 s68, 2.0, s68\n" \
  "s_cselect_b32 s59, 2.0, s59\n" \
  "s_cselect_b32 s69, 2.0, s69\n" \
  "s_add_i32 s63, s56, 1\n" \
  "v_mov_b64 v[196:197], 0\n" \
  "v_mov_b64 v[198:199], 0\n" \
  "v_mov_b64 v[200:201], 0\n" \
  "v_mov_b64 v[202:203], 0\n" \
  "v_mov_b64 v[160:161], 0\n" \
  "v_mov_b64 v[162:163], 0\n" \
  "v_mov_b64 v[164:165], 0\n" \
  "v_mov_b64 v[166:167], 0\n" \
  "v_mov_b64 v[168:169], 0\n" \
  "v_mov_b64 v[170:171], 0\n" \
  "v_mov_b64 v[172:173], 0\n" \
  "v_mov_b64 v[174:175], 0\n" \
  "v_mov_b64 v[176:177], 0\n" \
  "v_mov_b64 v[178:179], 0\n" \
  "v_mov_b64 v[180:181], 0\n" \
  "v_mov_b64 v[182:183], 0\n" \
  "v_mov_b64 v[184:185], 0\n" \
  "v_mov_b64 v[186:187], 0\n" \
  "v_mov_b64 v[188:189], 0\n" \
  "v_mov_b64 v[190:191], 0\n" \
  "v_mov_b64 v[192:193], 0\n" \
  "v_mov_b64 v[194:195], 0\n" \
  "buffer_store_dwordx4 v[246:249], %[la], %[X], s71 offen sc1\n" \
  "buffer_store_dwordx4 v[246:249], %[la], %[X], s71 offen offset:1024 sc1\n" \
  "s_waitcnt vmcnt(2)\n" \
  "v_cmp_gt_i32 vcc, s63, v244\n" \
  "s_barrier\n" \
  "ds_read_b128 v[104:107], %[mA] offset:0\n" \
  "ds_read_b128 v[108:111], %[mA] offset:1024\n" \
  "ds_read_b128 v[112:115], %[mA] offset:2048\n" \
  "ds_read_b128 v[116:119], %[mA] offset:3072\n" \
  "ds_read_b128 v[120:123], %[mA] offset:4096\n" \
  "buffer_store_dword v246, %[lm], %[X], s58 offen sc1\n" \
  "s_cbranch_vccnz .Lo_sf\n" \
  ".Lo_sf_back:\n" \
  "s_waitcnt lgkmcnt(4)\n" \
  "v_smfmac_f32_16x16x64_bf16 v[196:199], v[104:107], a[0:7], %[idx]\n" \
  "ds_read_b128 v[124:127], %[mA] offset:5120\n" \
  "v_smfmac_f32_16x16x64_bf16 v[200:203], v[104:107], a[8:15], %[idx]\n" \
  "s_waitcnt lgkmcnt(4)\n" \
  "v_smfmac_f32_16x16x64_bf16 v[196:199], v[108:111], a[16:23], %[idx]\n" \
  "ds_read_b128 v[128:131], %[mA] offset:6144\n" \
  "v_smfmac_f32_16x16x64_bf16 v[200:203], v[108:111], a[24:31], %[idx]\n" \
  "s_waitcnt lgkmcnt(4)\n" \
  "v_smfmac_f32_16x16x64_bf16 v[196:199], v[112:115], a[32:39], %[idx]\n" \
  "ds_read_b128 v[132:135], %[mA] offset:7168\n" \
  "v_smfmac_f32_16x16x64_bf16 v[200:203], v[112:115], a[40:47], %[idx]\n" \
  "s_waitcnt lgkmcnt(4)\n" \
  "v_smfmac_f32_16x16x64_bf16 v[196:199], v[116:119], a[48:55], %[idx]\n" \
  "ds_read_b128 v[136:139], %[mA8] offset:0\n" \
  "v_smfmac_f32_16x16x64_bf16 v[200:203], v[116:119], a[56:63], %[idx]\n" \
  "buffer_load_dwordx4 v[64:67], %[la], %[X], s69 offen offset:0 sc1\n" \
  "s_waitcnt lgkmcnt(4)\n" \
  "v_smfmac_f32_16x16x64_bf16 v[196:199], v[120:123], a[64:71], %[idx]\n" \
  "v_smfmac_f32_16x16x64_bf16 v[200:203], v[120:123], a[72:79], %[idx]\n" \
  "s_waitcnt lgkmcnt(3)\n" \
  "v_smfmac_f32_16x16x64_bf16 v[196:199], v[124:127], a[80:87], %[idx]\n" \
  "v_smfmac_f32_16x16x64_bf16 v[200:203], v[124:127], a[88:95], %[idx]\n" \
  "buffer_load_dwordx4 v[68:71], %[la], %[X], s69 offen offset:1024 sc1\n" \
  "s_waitcnt lgkmcnt(2)\n" \
  "v_smfmac_f32_16x16x64_bf16 v[196:199], v[128:131], a[96:103], %[idx]\n" \
  "v_smfmac_f32_16x16x64_bf16 v[200:203], v[128:131], a[104:111], %[idx]\n" \
  "s_waitcnt lgkmcnt(1)\n" \
  "v_smfmac_f32_16x16x64_bf16 v[196:199], v[132:135], a[112:119], %[idx]\n" \
  "v_smfmac_f32_16x16x64_bf16 v[200:203], v[132:135], a[120:127], %[idx]\n" \
  "buffer_load_dwordx4 v[72:75], %[la], %[X], s69 offen offset:2048 sc1\n" \
  "s_waitcnt lgkmcnt(0)\n" \
  "v_smfmac_f32_16x16x64_bf16 v[196:199], v[136:139], a[128:135], %[idx]\n" \
  "s_nop 2\n" \
  "v_smfmac_f32_16x16x64_bf16 v[200:203], v[136:139], a[136:143], %[idx]\n" \
  "s_nop 3\n" \
  "v_pk_add_f32 v[196:197], v[196:197], v[198:199]\n" \
  "s_nop 2\n" \
  "v_pk_add_f32 v[200:201], v[200:201], v[202:203]\n" \
  "v_cvt_pk_bf16_f32 v198, v196, v197\n" \
  "v_cvt_pk_bf16_f32 v199, v200, v201\n" \
  "ds_write2_b32 %[aW], v198, v199 offset1:16\n" \
  "v_cmp_u_f32 vcc, v196, v197\n" \
  "s_cbranch_vccnz .Lo_sm\n" \
  ".Lo_sm_back:\n" \
  "s_waitcnt lgkmcnt(0)\n" \
  "s_barrier\n" \
  "ds_read_b64_tr_b16 v[212:213], %[aR] offset:0\n" \
  "ds_read_b64_tr_b16 v[214:215], %[aR] offset:128\n" \
  "ds_read_b64_tr_b16 v[216:217], %[aR] offset:1024\n" \
  "ds_read_b64_tr_b16 v[218:219], %[aR] offset:1152\n" \
  "buffer_load_dwordx4 v[76:79], %[la], %[X], s69 offen offset:3072 sc1\n" \
  "s_waitcnt lgkmcnt(2)\n" \
  "v_smfmac_f32_16x16x64_bf16 v[160:163], v[212:215], a[144:151], %[idx]\n" \
  "buffer_load_dwordx4 v[80:83], v242, %[X], s69 offen offset:0 sc1\n" \
  "s_waitcnt lgkmcnt(0)\n" \
  "v_smfmac_f32_16x16x64_bf16 v[160:163], v[216:219], a[152:159], %[idx]\n" \
  "s_nop 1\n" \
  "v_smfmac_f32_16x16x64_bf16 v[164:167], v[212:215], a[160:167], %[idx]\n" \
  "s_nop 1\n" \
  "v_smfmac_f32_16x16x64_bf16 v[164:167], v[216:219], a[168:175], %[idx]\n" \
  "s_nop 1\n" \
  "v_smfmac_f32_16x16x64_bf16 v[168:171], v[212:215], a[176:183], %[idx]\n" \
  "buffer_load_dwordx4 v[84:87], v242, %[X], s69 offen offset:1024 sc1\n" \
  "v_add_f32 v160, v160, v162\n" \
  "v_smfmac_f32_16x16x64_bf16 v[168:171], v[216:219], a[184:191], %[idx]\n" \
  "v_add_f32 v161, v161, v163\n" \
  "v_smfmac_f32_16x16x64_bf16 v[172:175], v[212:215], a[192:199], %[idx]\n" \
  "v_cvt_pk_bf16_f32 v220, v160, v161\n" \
  "v_smfmac_f32_16x16x64_bf16 v[172:175], v[216:219], a[200:207], %[idx]\n" \
  "v_add_f32 v164, v164, v166\n" \
  "v_smfmac_f32_16x16x64_bf16 v[176:179], v[212:215], a[208:215], %[idx]\n" \
  "buffer_load_dwordx4 v[88:91], v242, %[X], s69 offen offset:2048 sc1\n" \
  "v_add_f32 v165, v165, v167\n" \
  "v_smfmac_f32_16x16x64_bf16 v[176:179], v[216:219], a[216:223], %[idx]\n" \
  "v_cvt_pk_bf16_f32 v221, v164, v165\n" \
  "v_smfmac_f32_16x16x64_bf16 v[180:183], v[212:215], a[224:231], %[idx]\n" \
  "v_add_f32 v168, v168, v170\n" \
  "v_smfmac_f32_16x16x64_bf16 v[180:183], v[216:219], a[232:239], %[idx]\n" \
  "v_add_f32 v169, v169, v171\n" \
  "v_smfmac_f32_16x16x64_bf16 v[184:187], v[212:215], a[240:247], %[idx]\n" \
  "buffer_load_dwordx4 v[92:95], v242, %[X], s69 offen offset:3072 sc1\n" \
  "v_cvt_pk_bf16_f32 v222, v168, v169\n" \
  "v_smfmac_f32_16x16x64_bf16 v[184:187], v[216:219], a[248:255], %[idx]\n" \
  "v_add_f32 v172, v172, v174\n" \
  "v_smfmac_f32_16x16x64_bf16 v[188:191], v[212:215], v[32:39], %[idx]\n" \
  "v_add_f32 v173, v173, v175\n" \
  "v_smfmac_f32_16x16x64_bf16 v[188:191], v[216:219], v[40:47], %[idx]\n" \
  "v_cvt_pk_bf16_f32 v223, v172, v173\n" \
  "v_smfmac_f32_16x16x64_bf16 v[192:195], v[212:215], v[48:55], %[idx]\n" \
  "v_add_f32 v176, v176, v178\n" \
  "v_smfmac_f32_16x16x64_bf16 v[192:195], v[216:219], v[56:63], %[idx]\n" \
  "v_add_f32 v177, v177, v179\n" \
  "v_permlane32_swap_b32 v220, v222\n" \
  "v_permlane32_swap_b32 v221, v223\n" \
  "v_cvt_pk_bf16_f32 v224, v176, v177\n" \
  "v_pk_add_f32 v[180:181], v[180:181], v[182:183]\n" \
  "v_cvt_pk_bf16_f32 v225, v180, v181\n" \
  "v_permlane16_swap_b32 v220, v221\n" \
  "v_permlane16_swap_b32 v222, v223\n" \
  "v_min_u32 v220, -2, v220\n" \
  "buffer_store_dwordx4 v[220:223], %[vmo], %[X], s66 offen sc1\n" \
  "s_add_i32 s66, s66, 0x10000\n" \
  "v_pk_add_f32 v[184:185], v[184:185], v[186:187]\n" \
  "v_cvt_pk_bf16_f32 v226, v184, v185\n" \
  "v_pk_add_f32 v[188:189], v[188:189], v[190:191]\n" \
  "v_cvt_pk_bf16_f32 v227, v188, v189\n" \
  "v_pk_add_f32 v[192:193], v[192:193], v[194:195]\n" \
  "v_cvt_pk_bf16_f32 v228, v192, v193\n" \
  "v_permlane32_swap_b32 v224, v226\n" \
  "v_permlane32_swap_b32 v225, v227\n" \
  "v_min_u32 v228, -2, v228\n" \
  "buffer_store_dword v228, %[vmo1], %[X], s66 offen sc1\n" \
  "v_permlane16_swap_b32 v224, v225\n" \
  "v_permlane16_swap_b32 v226, v227\n" \
  "v_min_u32 v224, -2, v224\n" \
  "buffer_store_dwordx4 v[224:227], %[vmo], %[X], s66 offen sc1\n" \
  "s_cmp_eq_u32 s56, 63\n" \
  "s_cbranch_scc1 .Lo_end\n" \
  "buffer_load_dword v245, off, %[X], %[ufo] sc1\n" \
  "s_memrealtime s[74:75]\n" \
  "s_waitcnt lgkmcnt(0)\n" \
  "s_sub_i32 s91, s74, s86\n" \
  "s_cmp_gt_u32 s91, 14\n" \
  "s_cbranch_scc1 .Lob_late\n" \
  ".Lob_al_ok:\n" \
  "buffer_load_dwordx4 v[96:99], %[la], %[X], s68 offen offset:0 sc1\n" \
  "s_nop 7\n" \
  "buffer_load_dwordx4 v[100:103], %[la], %[X], s68 offen offset:1024 sc1\n" \
  "s_nop 7\n" \
  "buffer_load_dwordx4 v[140:143], %[la], %[X], s59 offen offset:0 sc1\n" \
  "s_nop 7\n" \
  "buffer_load_dwordx4 v[144:147], %[la], %[X], s59 offen offset:1024 sc1\n" \
  "s_nop 7\n" \
  "buffer_load_dwordx4 v[148:151], %[la], %[X], s59 offen offset:2048 sc1\n" \
  "s_nop 7\n" \
  "buffer_load_dwordx4 v[152:155], %[la], %[X], s59 offen offset:3072 sc1\n" \
  "s_nop 7\n" \
  "buffer_load_dwordx4 v[156:159], v242, %[X], s59 offen offset:0 sc1\n" \
  "s_nop 7\n" \
  "s_nop 15\n" \
  "buffer_load_dwordx2 v[240:241], off, %[X], s94 offset:16 sc1\n" \
  "s_waitcnt vmcnt(8)\n" \
  "v_subrev_u32 v243, s56, v245\n" \
  "s_waitcnt vmcnt(5)\n" \
  "v_mfma_f32_16x16x32_bf16 v[204:207], v[64:67], v[140:143], 0\n" \
  "v_mfma_f32_16x16x32_bf16 v[208:211], v[68:71], v[140:143], 0\n" \
  "v_bitop3_b32 v243, v243, v140, s60 bitop3:0xf6\n" \
  "s_waitcnt vmcnt(4)\n" \
  "v_mfma_f32_16x16x32_bf16 v[204:207], v[72:75], v[144:147], v[204:207]\n" \
  "v_mfma_f32_16x16x32_bf16 v[208:211], v[76:79], v[144:147], v[208:211]\n" \
  "v_bitop3_b32 v243, v243, v144, s60 bitop3:0xf6\n" \
  "s_waitcnt vmcnt(3)\n" \
  "v_mfma_f32_16x16x32_bf16 v[204:207], v[80:83], v[148:151], v[204:207]\n" \
  "v_mfma_f32_16x16x32_bf16 v[208:211], v[84:87], v[148:151], v[208:211]\n" \
  "v_bitop3_b32 v243, v243, v148, s60 bitop3:0xf6\n" \
  "s_waitcnt vmcnt(2)\n" \
  "v_mfma_f32_16x16x32_bf16 v[204:207], v[88:91], v[152:155], v[204:207]\n" \
  "v_mfma_f32_16x16x32_bf16 v[208:211], v[92:95], v[152:155], v[208:211]\n" \
  "v_bitop3_b32 v243, v243, v152, s60 bitop3:0xf6\n" \
  "s_waitcnt vmcnt(1)\n" \
  "v_mfma_f32_16x16x32_bf16 v[204:207], v[96:99], v[156:159], v[204:207]\n" \
  "v_mfma_f32_16x16x32_bf16 v[208:211], v[100:103], v[156:159], v[208:211]\n" \
  "v_bitop3_b32 v238, v156, v157, s60 bitop3:0x7e\n" \
  "v_bitop3_b32 v239, v158, v159, s60 bitop3:0x7e\n" \
  "v_or3_b32 v243, v243, v238, v239\n" \
  "v_ashrrev_i32 v238, 31, v243\n" \
  "s_nop 3\n" \
  "v_add_f32_dpp v204, v208, v204 row_ror:8 row_mask:0xf bank_mask:0xf\n" \
  "v_add_f32_dpp v205, v209, v205 row_ror:8 row_mask:0xf bank_mask:0xf\n" \
  "v_add_f32_dpp v206, v210, v206 row_ror:8 row_mask:0xf bank_mask:0xf\n" \
  "v_add_f32_dpp v207, v211, v207 row_ror:8 row_mask:0xf bank_mask:0xf\n" \
  "ds_write_b128 %[rmW], v[204:207]\n" \
  "v_fma_f32 v238, v204, v205, v238\n" \
  "v_add_f32 v239, v206, v207\n" \
  "v_cmp_u_f32 vcc, v238, v239\n" \
  "s_cbranch_vccnz .Lo_sq\n" \
  ".Lo_sq_back:\n" \
  "s_waitcnt lgkmcnt(0)\n" \
  "s_barrier\n" \
  "ds_read2st64_b64 v[230:233], %[rmA] offset0:0 offset1:2\n" \
  "ds_read2st64_b64 v[234:237], %[rmA] offset0:4 offset1:6\n" \
  "s_waitcnt lgkmcnt(1)\n" \
  "v_pk_add_f32 v[230:231], v[230:231], v[232:233]\n" \
  "s_waitcnt lgkmcnt(0)\n" \
  "v_pk_add_f32 v[234:235], v[234:235], v[236:237]\n" \
  "v_pk_add_f32 v[230:231], v[230:231], v[234:235]\n" \
  "v_cvt_pk_bf16_f32 v230, v230, v231\n" \
  "v_xor_b32 v230, s60, v230\n" \
  "v_min_u32 v230, -2, v230\n" \
  "buffer_store_dword v230, %[lm], %[X], s61 offen sc1\n" \
  "s_waitcnt vmcnt(1)\n" \
  "s_cmp_lg_u32 s56, -1\n" \
  "s_cbranch_scc1 .Lo_top\n" \
  "s_mov_b32 s87, %[mr0]\n" \
  "s_cmp_lg_u32 %[grp], 0\n" \
  "s_cbranch_scc1 .Lo_w1\n" \
  "v_add_u32 v243, 0, %[wo]\n" \
  "global_load_dwordx4 a[0:3], v243, %[wp] offset:0\n" \
  "global_load_dwordx4 a[4:7], v243, %[wp] offset:1024\n" \
  "global_load_dwordx4 a[8:11], v243, %[wp] offset:2048\n" \
  "global_load_dwordx4 a[12:15], v243, %[wp] offset:3072\n" \
  "v_add_u32 v243, 4096, v243\n" \
  "global_load_dwordx4 a[16:19], v243, %[wp] offset:0\n" \
  "global_load_dwordx4 a[20:23], v243, %[wp] offset:1024\n" \
  "global_load_dwordx4 a[24:27], v243, %[wp] offset:2048\n" \
  "global_load_dwordx4 a[28:31], v243, %[wp] offset:3072\n" \
  "v_add_u32 v243, 4096, v243\n" \
  "global_load_dwordx4 a[32:35], v243, %[wp] offset:0\n" \
  "global_load_dwordx4 a[36:39], v243, %[wp] offset:1024\n" \
  "global_load_dwordx4 a[40:43], v243, %[wp] offset:2048\n" \
  "global_load_dwordx4 a[44:47], v243, %[wp] offset:3072\n" \
  "v_add_u32 v243, 4096, v243\n" \
  "global_load_dwordx4 a[48:51], v243, %[wp] offset:0\n" \
  "global_load_dwordx4 a[52:55], v243, %[wp] offset:1024\n" \
  "global_load_dwordx4 a[56:59], v243, %[wp] offset:2048\n" \
  "global_load_dwordx4 a[60:63], v243, %[wp] offset:3072\n" \
  "v_add_u32 v243, 4096, v243\n" \
  "global_load_dwordx4 a[64:67], v243, %[wp] offset:0\n" \
  "global_load_dwordx4 a[68:71], v243, %[wp] offset:1024\n" \
  "global_load_dwordx4 a[72:75], v243, %[wp] offset:2048\n" \
  "global_load_dwordx4 a[76:79], v243, %[wp] offset:3072\n" \
  "v_add_u32 v243, 4096, v243\n" \
  "global_load_dwordx4 a[80:83], v243, %[wp] offset:0\n" \
  "global_load_dwordx4 a[84:87], v243, %[wp] offset:1024\n" \
  "global_load_dwordx4 a[88:91], v243, %[wp] offset:2048\n" \
  "global_load_dwordx4 a[92:95], v243, %[wp] offset:3072\n" \
  "v_add_u32 v243, 4096, v243\n" \
  "global_load_dwordx4 a[96:99], v243, %[wp] offset:0\n" \
  "global_load_dwordx4 a[100:103], v243, %[wp] offset:1024\n" \
  "global_load_dwordx4 a[104:107], v243, %[wp] offset:2048\n" \
  "global_load_dwordx4 a[108:111], v243, %[wp] offset:3072\n" \
  "v_add_u32 v243, 4096, v243\n" \
  "global_load_dwordx4 a[112:115], v243, %[wp] offset:0\n" \
  "global_load_dwordx4 a[116:119], v243, %[wp] offset:1024\n" \
  "global_load_dwordx4 a[120:123], v243, %[wp] offset:2048\n" \
  "global_load_dwordx4 a[124:127], v243, %[wp] offset:3072\n" \
  "v_add_u32 v243, 4096, v243\n" \
  "global_load_dwordx4 a[128:131], v243, %[wp] offset:0\n" \
  "global_load_dwordx4 a[132:135], v243, %[wp] offset:1024\n" \
  "global_load_dwordx4 a[136:139], v243, %[wp] offset:2048\n" \
  "global_load_dwordx4 a[140:143], v243, %[wp] offset:3072\n" \
  "v_add_u32 v243, 4096, v243\n" \
  "global_load_dwordx4 a[144:147], v243, %[wp] offset:0\n" \
  "global_load_dwordx4 a[148:151], v243, %[wp] offset:1024\n" \
  "global_load_dwordx4 a[152:155], v243, %[wp] offset:2048\n" \
  "global_load_dwordx4 a[156:159], v243, %[wp] offset:3072\n" \
  "v_add_u32 v243, 4096, v243\n" \
  "global_load_dwordx4 a[160:163], v243, %[wp] offset:0\n" \
  "global_load_dwordx4 a[164:167], v243, %[wp] offset:1024\n" \
  "global_load_dwordx4 a[168:171], v243, %[wp] offset:2048\n" \
  "global_load_dwordx4 a[172:175], v243, %[wp] offset:3072\n" \
  "v_add_u32 v243, 4096, v243\n" \
  "global_load_dwordx4 a[176:179], v243, %[wp] offset:0\n" \
  "global_load_dwordx4 a[180:183], v243, %[wp] offset:1024\n" \
  "global_load_dwordx4 a[184:187], v243, %[wp] offset:2048\n" \
  "global_load_dwordx4 a[188:191], v243, %[wp] offset:3072\n" \
  "v_add_u32 v243, 4096, v243\n" \
  "global_load_dwordx4 a[192:195], v243, %[wp] offset:0\n" \
  "global_load_dwordx4 a[196:199], v243, %[wp] offset:1024\n" \
  "global_load_dwordx4 a[200:203], v243, %[wp] offset:2048\n" \
  "global_load_dwordx4 a[204:207], v243, %[wp] offset:3072\n" \
  "v_add_u32 v243, 4096, v243\n" \
  "global_load_dwordx4 a[208:211], v243, %[wp] offset:0\n" \
  "global_load_dwordx4 a[212:215], v243, %[wp] offset:1024\n" \
  "global_load_dwordx4 a[216:219], v243, %[wp] offset:2048\n" \
  "global_load_dwordx4 a[220:223], v243, %[wp] offset:3072\n" \
  "v_add_u32 v243, 4096, v243\n" \
  "global_load_dwordx4 a[224:227], v243, %[wp] offset:0\n" \
  "global_load_dwordx4 a[228:231], v243, %[wp] offset:1024\n" \
  "global_load_dwordx4 a[232:235], v243, %[wp] offset:2048\n" \
  "global_load_dwordx4 a[236:239], v243, %[wp] offset:3072\n" \
  "v_add_u32 v243, 4096, v243\n" \
  "global_load_dwordx4 a[240:243], v243, %[wp] offset:0\n" \
  "global_load_dwordx4 a[244:247], v243, %[wp] offset:1024\n" \
  "global_load_dwordx4 a[248:251], v243, %[wp] offset:2048\n" \
  "global_load_dwordx4 a[252:255], v243, %[wp] offset:3072\n" \
  "v_add_u32 v243, 4096, v243\n" \
  "global_load_dwordx4 v[32:35], v243, %[wp] offset:0\n" \
  "global_load_dwordx4 v[36:39], v243, %[wp] offset:1024\n" \
  "global_load_dwordx4 v[40:43], v243, %[wp] offset:2048\n" \
  "global_load_dwordx4 v[44:47], v243, %[wp] offset:3072\n" \
  "v_add_u32 v243, 4096, v243\n" \
  "global_load_dwordx4 v[48:51], v243, %[wp] offset:0\n" \
  "global_load_dwordx4 v[52:55], v243, %[wp] offset:1024\n" \
  "global_load_dwordx4 v[56:59], v243, %[wp] offset:2048\n" \
  "global_load_dwordx4 v[60:63], v243, %[wp] offset:3072\n" \
  "v_add_u32 v243, 4096, v243\n" \
  ".Lo_w1:\n" \
  "s_cmp_eq_u32 %[grp], 0\n" \
  "s_cbranch_scc1 .Loa_go\n" \
  "s_mov_b32 s64, 0\n" \
  "s_xor_b32 s75, %[bas], 0x2000\n" \
  ".Loa_g1:\n" \
  "buffer_load_dword v238, off, %[X], s75 offset:48 sc1\n" \
  "s_waitcnt vmcnt(0)\n" \
  "v_readfirstlane_b32 s74, v238\n" \
  "s_cmp_lg_u32 s74, 0\n" \
  "s_cbranch_scc1 .Loa_g2\n" \
  "s_sleep 8\n" \
  "s_add_i32 s64, s64, 1\n" \
  "s_cmp_lt_u32 s64, 400\n" \
  "s_cbranch_scc1 .Loa_g1\n" \
  ".Loa_g2:\n" \
  "s_add_i32 s74, s74, %[g1t]\n" \
  ".Loa_g3:\n" \
  "s_memrealtime s[76:77]\n" \
  "s_waitcnt lgkmcnt(0)\n" \
  "s_sub_i32 s76, s76, s74\n" \
  "s_cmp_gt_u32 s76, -3000\n" \
  "s_cbranch_scc1 .Loa_g3\n" \
  ".Loa_go:\n" \
  "s_waitcnt vmcnt(0)\n" \
  "v_mov_b64 v[240:241], 0\n" \
  "s_branch .Lo_top\n" \
  ".Lo_sq:\n" \
  "s_cmp_lt_i32 s56, 0\n" \
  "s_cbranch_scc1 .Lo_sq_back\n" \
  "s_mov_b32 s64, 0\n" \
  ".Lo_sqw:\n" \
  "buffer_load_dword v245, off, %[X], %[ufo] sc1\n" \
  "buffer_load_dwordx4 v[96:99], %[la], %[X], s68 offen offset:0 sc1\n" \
  "buffer_load_dwordx4 v[100:103], %[la], %[X], s68 offen offset:1024 sc1\n" \
  "buffer_load_dwordx4 v[140:143], %[la], %[X], s59 offen offset:0 sc1\n" \
  "buffer_load_dwordx4 v[144:147], %[la], %[X], s59 offen offset:1024 sc1\n" \
  "buffer_load_dwordx4 v[148:151], %[la], %[X], s59 offen offset:2048 sc1\n" \
  "buffer_load_dwordx4 v[152:155], %[la], %[X], s59 offen offset:3072 sc1\n" \
  "buffer_load_dwordx4 v[156:159], v242, %[X], s59 offen offset:0 sc1\n" \
  "s_waitcnt vmcnt(4)\n" \
  "v_subrev_u32 v243, s56, v245\n" \
  "s_waitcnt vmcnt(4)\n" \
  "v_mfma_f32_16x16x32_bf16 v[204:207], v[64:67], v[140:143], 0\n" \
  "v_mfma_f32_16x16x32_bf16 v[208:211], v[68:71], v[140:143], 0\n" \
  "v_bitop3_b32 v243, v243, v140, s60 bitop3:0xf6\n" \
  "s_waitcnt vmcnt(3)\n" \
  "v_mfma_f32_16x16x32_bf16 v[204:207], v[72:75], v[144:147], v[204:207]\n" \
  "v_mfma_f32_16x16x32_bf16 v[208:211], v[76:79], v[144:147], v[208:211]\n" \
  "v_bitop3_b32 v243, v243, v144, s60 bitop3:0xf6\n" \
  "s_waitcnt vmcnt(2)\n" \
  "v_mfma_f32_16x16x32_bf16 v[204:207], v[80:83], v[148:151], v[204:207]\n" \
  "v_mfma_f32_16x16x32_bf16 v[208:211], v[84:87], v[148:151], v[208:211]\n" \
  "v_bitop3_b32 v243, v243, v148, s60 bitop3:0xf6\n" \
  "s_waitcnt vmcnt(1)\n" \
  "v_mfma_f32_16x16x32_bf16 v[204:207], v[88:91], v[152:155], v[204:207]\n" \
  "v_mfma_f32_16x16x32_bf16 v[208:211], v[92:95], v[152:155], v[208:211]\n" \
  "v_bitop3_b32 v243, v243, v152, s60 bitop3:0xf6\n" \
  "s_waitcnt vmcnt(0)\n" \
  "v_mfma_f32_16x16x32_bf16 v[204:207], v[96:99], v[156:159], v[204:207]\n" \
  "v_mfma_f32_16x16x32_bf16 v[208:211], v[100:103], v[156:159], v[208:211]\n" \
  "v_bitop3_b32 v238, v156, v157, s60 bitop3:0x7e\n" \
  "v_bitop3_b32 v239, v158, v159, s60 bitop3:0x7e\n" \
  "v_or3_b32 v243, v243, v238, v239\n" \
  "v_max3_u32 v238, v97, v98, v99\n" \
  "v_max3_u32 v238, v238, v101, v102\n" \
  "v_max_u32 v238, v238, v103\n" \
  "v_and_b32 v238, %[w4], v238\n" \
  "v_ashrrev_i32 v243, 31, v243\n" \
  "v_max3_u32 v238, v238, v96, v100\n" \
  "v_max_u32 v238, v238, v243\n" \
  "v_cmp_eq_u32 vcc, -1, v238\n" \
  "s_cbranch_vccz .Lo_sqw_ok\n" \
  "s_add_i32 s64, s64, 1\n" \
  "s_and_b32 s77, s64, 1023\n" \
  "s_cbranch_scc1 .Lo_sqw\n" \
  "buffer_load_dword v239, off, %[F], 0 offset:1024 sc1\n" \
  "s_waitcnt vmcnt(0)\n" \
  "v_readfirstlane_b32 s77, v239\n" \
  "s_cmp_lg_u32 s77, 0\n" \
  "s_cbranch_scc1 .Lo_sqw_ok\n" \
  "s_cmp_lt_u32 s64, 400000\n" \
  "s_cbranch_scc1 .Lo_sqw\n" \
  "v_mov_b32 v239, 1\n" \
  "buffer_store_dword v239, off, %[F], 0 offset:1024 sc1\n" \
  ".Lo_sqw_ok:\n" \
  "v_add_f32_dpp v204, v208, v204 row_ror:8 row_mask:0xf bank_mask:0xf\n" \
  "v_add_f32_dpp v205, v209, v205 row_ror:8 row_mask:0xf bank_mask:0xf\n" \
  "v_add_f32_dpp v206, v210, v206 row_ror:8 row_mask:0xf bank_mask:0xf\n" \
  "v_add_f32_dpp v207, v211, v207 row_ror:8 row_mask:0xf bank_mask:0xf\n" \
  "ds_write_b128 %[rmW], v[204:207]\n" \
  "s_branch .Lo_sq_back\n" \
  ".Lo_sm:\n" \
  "s_cmp_lt_i32 s56, 0\n" \
  "s_cbranch_scc1 .Lo_sm_back\n" \
  "s_add_i32 s76, s57, -0x4000\n" \
  "v_mov_b64 v[196:197], 0\n" \
  "v_mov_b64 v[198:199], 0\n" \
  "v_mov_b64 v[200:201], 0\n" \
  "v_mov_b64 v[202:203], 0\n" \
  "s_mov_b32 s64, 0\n" \
  ".Lo_smw:\n" \
  "buffer_load_dwordx4 v[104:107], %[la], %[X], s76 offen offset:0 sc1\n" \
  "buffer_load_dwordx4 v[108:111], %[la], %[X], s76 offen offset:1024 sc1\n" \
  "buffer_load_dwordx4 v[112:115], %[la], %[X], s76 offen offset:2048 sc1\n" \
  "buffer_load_dwordx4 v[116:119], %[la], %[X], s76 offen offset:3072 sc1\n" \
  "buffer_load_dwordx4 v[120:123], v242, %[X], s76 offen offset:0 sc1\n" \
  "buffer_load_dwordx4 v[124:127], v242, %[X], s76 offen offset:1024 sc1\n" \
  "buffer_load_dwordx4 v[128:131], v242, %[X], s76 offen offset:2048 sc1\n" \
  "buffer_load_dwordx4 v[132:135], v242, %[X], s76 offen offset:3072 sc1\n" \
  "buffer_load_dwordx4 v[136:139], %[la], %[X], s57 offen offset:0 sc1\n" \
  "s_waitcnt vmcnt(0)\n" \
  "v_max3_u32 v239, v104, v105, v106\n" \
  "v_max3_u32 v239, v239, v107, v108\n" \
  "v_max3_u32 v239, v239, v109, v110\n" \
  "v_max3_u32 v239, v239, v111, v112\n" \
  "v_max3_u32 v239, v239, v113, v114\n" \
  "v_max3_u32 v239, v239, v115, v116\n" \
  "v_max3_u32 v239, v239, v117, v118\n" \
  "v_max3_u32 v239, v239, v119, v120\n" \
  "v_max3_u32 v239, v239, v121, v122\n" \
  "v_max3_u32 v239, v239, v123, v124\n" \
  "v_max3_u32 v239, v239, v125, v126\n" \
  "v_max3_u32 v239, v239, v127, v128\n" \
  "v_max3_u32 v239, v239, v129, v130\n" \
  "v_max3_u32 v239, v239, v131, v132\n" \
  "v_max3_u32 v239, v239, v133, v134\n" \
  "v_max3_u32 v239, v239, v135, v136\n" \
  "v_max3_u32 v239, v239, v137, v138\n" \
  "v_max_u32 v239, v239, v139\n" \
  "v_cmp_eq_u32 vcc, -1, v239\n" \
  "s_cbranch_vccz .Lo_smw_ok\n" \
  "s_add_i32 s64, s64, 1\n" \
  "s_and_b32 s77, s64, 1023\n" \
  "s_cbranch_scc1 .Lo_smw\n" \
  "buffer_load_dword v239, off, %[F], 0 offset:1024 sc1\n" \
  "s_waitcnt vmcnt(0)\n" \
  "v_readfirstlane_b32 s77, v239\n" \
  "s_cmp_lg_u32 s77, 0\n" \
  "s_cbranch_scc1 .Lo_smw_ok\n" \
  "s_cmp_lt_u32 s64, 400000\n" \
  "s_cbranch_scc1 .Lo_smw\n" \
  "v_mov_b32 v239, 1\n" \
  "buffer_store_dword v239, off, %[F], 0 offset:1024 sc1\n" \
  ".Lo_smw_ok:\n" \
  "v_smfmac_f32_16x16x64_bf16 v[196:199], v[104:107], a[0:7], %[idx]\n" \
  "v_smfmac_f32_16x16x64_bf16 v[200:203], v[104:107], a[8:15], %[idx]\n" \
  "v_smfmac_f32_16x16x64_bf16 v[196:199], v[108:111], a[16:23], %[idx]\n" \
  "v_smfmac_f32_16x16x64_bf16 v[200:203], v[108:111], a[24:31], %[idx]\n" \
  "v_smfmac_f32_16x16x64_bf16 v[196:199], v[112:115], a[32:39], %[idx]\n" \
  "v_smfmac_f32_16x16x64_bf16 v[200:203], v[112:115], a[40:47], %[idx]\n" \
  "v_smfmac_f32_16x16x64_bf16 v[196:199], v[116:119], a[48:55], %[idx]\n" \
  "v_smfmac_f32_16x16x64_bf16 v[200:203], v[116:119], a[56:63], %[idx]\n" \
  "v_smfmac_f32_16x16x64_bf16 v[196:199], v[120:123], a[64:71], %[idx]\n" \
  "v_smfmac_f32_16x16x64_bf16 v[200:203], v[120:123], a[72:79], %[idx]\n" \
  "v_smfmac_f32_16x16x64_bf16 v[196:199], v[124:127], a[80:87], %[idx]\n" \
  "v_smfmac_f32_16x16x64_bf16 v[200:203], v[124:127], a[88:95], %[idx]\n" \
  "v_smfmac_f32_16x16x64_bf16 v[196:199], v[128:131], a[96:103], %[idx]\n" \
  "v_smfmac_f32_16x16x64_bf16 v[200:203], v[128:131], a[104:111], %[idx]\n" \
  "v_smfmac_f32_16x16x64_bf16 v[196:199], v[132:135], a[112:119], %[idx]\n" \
  "v_smfmac_f32_16x16x64_bf16 v[200:203], v[132:135], a[120:127], %[idx]\n" \
  "v_smfmac_f32_16x16x64_bf16 v[196:199], v[136:139], a[128:135], %[idx]\n" \
  "v_smfmac_f32_16x16x64_bf16 v[200:203], v[136:139], a[136:143], %[idx]\n" \
  "s_nop 7\n" \
  "v_pk_add_f32 v[196:197], v[196:197], v[198:199]\n" \
  "v_pk_add_f32 v[200:201], v[200:201], v[202:203]\n" \
  "v_cvt_pk_bf16_f32 v198, v196, v197\n" \
  "v_cvt_pk_bf16_f32 v199, v200, v201\n" \
  "ds_write2_b32 %[aW], v198, v199 offset1:16\n" \
  "s_branch .Lo_sm_back\n" \
  ".Lo_sf:\n" \
  "s_mov_b32 s64, 0\n" \
  ".Lo_sfw:\n" \
  "buffer_load_dword v244, %[flo], %[X], 0 offen sc1\n" \
  "s_waitcnt vmcnt(0)\n" \
  "v_cmp_gt_i32 vcc, s63, v244\n" \
  "s_cbranch_vccz .Lo_sfw_ok\n" \
  "s_sleep 1\n" \
  "s_add_i32 s64, s64, 1\n" \
  "s_and_b32 s77, s64, 1023\n" \
  "s_cbranch_scc1 .Lo_sfw\n" \
  "buffer_load_dword v239, off, %[F], 0 offset:1024 sc1\n" \
  "s_waitcnt vmcnt(0)\n" \
  "v_readfirstlane_b32 s77, v239\n" \
  "s_cmp_lg_u32 s77, 0\n" \
  "s_cbranch_scc1 .Lo_sfw_ok\n" \
  "s_cmp_lt_u32 s64, 400000\n" \
  "s_cbranch_scc1 .Lo_sfw\n" \
  "v_mov_b32 v239, 1\n" \
  "buffer_store_dword v239, off, %[F], 0 offset:1024 sc1\n" \
  ".Lo_sfw_ok:\n" \
  "s_branch .Lo_sf_back\n" \
  ".Loa_late:\n" \
  "s_cmp_lt_i32 s56, 0\n" \
  "s_cbranch_scc1 .Loa_al_ok\n" \
  "s_cmp_gt_u32 s91, -3000\n" \
  "s_cbranch_scc0 .Loa_late2\n" \
  ".Loa_wt:\n" \
  "s_memrealtime s[76:77]\n" \
  "s_waitcnt lgkmcnt(0)\n" \
  "s_sub_i32 s76, s76, s65\n" \
  "s_cmp_lt_i32 s76, 0\n" \
  "s_cbranch_scc1 .Loa_wt\n" \
  "s_branch .Loa_al_ok\n" \
  ".Loa_late2:\n" \
  "s_cmp_lg_u32 s65, 0\n" \
  "s_cbranch_scc1 .Loa_late3\n" \
  "s_mov_b32 s64, 0\n" \
  ".Loa_tw:\n" \
  "buffer_load_dword v238, off, %[X], s94 sc1\n" \
  "s_waitcnt vmcnt(0)\n" \
  "v_readfirstlane_b32 s65, v238\n" \
  "s_cmp_lg_u32 s65, 0\n" \
  "s_cbranch_scc1 .Loa_tw_ok\n" \
  "s_sleep 1\n" \
  "s_add_i32 s64, s64, 1\n" \
  "s_cmp_lt_u32 s64, 200\n" \
  "s_cbranch_scc1 .Loa_tw\n" \
  ".Loa_tw_ok:\n" \
  "s_sub_i32 s91, s74, s65\n" \
  "s_cmp_gt_u32 s91, -3000\n" \
  "s_cbranch_scc1 .Loa_wt\n" \
  ".Loa_late3:\n" \
  "s_branch .Loa_al_ok\n" \
  ".Lob_late:\n" \
  "s_cmp_lt_i32 s56, 0\n" \
  "s_cbranch_scc1 .Lob_al_ok\n" \
  "s_cmp_gt_u32 s91, -3000\n" \
  "s_cbranch_scc0 .Lob_late2\n" \
  ".Lob_wt:\n" \
  "s_memrealtime s[76:77]\n" \
  "s_waitcnt lgkmcnt(0)\n" \
  "s_sub_i32 s76, s76, s86\n" \
  "s_cmp_lt_i32 s76, 0\n" \
  "s_cbranch_scc1 .Lob_wt\n" \
  "s_branch .Lob_al_ok\n" \
  ".Lob_late2:\n" \
  "s_cmp_lg_u32 s86, 0\n" \
  "s_cbranch_scc1 .Lob_late3\n" \
  "s_mov_b32 s64, 0\n" \
  ".Lob_tw:\n" \
  "buffer_load_dword v238, off, %[X], s94 offset:4 sc1\n" \
  "s_waitcnt vmcnt(0)\n" \
  "v_readfirstlane_b32 s86, v238\n" \
  "s_cmp_lg_u32 s86, 0\n" \
  "s_cbranch_scc1 .Lob_tw_ok\n" \
  "s_sleep 1\n" \
  "s_add_i32 s64, s64, 1\n" \
  "s_cmp_lt_u32 s64, 200\n" \
  "s_cbranch_scc1 .Lob_tw\n" \
  ".Lob_tw_ok:\n" \
  "s_sub_i32 s91, s74, s86\n" \
  "s_cmp_gt_u32 s91, -3000\n" \
  "s_cbranch_scc1 .Lob_wt\n" \
  ".Lob_late3:\n" \
  "s_branch .Lob_al_ok\n" \
  ".Lo_end:\n"
#define O_CLOB "v32", "v33", "v34", "v35", "v36", "v37", "v38", "v39", "v40", "v41", "v42", "v43", "v44", "v45", "v46", "v47", "v48", "v49", "v50", "v51", "v52", "v53", "v54", "v55", "v56", "v57", "v58", "v59", "v60", "v61", "v62", "v63", "v64", "v65", "v66", "v67", "v68", "v69", "v70", "v71", "v72", "v73", "v74", "v75", "v76", "v77", "v78", "v79", "v80", "v81", "v82", "v83", "v84", "v85", "v86", "v87", "v88", "v89", "v90", "v91", "v92", "v93", "v94", "v95", "v96", "v97", "v98", "v99", "v100", "v101", "v102", "v103", "v104", "v105", "v106", "v107", "v108", "v109", "v110", "v111", "v112", "v113", "v114", "v115", "v116", "v117", "v118", "v119", "v120", "v121", "v122", "v123", "v124", "v125", "v126", "v127", "v128", "v129", "v130", "v131", "v132", "v133", "v134", "v135", "v136", "v137", "v138", "v139", "v140", "v141", "v142", "v143", "v144", "v145", "v146", "v147", "v148", "v149", "v150", "v151", "v152", "v153", "v154", "v155", "v156", "v157", "v158", "v159", "v160", "v161", "v162", "v163", "v164", "v165", "v166", "v167", "v168", "v169", "v170", "v171", "v172", "v173", "v174", "v175", "v176", "v177", "v178", "v179", "v180", "v181", "v182", "v183", "v184", "v185", "v186", "v187", "v188", "v189", "v190", "v191", "v192", "v193", "v194", "v195", "v196", "v197", "v198", "v199", "v200", "v201", "v202", "v203", "v204", "v205", "v206", "v207", "v208", "v209", "v210", "v211", "v212", "v213", "v214", "v215", "v216", "v217", "v218", "v219", "v220", "v221", "v222", "v223", "v224", "v225", "v226", "v227", "v228", "v229", "v230", "v231", "v232", "v233", "v234", "v235", "v236", "v237", "v238", "v239", "v240", "v241", "v242", "v243", "v244", "v245", "v246", "v247", "v248", "v249", "v250", "v251", "v252", "v253", "v254", "v255", "s56", "s57", "s58", "s59", "s60", "s61", "s62", "s63", "s64", "s65", "s66", "s67", "s68", "s69", "s70", "s71", "s72", "s73", "s74", "s75", "s76", "s77", "s78", "s79", "s80", "s81", "s82", "s83", "s84", "s85", "s86", "s87", "s88", "s89", "s90", "s91", "s92", "s93", "s94", "a0", "a1", "a2", "a3", "a4", "a5", "a6", "a7", "a8", "a9", "a10", "a11", "a12", "a13", "a14", "a15", "a16", "a17", "a18", "a19", "a20", "a21", "a22", "a23", "a24", "a25", "a26", "a27", "a28", "a29", "a30", "a31", "a32", "a33", "a34", "a35", "a36", "a37", "a38", "a39", "a40", "a41", "a42", "a43", "a44", "a45", "a46", "a47", "a48", "a49", "a50", "a51", "a52", "a53", "a54", "a55", "a56", "a57", "a58", "a59", "a60", "a61", "a62", "a63", "a64", "a65", "a66", "a67", "a68", "a69", "a70", "a71", "a72", "a73", "a74", "a75", "a76", "a77", "a78", "a79", "a80", "a81", "a82", "a83", "a84", "a85", "a86", "a87", "a88", "a89", "a90", "a91", "a92", "a93", "a94", "a95", "a96", "a97", "a98", "a99", "a100", "a101", "a102", "a103", "a104", "a105", "a106", "a107", "a108", "a109", "a110", "a111", "a112", "a113", "a114", "a115", "a116", "a117", "a118", "a119", "a120", "a121", "a122", "a123", "a124", "a125", "a126", "a127", "a128", "a129", "a130", "a131", "a132", "a133", "a134", "a135", "a136", "a137", "a138", "a139", "a140", "a141", "a142", "a143", "a144", "a145", "a146", "a147", "a148", "a149", "a150", "a151", "a152", "a153", "a154", "a155", "a156", "a157", "a158", "a159", "a160", "a161", "a162", "a163", "a164", "a165", "a166", "a167", "a168", "a169", "a170", "a171", "a172", "a173", "a174", "a175", "a176", "a177", "a178", "a179", "a180", "a181", "a182", "a183", "a184", "a185", "a186", "a187", "a188", "a189", "a190", "a191", "a192", "a193", "a194", "a195", "a196", "a197", "a198", "a199", "a200", "a201", "a202", "a203", "a204", "a205", "a206", "a207", "a208", "a209", "a210", "a211", "a212", "a213", "a214", "a215", "a216", "a217", "a218", "a219", "a220", "a221", "a222", "a223", "a224", "a225", "a226", "a227", "a228", "a229", "a230", "a231", "a232", "a233", "a234", "a235", "a236", "a237", "a238", "a239", "a240", "a241", "a242", "a243", "a244", "a245", "a246", "a247", "a248", "a249", "a250", "a251", "a252", "a253", "a254", "a255", "vcc", "scc", "m0", "memory"
#define U_ASM \
  "s_nop 4\n" \
  "s_mov_b32 s56, -2\n" \
  "v_add_u32 v183, 0x1000, %[la]\n" \
  "s_mov_b32 s78, 0\n" \
  "s_mov_b32 s79, 0\n" \
  "s_mov_b32 s80, 0\n" \
  "s_mov_b32 s81, -1\n" \
  "s_mov_b32 s87, 2.0\n" \
  "s_mov_b32 s88, 2.0\n" \
  "s_mov_b32 s66, 589824\n" \
  "s_add_i32 s94, %[bas], -32\n" \
  "s_waitcnt vmcnt(0)\n" \
  ".Lu_top:\n" \
  "s_add_i32 s56, s56, 1\n" \
  "s_add_i32 s94, s94, 16\n" \
  "v_readfirstlane_b32 s64, v182\n" \
  "s_bfe_i32 s62, s56, 0x10001\n" \
  "s_and_b32 s60, s62, 0x80008000\n" \
  "s_mul_i32 s59, s56, 36864\n" \
  "s_add_i32 s59, s59, %[ow]\n" \
  "s_lshl_b32 s61, s56, 17\n" \
  "s_and_b32 s61, s61, 0x20000\n" \
  "s_add_i32 s57, %[pt0], s61\n" \
  "s_add_i32 s66, s66, 589824\n" \
  "s_cmp_eq_u32 s66, 1769472\n" \
  "s_cselect_b32 s66, 0, s66\n" \
  "s_add_i32 s70, %[vm0], s66\n" \
  "s_cmp_lt_i32 s56, 0\n" \
  "s_cselect_b32 s59, 2.0, s59\n" \
  "s_cselect_b32 s57, 2.0, s57\n" \
  "s_cselect_b32 s70, 2.0, s70\n" \
  "s_add_i32 s58, s57, 0x4000\n" \
  "s_add_i32 s71, s70, 0x4000\n" \
  "s_cmp_lg_u32 s64, 0\n" \
  "s_cselect_b32 s76, %[uc2], 0\n" \
  "s_add_i32 s64, s64, s76\n" \
  "s_memrealtime s[74:75]\n" \
  "s_waitcnt lgkmcnt(0)\n" \
  "s_sub_i32 s91, s74, s64\n" \
  "s_cmp_gt_u32 s91, 14\n" \
  "s_cbranch_scc1 .Lub_late\n" \
  ".Lub_al_ok:\n" \
  "buffer_load_dwordx4 v[128:131], %[la], %[X], s70 offen offset:0 sc1\n" \
  "s_add_i32 s62, s56, 1\n" \
  "s_and_b32 s65, s62, 7\n" \
  "s_lshl_b32 s65, s65, 22\n" \
  "s_add_i32 s65, s65, s88\n" \
  "s_cmp_lt_i32 s56, 63\n" \
  "s_cselect_b32 s65, s65, 2.0\n" \
  "s_min_i32 s61, s62, 63\n" \
  "s_add_i32 s61, s61, 1\n" \
  "v_mov_b32 v185, s62\n" \
  "buffer_load_dwordx4 v[132:135], %[la], %[X], s70 offen offset:1024 sc1\n" \
  "s_nop 7\n" \
  "buffer_load_dwordx4 v[136:139], %[la], %[X], s71 offen offset:0 sc1\n" \
  "s_nop 7\n" \
  "buffer_load_dwordx4 v[140:143], %[la], %[X], s71 offen offset:1024 sc1\n" \
  "s_nop 7\n" \
  "buffer_load_dwordx4 v[40:43], %[la], %[X], s57 offen offset:2048 sc1\n" \
  "s_nop 7\n" \
  "buffer_load_dwordx4 v[44:47], %[la], %[X], s57 offen offset:3072 sc1\n" \
  "s_nop 7\n" \
  "buffer_load_dwordx4 v[48:51], v183, %[X], s57 offen offset:0 sc1\n" \
  "s_nop 7\n" \
  "buffer_load_dwordx4 v[52:55], v183, %[X], s57 offen offset:1024 sc1\n" \
  "s_waitcnt vmcnt(8)\n" \
  "v_accvgpr_read_b32 v96, a0\n" \
  "v_accvgpr_read_b32 v97, a1\n" \
  "v_accvgpr_read_b32 v98, a8\n" \
  "v_accvgpr_read_b32 v99, a9\n" \
  "v_accvgpr_read_b32 v100, a4\n" \
  "v_accvgpr_read_b32 v101, a5\n" \
  "v_accvgpr_read_b32 v102, a12\n" \
  "v_accvgpr_read_b32 v103, a13\n" \
  "buffer_load_dwordx4 v[56:59], v183, %[X], s57 offen offset:2048 sc1\n" \
  "v_accvgpr_read_b32 v104, a16\n" \
  "v_accvgpr_read_b32 v105, a17\n" \
  "v_accvgpr_read_b32 v106, a24\n" \
  "v_accvgpr_read_b32 v107, a25\n" \
  "v_accvgpr_read_b32 v108, a20\n" \
  "v_accvgpr_read_b32 v109, a21\n" \
  "v_accvgpr_read_b32 v110, a28\n" \
  "v_accvgpr_read_b32 v111, a29\n" \
  "buffer_load_dwordx4 v[60:63], v183, %[X], s57 offen offset:3072 sc1\n" \
  "v_accvgpr_read_b32 v112, a32\n" \
  "v_accvgpr_read_b32 v113, a33\n" \
  "v_accvgpr_read_b32 v114, a40\n" \
  "v_accvgpr_read_b32 v115, a41\n" \
  "v_accvgpr_read_b32 v116, a36\n" \
  "v_accvgpr_read_b32 v117, a37\n" \
  "v_accvgpr_read_b32 v118, a44\n" \
  "v_accvgpr_read_b32 v119, a45\n" \
  "buffer_load_dwordx4 v[64:67], %[la], %[X], s58 offen offset:0 sc1\n" \
  "v_accvgpr_read_b32 v120, a48\n" \
  "v_accvgpr_read_b32 v121, a49\n" \
  "v_accvgpr_read_b32 v122, a56\n" \
  "v_accvgpr_read_b32 v123, a57\n" \
  "v_accvgpr_read_b32 v124, a52\n" \
  "v_accvgpr_read_b32 v125, a53\n" \
  "v_accvgpr_read_b32 v126, a60\n" \
  "v_accvgpr_read_b32 v127, a61\n" \
  "buffer_load_dwordx4 v[68:71], %[la], %[X], s58 offen offset:1024 sc1\n" \
  "s_nop 7\n" \
  "buffer_load_dwordx4 v[72:75], %[la], %[X], s58 offen offset:2048 sc1\n" \
  "s_nop 7\n" \
  "buffer_load_dwordx4 v[76:79], %[la], %[X], s58 offen offset:3072 sc1\n" \
  "s_nop 7\n" \
  "buffer_load_dword v184, %[flo], %[X], 0 offen sc1\n" \
  "s_nop 7\n" \
  "s_waitcnt vmcnt(10)\n" \
  "v_mfma_f32_16x16x32_bf16 v[148:151], v[128:131], v[40:43], 0\n" \
  "v_bitop3_b32 v176, v40, v41, s60 bitop3:0x7e\n" \
  "v_bitop3_b32 v177, v42, v43, s60 bitop3:0x7e\n" \
  "v_mfma_f32_16x16x32_bf16 v[156:159], v[136:139], v[40:43], 0\n" \
  "buffer_load_dwordx4 v[32:35], %[la], %[X], s57 offen offset:0 sc1\n" \
  "s_waitcnt vmcnt(10)\n" \
  "v_mfma_f32_16x16x32_bf16 v[148:151], v[132:135], v[44:47], v[148:151]\n" \
  "v_bitop3_b32 v178, v44, v45, s60 bitop3:0x7e\n" \
  "v_or3_b32 v176, v176, v177, v178\n" \
  "v_mfma_f32_16x16x32_bf16 v[156:159], v[140:143], v[44:47], v[156:159]\n" \
  "v_bitop3_b32 v177, v46, v47, s60 bitop3:0x7e\n" \
  "buffer_load_dwordx4 v[36:39], %[la], %[X], s57 offen offset:1024 sc1\n" \
  "s_waitcnt vmcnt(10)\n" \
  "v_mfma_f32_16x16x32_bf16 v[148:151], a[0:3], v[48:51], v[148:151]\n" \
  "v_mfma_f32_16x16x32_bf16 v[156:159], a[32:35], v[48:51], v[156:159]\n" \
  "buffer_load_dwordx4 v[80:83], v183, %[X], s58 offen offset:0 sc1\n" \
  "v_max3_u32 v180, v129, v130, v131\n" \
  "v_max3_u32 v180, v180, v133, v134\n" \
  "v_max_u32 v180, v180, v135\n" \
  "v_and_b32 v180, %[w40], v180\n" \
  "v_max3_u32 v180, v180, v128, v132\n" \
  "v_max3_u32 v181, v137, v138, v139\n" \
  "v_max3_u32 v181, v181, v141, v142\n" \
  "v_max_u32 v181, v181, v143\n" \
  "v_and_b32 v181, %[w41], v181\n" \
  "v_max3_u32 v181, v181, v136, v140\n" \
  "s_waitcnt vmcnt(10)\n" \
  "v_mfma_f32_16x16x32_bf16 v[148:151], a[4:7], v[52:55], v[148:151]\n" \
  "v_bitop3_b32 v178, v48, v52, s60 bitop3:0x7e\n" \
  "v_or3_b32 v176, v176, v177, v178\n" \
  "v_mfma_f32_16x16x32_bf16 v[156:159], a[36:39], v[52:55], v[156:159]\n" \
  "buffer_load_dwordx4 v[84:87], v183, %[X], s58 offen offset:1024 sc1\n" \
  "s_waitcnt vmcnt(10)\n" \
  "v_mfma_f32_16x16x32_bf16 v[148:151], a[8:11], v[56:59], v[148:151]\n" \
  "v_mfma_f32_16x16x32_bf16 v[156:159], a[40:43], v[56:59], v[156:159]\n" \
  "buffer_load_dwordx4 v[88:91], v183, %[X], s58 offen offset:2048 sc1\n" \
  "s_waitcnt vmcnt(10)\n" \
  "v_mfma_f32_16x16x32_bf16 v[148:151], a[12:15], v[60:63], v[148:151]\n" \
  "v_bitop3_b32 v177, v56, v60, s60 bitop3:0x7e\n" \
  "v_mfma_f32_16x16x32_bf16 v[156:159], a[44:47], v[60:63], v[156:159]\n" \
  "buffer_load_dwordx4 v[92:95], v183, %[X], s58 offen offset:3072 sc1\n" \
  "s_waitcnt vmcnt(10)\n" \
  "v_mfma_f32_16x16x32_bf16 v[148:151], a[16:19], v[64:67], v[148:151]\n" \
  "v_mfma_f32_16x16x32_bf16 v[156:159], a[48:51], v[64:67], v[156:159]\n" \
  "buffer_load_dword v182, off, %[X], s94 offset:20 sc1\n" \
  "s_waitcnt vmcnt(10)\n" \
  "v_mfma_f32_16x16x32_bf16 v[148:151], a[20:23], v[68:71], v[148:151]\n" \
  "v_bitop3_b32 v178, v64, v68, s60 bitop3:0x7e\n" \
  "v_or3_b32 v176, v176, v177, v178\n" \
  "v_mfma_f32_16x16x32_bf16 v[156:159], a[52:55], v[68:71], v[156:159]\n" \
  "s_waitcnt vmcnt(9)\n" \
  "v_mfma_f32_16x16x32_bf16 v[148:151], a[24:27], v[72:75], v[148:151]\n" \
  "v_bitop3_b32 v176, v176, v72, s60 bitop3:0xf6\n" \
  "v_mfma_f32_16x16x32_bf16 v[156:159], a[56:59], v[72:75], v[156:159]\n" \
  "s_waitcnt vmcnt(8)\n" \
  "v_mfma_f32_16x16x32_bf16 v[148:151], a[28:31], v[76:79], v[148:151]\n" \
  "v_bitop3_b32 v176, v176, v76, s60 bitop3:0xf6\n" \
  "v_mfma_f32_16x16x32_bf16 v[156:159], a[60:63], v[76:79], v[156:159]\n" \
  "s_waitcnt vmcnt(7)\n" \
  "v_subrev_u32 v184, s61, v184\n" \
  "v_or_b32 v176, v176, v184\n" \
  "v_ashrrev_i32 v176, 31, v176\n" \
  "v_max3_u32 v180, v180, v181, v176\n" \
  "v_cmp_eq_u32 vcc, -1, v180\n" \
  "s_cbranch_vccnz .Lu_sp\n" \
  ".Lu_sp_back:\n" \
  "s_waitcnt vmcnt(6)\n" \
  "v_mfma_f32_16x16x32_bf16 v[144:147], v[128:131], v[32:35], 0\n" \
  "v_bitop3_b32 v176, v32, v33, s60 bitop3:0x7e\n" \
  "v_bitop3_b32 v177, v34, v35, s60 bitop3:0x7e\n" \
  "v_mfma_f32_16x16x32_bf16 v[152:155], v[136:139], v[32:35], 0\n" \
  "ds_write_b128 %[rtW], v[148:151] offset:1024\n" \
  "s_waitcnt vmcnt(5)\n" \
  "v_mfma_f32_16x16x32_bf16 v[144:147], v[132:135], v[36:39], v[144:147]\n" \
  "v_bitop3_b32 v178, v36, v37, s60 bitop3:0x7e\n" \
  "v_or3_b32 v176, v176, v177, v178\n" \
  "v_mfma_f32_16x16x32_bf16 v[152:155], v[140:143], v[36:39], v[152:155]\n" \
  "v_bitop3_b32 v177, v38, v39, s60 bitop3:0x7e\n" \
  "ds_write_b128 %[rtW], v[156:159] offset:9216\n" \
  "s_waitcnt vmcnt(4)\n" \
  "v_mfma_f32_16x16x32_bf16 v[144:147], v[96:99], v[80:83], v[144:147]\n" \
  "v_bitop3_b32 v178, v80, v82, s60 bitop3:0x7e\n" \
  "v_or3_b32 v176, v176, v177, v178\n" \
  "v_mfma_f32_16x16x32_bf16 v[152:155], v[112:115], v[80:83], v[152:155]\n" \
  "s_waitcnt vmcnt(3)\n" \
  "v_mfma_f32_16x16x32_bf16 v[144:147], v[100:103], v[84:87], v[144:147]\n" \
  "v_bitop3_b32 v177, v84, v86, s60 bitop3:0x7e\n" \
  "v_mfma_f32_16x16x32_bf16 v[152:155], v[116:119], v[84:87], v[152:155]\n" \
  "buffer_load_dwordx4 a[0:3], %[la], %[X], s65 offen offset:0 sc1\n" \
  "s_waitcnt vmcnt(3)\n" \
  "v_mfma_f32_16x16x32_bf16 v[144:147], v[104:107], v[88:91], v[144:147]\n" \
  "v_bitop3_b32 v178, v88, v90, s60 bitop3:0x7e\n" \
  "v_or3_b32 v176, v176, v177, v178\n" \
  "v_mfma_f32_16x16x32_bf16 v[152:155], v[120:123], v[88:91], v[152:155]\n" \
  "buffer_load_dwordx4 a[4:7], %[la], %[X], s65 offen offset:1024 sc1\n" \
  "s_waitcnt vmcnt(3)\n" \
  "v_mfma_f32_16x16x32_bf16 v[144:147], v[108:111], v[92:95], v[144:147]\n" \
  "v_bitop3_b32 v176, v176, v92, s60 bitop3:0xf6\n" \
  "v_bitop3_b32 v176, v176, v94, s60 bitop3:0xf6\n" \
  "v_mfma_f32_16x16x32_bf16 v[152:155], v[124:127], v[92:95], v[152:155]\n" \
  "buffer_load_dwordx4 a[8:11], %[la], %[X], s65 offen offset:2048 sc1\n" \
  "v_cmp_gt_i32 vcc, 0, v176\n" \
  "s_cbranch_vccnz .Lu_sr\n" \
  ".Lu_sr_back:\n" \
  "s_nop 1\n" \
  "ds_write_b128 %[rtW], v[144:147] offset:0\n" \
  "buffer_load_dwordx4 a[12:15], %[la], %[X], s65 offen offset:3072 sc1\n" \
  "s_nop 0\n" \
  "ds_write_b128 %[rtW], v[152:155] offset:8192\n" \
  "buffer_load_dwordx4 a[16:19], v183, %[X], s65 offen offset:0 sc1\n" \
  "s_waitcnt lgkmcnt(0)\n" \
  "s_barrier\n" \
  "ds_read2st64_b64 v[160:163], %[rtA] offset0:0 offset1:4\n" \
  "ds_read2st64_b64 v[164:167], %[rtA] offset0:8 offset1:12\n" \
  "ds_read2st64_b64 v[168:171], %[rtA] offset0:16 offset1:20\n" \
  "ds_read2st64_b64 v[172:175], %[rtA] offset0:24 offset1:28\n" \
  "buffer_store_dword v185, %[ufo], %[X], s87 offen sc1\n" \
  "buffer_load_dwordx4 a[20:23], v183, %[X], s65 offen offset:1024 sc1\n" \
  "s_waitcnt lgkmcnt(2)\n" \
  "v_pk_add_f32 v[160:161], v[160:161], v[162:163]\n" \
  "v_pk_add_f32 v[164:165], v[164:165], v[166:167]\n" \
  "v_pk_add_f32 v[160:161], v[160:161], v[164:165]\n" \
  "v_cvt_pk_bf16_f32 v160, v160, v161\n" \
  "v_xor_b32 v160, s60, v160\n" \
  "v_min_u32 v160, -2, v160\n" \
  "buffer_store_dword v160, %[lo], %[OS], s59 offen offset:0 sc1\n" \
  "buffer_load_dwordx4 a[24:27], v183, %[X], s65 offen offset:2048 sc1\n" \
  "s_waitcnt lgkmcnt(0)\n" \
  "v_pk_add_f32 v[168:169], v[168:169], v[170:171]\n" \
  "v_pk_add_f32 v[172:173], v[172:173], v[174:175]\n" \
  "v_pk_add_f32 v[168:169], v[168:169], v[172:173]\n" \
  "v_cvt_pk_bf16_f32 v168, v168, v169\n" \
  "v_xor_b32 v168, s60, v168\n" \
  "v_min_u32 v168, -2, v168\n" \
  "buffer_store_dword v168, %[lo], %[OS], s59 offen offset:256 sc1\n" \
  "s_barrier\n" \
  "s_waitcnt vmcnt(10)\n" \
  "buffer_load_dwordx4 a[28:31], v183, %[X], s65 offen offset:3072 sc1\n" \
  "s_add_i32 s65, s65, 0x10000\n" \
  "s_nop 7\n" \
  "buffer_load_dwordx4 a[32:35], %[la], %[X], s65 offen offset:0 sc1\n" \
  "s_nop 7\n" \
  "buffer_load_dwordx4 a[36:39], %[la], %[X], s65 offen offset:1024 sc1\n" \
  "s_nop 7\n" \
  "buffer_load_dwordx4 a[40:43], %[la], %[X], s65 offen offset:2048 sc1\n" \
  "s_nop 7\n" \
  "buffer_load_dwordx4 a[44:47], %[la], %[X], s65 offen offset:3072 sc1\n" \
  "s_nop 7\n" \
  "buffer_load_dwordx4 a[48:51], v183, %[X], s65 offen offset:0 sc1\n" \
  "s_nop 7\n" \
  "buffer_load_dwordx4 a[52:55], v183, %[X], s65 offen offset:1024 sc1\n" \
  "s_nop 7\n" \
  "buffer_load_dwordx4 a[56:59], v183, %[X], s65 offen offset:2048 sc1\n" \
  "s_nop 7\n" \
  "buffer_load_dwordx4 a[60:63], v183, %[X], s65 offen offset:3072 sc1\n" \
  "s_add_i32 s65, s65, 0x10000\n" \
  "s_nop 7\n" \
  "s_cmp_lg_u32 s56, s81\n" \
  "s_cbranch_scc1 .Lu_top\n" \
  "s_cmp_eq_u32 s56, -1\n" \
  "s_cbranch_scc0 .Lu_end\n" \
  "s_mov_b32 s81, 63\n" \
  "s_mov_b32 s87, 0\n" \
  "s_mov_b32 s88, %[vt0]\n" \
  "s_cmp_eq_u32 %[grp], 0\n" \
  "s_cbranch_scc1 .Lub_go\n" \
  "s_mov_b32 s63, 0\n" \
  "s_xor_b32 s75, %[bas], 0x2000\n" \
  ".Lub_g1:\n" \
  "buffer_load_dword v180, off, %[X], s75 offset:48 sc1\n" \
  "s_waitcnt vmcnt(0)\n" \
  "v_readfirstlane_b32 s74, v180\n" \
  "s_cmp_lg_u32 s74, 0\n" \
  "s_cbranch_scc1 .Lub_g2\n" \
  "s_sleep 8\n" \
  "s_add_i32 s63, s63, 1\n" \
  "s_cmp_lt_u32 s63, 400\n" \
  "s_cbranch_scc1 .Lub_g1\n" \
  ".Lub_g2:\n" \
  "s_add_i32 s74, s74, %[g1t]\n" \
  ".Lub_g3:\n" \
  "s_memrealtime s[76:77]\n" \
  "s_waitcnt lgkmcnt(0)\n" \
  "s_sub_i32 s76, s76, s74\n" \
  "s_cmp_gt_u32 s76, -3000\n" \
  "s_cbranch_scc1 .Lub_g3\n" \
  ".Lub_go:\n" \
  "s_add_i32 s62, s56, 1\n" \
  "s_and_b32 s65, s62, 7\n" \
  "s_lshl_b32 s65, s65, 22\n" \
  "s_add_i32 s65, s65, s88\n" \
  "s_cmp_lt_i32 s56, 63\n" \
  "s_cselect_b32 s65, s65, 2.0\n" \
  "s_min_i32 s61, s62, 63\n" \
  "s_add_i32 s61, s61, 1\n" \
  "s_mov_b32 s63, 0\n" \
  ".Lu_enw:\n" \
  "buffer_load_dword v184, %[flo], %[X], 0 offen sc1\n" \
  "s_waitcnt vmcnt(0)\n" \
  "v_cmp_gt_i32 vcc, s61, v184\n" \
  "s_cbranch_vccz .Lu_enw_ok\n" \
  "s_sleep 4\n" \
  "s_add_i32 s63, s63, 1\n" \
  "s_and_b32 s77, s63, 1023\n" \
  "s_cbranch_scc1 .Lu_enw\n" \
  "buffer_load_dword v181, off, %[F], 0 offset:1024 sc1\n" \
  "s_waitcnt vmcnt(0)\n" \
  "v_readfirstlane_b32 s77, v181\n" \
  "s_cmp_lg_u32 s77, 0\n" \
  "s_cbranch_scc1 .Lu_enw_ok\n" \
  "s_cmp_lt_u32 s63, 400000\n" \
  "s_cbranch_scc1 .Lu_enw\n" \
  "v_mov_b32 v181, 1\n" \
  "buffer_store_dword v181, off, %[F], 0 offset:1024 sc1\n" \
  ".Lu_enw_ok:\n" \
  "buffer_load_dwordx4 a[0:3], %[la], %[X], s65 offen offset:0 sc1\n" \
  "buffer_load_dwordx4 a[4:7], %[la], %[X], s65 offen offset:1024 sc1\n" \
  "buffer_load_dwordx4 a[8:11], %[la], %[X], s65 offen offset:2048 sc1\n" \
  "buffer_load_dwordx4 a[12:15], %[la], %[X], s65 offen offset:3072 sc1\n" \
  "buffer_load_dwordx4 a[16:19], v183, %[X], s65 offen offset:0 sc1\n" \
  "buffer_load_dwordx4 a[20:23], v183, %[X], s65 offen offset:1024 sc1\n" \
  "buffer_load_dwordx4 a[24:27], v183, %[X], s65 offen offset:2048 sc1\n" \
  "buffer_load_dwordx4 a[28:31], v183, %[X], s65 offen offset:3072 sc1\n" \
  "s_add_i32 s65, s65, 0x10000\n" \
  "buffer_load_dwordx4 a[32:35], %[la], %[X], s65 offen offset:0 sc1\n" \
  "buffer_load_dwordx4 a[36:39], %[la], %[X], s65 offen offset:1024 sc1\n" \
  "buffer_load_dwordx4 a[40:43], %[la], %[X], s65 offen offset:2048 sc1\n" \
  "buffer_load_dwordx4 a[44:47], %[la], %[X], s65 offen offset:3072 sc1\n" \
  "buffer_load_dwordx4 a[48:51], v183, %[X], s65 offen offset:0 sc1\n" \
  "buffer_load_dwordx4 a[52:55], v183, %[X], s65 offen offset:1024 sc1\n" \
  "buffer_load_dwordx4 a[56:59], v183, %[X], s65 offen offset:2048 sc1\n" \
  "buffer_load_dwordx4 a[60:63], v183, %[X], s65 offen offset:3072 sc1\n" \
  "s_add_i32 s65, s65, 0x10000\n" \
  "v_mov_b32 v182, 0\n" \
  "s_branch .Lu_top\n" \
  ".Lu_sp:\n" \
  "s_cmp_lt_i32 s56, 0\n" \
  "s_cbranch_scc1 .Lu_sp_back\n" \
  "s_mov_b32 s63, 0\n" \
  ".Lu_spw:\n" \
  "buffer_load_dwordx4 v[128:131], %[la], %[X], s70 offen offset:0 sc1\n" \
  "buffer_load_dwordx4 v[132:135], %[la], %[X], s70 offen offset:1024 sc1\n" \
  "buffer_load_dwordx4 v[136:139], %[la], %[X], s71 offen offset:0 sc1\n" \
  "buffer_load_dwordx4 v[140:143], %[la], %[X], s71 offen offset:1024 sc1\n" \
  "buffer_load_dwordx4 v[40:43], %[la], %[X], s57 offen offset:2048 sc1\n" \
  "buffer_load_dwordx4 v[44:47], %[la], %[X], s57 offen offset:3072 sc1\n" \
  "buffer_load_dwordx4 v[48:51], v183, %[X], s57 offen offset:0 sc1\n" \
  "buffer_load_dwordx4 v[52:55], v183, %[X], s57 offen offset:1024 sc1\n" \
  "buffer_load_dwordx4 v[56:59], v183, %[X], s57 offen offset:2048 sc1\n" \
  "buffer_load_dwordx4 v[60:63], v183, %[X], s57 offen offset:3072 sc1\n" \
  "buffer_load_dwordx4 v[64:67], %[la], %[X], s58 offen offset:0 sc1\n" \
  "buffer_load_dwordx4 v[68:71], %[la], %[X], s58 offen offset:1024 sc1\n" \
  "buffer_load_dwordx4 v[72:75], %[la], %[X], s58 offen offset:2048 sc1\n" \
  "buffer_load_dwordx4 v[76:79], %[la], %[X], s58 offen offset:3072 sc1\n" \
  "buffer_load_dword v184, %[flo], %[X], 0 offen sc1\n" \
  "s_waitcnt vmcnt(0)\n" \
  "v_max3_u32 v180, v129, v130, v131\n" \
  "v_max3_u32 v180, v180, v133, v134\n" \
  "v_max_u32 v180, v180, v135\n" \
  "v_and_b32 v180, %[w40], v180\n" \
  "v_max3_u32 v180, v180, v128, v132\n" \
  "v_max3_u32 v181, v137, v138, v139\n" \
  "v_max3_u32 v181, v181, v141, v142\n" \
  "v_max_u32 v181, v181, v143\n" \
  "v_and_b32 v181, %[w41], v181\n" \
  "v_max3_u32 v181, v181, v136, v140\n" \
  "v_bitop3_b32 v176, v40, v41, s60 bitop3:0x7e\n" \
  "v_bitop3_b32 v177, v42, v43, s60 bitop3:0x7e\n" \
  "v_bitop3_b32 v178, v44, v45, s60 bitop3:0x7e\n" \
  "v_or3_b32 v176, v176, v177, v178\n" \
  "v_bitop3_b32 v177, v46, v47, s60 bitop3:0x7e\n" \
  "v_bitop3_b32 v178, v48, v52, s60 bitop3:0x7e\n" \
  "v_or3_b32 v176, v176, v177, v178\n" \
  "v_bitop3_b32 v177, v56, v60, s60 bitop3:0x7e\n" \
  "v_bitop3_b32 v178, v64, v68, s60 bitop3:0x7e\n" \
  "v_or3_b32 v176, v176, v177, v178\n" \
  "v_bitop3_b32 v176, v176, v72, s60 bitop3:0xf6\n" \
  "v_bitop3_b32 v176, v176, v76, s60 bitop3:0xf6\n" \
  "v_subrev_u32 v184, s61, v184\n" \
  "v_or_b32 v176, v176, v184\n" \
  "v_ashrrev_i32 v176, 31, v176\n" \
  "v_max3_u32 v180, v180, v181, v176\n" \
  "v_cmp_eq_u32 vcc, -1, v180\n" \
  "s_cbranch_vccz .Lu_spw_ok\n" \
  "s_sleep 2\n" \
  "s_add_i32 s63, s63, 1\n" \
  "s_and_b32 s77, s63, 1023\n" \
  "s_cbranch_scc1 .Lu_spw\n" \
  "buffer_load_dword v181, off, %[F], 0 offset:1024 sc1\n" \
  "s_waitcnt vmcnt(0)\n" \
  "v_readfirstlane_b32 s77, v181\n" \
  "s_cmp_lg_u32 s77, 0\n" \
  "s_cbranch_scc1 .Lu_spw_ok\n" \
  "s_cmp_lt_u32 s63, 400000\n" \
  "s_cbranch_scc1 .Lu_spw\n" \
  "v_mov_b32 v181, 1\n" \
  "buffer_store_dword v181, off, %[F], 0 offset:1024 sc1\n" \
  ".Lu_spw_ok:\n" \
  "buffer_load_dwordx4 v[32:35], %[la], %[X], s57 offen offset:0 sc1\n" \
  "buffer_load_dwordx4 v[36:39], %[la], %[X], s57 offen offset:1024 sc1\n" \
  "buffer_load_dwordx4 v[80:83], v183, %[X], s58 offen offset:0 sc1\n" \
  "buffer_load_dwordx4 v[84:87], v183, %[X], s58 offen offset:1024 sc1\n" \
  "buffer_load_dwordx4 v[88:91], v183, %[X], s58 offen offset:2048 sc1\n" \
  "buffer_load_dwordx4 v[92:95], v183, %[X], s58 offen offset:3072 sc1\n" \
  "v_mfma_f32_16x16x32_bf16 v[148:151], v[128:131], v[40:43], 0\n" \
  "v_mfma_f32_16x16x32_bf16 v[156:159], v[136:139], v[40:43], 0\n" \
  "v_mfma_f32_16x16x32_bf16 v[148:151], v[132:135], v[44:47], v[148:151]\n" \
  "v_mfma_f32_16x16x32_bf16 v[156:159], v[140:143], v[44:47], v[156:159]\n" \
  "v_mfma_f32_16x16x32_bf16 v[148:151], a[0:3], v[48:51], v[148:151]\n" \
  "v_mfma_f32_16x16x32_bf16 v[156:159], a[32:35], v[48:51], v[156:159]\n" \
  "v_mfma_f32_16x16x32_bf16 v[148:151], a[4:7], v[52:55], v[148:151]\n" \
  "v_mfma_f32_16x16x32_bf16 v[156:159], a[36:39], v[52:55], v[156:159]\n" \
  "v_mfma_f32_16x16x32_bf16 v[148:151], a[8:11], v[56:59], v[148:151]\n" \
  "v_mfma_f32_16x16x32_bf16 v[156:159], a[40:43], v[56:59], v[156:159]\n" \
  "v_mfma_f32_16x16x32_bf16 v[148:151], a[12:15], v[60:63], v[148:151]\n" \
  "v_mfma_f32_16x16x32_bf16 v[156:159], a[44:47], v[60:63], v[156:159]\n" \
  "v_mfma_f32_16x16x32_bf16 v[148:151], a[16:19], v[64:67], v[148:151]\n" \
  "v_mfma_f32_16x16x32_bf16 v[156:159], a[48:51], v[64:67], v[156:159]\n" \
  "v_mfma_f32_16x16x32_bf16 v[148:151], a[20:23], v[68:71], v[148:151]\n" \
  "v_mfma_f32_16x16x32_bf16 v[156:159], a[52:55], v[68:71], v[156:159]\n" \
  "v_mfma_f32_16x16x32_bf16 v[148:151], a[24:27], v[72:75], v[148:151]\n" \
  "v_mfma_f32_16x16x32_bf16 v[156:159], a[56:59], v[72:75], v[156:159]\n" \
  "v_mfma_f32_16x16x32_bf16 v[148:151], a[28:31], v[76:79], v[148:151]\n" \
  "v_mfma_f32_16x16x32_bf16 v[156:159], a[60:63], v[76:79], v[156:159]\n" \
  "s_waitcnt vmcnt(0)\n" \
  "s_branch .Lu_sp_back\n" \
  ".Lu_sr:\n" \
  "s_cmp_lt_i32 s56, 0\n" \
  "s_cbranch_scc1 .Lu_sr_back\n" \
  "s_mov_b32 s63, 0\n" \
  ".Lu_srw:\n" \
  "buffer_load_dwordx4 v[32:35], %[la], %[X], s57 offen offset:0 sc1\n" \
  "buffer_load_dwordx4 v[36:39], %[la], %[X], s57 offen offset:1024 sc1\n" \
  "buffer_load_dwordx4 v[80:83], v183, %[X], s58 offen offset:0 sc1\n" \
  "buffer_load_dwordx4 v[84:87], v183, %[X], s58 offen offset:1024 sc1\n" \
  "buffer_load_dwordx4 v[88:91], v183, %[X], s58 offen offset:2048 sc1\n" \
  "buffer_load_dwordx4 v[92:95], v183, %[X], s58 offen offset:3072 sc1\n" \
  "s_waitcnt vmcnt(0)\n" \
  "v_bitop3_b32 v176, v32, v33, s60 bitop3:0x7e\n" \
  "v_bitop3_b32 v177, v34, v35, s60 bitop3:0x7e\n" \
  "v_bitop3_b32 v178, v36, v37, s60 bitop3:0x7e\n" \
  "v_or3_b32 v176, v176, v177, v178\n" \
  "v_bitop3_b32 v177, v38, v39, s60 bitop3:0x7e\n" \
  "v_bitop3_b32 v178, v80, v82, s60 bitop3:0x7e\n" \
  "v_or3_b32 v176, v176, v177, v178\n" \
  "v_bitop3_b32 v177, v84, v86, s60 bitop3:0x7e\n" \
  "v_bitop3_b32 v178, v88, v90, s60 bitop3:0x7e\n" \
  "v_or3_b32 v176, v176, v177, v178\n" \
  "v_bitop3_b32 v176, v176, v92, s60 bitop3:0xf6\n" \
  "v_bitop3_b32 v176, v176, v94, s60 bitop3:0xf6\n" \
  "v_cmp_gt_i32 vcc, 0, v176\n" \
  "s_cbranch_vccz .Lu_srw_ok\n" \
  "s_sleep 2\n" \
  "s_add_i32 s63, s63, 1\n" \
  "s_and_b32 s77, s63, 1023\n" \
  "s_cbranch_scc1 .Lu_srw\n" \
  "buffer_load_dword v181, off, %[F], 0 offset:1024 sc1\n" \
  "s_waitcnt vmcnt(0)\n" \
  "v_readfirstlane_b32 s77, v181\n" \
  "s_cmp_lg_u32 s77, 0\n" \
  "s_cbranch_scc1 .Lu_srw_ok\n" \
  "s_cmp_lt_u32 s63, 400000\n" \
  "s_cbranch_scc1 .Lu_srw\n" \
  "v_mov_b32 v181, 1\n" \
  "buffer_store_dword v181, off, %[F], 0 offset:1024 sc1\n" \
  ".Lu_srw_ok:\n" \
  "v_mfma_f32_16x16x32_bf16 v[144:147], v[128:131], v[32:35], 0\n" \
  "v_mfma_f32_16x16x32_bf16 v[152:155], v[136:139], v[32:35], 0\n" \
  "v_mfma_f32_16x16x32_bf16 v[144:147], v[132:135], v[36:39], v[144:147]\n" \
  "v_mfma_f32_16x16x32_bf16 v[152:155], v[140:143], v[36:39], v[152:155]\n" \
  "v_mfma_f32_16x16x32_bf16 v[144:147], v[96:99], v[80:83], v[144:147]\n" \
  "v_mfma_f32_16x16x32_bf16 v[152:155], v[112:115], v[80:83], v[152:155]\n" \
  "v_mfma_f32_16x16x32_bf16 v[144:147], v[100:103], v[84:87], v[144:147]\n" \
  "v_mfma_f32_16x16x32_bf16 v[152:155], v[116:119], v[84:87], v[152:155]\n" \
  "v_mfma_f32_16x16x32_bf16 v[144:147], v[104:107], v[88:91], v[144:147]\n" \
  "v_mfma_f32_16x16x32_bf16 v[152:155], v[120:123], v[88:91], v[152:155]\n" \
  "v_mfma_f32_16x16x32_bf16 v[144:147], v[108:111], v[92:95], v[144:147]\n" \
  "v_mfma_f32_16x16x32_bf16 v[152:155], v[124:127], v[92:95], v[152:155]\n" \
  "s_nop 7\n" \
  "s_branch .Lu_sr_back\n" \
  ".Lub_late:\n" \
  "s_cmp_lt_i32 s56, 0\n" \
  "s_cbranch_scc1 .Lub_al_ok\n" \
  "s_cmp_gt_u32 s91, -3000\n" \
  "s_cbranch_scc0 .Lub_late2\n" \
  ".Lub_wt:\n" \
  "s_memrealtime s[76:77]\n" \
  "s_waitcnt lgkmcnt(0)\n" \
  "s_sub_i32 s76, s76, s64\n" \
  "s_cmp_lt_i32 s76, 0\n" \
  "s_cbranch_scc1 .Lub_wt\n" \
  "s_branch .Lub_al_ok\n" \
  ".Lub_late2:\n" \
  "s_cmp_lg_u32 s64, 0\n" \
  "s_cbranch_scc1 .Lub_late3\n" \
  "s_mov_b32 s63, 0\n" \
  ".Lub_tw:\n" \
  "buffer_load_dword v180, off, %[X], s94 offset:4 sc1\n" \
  "s_waitcnt vmcnt(0)\n" \
  "v_readfirstlane_b32 s64, v180\n" \
  "s_cmp_lg_u32 s64, 0\n" \
  "s_cbranch_scc1 .Lub_tw_ok\n" \
  "s_sleep 1\n" \
  "s_add_i32 s63, s63, 1\n" \
  "s_cmp_lt_u32 s63, 200\n" \
  "s_cbranch_scc1 .Lub_tw\n" \
  ".Lub_tw_ok:\n" \
  "s_sub_i32 s91, s74, s64\n" \
  "s_cmp_gt_u32 s91, -3000\n" \
  "s_cbranch_scc1 .Lub_wt\n" \
  ".Lub_late3:\n" \
  "s_branch .Lub_al_ok\n" \
  ".Lu_end:\n" \
  "s_waitcnt vmcnt(0)\n"
#define U_CLOB "v32", "v33", "v34", "v35", "v36", "v37", "v38", "v39", "v40", "v41", "v42", "v43", "v44", "v45", "v46", "v47", "v48", "v49", "v50", "v51", "v52", "v53", "v54", "v55", "v56", "v57", "v58", "v59", "v60", "v61", "v62", "v63", "v64", "v65", "v66", "v67", "v68", "v69", "v70", "v71", "v72", "v73", "v74", "v75", "v76", "v77", "v78", "v79", "v80", "v81", "v82", "v83", "v84", "v85", "v86", "v87", "v88", "v89", "v90", "v91", "v92", "v93", "v94", "v95", "v96", "v97", "v98", "v99", "v100", "v101", "v102", "v103", "v104", "v105", "v106", "v107", "v108", "v109", "v110", "v111", "v112", "v113", "v114", "v115", "v116", "v117", "v118", "v119", "v120", "v121", "v122", "v123", "v124", "v125", "v126", "v127", "v128", "v129", "v130", "v131", "v132", "v133", "v134", "v135", "v136", "v137", "v138", "v139", "v140", "v141", "v142", "v143", "v144", "v145", "v146", "v147", "v148", "v149", "v150", "v151", "v152", "v153", "v154", "v155", "v156", "v157", "v158", "v159", "v160", "v161", "v162", "v163", "v164", "v165", "v166", "v167", "v168", "v169", "v170", "v171", "v172", "v173", "v174", "v175", "v176", "v177", "v178", "v179", "v180", "v181", "v182", "v183", "v184", "v185", "v186", "v187", "v188", "v189", "v190", "v191", "v192", "v193", "v194", "v195", "v196", "v197", "v198", "v199", "v200", "v201", "v202", "v203", "v204", "v205", "v206", "v207", "v208", "v209", "v210", "v211", "v212", "v213", "v214", "v215", "v216", "v217", "v218", "v219", "v220", "v221", "v222", "v223", "v224", "v225", "v226", "v227", "v228", "v229", "v230", "v231", "v232", "v233", "v234", "v235", "v236", "v237", "v238", "v239", "v240", "v241", "v242", "v243", "v244", "v245", "v246", "v247", "v248", "v249", "v250", "v251", "v252", "v253", "v254", "v255", "s56", "s57", "s58", "s59", "s60", "s61", "s62", "s63", "s64", "s65", "s66", "s67", "s68", "s69", "s70", "s71", "s72", "s73", "s74", "s75", "s76", "s77", "s78", "s79", "s80", "s81", "s82", "s83", "s84", "s85", "s86", "s87", "s88", "s89", "s90", "s91", "s92", "s93", "s94", "a0", "a1", "a2", "a3", "a4", "a5", "a6", "a7", "a8", "a9", "a10", "a11", "a12", "a13", "a14", "a15", "a16", "a17", "a18", "a19", "a20", "a21", "a22", "a23", "a24", "a25", "a26", "a27", "a28", "a29", "a30", "a31", "a32", "a33", "a34", "a35", "a36", "a37", "a38", "a39", "a40", "a41", "a42", "a43", "a44", "a45", "a46", "a47", "a48", "a49", "a50", "a51", "a52", "a53", "a54", "a55", "a56", "a57", "a58", "a59", "a60", "a61", "a62", "a63", "a64", "a65", "a66", "a67", "a68", "a69", "a70", "a71", "a72", "a73", "a74", "a75", "a76", "a77", "a78", "a79", "a80", "a81", "a82", "a83", "a84", "a85", "a86", "a87", "a88", "a89", "a90", "a91", "a92", "a93", "a94", "a95", "a96", "a97", "a98", "a99", "a100", "a101", "a102", "a103", "a104", "a105", "a106", "a107", "a108", "a109", "a110", "a111", "a112", "a113", "a114", "a115", "a116", "a117", "a118", "a119", "a120", "a121", "a122", "a123", "a124", "a125", "a126", "a127", "a128", "a129", "a130", "a131", "a132", "a133", "a134", "a135", "a136", "a137", "a138", "a139", "a140", "a141", "a142", "a143", "a144", "a145", "a146", "a147", "a148", "a149", "a150", "a151", "a152", "a153", "a154", "a155", "a156", "a157", "a158", "a159", "a160", "a161", "a162", "a163", "a164", "a165", "a166", "a167", "a168", "a169", "a170", "a171", "a172", "a173", "a174", "a175", "a176", "a177", "a178", "a179", "a180", "a181", "a182", "a183", "a184", "a185", "a186", "a187", "a188", "a189", "a190", "a191", "a192", "a193", "a194", "a195", "a196", "a197", "a198", "a199", "a200", "a201", "a202", "a203", "a204", "a205", "a206", "a207", "a208", "a209", "a210", "a211", "a212", "a213", "a214", "a215", "a216", "a217", "a218", "a219", "a220", "a221", "a222", "a223", "a224", "a225", "a226", "a227", "a228", "a229", "a230", "a231", "a232", "a233", "a234", "a235", "a236", "a237", "a238", "a239", "a240", "a241", "a242", "a243", "a244", "a245", "a246", "a247", "a248", "a249", "a250", "a251", "a252", "a253", "a254", "a255", "vcc", "scc", "m0", "memory"
#define TAU 236
#define SH1 40
#define MRG 2
#define XST 150
#define G1T 100
#define VTC -8
#define UC2 5
#define LAG 355
#define SCHED [tau] "s"((220)), [g1t] "s"((100)), [bas] "s"(US(gb + CTL_CH * CH + 64u)), [rdy] "v"(tid == 0 ? 0u : DROP)
__device__ __forceinline__ unsigned qrow(int r) { return (unsigned)(L_QI + (r >> 4) * 2816 + (r & 15) * 160 + ((r >> 2) & 3) * 8); }
__device__ __forceinline__ void qk_wg(const MP& p, int role, int grp, int h, int rep, unsigned gb) {
  const int tid = threadIdx.x, lane = tid & 63, w = __builtin_amdgcn_readfirstlane(tid >> 6);
  const int fr = lane & 15, g = lane >> 4;
  const int jj = (h >> 1) & 3, hh = h & 1;
  const unsigned pb = w < NPR ? (unsigned)(16 * (w >> 1) + 4 * (w & 1) + (h >> 3)) * CH + (unsigned)(((h >> 3) & 1) * 1024) : DROP;
  const unsigned ptb = (unsigned)(PT_CH - PM_CH) * CH + (unsigned)(h >> 3) * 2u * CH + 8192u - 2u * gb;
  const int qc = 8 * (w & 1) + (fr & 7), T = w >> 1;
  const unsigned po1 = fr < 8 ? pb + (unsigned)(jj * 1024 + (g * 16 + hh * 8 + fr) * 16) : T ? ptb + phys((unsigned)((4 + (h & 7)) * 1024 + (g * 16 + qc) * 16)) : DROP;
  const unsigned po2 = fr < 8 ? pb + (unsigned)(4096 + (jj * 16 + hh * 8 + fr) * 16 + g * 4) : ptb + (unsigned)((T * 2 + hh) * 1024 + (jj * 16 + qc) * 16 + g * 4);
  const unsigned po3 = fr < 8 || T ? DROP : ptb + phys((unsigned)((12 + (jj & 2) + hh) * 1024 + (g * 16 + qc) * 16 + (jj & 1) * 8));
  const u16* wb = p.WP + (size_t)h * (288 * 512);
  unsigned hA = (unsigned)(65536 + (fr >> 3) * RGB + g * 128 + (lane & 7) * 16);
  asm volatile(QK_ASM
    : [hA] "+v"(hA)
    : [X] "s"(mkd(p.win, WIN_BYTES)), [HS] "s"(mkd(p.H + (size_t)grp * (NSEG * SEGB / 2), NSEG * SEGB)), [F] "s"(mkd(p.ct, CT_TOTAL * 4u)),
      [mr0] "s"(US(gb + (unsigned)(MEM_CH + 2 * rep) * CH)), [pm0] "s"(US(gb + PM_CH * CH)), [hl0] "s"(US((unsigned)w * RGB)), [wp] "s"(wb), [grp] "s"(US(grp)),
      SCHED, [x0] "s"((200)), [mrg] "s"((2)), [rpo] "v"(tid == 0 ? gb + CTL_CH * CH + (unsigned)(2048 + 256 * rep) : DROP),
      [cpo] "v"(h == 0 ? gb + CTL_CH * CH + (unsigned)(2048 + 256 * (lane & 7)) : DROP), [cpw] "v"(h == 0 && tid == 0 ? 0u : DROP),
      [cpa] "v"(h == 0 && grp ? 5u * 16u : DROP), [lag] "s"((340)), [dn] "s"(-(4)), [up] "s"((8)), [sh1] "s"((100)), [gg] "s"(US(grp * 130)), [c2] "s"((42)), [c4] "s"((20)),
      [mo] "v"((unsigned)lane * 16u), [idx] "v"((lane & 2) ? 0xEEEEu : 0x4444u),
      [qW] "v"(qrow(32 * w + fr) + 4 * g), [kW] "v"(qrow(32 * w + fr) + 128 + 8 * g),
      [qR] "v"(qrow(4 * g + (fr >> 2)) + ((fr & 2) ? 16 + 32 * w : 0) + 8 * (fr & 1)), [kR] "v"(qrow(4 * g + (fr >> 2)) + 128 + 8 * (fr & 3)),
      [qtW] "v"(qrow(32 * w + fr) + 16 + 32 * (g >> 1) + 8 * (g & 1)), [ktW] "v"(L_KT + (unsigned)(w * 1024 + lane * 16)), [ktR] "v"(L_KT + (unsigned)lane * 16u),
      [hvo] "v"((unsigned)w * RGB + (unsigned)lane * 16u), [po1] "v"(po1), [po2] "v"(po2), [po3] "v"(po3), [pcl] "v"(fr == 0 ? po1 : lane == 1 ? po2 : DROP),
      [lim] "v"(fr >= 8 ? 16 * T + qc - 4 * g : 1000), [wo] "v"((unsigned)(w * (4 * 18 * 1024) + lane * 16))
    : QK_CLOB);
}

__device__ __forceinline__ void vt_wg(const MP& p, int role, int grp, int h, int rep, unsigned gb) {
  const int tid = threadIdx.x, lane = tid & 63, w = __builtin_amdgcn_readfirstlane(tid >> 6);
  const int fr = lane & 15, g = lane >> 4;
  unsigned hA = (unsigned)(65536 + (fr >> 3) * RGB + g * 128 + (lane & 7) * 16);
  asm volatile(VT_ASM
    : [hA] "+v"(hA)
    : [X] "s"(mkd(p.win, WIN_BYTES)), [HS] "s"(mkd(p.H + (size_t)grp * (NSEG * SEGB / 2), NSEG * SEGB)),
      [F] "s"(mkd(p.ct, CT_TOTAL * 4u)), [hl0] "s"(US((unsigned)w * RGB)), [wp] "s"(p.VP + (size_t)h * (288 * 512)), [grp] "s"(US(grp)),
      SCHED, [x0] "s"((200)), [vtc] "s"((-14)), [rpo] "v"(tid == 0 ? gb + CTL_CH * CH + (unsigned)(3072 + 256 * rep) : DROP),
      [la] "v"((unsigned)lane * 16u), [vW] "v"(L_VI + (unsigned)(w * 1024 + lane * 16)), [vR] "v"(L_VI + (unsigned)lane * 16u), [hvo] "v"((unsigned)w * RGB + (unsigned)lane * 16u),
      [px] "s"(US(4u * CH + (w == 3 ? 8192u - 2u * gb : 0u))), [sto] "v"(gb + (unsigned)(VT_CH + 36 * w + (h >> 3)) * CH + (unsigned)((h & 7) * 1024 + lane * 16)), [flo] "v"(tid == 0 ? gb + CTL_CH * CH + (unsigned)(4096 + (h >> 3) * 128 + (h & 7) * 4) : DROP),
      [wo] "v"((unsigned)(w * (72 * 1024) + lane * 16))
    : VT_CLOB);
}

__device__ __forceinline__ int o_head(int j) { return 16 * (j / 18) + ((j % 18) & 15); }
__device__ __forceinline__ void o_wg(const MP& p, int role, int grp, int j, int rep, unsigned gb) {
  const int tid = threadIdx.x, lane = tid & 63, w = __builtin_amdgcn_readfirstlane(tid >> 6);
  const int fr = lane & 15, g = lane >> 4;
  const int wsl = (fr * 4 + (g ^ ((fr >> 1) & 3))) * 16;
  const int mr = 2 * g + ((lane >> 2) & 1);
  const int h = o_head(j);
  const unsigned vb = j % 18 < 16 ? (gb ^ 8192u) + (unsigned)(VM_CH + 9 * w) * CH + (unsigned)((h >> 3) * 2048 + (h & 1) * 1024 + ((h >> 1) & 3) * 256 + fr * 16) : DROP;
  unsigned la = (unsigned)lane * 16u;
  asm volatile(O_ASM
    : [la] "+v"(la)
    : [X] "s"(mkd(p.win, WIN_BYTES)),
      [F] "s"(mkd(p.ct, CT_TOTAL * 4u)), [mw0] "s"(US(gb + (unsigned)(MEM_CH + 2 * w) * CH)), [mr0] "s"(US(gb + (unsigned)(MEM_CH + 2 * rep) * CH)), [ufo] "s"(US(gb + CTL_CH * CH + (unsigned)(5120 + (j >> 1) * 128))),
      [pm0] "s"(US(gb + (unsigned)(PM_CH + (NPR == 4 ? 16 * (rep >> 1) + 4 * (rep & 1) : NPR == 2 ? 4 * (rep >> 1) : 0) + w) * CH + (unsigned)(w & 1) * 1024u)), [vm0] "s"(US((gb ^ 8192u) + (unsigned)(VM_CH + j) * CH + (unsigned)w * 2048u)), [vt0] "s"(US((gb ^ (j < 32 ? 0u : 8192u)) + (unsigned)(VT_CH + 4 * j + w) * CH)),
      [ml0] "s"(US(LO_MEM + (unsigned)w * 1024u)), [wp] "s"(p.VP + (size_t)h * (288 * 512)), [grp] "s"(US(grp)),
      [g1t] "s"((100)), [bas] "s"(US(gb + CTL_CH * CH + 64u)), [w4] "s"(US(j % 9 == 8 ? -1 : 0)),
      [idx] "v"((lane & 2) ? 0xEEEEu : 0x4444u), [mA] "v"(LO_MEM + (unsigned)lane * 16u), [mA8] "v"(LO_MEM + (unsigned)(8192 + g * 1024 + fr * 16)),
      [c0] "v"((unsigned)(w * 1024 + lane * 16)), [c2] "v"(lane < 16 ? (unsigned)(w * 256 + lane * 16) : DROP),
      [aW] "v"(LO_A + (unsigned)((w * 16 + (fr >> 2) * 4 + (fr & 1)) * 32 + g * 8 + (fr & 2) * 2)), [aR] "v"(LO_A + (unsigned)((g * 8 + (fr >> 2)) * 32 + (fr & 3) * 8)),
      [vmo] "v"(vb + (unsigned)g * CH), [vmo1] "v"(vb + 4u * CH + (unsigned)g * 4u), [flo] "v"(gb + CTL_CH * CH + (unsigned)(4096 + w * 128 + (lane & 7) * 4)),
      [rmW] "v"(LO_RM + (unsigned)(w * 1024 + wsl)), [rmA] "v"(LO_RM + (unsigned)((mr * 4 + ((lane & 3) ^ ((mr >> 1) & 3))) * 16 + (lane & 8))),
      [lm] "v"(phys((unsigned)(j * 256 + lane * 4))), [wo] "v"((unsigned)(w * (72 * 1024) + lane * 16))
    : O_CLOB);
}

__device__ __forceinline__ void u_wg(const MP& p, int role, int grp, int u, unsigned gb) {
  const int tid = threadIdx.x, lane = tid & 63, w = __builtin_amdgcn_readfirstlane(tid >> 6);
  const int fr = lane & 15, g = lane >> 4, tr = (lane >> 2) & 7;
  unsigned la = (unsigned)lane * 16u;
  asm volatile(U_ASM
    : [la] "+v"(la)
    : [X] "s"(mkd(p.win, WIN_BYTES)), [OS] "s"(mkd(p.OT + (size_t)grp * (NSEG * SEGB / 2), NSEG * SEGB)), [F] "s"(mkd(p.ct, CT_TOTAL * 4u)),
      [pt0] "s"(US((gb ^ 8192u) + (unsigned)(PT_CH + 2 * w) * CH)), [vm0] "s"(US((gb ^ 8192u) + (unsigned)(VM_CH + 2 * u) * CH + (unsigned)w * 2048u)),
      [vt0] "s"(US((gb ^ (u < 16 ? 0u : 8192u)) + (unsigned)(VT_CH + 8 * u + w) * CH)),
      [ow] "s"(US((unsigned)w * RGB)), [grp] "s"(US(grp)), [g1t] "s"((100)), [bas] "s"(US(gb + CTL_CH * CH + 64u)), [w40] "s"(US(2 * u % 9 == 8 ? -1 : 0)), [w41] "s"(US((2 * u + 1) % 9 == 8 ? -1 : 0)),
      [uc2] "s"((5)),
      [flo] "v"(gb + CTL_CH * CH + (unsigned)(4096 + w * 128 + (lane & 7) * 4)), [ufo] "v"(tid == 0 ? gb + CTL_CH * CH + (unsigned)(5120 + u * 128) : DROP),
      [rtW] "v"((unsigned)(w * 2048 + (fr * 4 + (g ^ ((fr >> 1) & 3))) * 16)),
      [rtA] "v"((unsigned)((w >> 1) * 1024 + ((8 * (w & 1) + tr) * 4 + ((2 * (lane >> 5) + ((lane >> 1) & 1)) ^ ((tr >> 1) & 3))) * 16 + (lane & 1) * 8)),
      [lo] "v"((unsigned)(u * 512 + lane * 4))
    : U_CLOB);
}

#define YSLP 2
struct Row9 { unsigned u[9]; };
__device__ __forceinline__ float rnd(float a) { return __uint_as_float(pk2(0.f, a)); }
__device__ __forceinline__ void ln18(const f32x2 (&x)[9], f32x2 (&o)[9], float* st, int lane, int w) {
  f32x2 s2 = x[0];
#pragma unroll
  for (int i = 1; i < 9; ++i) s2 += x[i];
  const float mw = i_red8add(s2[0] + s2[1]) * (1.0f / 144.0f);
  f32x2 q2 = {0.f, 0.f};
#pragma unroll
  for (int i = 0; i < 9; ++i) { const f32x2 d = x[i] - mw; q2 += d * d; }
  const float qw = i_red8add(q2[0] + q2[1]);
  if (lane < 8) *(f32x2*)(st + (lane * 4 + w) * 2) = (f32x2){mw, qw};
  BAR();
  const f32x4 a = *(const f32x4*)(st + (lane & 7) * 8), b = *(const f32x4*)(st + (lane & 7) * 8 + 4);
  const float mean = ((a[0] + a[2]) + (b[0] + b[2])) * 0.25f;
  const float d0 = a[0] - mean, d1 = a[2] - mean, d2 = b[0] - mean, d3 = b[2] - mean;
  const float m2 = ((a[1] + a[3]) + (b[1] + b[3])) + 144.0f * ((d0 * d0 + d1 * d1) + (d2 * d2 + d3 * d3));
  const float var = rnd(m2 * (1.0f / DM));
  const float m = rnd(mean), rden = __builtin_amdgcn_rcpf(rnd(__builtin_amdgcn_sqrtf(rnd(var + 1e-5f))));
#pragma unroll
  for (int i = 0; i < 9; ++i) { const f32x2 d = x[i] - m; o[i] = (f32x2){rnd(d[0]), rnd(d[1])} * rden; }
}
__device__ __forceinline__ void mix_rows(Row9& xa, const Row9& ya, const rsrc_t H, unsigned so, unsigned o0, unsigned oe, float* st, int lane, int w) {
  f32x2 x[9], y[9], ln[9];
#pragma unroll
  for (int i = 0; i < 9; ++i) { x[i] = (f32x2){lo2f(xa.u[i]), hi2f(xa.u[i])}; y[i] = (f32x2){lo2f(ya.u[i]), hi2f(ya.u[i])}; }
  ln18(x, ln, st, lane, w);
#pragma unroll
  for (int i = 0; i < 9; ++i) {
    const f32x2 m = (f32x2){rnd(ln[i][0]), rnd(ln[i][1])} * y[i];
    const f32x2 u = x[i] + (f32x2){rnd(m[0]), rnd(m[1])};
    x[i] = (f32x2){rnd(u[0]), rnd(u[1])};
  }
  ln18(x, ln, st + 64, lane, w);
  unsigned h[9];
#pragma unroll
  for (int i = 0; i < 9; ++i) h[i] = nosent(pk2(ln[i][0], ln[i][1]));
  st16(H, o0, so, (v4u){h[0], h[1], h[2], h[3]});
  st16(H, o0 + 4096u, so, (v4u){h[4], h[5], h[6], h[7]});
  st4(H, oe, so, h[8]);
#pragma unroll
  for (int i = 0; i < 9; ++i) xa.u[i] = pk2(x[i][0], x[i][1]);
}
template <int SLP>
__device__ __forceinline__ void wait9(const rsrc_t O, const rsrc_t F, unsigned so, unsigned o0, unsigned o1, unsigned oe, v4u (&v)[2], unsigned& e) {
  unsigned spins = 0;
#pragma nounroll
  while (true) {
    asm volatile("" ::: "memory");
    v[0] = ld16(O, o0, so); v[1] = ld16(O, o1, so); e = ld4(O, oe, so);
    if (__all(mx4(mx4(e, v[0]), v[1]) != SENT)) break;
    if (spin_fail(spins, F)) break;
    __builtin_amdgcn_s_sleep(SLP);
  }
}
__device__ __forceinline__ void add_rows(Row9& xa, const v4u (&v)[2], unsigned e) {
#pragma unroll
  for (int i = 0; i < 8; ++i) xa.u[i] = pk2(lo2f(xa.u[i]) + lo2f(v[i >> 2][i & 3]), hi2f(xa.u[i]) + hi2f(v[i >> 2][i & 3]));
  xa.u[8] = pk2(lo2f(xa.u[8]) + lo2f(e), hi2f(xa.u[8]) + hi2f(e));
}
__device__ __forceinline__ void arm_slots(const MP& p, int s, int rg) {
  const rsrc_t O = mkr(p.OT, 2u * NSEG * SEGB), H = mkr(p.H, 2u * NSEG * SEGB);
  const unsigned so = __builtin_amdgcn_readfirstlane((unsigned)s * SEGB + (unsigned)rg * RGB), o0 = threadIdx.x * 16u;
  const v4u sent = {SENT, SENT, SENT, SENT};
#pragma unroll
  for (int i = 0; i < 3; ++i) {
    const unsigned o = i < 2 || threadIdx.x < 64 ? o0 + 4096u * i : DROP;
    st16(H, o, s + 5 < NSEG ? so + 5u * SEGB : DROP, sent);
    st16(O, o, so, sent); st16(O, o, NSEG * SEGB + so, sent); st16(H, o, s + 2 < NSEG ? (NSEG + 2) * SEGB + so : DROP, sent);
  }
}
#define LR_Y 1024
#define LR_SV 10240
#define RSLP 4
__device__ __forceinline__ void row_wg(const MP& p, char* smem, int role, int rw, unsigned flip) {
  const int tid = threadIdx.x, lane = tid & 63, w = __builtin_amdgcn_readfirstlane(tid >> 6);
  const rsrc_t F = mkr(p.ct, CT_TOTAL * 4u), O = mkr(p.OT, 2u * NSEG * SEGB), H = mkr(p.H, 2u * NSEG * SEGB), Y = mkr(p.Y, DM * YS), X = mkr(p.win, WIN_BYTES);
  float* st = (float*)smem;
  const int r8 = tid & 7, pj = (tid >> 3) & 7;
  const unsigned o0 = (unsigned)tid * 16u;
  const unsigned oe = (unsigned)(8192 + pj * 128 + r8 * 16 + w * 4);
  const int rg = rw & 3, s0 = rw >> 2;
  const char* yR = smem + LR_Y + (8 * ((tid >> 4) * 2 + ((lane >> 1) & 1)) + ((lane & 15) >> 2)) * 16 + (lane & 1) * 8;
  const char* yE = smem + LR_Y + (512 + 8 * pj + 2 * w) * 16 + r8 * 2;
  Row9 xp;
#pragma unroll
  for (int i = 0; i < 9; ++i) xp.u[i] = 0u;
#pragma nounroll
  for (int job = rw < 12 ? -1 : 0; job < 30; ++job) {
    const bool isA = job <= 0 || (job & 1);
    const int k = job <= 0 ? 0 : (isA ? (job + 1) >> 1 : (job >> 1) - 1);
    const int s = s0 + 5 * k;
    if (s >= (isA ? NSEG : NSEG + 5)) continue;
    const unsigned to = CTL_CH * CH + 64u + 16u * (unsigned)s + 4u;
    const int tc = (132);
    const unsigned so = __builtin_amdgcn_readfirstlane((unsigned)s * SEGB + (unsigned)rg * RGB);
    char* sv = smem + LR_SV + (k % 3) * 2 * RGB;
    const size_t go = (size_t)(s * SEG + rg * 8 + r8) * DM;
    Row9 xa, ya;
    v4u v[2];
    unsigned e;
    if (isA) {
      const v4u x0 = *(const v4u*)(p.x0 + go + (tid >> 3) * 8), x1 = *(const v4u*)(p.x0 + go + 256 + (tid >> 3) * 8);
      xa.u[8] = *(const unsigned*)(p.x0 + go + 512 + 8 * pj + 2 * w);
#pragma unroll
      for (int i = 0; i < 4; ++i) { xa.u[i] = x0[i]; xa.u[4 + i] = x1[i]; }
      if (job > 0) arm_slots(p, s, rg);
      if (job == 0) {
        unsigned spins = 0;
#pragma nounroll
        while (!__all(ld4(F, (unsigned)(CT_YC + (lane & 7) * 32) * 4u, 0) >= 72u)) { if (spin_fail(spins, F)) break; __builtin_amdgcn_s_sleep(4); asm volatile("" ::: "memory"); }
      }
      if (job >= 0) wait9<YSLP>(Y, F, (unsigned)(s * SEG + rg * 8) * 2u, (unsigned)tid * YS, (unsigned)(tid + 256) * YS, (unsigned)(512 + (tid >> 2)) * YS + (unsigned)(tid & 3) * 4u, v, e);
      else { v[0] = x0; v[1] = x1; e = xa.u[8]; }
      VM0();
      *(v4u*)(smem + LR_Y + o0) = v[0]; *(v4u*)(smem + LR_Y + o0 + 4096) = v[1]; *(unsigned*)(smem + LR_Y + 8192 + tid * 4) = e;
      BAR();
#pragma unroll
      for (int i = 0; i < 2; ++i) {
        const v2u x = trd(yR + 4096 * i), y = trd(yR + 4096 * i + 64);
        ya.u[4 * i] = x.x; ya.u[4 * i + 1] = x.y; ya.u[4 * i + 2] = y.x; ya.u[4 * i + 3] = y.y;
      }
      ya.u[8] = (unsigned)*(const u16*)yE | ((unsigned)*(const u16*)(yE + 16) << 16);
    } else if (s < NSEG) {
#pragma unroll
      for (int i = 0; i < 2; ++i) {
        const v4u a = *(const v4u*)(sv + o0 + 4096 * i), b = *(const v4u*)(sv + RGB + o0 + 4096 * i);
#pragma unroll
        for (int q = 0; q < 4; ++q) { xa.u[4 * i + q] = a[q]; ya.u[4 * i + q] = b[q]; }
      }
      xa.u[8] = *(const unsigned*)(sv + 8192 + tid * 4); ya.u[8] = *(const unsigned*)(sv + RGB + 8192 + tid * 4);
      wait_time(X, flip * 8192u + to, tc);
      wait9<RSLP>(O, F, so, o0, o0 + 4096u, oe, v, e);
      add_rows(xa, v, e);
    }
    if (s < NSEG) mix_rows(xa, ya, H, job < 0 ? 0x40000000u : (isA ? so : NSEG * SEGB + so), o0, oe, st, lane, w);
    if (isA) {
#pragma unroll
      for (int i = 0; i < 2; ++i) {
        *(v4u*)(sv + o0 + 4096 * i) = (v4u){xa.u[4 * i], xa.u[4 * i + 1], xa.u[4 * i + 2], xa.u[4 * i + 3]};
        *(v4u*)(sv + RGB + o0 + 4096 * i) = (v4u){ya.u[4 * i], ya.u[4 * i + 1], ya.u[4 * i + 2], ya.u[4 * i + 3]};
      }
      *(unsigned*)(sv + 8192 + tid * 4) = xa.u[8]; *(unsigned*)(sv + RGB + 8192 + tid * 4) = ya.u[8];
    } else {
      if (k > 0) {
        wait_time(X, (flip ^ 1u) * 8192u + to - 80u, tc);
        wait9<RSLP>(O, F, (NSEG - 5) * SEGB + so, o0, o0 + 4096u, oe, v, e);
        add_rows(xp, v, e);
        u16* po = p.out + go - 5 * SEG * DM;
        *(v4u*)(po + (tid >> 3) * 8) = (v4u){xp.u[0], xp.u[1], xp.u[2], xp.u[3]};
        *(v4u*)(po + 256 + (tid >> 3) * 8) = (v4u){xp.u[4], xp.u[5], xp.u[6], xp.u[7]};
        *(unsigned*)(po + 512 + 8 * pj + 2 * w) = xp.u[8];
      }
      xp = xa;
    }
  }
}

#define CREV_STRIDE 2208
#define LC_KVS 35328
#define LC_RED 43008
__device__ __forceinline__ unsigned rot16(unsigned u) { return __builtin_amdgcn_alignbit(u, u, 16); }
#define LC_IN 61440
__device__ __forceinline__ void crev_put(u16* dst, int stride, const v4u& c0, const v4u& c1) {
  const unsigned e[8] = {rot16(c0.w), rot16(c0.z), rot16(c0.y), rot16(c0.x), rot16(c1.w), rot16(c1.z), rot16(c1.y), rot16(c1.x)};
  unsigned u[7];
#pragma unroll
  for (int k = 0; k < 7; ++k) u[k] = __builtin_amdgcn_alignbit(e[k + 1], e[k], 16);
#pragma unroll
  for (int a = 0; a < 4; ++a) {
    *(v4u*)(dst + (2 * a) * stride) = (v4u){e[a], e[a + 1], e[a + 2], e[a + 3]};
    *(v4u*)(dst + (2 * a + 1) * stride) = (v4u){u[a], u[a + 1], u[a + 2], u[a + 3]};
  }
}
__device__ __forceinline__ unsigned y2pair(float y0, float y1, unsigned kv, float Dv) {
  const unsigned yy = pk2(y0, y1), kd = pk2(lo2f(kv) * Dv, hi2f(kv) * Dv);
  return nosent(pk2(lo2f(yy) + lo2f(kd), hi2f(yy) + hi2f(kd)));
}
__device__ __forceinline__ void conv_hi(char* smem, const char* in, float Dv, const rsrc_t Y, int d, int tid) {
  const int lane = tid & 63, w = tid >> 6;
  const v4u c0 = *(const v4u*)(in + tid * 16), kq = *(const v4u*)(in + 4096 + tid * 16);
  v4u c1 = *(const v4u*)(in + (tid ? tid * 16 - 16 : 0));
  if (tid == 0) c1 = (v4u){0u, 0u, 0u, 0u};
  u16* crev = (u16*)smem;
  u16* kvs = (u16*)(smem + LC_KVS);
  float* red = (float*)(smem + LC_RED);
  crev_put(crev + (263 - tid) * 8, CREV_STRIDE, c0, c1);
  *(v4u*)&kvs[(32 + (tid >> 2)) * 40 + (tid & 3) * 8] = kq;
  BAR();
  const int i = lane & 31, h = lane >> 5, X0 = 2048 + 63;
  const int rho = (X0 - i) & 7;
  const u16* aB = crev + rho * CREV_STRIDE + (X0 - (32 * w + i - 8 * h) - rho);
  const u16* bB = kvs + (64 + i - w) * 40 + 8 * h;
  f32x16 acc;
#pragma unroll
  for (int r = 0; r < 16; ++r) acc[r] = 0.f;
  v4u A[8], B[8];
#pragma unroll
  for (int q = 0; q < 8; ++q) { A[q] = *(const v4u*)(aB - 128 * (q >> 1) + 16 * (q & 1)); B[q] = *(const v4u*)(bB - 160 * (q >> 1) + 16 * (q & 1)); }
  asm volatile("s_nop 1" : "+v"(acc));
#pragma nounroll
  for (int m4 = 1; m4 <= 4; ++m4) {
    const u16* an = aB - 512 * (m4 & 3);
    const u16* bn = bB - 640 * (m4 & 3);
    PIN();
#pragma unroll
    for (int q = 0; q < 8; ++q) {
      asm volatile("v_mfma_f32_32x32x16_bf16 %0, %1, %2, %0" : "+v"(acc) : "v"(A[q]), "v"(B[q]));
      A[q] = *(const v4u*)(an - 128 * (q >> 1) + 16 * (q & 1)); B[q] = *(const v4u*)(bn - 160 * (q >> 1) + 16 * (q & 1));
      PIN();
    }
  }
  asm volatile("s_nop 7\n\ts_nop 7" : "+v"(acc));
#pragma unroll
  for (int r = 0; r < 4; ++r) *(f32x4*)&red[(w * 32 + i) * 36 + 8 * r + 4 * h] = (f32x4){acc[4 * r], acc[4 * r + 1], acc[4 * r + 2], acc[4 * r + 3]};
  BAR();
  if (tid >= 128) {
    const int l = tid & 127;
    const float* rr = red + (l >> 2) * 36 + (l & 3) * 8;
    f32x4 y[2];
#pragma unroll
    for (int e = 0; e < 2; ++e)
      y[e] = (*(const f32x4*)(rr + 4 * e) + *(const f32x4*)(rr + 32 * 36 + 4 * e)) + (*(const f32x4*)(rr + 64 * 36 + 4 * e) + *(const f32x4*)(rr + 96 * 36 + 4 * e));
    st16(Y, (unsigned)tid * 16u, (unsigned)d * YS,
         (v4u){y2pair(y[0][0], y[0][1], kq.x, Dv), y2pair(y[0][2], y[0][3], kq.y, Dv), y2pair(y[1][0], y[1][1], kq.z, Dv), y2pair(y[1][2], y[1][3], kq.w, Dv)});
  }
}
#define CL_STRIDE 1056
#define LC_WAVE 22016
__device__ __forceinline__ void conv_lo(char* sm, const v4u (&cw)[3], const v4u (&kw)[2], float Dv, const rsrc_t Y, int d, int lane) {
  u16* crev = (u16*)sm;
  u16* kvs = (u16*)(sm + 16 * CL_STRIDE);
  const v4u z = {0u, 0u, 0u, 0u};
  crev_put(crev + (127 - 2 * lane) * 8, CL_STRIDE, cw[1], lane ? cw[0] : z);
  crev_put(crev + (126 - 2 * lane) * 8, CL_STRIDE, cw[2], cw[1]);
  if (lane < 32) *(v4u*)(crev + (lane >> 2) * CL_STRIDE + (128 + (lane & 3)) * 8) = z;
  *(v4u*)&kvs[(lane >> 1) * 40 + (lane & 1) * 16] = z; *(v4u*)&kvs[(lane >> 1) * 40 + (lane & 1) * 16 + 8] = z;
  *(v4u*)&kvs[(32 + (lane >> 1)) * 40 + (lane & 1) * 16] = kw[0]; *(v4u*)&kvs[(32 + (lane >> 1)) * 40 + (lane & 1) * 16 + 8] = kw[1];
  asm volatile("" ::: "memory");
  const int i = lane & 31, h = lane >> 5;
  const int rho = (1023 - i) & 7;
  const u16* aB = crev + rho * CL_STRIDE + (1023 - (i - 8 * h) - rho);
  const u16* bB = kvs + (32 + i) * 40 + 8 * h;
  f32x16 acc;
#pragma unroll
  for (int r = 0; r < 16; ++r) acc[r] = 0.f;
  v4u A[8], B[8];
#pragma unroll
  for (int q = 0; q < 8; ++q) { A[q] = *(const v4u*)(aB - 32 * (q >> 1) + 16 * (q & 1)); B[q] = *(const v4u*)(bB - 40 * (q >> 1) + 16 * (q & 1)); }
  asm volatile("s_nop 1" : "+v"(acc));
#pragma nounroll
  for (int m4 = 1; m4 <= 8; ++m4) {
    const u16* an = aB - 128 * (m4 & 7);
    const u16* bn = bB - 160 * (m4 & 7);
    PIN();
#pragma unroll
    for (int q = 0; q < 8; ++q) {
      asm volatile("v_mfma_f32_32x32x16_bf16 %0, %1, %2, %0" : "+v"(acc) : "v"(A[q]), "v"(B[q]));
      A[q] = *(const v4u*)(an - 32 * (q >> 1) + 16 * (q & 1)); B[q] = *(const v4u*)(bn - 40 * (q >> 1) + 16 * (q & 1));
      PIN();
    }
  }
  v2u kv4[4];
#pragma unroll
  for (int r = 0; r < 4; ++r) kv4[r] = *(const v2u*)&kvs[(32 + i) * 40 + 8 * r + 4 * h];
  asm volatile("s_nop 7\n\ts_nop 7" : "+v"(acc));
  v2u o[4];
#pragma unroll
  for (int r = 0; r < 4; ++r) o[r] = (v2u){y2pair(acc[4 * r], acc[4 * r + 1], kv4[r].x, Dv), y2pair(acc[4 * r + 2], acc[4 * r + 3], kv4[r].y, Dv)};
#pragma unroll
  for (int r = 0; r < 4; ++r) *(v2u*)&kvs[i * 40 + 8 * r + 4 * h] = o[r];
  asm volatile("" ::: "memory");
#pragma unroll
  for (int k = 0; k < 2; ++k) st16(Y, (unsigned)(k * 64 + lane) * 16u, (unsigned)d * YS, *(const v4u*)&kvs[(16 * k + (lane >> 2)) * 40 + 8 * (lane & 3)]);
}

#define NQE 8
#define NVE 8
#define NOE 10
__global__ __launch_bounds__(256, 1) void k_main(MP p) {
  extern __shared__ __attribute__((aligned(16))) char smem[];
  const int tid = threadIdx.x;
  unsigned cpf = 0;
  {
    unsigned long long pc; unsigned len;
    asm volatile("s_getpc_b64 %0\n.Lcpf:\n\ts_mov_b32 %1, _Z5k_pre4PreP-.Lcpf" : "=s"(pc), "=s"(len));
    cpf = __builtin_amdgcn_raw_buffer_load_b32(mkr((const void*)pc, (int)len > 0 ? len & ~3u : 0u), (unsigned)(tid + 256 * ((blockIdx.x >> 3) & 3)) * 128u, 0, 0);
  }
  unsigned xcc; asm volatile("s_getreg_b32 %0, hwreg(HW_REG_XCC_ID)" : "=s"(xcc));
  xcc &= 7u;
  int* rl = (int*)(smem + L_ROLE);
  if (tid == 0) {
    const unsigned fl = p.ct[CT_FLIP];
    int r = 0;
#pragma nounroll
    for (unsigned k = 0; k < 8; ++k) {
      const unsigned x = xcc ^ k;
      const unsigned tk = __hip_atomic_fetch_add(&p.ct[CT_TICK + x * 32], 1u, __ATOMIC_RELAXED, __HIP_MEMORY_SCOPE_AGENT);
      if (tk < 32u) { r = (int)(x * 32u + tk); break; }
    }
    rl[0] = r; rl[1] = (int)(fl & 1u);
  }
  asm volatile("" :: "v"(cpf));
  BAR();
  int role = __builtin_amdgcn_readfirstlane(rl[0]);
  {
    const int o = (role >> 5) & 1, a = (role >> 6) & 1, tk = role & 31;
    const int c1 = o ? 16 - NQE : NQE, c2 = c1 + (o ? 16 - NVE : NVE), c3 = c2 + (o ? 18 - NOE : NOE), c4 = c3 + (o ? 9 - NOE / 2 : NOE / 2);
    const int r = tk < c1 ? 16 * a + o * NQE + tk : tk < c2 ? 32 + 16 * a + o * NVE + tk - c1 : tk < c3 ? 64 + 18 * a + o * NOE + tk - c2
                : tk < c4 ? 100 + 9 * a + o * (NOE / 2) + tk - c3 : 118 + 5 * a + o * (32 - NQE - NVE - NOE - NOE / 2) + tk - c4;
    role = (role & 128) + r;
  }
  const unsigned flip = (unsigned)__builtin_amdgcn_readfirstlane(rl[1]);
  const int i = role & 127, grp = role >> 7;
  const unsigned gb = (((unsigned)grp) ^ flip) * 8192u;
  const int rep = (int)(xcc & 3u);
  const int rw = (grp ^ 1) * 10 + (i - 118);
  if (grp || i >= 100) {
    const int lane = tid & 63, w = __builtin_amdgcn_readfirstlane(tid >> 6);
    const int e2 = grp ? i : i + 18, d1 = (i < 118 ? e2 : (rw < 12 ? 200 : 124 + rw)) * 4 + w;
    const int n2 = grp && i < 118 ? (e2 < 104 ? 5 : 4) : 0;
    const rsrc_t Y = mkr(p.Y, DM * YS);
    const int dl = lane == 0 ? d1 : e2 + 118 * (lane - 1);
    const unsigned dv = (unsigned)p.ssm_D[(unsigned)dl < DM ? dl : 0] << 16;
    v4u cw[3], kw[2], i2[5][2];
    {
      const size_t o = (size_t)(d1 < DM ? d1 : 0) * S_LEN + lane * 16;
      cw[0] = *(const v4u*)&p.ssm_k[lane ? o - 8 : o]; cw[1] = *(const v4u*)&p.ssm_k[o]; cw[2] = *(const v4u*)&p.ssm_k[o + 8];
      kw[0] = *(const v4u*)&p.kvT[o]; kw[1] = *(const v4u*)&p.kvT[o + 8];
    }
    VM0();
    if (i >= 118) arm_slots(p, rw >> 2, rw & 3);
    if (d1 < DM) conv_lo(smem + w * LC_WAVE, cw, kw, __uint_as_float(__builtin_amdgcn_readlane(dv, 0)), Y, d1, lane);
    if (d1 < DM && lane == 0) __hip_atomic_fetch_add(&p.ct[CT_YC + (d1 & 7) * 32], 1u, __ATOMIC_RELAXED, __HIP_MEMORY_SCOPE_AGENT);
    if (n2) {
#pragma unroll
      for (int c = 0; c < 5; ++c) {
        const int d = e2 + 118 * c < DM ? e2 + 118 * c : e2;
        i2[c][0] = *(const v4u*)&p.ssm_k[(size_t)d * S_LEN + tid * 8]; i2[c][1] = *(const v4u*)&p.kvT[(size_t)d * S_LEN + tid * 8];
      }
      BAR();
#pragma unroll
      for (int k = 0; k < 5; ++k) { *(v4u*)(smem + LC_IN + k * 8192 + tid * 16) = i2[k][0]; *(v4u*)(smem + LC_IN + 4096 + k * 8192 + tid * 16) = i2[k][1]; }
      if (tid < 64) *(v4u*)(smem + ((tid >> 3) * CREV_STRIDE + (264 + (tid & 7)) * 8) * 2) = (v4u){0u, 0u, 0u, 0u};
      if (tid < 128) *(v4u*)(smem + LC_KVS + ((tid >> 2) * 40 + (tid & 3) * 8) * 2) = (v4u){0u, 0u, 0u, 0u};
      BAR();
#pragma nounroll
      for (int c = 0; c < n2; ++c) {
        const u16* wb = (i < 32 ? p.WP : p.VP) + (size_t)(i < 64 ? i & 31 : o_head(i - 64) & 31) * (288 * 512);
        if (c == 1) load_w<0, 8>(wb); else if (c == 2) load_w<8, 16>(wb); else if (c == 3) load_w<16, 24>(wb); else if (c == 4) load_w<24, 32>(wb);
        conv_hi(smem, smem + LC_IN + c * 8192, __uint_as_float(__builtin_amdgcn_readlane(dv, c + 1)), Y, e2 + 118 * c, tid);
        AOWN();
      }
    }
    BAR();
  }
  if (i < 32) qk_wg(p, role, grp, i, rep, gb);
  else if (i < 64) vt_wg(p, role, grp, i - 32, rep, gb);
  else if (i < 100) o_wg(p, role, grp, i - 64, rep, gb);
  else if (i < 118) u_wg(p, role, grp, i - 100, gb);
  else row_wg(p, smem, role, rw, flip);
}

#define PRE_KV 144
#define PRE_PW 192
#define PRE_PO 128
#define PRE_FILL 96
#define WAUX 16
#define FAUX 16
#define STW(base, bytes, idx16, v) __builtin_amdgcn_raw_buffer_store_b128(v, mkr(base, bytes), (unsigned)(idx16) * 16u, 0, FAUX)
struct PreP {
  const u16* kk; const u16* vv; const u16* shift_k; u16* kvT;
  const u16* Wq; const u16* Wk; const u16* Wv; const u16* Wo; u16* WP; u16* VP;
  const u16* mem0; v4u* win; v4u* Y; v4u* H; unsigned* ct;
};
__global__ __launch_bounds__(256) void k_pre(PreP p) {
  __shared__ __attribute__((aligned(16))) u16 sh[128 * 152];
  const int tid = threadIdx.x;
  int bid = blockIdx.x;
  if (bid < 32) {
    unsigned long long pc; int len, back;
    asm volatile("s_getpc_b64 %0\n.Lcpf0:\n\ts_mov_b32 %1, code_end-.Lcpf0\n\ts_mov_b32 %2, .Lcpf0-_Z6k_main2MP" : "=s"(pc), "=s"(len), "=s"(back));
    const bool ok = len > 0 && back > 0;
    const unsigned v = __builtin_amdgcn_raw_buffer_load_b32(mkr((const void*)pc, ok ? (unsigned)len & ~3u : 0u), (unsigned)tid * 128u, 0, 0);
    const unsigned u = __builtin_amdgcn_raw_buffer_load_b32(mkr((const void*)(pc - (unsigned)back), ok ? (unsigned)back & ~3u : 0u), (unsigned)(tid + 256 * (bid >> 3)) * 128u, 0, 0);
    asm volatile("" :: "v"(v), "v"(u));
  }
  if (bid == 0) {
    for (int i = tid; i < CT_TOTAL; i += 256) if (i < CT_FLIP || i >= CT_YC) p.ct[i] = 0u;
    const rsrc_t X = mkr(p.win, WIN_BYTES);
    unsigned xcc; asm volatile("s_getreg_b32 %0, hwreg(HW_REG_XCC_ID)" : "=s"(xcc));
    int* best = (int*)sh;
    int b0 = 1 << 30, b1 = 1 << 30;
    unsigned acc = 0;
    const unsigned ob = CAL_CH * CH + (unsigned)(tid >> 6) * 2048u;
    st16(X, ob + (unsigned)(((tid & 63) >> 3) * 256 + (tid & 7) * 16), 0, (v4u){0u, 0u, 0u, 0u});
    st16(X, ob + (unsigned)(((tid & 63) >> 3) * 256 + (tid & 7) * 16), 8192u, (v4u){0u, 0u, 0u, 0u});
    asm volatile("s_waitcnt vmcnt(0)" ::: "memory");
#pragma nounroll
    for (int i = 0; i < 16; ++i) {
      const unsigned o = ob + (unsigned)(i & 1) * 8192u + (unsigned)(i >> 1) * 256u;
      long long t0 = __builtin_amdgcn_s_memtime();
      unsigned v = ld4(X, o, 0);
      asm volatile("s_waitcnt vmcnt(0)" : "+v"(v) :: "memory");
      long long t1 = __builtin_amdgcn_s_memtime();
      acc += v;
      const int d = (int)(t1 - t0);
      if (i & 1) b1 = d < b1 ? d : b1; else b0 = d < b0 ? d : b0;
    }
    if ((tid & 63) == 0) { best[(tid >> 6) * 2] = b0; best[(tid >> 6) * 2 + 1] = b1; }
    __syncthreads();
    if (tid == 0) {
      for (int i = 1; i < 4; ++i) { b0 = best[2 * i] < b0 ? best[2 * i] : b0; b1 = best[2 * i + 1] < b1 ? best[2 * i + 1] : b1; }
      p.ct[CT_FLIP] = ((b1 < b0 ? 1u : 0u) ^ ((xcc >> 2) & 1u)) + acc;
    }
    return;
  }
  bid -= 1;
  if (bid < PRE_PO) {
    const int h = bid >> 2, wv = bid & 3;
    v4u ld[9];
#pragma unroll
    for (int i = 0; i < 9; ++i) { int c = tid + 256 * i, r = c / 18, q = c % 18; ld[i] = *(const v4u*)&p.Wo[(long)(h * DH + r) * DM + 144 * wv + 8 * q]; }
#pragma unroll
    for (int i = 0; i < 9; ++i) { int c = tid + 256 * i, r = c / 18, q = c % 18; *(v4u*)&sh[r * 152 + 8 * q] = ld[i]; }
    __syncthreads();
    const int ks = tid >> 6, l = tid & 63, fr = l & 15, g = l >> 4;
#pragma unroll
    for (int ct = 0; ct < 9; ++ct) {
      const u16* a0 = &sh[(32 * ks + 4 * g + (fr >> 2)) * 152 + 16 * ct + 4 * (fr & 3)];
      const v2u x = trd((const char*)a0), y = trd((const char*)(a0 + 16 * 152));
      __builtin_amdgcn_raw_buffer_store_b128((v4u){x.x, x.y, y.x, y.y}, mkr(p.VP, 2u * HD * DM * 2u), (unsigned)(((h * 4 + wv) * 72 + 36 + 4 * ct + ks) * 64 + l) * 16u, 0, WAUX);
    }
    return;
  }
  bid -= PRE_PO;
  if (bid < PRE_KV) {
    const int d0 = (bid % 9) * 64, s0 = (bid / 9) * 128;
    const int dq = tid & 7, sr = tid >> 3;
    const int d = d0 + dq * 8, s = s0 + 4 * sr;
    v4u kr[7], vr[4], sw[4];
#pragma unroll
    for (int a = 0; a < 7; ++a) kr[a] = (s - 3 + a >= 0) ? *(const v4u*)&p.kk[(long)(s - 3 + a) * DM + d] : (v4u){0, 0, 0, 0};
#pragma unroll
    for (int a = 0; a < 4; ++a) { vr[a] = *(const v4u*)&p.vv[(long)(s + a) * DM + d]; sw[a] = *(const v4u*)&p.shift_k[d * 4 + 8 * a]; }
#pragma unroll
    for (int jp = 0; jp < 4; ++jp) {
      const float w0[4] = {lo2f(sw[jp].x), hi2f(sw[jp].x), lo2f(sw[jp].y), hi2f(sw[jp].y)}, w1[4] = {lo2f(sw[jp].z), hi2f(sw[jp].z), lo2f(sw[jp].w), hi2f(sw[jp].w)};
      unsigned o[4];
#pragma unroll
      for (int r = 0; r < 4; ++r) {
        unsigned acc = pk2(lo2f(kr[r][jp]) * w0[3], hi2f(kr[r][jp]) * w1[3]);
#pragma unroll
        for (int a = 1; a < 4; ++a) {
          const unsigned m = pk2(lo2f(kr[r + a][jp]) * w0[3 - a], hi2f(kr[r + a][jp]) * w1[3 - a]);
          acc = pk2(lo2f(acc) + lo2f(m), hi2f(acc) + hi2f(m));
        }
        o[r] = pk2(lo2f(acc) * lo2f(vr[r][jp]), hi2f(acc) * hi2f(vr[r][jp]));
      }
      *(v2u*)&sh[(dq * 8 + 2 * jp) * 136 + 4 * sr] = (v2u){(o[0] & 0xFFFFu) | (o[1] << 16), (o[2] & 0xFFFFu) | (o[3] << 16)};
      *(v2u*)&sh[(dq * 8 + 2 * jp + 1) * 136 + 4 * sr] = (v2u){(o[0] >> 16) | (o[1] & 0xFFFF0000u), (o[2] >> 16) | (o[3] & 0xFFFF0000u)};
    }
    __syncthreads();
#pragma unroll
    for (int i = 0; i < 4; ++i) {
      const int c = tid + 256 * i, dd = c >> 4, q = c & 15;
      STW(p.kvT, DM * S_LEN * 2u, (d0 + dd) * (S_LEN / 8) + s0 / 8 + q, *(const v4u*)&sh[dd * 136 + 8 * q]);
    }
    return;
  }
  bid -= PRE_KV;
  if (bid < PRE_PW) {
    u16 (*t)[136] = (u16 (*)[136])sh;
    const int kq = bid % 6, h = bid / 6;
    v4u ld[3][6];
#pragma unroll
    for (int i = 0; i < 6; ++i) {
      int c = tid + 256 * i, r = c >> 4, q = c & 15;
      ld[0][i] = *reinterpret_cast<const v4u*>(&p.Wq[(long)(96 * kq + r) * HD + h * DH + q * 8]);
      ld[1][i] = *reinterpret_cast<const v4u*>(&p.Wk[(long)(96 * kq + r) * HD + h * DH + q * 8]);
      ld[2][i] = *reinterpret_cast<const v4u*>(&p.Wv[(long)(96 * kq + r) * HD + h * DH + q * 8]);
    }
#pragma unroll
    for (int which = 0; which < 3; ++which) {
      if (which) __syncthreads();
#pragma unroll
      for (int i = 0; i < 6; ++i) {
        int c = tid + 256 * i, r = c >> 4, q = c & 15;
        *reinterpret_cast<v4u*>(&t[r][q * 8]) = ld[which][i];
      }
      __syncthreads();
#pragma unroll
      for (int i = 0; i < 6; ++i) {
        int c = tid + 256 * i, k3 = c >> 9, tile = (c >> 6) & 7, l = c & 63, fr = l & 15, g = l >> 4;
        const u16* a0 = &t[32 * k3 + 8 * g + (fr >> 2)][16 * tile + 4 * (fr & 3)];
        v2u x = __builtin_bit_cast(v2u, __builtin_amdgcn_ds_read_tr16_b64_v4i16((__attribute__((address_space(3))) s16x4*)a0));
        v2u y = __builtin_bit_cast(v2u, __builtin_amdgcn_ds_read_tr16_b64_v4i16((__attribute__((address_space(3))) s16x4*)(a0 + 4 * 136)));
        const int ks = 3 * kq + k3, f = which < 2 ? 8 * (ks >> 1) + 4 * which + 2 * (tile & 1) + (ks & 1) : 4 * (ks >> 1) + 2 * (tile & 1) + (ks & 1);
        __builtin_amdgcn_raw_buffer_store_b128((v4u){x.x, x.y, y.x, y.y}, mkr(which < 2 ? p.WP : p.VP, 2u * HD * DM * 2u), (unsigned)(((h * 4 + (tile >> 1)) * 72 + f) * 64 + l) * 16u, 0, WAUX);
      }
    }
    return;
  }
  bid -= PRE_PW;
  {
    const v4u ones = {0xFFFFFFFFu, 0xFFFFFFFFu, 0xFFFFFFFFu, 0xFFFFFFFFu};
    for (unsigned i = bid * 256 + tid; i < (VM_CH + 108u) * CH / 16u; i += PRE_FILL * 256) {
      if (i >= (CTL_CH + 1u) * CH / 16u && i < PT_CH * CH / 16u) continue;
      v4u v = i / (CH / 16u) == CTL_CH ? (v4u){0u, 0u, 0u, 0u} : ones;
      const unsigned c = ((i >> 10) & 1u) * 512u + (i & 511u);
      if (i < 2u * NREP * CH / 16u && c < 576u) {
        const unsigned* s = (const unsigned*)&p.mem0[(2 * ((c >> 2) & 3) + (c & 1)) * DM + (c >> 4) * 16 + (c & 2)];
        v = (v4u){nosent(s[0]), nosent(s[2]), nosent(s[4]), nosent(s[6])};
      }
      STW(p.win, WIN_BYTES, i, v);
    }
    for (unsigned i = bid * 256 + tid; i < DM * 256; i += PRE_FILL * 256) STW(p.Y, DM * YS, (i >> 8) * (YS / 16) + (i & 255), ones);
    for (unsigned i = bid * 256 + tid; i < 5 * SEGB / 16; i += PRE_FILL * 256) STW(p.H, 2u * NSEG * SEGB, i, ones);
    if (bid * 256 + tid < 2 * SEGB / 16) STW(p.H, 2u * NSEG * SEGB, NSEG * SEGB / 16 + bid * 256 + tid, ones);
  }
}

extern "C" __device__ __attribute__((used, noinline)) void code_end() {}

extern "C" void kernel_launch(void* const* d_in, const int* in_sizes, int n_in, void* d_out, int out_size, void* d_ws,
                              size_t ws_size, hipStream_t stream) {
  const u16* A_q = (const u16*)d_in[0];
  const u16* A_k = (const u16*)d_in[1];
  const u16* A_v = (const u16*)d_in[2];
  const u16* A_shift = (const u16*)d_in[3];
  const u16* A_ssmk = (const u16*)d_in[4];
  const u16* A_ssmD = (const u16*)d_in[5];
  const u16* B_mem0 = (const u16*)d_in[7];
  const u16* B_Wq = (const u16*)d_in[8];
  const u16* B_Wk = (const u16*)d_in[9];
  const u16* B_Wv = (const u16*)d_in[10];
  const u16* B_Wo = (const u16*)d_in[11];

  char* ws = (char*)(((uintptr_t)d_ws + ((2u << 20) - 1)) & ~(uintptr_t)((2u << 20) - 1));
  size_t off = 0;
  auto alloc = [&](size_t bytes) { char* q = ws + off; off += (bytes + 4095) & ~(size_t)4095; return q; };
  unsigned* win = (unsigned*)alloc(WIN_BYTES);
  unsigned* ct = (unsigned*)alloc(CT_TOTAL * 4);
  u16* Y = (u16*)alloc((size_t)DM * YS);
  u16* H = (u16*)alloc((size_t)2 * NSEG * SEGB);
  u16* OT = (u16*)alloc((size_t)2 * NSEG * SEGB);
  u16* WP = (u16*)alloc((size_t)2 * HD * DM * 2);
  u16* VP = (u16*)alloc((size_t)2 * HD * DM * 2);
  u16* kvT = (u16*)alloc((size_t)DM * S_LEN * 2);

  PreP pp;
  pp.kk = A_k; pp.vv = A_v; pp.shift_k = A_shift; pp.kvT = kvT;
  pp.Wq = B_Wq; pp.Wk = B_Wk; pp.Wv = B_Wv; pp.Wo = B_Wo; pp.WP = WP; pp.VP = VP;
  pp.mem0 = B_mem0; pp.win = (v4u*)win; pp.Y = (v4u*)Y; pp.H = (v4u*)H; pp.ct = ct;
  k_pre<<<1 + PRE_PO + PRE_KV + PRE_PW + PRE_FILL, 256, 0, stream>>>(pp);
  MP mp;
  mp.WP = WP; mp.VP = VP; mp.x0 = A_q; mp.kvT = kvT; mp.ssm_k = A_ssmk; mp.ssm_D = A_ssmD; mp.mem0 = B_mem0;
  mp.out = (u16*)d_out; mp.win = win; mp.ct = ct; mp.Y = Y; mp.H = H; mp.OT = OT;
  const int smem_bytes = L_TOTAL;
  (void)hipFuncSetAttribute((const void*)k_main, hipFuncAttributeMaxDynamicSharedMemorySize, smem_bytes);
  k_main<<<256, 256, smem_bytes, stream>>>(mp);
}
```

```cpp
#include <hip/hip_runtime.h>
#include <hip/hip_bf16.h>
#include <stdint.h>

typedef unsigned short u16;
using bf16x8 = __attribute__((ext_vector_type(8))) short;
using s16x4 = __attribute__((ext_vector_type(4))) short;
using f32x4 = __attribute__((ext_vector_type(4))) float;
using f32x2 = __attribute__((ext_vector_type(2))) float;
using f32x16 = __attribute__((ext_vector_type(16))) float;
using bfx2 = __attribute__((ext_vector_type(2))) __bf16;
typedef unsigned v4u __attribute__((ext_vector_type(4)));
typedef unsigned v2u __attribute__((ext_vector_type(2)));

#define S_LEN 2048
#define DM 576
#define NH 32
#define DH 128
#define HD (NH * DH)
#define SEG 32
#define NSEG 64
#define RGB 9216u
#define SEGB (4u * RGB)

__device__ __forceinline__ unsigned pk2(float a, float b) { f32x2 v = {a, b}; bfx2 r = __builtin_convertvector(v, bfx2); return __builtin_bit_cast(unsigned, r); }
__device__ __forceinline__ float lo2f(unsigned u) { return __uint_as_float(u << 16); }
__device__ __forceinline__ float hi2f(unsigned u) { return __uint_as_float(u & 0xFFFF0000u); }
__device__ __forceinline__ unsigned nosent(unsigned u) { return u < 0xFFFFFFFEu ? u : 0xFFFFFFFEu; }
__device__ __forceinline__ bf16x8 asb8(v4u v) { return __builtin_bit_cast(bf16x8, v); }
#define MFMA32(a, b, c) __builtin_amdgcn_mfma_f32_16x16x32_bf16(asb8(a), asb8(b), c, 0, 0, 0)

typedef __amdgpu_buffer_rsrc_t rsrc_t;
__device__ __forceinline__ rsrc_t mkr(const void* p, unsigned bytes) { return __builtin_amdgcn_make_buffer_rsrc((void*)p, 0, bytes, 0x00020000); }
__device__ __forceinline__ void st16(rsrc_t r, unsigned vo, unsigned so, v4u v) {
  asm volatile("" : "+v"(v));
  __builtin_amdgcn_raw_buffer_store_b128(v, r, vo, so, 16);
  asm volatile("s_nop 1" :: "v"(v));
}
__device__ __forceinline__ void st4(rsrc_t r, unsigned vo, unsigned so, unsigned v) { __builtin_amdgcn_raw_buffer_store_b32(v, r, vo, so, 16); }
__device__ __forceinline__ v4u ld16(rsrc_t r, unsigned vo, unsigned so) { return __builtin_amdgcn_raw_buffer_load_b128(r, vo, so, 16); }
__device__ __forceinline__ unsigned ld4(rsrc_t r, unsigned vo, unsigned so) { return __builtin_amdgcn_raw_buffer_load_b32(r, vo, so, 16); }

#define WIN_BYTES (36u << 20)
#define CH 16384u
#define NREP 4
#define NPR 4
#define MEM_CH 0
#define PM_CH 32
#define PT_CH 128
#define VM_CH 144
#define VT_CH 256
#define CTL_CH 64
#define CAL_CH 127
#define CT_TICK 0
#define CT_TMO 256
#define CT_FLIP 352
#define CT_YC 384
#define CT_TOTAL 640
#define YS 4352u
#define SENT 0xFFFFFFFFu
#define SPIN_CAP 400000u

struct MP {
  const u16* Wq; const u16* Wk; const u16* Wv; const u16* Wo; const u16* x0; const u16* kvT; const u16* ssm_k; const u16* ssm_D; const u16* mem0;
  u16* out; unsigned* win; unsigned* ct; u16* Y; u16* H; u16* OT;
};

#define BAR() do { asm volatile("s_waitcnt lgkmcnt(0)\n\ts_barrier" ::: "memory"); } while (0)
#define VM0() __builtin_amdgcn_s_waitcnt(0x0F70)
#define PIN() __builtin_amdgcn_sched_barrier(0)
__device__ __forceinline__ unsigned umax3(unsigned a, unsigned b, unsigned c) { return __builtin_elementwise_max(__builtin_elementwise_max(a, b), c); }
__device__ __forceinline__ unsigned mx4(unsigned m, v4u v) { return umax3(umax3(m, v.x, v.y), v.z, v.w); }
__device__ __forceinline__ unsigned phys(unsigned o) { return (o >> 13) * CH + (o & 8191u); }
template <int N> struct IC { static constexpr int v = N; };
template <int B, int E, typename F> __device__ __forceinline__ void sfor(F&& f) { if constexpr (B < E) { f(IC<B>{}); sfor<B + 1, E>(f); } }

__device__ __forceinline__ bool spin_fail(unsigned& spins, rsrc_t F) {
  if (__builtin_expect((++spins & 1023u) != 0u, 1)) return false;
  if (spins > SPIN_CAP) st4(F, CT_TMO * 4u, 0, 1u);
  VM0();
  return (__builtin_amdgcn_readfirstlane(ld4(F, CT_TMO * 4u, 0)) != 0) | (spins > SPIN_CAP);
}

#define RED8(name, ins) __device__ __forceinline__ float name(float a) { float d, t; asm volatile("s_nop 1\n\t" ins "_dpp %0, %2, %2 row_ror:8 row_mask:0xf bank_mask:0xf\n\t" \
    "v_mov_b32 %1, %0\n\ts_nop 1\n\tv_permlane16_swap_b32 %0, %1\n\t" ins " %0, %0, %1\n\tv_mov_b32 %1, %0\n\ts_nop 1\n\tv_permlane32_swap_b32 %0, %1\n\t" ins " %0, %0, %1" \
    : "=&v"(d), "=&v"(t) : "v"(a)); return d; }
RED8(i_red8add, "v_add_f32")

__device__ __forceinline__ void wait_time(const rsrc_t X, unsigned o, int c) {
  unsigned v = 0;
#pragma nounroll
  for (int i = 0; i < 2000; ++i) {
    asm volatile("" ::: "memory");
    v = __builtin_amdgcn_readfirstlane(ld4(X, o, 0));
    if (v) break;
    __builtin_amdgcn_s_sleep(12);
  }
  v += (unsigned)c;
#pragma nounroll
  while ((unsigned)__builtin_amdgcn_s_memrealtime() - v > 0xFFFFF000u) __builtin_amdgcn_s_sleep(1);
}
__device__ __forceinline__ v4u mkd(const void* p, unsigned bytes) { return (v4u){(unsigned)(uintptr_t)p, (unsigned)((uintptr_t)p >> 32) & 0xFFFFu, bytes, 0x00020000u}; }
typedef __attribute__((address_space(3))) char* lds_ptr;
#define DROP 0x40000000u
#define US(x) __builtin_amdgcn_readfirstlane((int)(x))

#define L_QI 36864
#define L_KT 102400
#define L_VI 36864
#define LO_MEM 0
#define LO_A 12288
#define LO_RM 16384
#define L_ROLE 137728
#define L_TOTAL 137792
__device__ __forceinline__ v2u trd(const char* a) { return __builtin_bit_cast(v2u, __builtin_amdgcn_ds_read_tr16_b64_v4i16((__attribute__((address_space(3))) s16x4*)a)); }
#define QK_ASM \
  "s_nop 4\n" \
  "v_xor_b32 v64, 0x80, %[wv]\n" \
  "v_xor_b32 v65, 32, %[wr]\n" \
  "v_add_u32 v66, 0x10000, %[wr]\n" \
  "v_add_u32 v67, 0x10000, v65\n" \
  "s_cmp_eq_u32 %[grp], 0\n" \
  "s_cbranch_scc1 .Lqk_ws1\n" \
  "s_sleep 8\n" \
  ".Lqk_ws1:\n" \
  "s_add_i32 m0, %[wl], 0\n" \
  "s_mov_b32 s76, 0\n" \
  "buffer_load_dwordx4 %[wv], %[WA], s76 offen lds\n" \
  "s_add_i32 m0, %[wl], 1024\n" \
  "s_mov_b32 s76, 32768\n" \
  "buffer_load_dwordx4 %[wv], %[WA], s76 offen lds\n" \
  "s_add_i32 m0, %[wl], 2048\n" \
  "s_mov_b32 s76, 65536\n" \
  "buffer_load_dwordx4 v64, %[WA], s76 offen lds\n" \
  "s_add_i32 m0, %[wl], 3072\n" \
  "s_mov_b32 s76, 98304\n" \
  "buffer_load_dwordx4 v64, %[WA], s76 offen lds\n" \
  "s_cmp_eq_u32 %[grp], 0\n" \
  "s_cbranch_scc1 .Lqk_ws2\n" \
  "s_sleep 8\n" \
  ".Lqk_ws2:\n" \
  "s_add_i32 m0, %[wl], 16384\n" \
  "s_mov_b32 s76, 0\n" \
  "buffer_load_dwordx4 %[wv], %[WB], s76 offen lds\n" \
  "s_add_i32 m0, %[wl], 17408\n" \
  "s_mov_b32 s76, 32768\n" \
  "buffer_load_dwordx4 %[wv], %[WB], s76 offen lds\n" \
  "s_add_i32 m0, %[wl], 18432\n" \
  "s_mov_b32 s76, 65536\n" \
  "buffer_load_dwordx4 v64, %[WB], s76 offen lds\n" \
  "s_add_i32 m0, %[wl], 19456\n" \
  "s_mov_b32 s76, 98304\n" \
  "buffer_load_dwordx4 v64, %[WB], s76 offen lds\n" \
  "s_cmp_eq_u32 %[grp], 0\n" \
  "s_cbranch_scc1 .Lqk_ws3\n" \
  "s_sleep 8\n" \
  ".Lqk_ws3:\n" \
  "s_add_i32 m0, %[wl], 32768\n" \
  "s_mov_b32 s76, 524288\n" \
  "buffer_load_dwordx4 %[wv], %[WA], s76 offen lds\n" \
  "s_add_i32 m0, %[wl], 33792\n" \
  "s_mov_b32 s76, 557056\n" \
  "buffer_load_dwordx4 %[wv], %[WA], s76 offen lds\n" \
  "s_add_i32 m0, %[wl], 34816\n" \
  "s_mov_b32 s76, 589824\n" \
  "buffer_load_dwordx4 v64, %[WA], s76 offen lds\n" \
  "s_add_i32 m0, %[wl], 35840\n" \
  "s_mov_b32 s76, 622592\n" \
  "buffer_load_dwordx4 v64, %[WA], s76 offen lds\n" \
  "s_cmp_eq_u32 %[grp], 0\n" \
  "s_cbranch_scc1 .Lqk_ws4\n" \
  "s_sleep 8\n" \
  ".Lqk_ws4:\n" \
  "s_add_i32 m0, %[wl], 49152\n" \
  "s_mov_b32 s76, 524288\n" \
  "buffer_load_dwordx4 %[wv], %[WB], s76 offen lds\n" \
  "s_add_i32 m0, %[wl], 50176\n" \
  "s_mov_b32 s76, 557056\n" \
  "buffer_load_dwordx4 %[wv], %[WB], s76 offen lds\n" \
  "s_add_i32 m0, %[wl], 51200\n" \
  "s_mov_b32 s76, 589824\n" \
  "buffer_load_dwordx4 v64, %[WB], s76 offen lds\n" \
  "s_add_i32 m0, %[wl], 52224\n" \
  "s_mov_b32 s76, 622592\n" \
  "buffer_load_dwordx4 v64, %[WB], s76 offen lds\n" \
  "s_cmp_eq_u32 %[grp], 0\n" \
  "s_cbranch_scc1 .Lqk_ws5\n" \
  "s_sleep 8\n" \
  ".Lqk_ws5:\n" \
  "s_add_i32 m0, %[wl], 65536\n" \
  "s_mov_b32 s76, 1048576\n" \
  "buffer_load_dwordx4 %[wv], %[WA], s76 offen lds\n" \
  "s_add_i32 m0, %[wl], 66560\n" \
  "s_mov_b32 s76, 1081344\n" \
  "buffer_load_dwordx4 %[wv], %[WA], s76 offen lds\n" \
  "s_add_i32 m0, %[wl], 67584\n" \
  "s_mov_b32 s76, 1114112\n" \
  "buffer_load_dwordx4 v64, %[WA], s76 offen lds\n" \
  "s_add_i32 m0, %[wl], 68608\n" \
  "s_mov_b32 s76, 1146880\n" \
  "buffer_load_dwordx4 v64, %[WA], s76 offen lds\n" \
  "s_cmp_eq_u32 %[grp], 0\n" \
  "s_cbranch_scc1 .Lqk_ws6\n" \
  "s_sleep 8\n" \
  ".Lqk_ws6:\n" \
  "s_add_i32 m0, %[wl], 81920\n" \
  "s_mov_b32 s76, 1048576\n" \
  "buffer_load_dwordx4 %[wv], %[WB], s76 offen lds\n" \
  "s_add_i32 m0, %[wl], 82944\n" \
  "s_mov_b32 s76, 1081344\n" \
  "buffer_load_dwordx4 %[wv], %[WB], s76 offen lds\n" \
  "s_add_i32 m0, %[wl], 83968\n" \
  "s_mov_b32 s76, 1114112\n" \
  "buffer_load_dwordx4 v64, %[WB], s76 offen lds\n" \
  "s_add_i32 m0, %[wl], 84992\n" \
  "s_mov_b32 s76, 1146880\n" \
  "buffer_load_dwordx4 v64, %[WB], s76 offen lds\n" \
  "s_cmp_eq_u32 %[grp], 0\n" \
  "s_cbranch_scc1 .Lqk_ws7\n" \
  "s_sleep 8\n" \
  ".Lqk_ws7:\n" \
  "s_add_i32 m0, %[wl], 98304\n" \
  "s_mov_b32 s76, 1572864\n" \
  "buffer_load_dwordx4 %[wv], %[WA], s76 offen lds\n" \
  "s_add_i32 m0, %[wl], 99328\n" \
  "s_mov_b32 s76, 1605632\n" \
  "buffer_load_dwordx4 %[wv], %[WA], s76 offen lds\n" \
  "s_add_i32 m0, %[wl], 100352\n" \
  "s_mov_b32 s76, 1638400\n" \
  "buffer_load_dwordx4 v64, %[WA], s76 offen lds\n" \
  "s_add_i32 m0, %[wl], 101376\n" \
  "s_mov_b32 s76, 1671168\n" \
  "buffer_load_dwordx4 v64, %[WA], s76 offen lds\n" \
  "s_cmp_eq_u32 %[grp], 0\n" \
  "s_cbranch_scc1 .Lqk_ws8\n" \
  "s_sleep 8\n" \
  ".Lqk_ws8:\n" \
  "s_add_i32 m0, %[wl], 114688\n" \
  "s_mov_b32 s76, 1572864\n" \
  "buffer_load_dwordx4 %[wv], %[WB], s76 offen lds\n" \
  "s_add_i32 m0, %[wl], 115712\n" \
  "s_mov_b32 s76, 1605632\n" \
  "buffer_load_dwordx4 %[wv], %[WB], s76 offen lds\n" \
  "s_add_i32 m0, %[wl], 116736\n" \
  "s_mov_b32 s76, 1638400\n" \
  "buffer_load_dwordx4 v64, %[WB], s76 offen lds\n" \
  "s_add_i32 m0, %[wl], 117760\n" \
  "s_mov_b32 s76, 1671168\n" \
  "buffer_load_dwordx4 v64, %[WB], s76 offen lds\n" \
  "s_waitcnt vmcnt(28)\n" \
  "s_waitcnt lgkmcnt(0)\n" \
  "s_barrier\n" \
  "ds_read_b64_tr_b16 a[0:1], %[wr] offset:0\n" \
  "ds_read_b64_tr_b16 a[2:3], %[wr] offset:1024\n" \
  "ds_read_b64_tr_b16 a[4:5], %[wr] offset:8192\n" \
  "ds_read_b64_tr_b16 a[6:7], %[wr] offset:9216\n" \
  "ds_read_b64_tr_b16 a[8:9], v65 offset:0\n" \
  "ds_read_b64_tr_b16 a[10:11], v65 offset:1024\n" \
  "ds_read_b64_tr_b16 a[12:13], v65 offset:8192\n" \
  "ds_read_b64_tr_b16 a[14:15], v65 offset:9216\n" \
  "s_waitcnt vmcnt(24)\n" \
  "s_waitcnt lgkmcnt(0)\n" \
  "s_barrier\n" \
  "s_cmp_eq_u32 %[grp], 0\n" \
  "s_cbranch_scc1 .Lqk_ws9\n" \
  "s_sleep 8\n" \
  ".Lqk_ws9:\n" \
  "s_add_i32 m0, %[wl], 0\n" \
  "s_mov_b32 s76, 2097152\n" \
  "buffer_load_dwordx4 %[wv], %[WA], s76 offen lds\n" \
  "s_add_i32 m0, %[wl], 1024\n" \
  "s_mov_b32 s76, 2129920\n" \
  "buffer_load_dwordx4 %[wv], %[WA], s76 offen lds\n" \
  "s_add_i32 m0, %[wl], 2048\n" \
  "s_mov_b32 s76, 2162688\n" \
  "buffer_load_dwordx4 v64, %[WA], s76 offen lds\n" \
  "s_add_i32 m0, %[wl], 3072\n" \
  "s_mov_b32 s76, 2195456\n" \
  "buffer_load_dwordx4 v64, %[WA], s76 offen lds\n" \
  "ds_read_b64_tr_b16 a[16:17], %[wr] offset:16384\n" \
  "ds_read_b64_tr_b16 a[18:19], %[wr] offset:17408\n" \
  "ds_read_b64_tr_b16 a[20:21], %[wr] offset:24576\n" \
  "ds_read_b64_tr_b16 a[22:23], %[wr] offset:25600\n" \
  "ds_read_b64_tr_b16 a[24:25], v65 offset:16384\n" \
  "ds_read_b64_tr_b16 a[26:27], v65 offset:17408\n" \
  "ds_read_b64_tr_b16 a[28:29], v65 offset:24576\n" \
  "ds_read_b64_tr_b16 a[30:31], v65 offset:25600\n" \
  "s_waitcnt vmcnt(24)\n" \
  "s_waitcnt lgkmcnt(0)\n" \
  "s_barrier\n" \
  "s_cmp_eq_u32 %[grp], 0\n" \
  "s_cbranch_scc1 .Lqk_ws10\n" \
  "s_sleep 8\n" \
  ".Lqk_ws10:\n" \
  "s_add_i32 m0, %[wl], 16384\n" \
  "s_mov_b32 s76, 2097152\n" \
  "buffer_load_dwordx4 %[wv], %[WB], s76 offen lds\n" \
  "s_add_i32 m0, %[wl], 17408\n" \
  "s_mov_b32 s76, 2129920\n" \
  "buffer_load_dwordx4 %[wv], %[WB], s76 offen lds\n" \
  "s_add_i32 m0, %[wl], 18432\n" \
  "s_mov_b32 s76, 2162688\n" \
  "buffer_load_dwordx4 v64, %[WB], s76 offen lds\n" \
  "s_add_i32 m0, %[wl], 19456\n" \
  "s_mov_b32 s76, 2195456\n" \
  "buffer_load_dwordx4 v64, %[WB], s76 offen lds\n" \
  "ds_read_b64_tr_b16 a[32:33], %[wr] offset:32768\n" \
  "ds_read_b64_tr_b16 a[34:35], %[wr] offset:33792\n" \
  "ds_read_b64_tr_b16 a[36:37], %[wr] offset:40960\n" \
  "ds_read_b64_tr_b16 a[38:39], %[wr] offset:41984\n" \
  "ds_read_b64_tr_b16 a[40:41], v65 offset:32768\n" \
  "ds_read_b64_tr_b16 a[42:43], v65 offset:33792\n" \
  "ds_read_b64_tr_b16 a[44:45], v65 offset:40960\n" \
  "ds_read_b64_tr_b16 a[46:47], v65 offset:41984\n" \
  "s_waitcnt vmcnt(24)\n" \
  "s_waitcnt lgkmcnt(0)\n" \
  "s_barrier\n" \
  "s_cmp_eq_u32 %[grp], 0\n" \
  "s_cbranch_scc1 .Lqk_ws11\n" \
  "s_sleep 8\n" \
  ".Lqk_ws11:\n" \
  "s_add_i32 m0, %[wl], 32768\n" \
  "s_mov_b32 s76, 2621440\n" \
  "buffer_load_dwordx4 %[wv], %[WA], s76 offen lds\n" \
  "s_add_i32 m0, %[wl], 33792\n" \
  "s_mov_b32 s76, 2654208\n" \
  "buffer_load_dwordx4 %[wv], %[WA], s76 offen lds\n" \
  "s_add_i32 m0, %[wl], 34816\n" \
  "s_mov_b32 s76, 2686976\n" \
  "buffer_load_dwordx4 v64, %[WA], s76 offen lds\n" \
  "s_add_i32 m0, %[wl], 35840\n" \
  "s_mov_b32 s76, 2719744\n" \
  "buffer_load_dwordx4 v64, %[WA], s76 offen lds\n" \
  "ds_read_b64_tr_b16 a[48:49], %[wr] offset:49152\n" \
  "ds_read_b64_tr_b16 a[50:51], %[wr] offset:50176\n" \
  "ds_read_b64_tr_b16 a[52:53], %[wr] offset:57344\n" \
  "ds_read_b64_tr_b16 a[54:55], %[wr] offset:58368\n" \
  "ds_read_b64_tr_b16 a[56:57], v65 offset:49152\n" \
  "ds_read_b64_tr_b16 a[58:59], v65 offset:50176\n" \
  "ds_read_b64_tr_b16 a[60:61], v65 offset:57344\n" \
  "ds_read_b64_tr_b16 a[62:63], v65 offset:58368\n" \
  "s_waitcnt vmcnt(24)\n" \
  "s_waitcnt lgkmcnt(0)\n" \
  "s_barrier\n" \
  "s_cmp_eq_u32 %[grp], 0\n" \
  "s_cbranch_scc1 .Lqk_ws12\n" \
  "s_sleep 8\n" \
  ".Lqk_ws12:\n" \
  "s_add_i32 m0, %[wl], 49152\n" \
  "s_mov_b32 s76, 2621440\n" \
  "buffer_load_dwordx4 %[wv], %[WB], s76 offen lds\n" \
  "s_add_i32 m0, %[wl], 50176\n" \
  "s_mov_b32 s76, 2654208\n" \
  "buffer_load_dwordx4 %[wv], %[WB], s76 offen lds\n" \
  "s_add_i32 m0, %[wl], 51200\n" \
  "s_mov_b32 s76, 2686976\n" \
  "buffer_load_dwordx4 v64, %[WB], s76 offen lds\n" \
  "s_add_i32 m0, %[wl], 52224\n" \
  "s_mov_b32 s76, 2719744\n" \
  "buffer_load_dwordx4 v64, %[WB], s76 offen lds\n" \
  "ds_read_b64_tr_b16 a[64:65], v66 offset:0\n" \
  "ds_read_b64_tr_b16 a[66:67], v66 offset:1024\n" \
  "ds_read_b64_tr_b16 a[68:69], v66 offset:8192\n" \
  "ds_read_b64_tr_b16 a[70:71], v66 offset:9216\n" \
  "ds_read_b64_tr_b16 a[72:73], v67 offset:0\n" \
  "ds_read_b64_tr_b16 a[74:75], v67 offset:1024\n" \
  "ds_read_b64_tr_b16 a[76:77], v67 offset:8192\n" \
  "ds_read_b64_tr_b16 a[78:79], v67 offset:9216\n" \
  "s_waitcnt vmcnt(24)\n" \
  "s_waitcnt lgkmcnt(0)\n" \
  "s_barrier\n" \
  "s_cmp_eq_u32 %[grp], 0\n" \
  "s_cbranch_scc1 .Lqk_ws13\n" \
  "s_sleep 8\n" \
  ".Lqk_ws13:\n" \
  "s_add_i32 m0, %[wl], 65536\n" \
  "s_mov_b32 s76, 3145728\n" \
  "buffer_load_dwordx4 %[wv], %[WA], s76 offen lds\n" \
  "s_add_i32 m0, %[wl], 66560\n" \
  "s_mov_b32 s76, 3178496\n" \
  "buffer_load_dwordx4 %[wv], %[WA], s76 offen lds\n" \
  "s_add_i32 m0, %[wl], 67584\n" \
  "s_mov_b32 s76, 3211264\n" \
  "buffer_load_dwordx4 v64, %[WA], s76 offen lds\n" \
  "s_add_i32 m0, %[wl], 68608\n" \
  "s_mov_b32 s76, 3244032\n" \
  "buffer_load_dwordx4 v64, %[WA], s76 offen lds\n" \
  "ds_read_b64_tr_b16 a[80:81], v66 offset:16384\n" \
  "ds_read_b64_tr_b16 a[82:83], v66 offset:17408\n" \
  "ds_read_b64_tr_b16 a[84:85], v66 offset:24576\n" \
  "ds_read_b64_tr_b16 a[86:87], v66 offset:25600\n" \
  "ds_read_b64_tr_b16 a[88:89], v67 offset:16384\n" \
  "ds_read_b64_tr_b16 a[90:91], v67 offset:17408\n" \
  "ds_read_b64_tr_b16 a[92:93], v67 offset:24576\n" \
  "ds_read_b64_tr_b16 a[94:95], v67 offset:25600\n" \
  "s_waitcnt vmcnt(24)\n" \
  "s_waitcnt lgkmcnt(0)\n" \
  "s_barrier\n" \
  "s_cmp_eq_u32 %[grp], 0\n" \
  "s_cbranch_scc1 .Lqk_ws14\n" \
  "s_sleep 8\n" \
  ".Lqk_ws14:\n" \
  "s_add_i32 m0, %[wl], 81920\n" \
  "s_mov_b32 s76, 3145728\n" \
  "buffer_load_dwordx4 %[wv], %[WB], s76 offen lds\n" \
  "s_add_i32 m0, %[wl], 82944\n" \
  "s_mov_b32 s76, 3178496\n" \
  "buffer_load_dwordx4 %[wv], %[WB], s76 offen lds\n" \
  "s_add_i32 m0, %[wl], 83968\n" \
  "s_mov_b32 s76, 3211264\n" \
  "buffer_load_dwordx4 v64, %[WB], s76 offen lds\n" \
  "s_add_i32 m0, %[wl], 84992\n" \
  "s_mov_b32 s76, 3244032\n" \
  "buffer_load_dwordx4 v64, %[WB], s76 offen lds\n" \
  "ds_read_b64_tr_b16 a[96:97], v66 offset:32768\n" \
  "ds_read_b64_tr_b16 a[98:99], v66 offset:33792\n" \
  "ds_read_b64_tr_b16 a[100:101], v66 offset:40960\n" \
  "ds_read_b64_tr_b16 a[102:103], v66 offset:41984\n" \
  "ds_read_b64_tr_b16 a[104:105], v67 offset:32768\n" \
  "ds_read_b64_tr_b16 a[106:107], v67 offset:33792\n" \
  "ds_read_b64_tr_b16 a[108:109], v67 offset:40960\n" \
  "ds_read_b64_tr_b16 a[110:111], v67 offset:41984\n" \
  "s_waitcnt vmcnt(24)\n" \
  "s_waitcnt lgkmcnt(0)\n" \
  "s_barrier\n" \
  "s_cmp_eq_u32 %[grp], 0\n" \
  "s_cbranch_scc1 .Lqk_ws15\n" \
  "s_sleep 8\n" \
  ".Lqk_ws15:\n" \
  "s_add_i32 m0, %[wl], 98304\n" \
  "s_mov_b32 s76, 3670016\n" \
  "buffer_load_dwordx4 %[wv], %[WA], s76 offen lds\n" \
  "s_add_i32 m0, %[wl], 99328\n" \
  "s_mov_b32 s76, 3702784\n" \
  "buffer_load_dwordx4 %[wv], %[WA], s76 offen lds\n" \
  "s_add_i32 m0, %[wl], 100352\n" \
  "s_mov_b32 s76, 3735552\n" \
  "buffer_load_dwordx4 v64, %[WA], s76 offen lds\n" \
  "s_add_i32 m0, %[wl], 101376\n" \
  "s_mov_b32 s76, 3768320\n" \
  "buffer_load_dwordx4 v64, %[WA], s76 offen lds\n" \
  "ds_read_b64_tr_b16 a[112:113], v66 offset:49152\n" \
  "ds_read_b64_tr_b16 a[114:115], v66 offset:50176\n" \
  "ds_read_b64_tr_b16 a[116:117], v66 offset:57344\n" \
  "ds_read_b64_tr_b16 a[118:119], v66 offset:58368\n" \
  "ds_read_b64_tr_b16 a[120:121], v67 offset:49152\n" \
  "ds_read_b64_tr_b16 a[122:123], v67 offset:50176\n" \
  "ds_read_b64_tr_b16 a[124:125], v67 offset:57344\n" \
  "ds_read_b64_tr_b16 a[126:127], v67 offset:58368\n" \
  "s_waitcnt vmcnt(24)\n" \
  "s_waitcnt lgkmcnt(0)\n" \
  "s_barrier\n" \
  "s_cmp_eq_u32 %[grp], 0\n" \
  "s_cbranch_scc1 .Lqk_ws16\n" \
  "s_sleep 8\n" \
  ".Lqk_ws16:\n" \
  "s_add_i32 m0, %[wl], 114688\n" \
  "s_mov_b32 s76, 3670016\n" \
  "buffer_load_dwordx4 %[wv], %[WB], s76 offen lds\n" \
  "s_add_i32 m0, %[wl], 115712\n" \
  "s_mov_b32 s76, 3702784\n" \
  "buffer_load_dwordx4 %[wv], %[WB], s76 offen lds\n" \
  "s_add_i32 m0, %[wl], 116736\n" \
  "s_mov_b32 s76, 3735552\n" \
  "buffer_load_dwordx4 v64, %[WB], s76 offen lds\n" \
  "s_add_i32 m0, %[wl], 117760\n" \
  "s_mov_b32 s76, 3768320\n" \
  "buffer_load_dwordx4 v64, %[WB], s76 offen lds\n" \
  "ds_read_b64_tr_b16 a[128:129], %[wr] offset:0\n" \
  "ds_read_b64_tr_b16 a[130:131], %[wr] offset:1024\n" \
  "ds_read_b64_tr_b16 a[132:133], %[wr] offset:8192\n" \
  "ds_read_b64_tr_b16 a[134:135], %[wr] offset:9216\n" \
  "ds_read_b64_tr_b16 a[136:137], v65 offset:0\n" \
  "ds_read_b64_tr_b16 a[138:139], v65 offset:1024\n" \
  "ds_read_b64_tr_b16 a[140:141], v65 offset:8192\n" \
  "ds_read_b64_tr_b16 a[142:143], v65 offset:9216\n" \
  "s_waitcnt vmcnt(24)\n" \
  "s_waitcnt lgkmcnt(0)\n" \
  "s_barrier\n" \
  "s_cmp_eq_u32 %[grp], 0\n" \
  "s_cbranch_scc1 .Lqk_ws17\n" \
  "s_sleep 8\n" \
  ".Lqk_ws17:\n" \
  "s_add_i32 m0, %[wl], 0\n" \
  "s_mov_b32 s76, 4194304\n" \
  "buffer_load_dwordx4 %[wv], %[WA], s76 offen lds\n" \
  "s_add_i32 m0, %[wl], 1024\n" \
  "s_mov_b32 s76, 4227072\n" \
  "buffer_load_dwordx4 %[wv], %[WA], s76 offen lds\n" \
  "s_add_i32 m0, %[wl], 2048\n" \
  "s_mov_b32 s76, 4259840\n" \
  "buffer_load_dwordx4 v64, %[WA], s76 offen lds\n" \
  "s_add_i32 m0, %[wl], 3072\n" \
  "s_mov_b32 s76, 4292608\n" \
  "buffer_load_dwordx4 v64, %[WA], s76 offen lds\n" \
  "ds_read_b64_tr_b16 a[144:145], %[wr] offset:16384\n" \
  "ds_read_b64_tr_b16 a[146:147], %[wr] offset:17408\n" \
  "ds_read_b64_tr_b16 a[148:149], %[wr] offset:24576\n" \
  "ds_read_b64_tr_b16 a[150:151], %[wr] offset:25600\n" \
  "ds_read_b64_tr_b16 a[152:153], v65 offset:16384\n" \
  "ds_read_b64_tr_b16 a[154:155], v65 offset:17408\n" \
  "ds_read_b64_tr_b16 a[156:157], v65 offset:24576\n" \
  "ds_read_b64_tr_b16 a[158:159], v65 offset:25600\n" \
  "s_waitcnt vmcnt(24)\n" \
  "s_waitcnt lgkmcnt(0)\n" \
  "s_barrier\n" \
  "s_cmp_eq_u32 %[grp], 0\n" \
  "s_cbranch_scc1 .Lqk_ws18\n" \
  "s_sleep 8\n" \
  ".Lqk_ws18:\n" \
  "s_add_i32 m0, %[wl], 16384\n" \
  "s_mov_b32 s76, 4194304\n" \
  "buffer_load_dwordx4 %[wv], %[WB], s76 offen lds\n" \
  "s_add_i32 m0, %[wl], 17408\n" \
  "s_mov_b32 s76, 4227072\n" \
  "buffer_load_dwordx4 %[wv], %[WB], s76 offen lds\n" \
  "s_add_i32 m0, %[wl], 18432\n" \
  "s_mov_b32 s76, 4259840\n" \
  "buffer_load_dwordx4 v64, %[WB], s76 offen lds\n" \
  "s_add_i32 m0, %[wl], 19456\n" \
  "s_mov_b32 s76, 4292608\n" \
  "buffer_load_dwordx4 v64, %[WB], s76 offen lds\n" \
  "ds_read_b64_tr_b16 a[160:161], %[wr] offset:32768\n" \
  "ds_read_b64_tr_b16 a[162:163], %[wr] offset:33792\n" \
  "ds_read_b64_tr_b16 a[164:165], %[wr] offset:40960\n" \
  "ds_read_b64_tr_b16 a[166:167], %[wr] offset:41984\n" \
  "ds_read_b64_tr_b16 a[168:169], v65 offset:32768\n" \
  "ds_read_b64_tr_b16 a[170:171], v65 offset:33792\n" \
  "ds_read_b64_tr_b16 a[172:173], v65 offset:40960\n" \
  "ds_read_b64_tr_b16 a[174:175], v65 offset:41984\n" \
  "s_waitcnt vmcnt(24)\n" \
  "s_waitcnt lgkmcnt(0)\n" \
  "s_barrier\n" \
  "ds_read_b64_tr_b16 a[176:177], %[wr] offset:49152\n" \
  "ds_read_b64_tr_b16 a[178:179], %[wr] offset:50176\n" \
  "ds_read_b64_tr_b16 a[180:181], %[wr] offset:57344\n" \
  "ds_read_b64_tr_b16 a[182:183], %[wr] offset:58368\n" \
  "ds_read_b64_tr_b16 a[184:185], v65 offset:49152\n" \
  "ds_read_b64_tr_b16 a[186:187], v65 offset:50176\n" \
  "ds_read_b64_tr_b16 a[188:189], v65 offset:57344\n" \
  "ds_read_b64_tr_b16 a[190:191], v65 offset:58368\n" \
  "s_waitcnt vmcnt(20)\n" \
  "s_waitcnt lgkmcnt(0)\n" \
  "s_barrier\n" \
  "ds_read_b64_tr_b16 a[192:193], v66 offset:0\n" \
  "ds_read_b64_tr_b16 a[194:195], v66 offset:1024\n" \
  "ds_read_b64_tr_b16 a[196:197], v66 offset:8192\n" \
  "ds_read_b64_tr_b16 a[198:199], v66 offset:9216\n" \
  "ds_read_b64_tr_b16 a[200:201], v67 offset:0\n" \
  "ds_read_b64_tr_b16 a[202:203], v67 offset:1024\n" \
  "ds_read_b64_tr_b16 a[204:205], v67 offset:8192\n" \
  "ds_read_b64_tr_b16 a[206:207], v67 offset:9216\n" \
  "s_waitcnt vmcnt(16)\n" \
  "s_waitcnt lgkmcnt(0)\n" \
  "s_barrier\n" \
  "ds_read_b64_tr_b16 a[208:209], v66 offset:16384\n" \
  "ds_read_b64_tr_b16 a[210:211], v66 offset:17408\n" \
  "ds_read_b64_tr_b16 a[212:213], v66 offset:24576\n" \
  "ds_read_b64_tr_b16 a[214:215], v66 offset:25600\n" \
  "ds_read_b64_tr_b16 a[216:217], v67 offset:16384\n" \
  "ds_read_b64_tr_b16 a[218:219], v67 offset:17408\n" \
  "ds_read_b64_tr_b16 a[220:221], v67 offset:24576\n" \
  "ds_read_b64_tr_b16 a[222:223], v67 offset:25600\n" \
  "s_waitcnt vmcnt(12)\n" \
  "s_waitcnt lgkmcnt(0)\n" \
  "s_barrier\n" \
  "ds_read_b64_tr_b16 a[224:225], v66 offset:32768\n" \
  "ds_read_b64_tr_b16 a[226:227], v66 offset:33792\n" \
  "ds_read_b64_tr_b16 a[228:229], v66 offset:40960\n" \
  "ds_read_b64_tr_b16 a[230:231], v66 offset:41984\n" \
  "ds_read_b64_tr_b16 a[232:233], v67 offset:32768\n" \
  "ds_read_b64_tr_b16 a[234:235], v67 offset:33792\n" \
  "ds_read_b64_tr_b16 a[236:237], v67 offset:40960\n" \
  "ds_read_b64_tr_b16 a[238:239], v67 offset:41984\n" \
  "s_waitcnt vmcnt(8)\n" \
  "s_waitcnt lgkmcnt(0)\n" \
  "s_barrier\n" \
  "ds_read_b64_tr_b16 a[240:241], v66 offset:49152\n" \
  "ds_read_b64_tr_b16 a[242:243], v66 offset:50176\n" \
  "ds_read_b64_tr_b16 a[244:245], v66 offset:57344\n" \
  "ds_read_b64_tr_b16 a[246:247], v66 offset:58368\n" \
  "ds_read_b64_tr_b16 a[248:249], v67 offset:49152\n" \
  "ds_read_b64_tr_b16 a[250:251], v67 offset:50176\n" \
  "ds_read_b64_tr_b16 a[252:253], v67 offset:57344\n" \
  "ds_read_b64_tr_b16 a[254:255], v67 offset:58368\n" \
  "s_waitcnt vmcnt(4)\n" \
  "s_waitcnt lgkmcnt(0)\n" \
  "s_barrier\n" \
  "ds_read_b64_tr_b16 v[32:33], %[wr] offset:0\n" \
  "ds_read_b64_tr_b16 v[34:35], %[wr] offset:1024\n" \
  "ds_read_b64_tr_b16 v[36:37], %[wr] offset:8192\n" \
  "ds_read_b64_tr_b16 v[38:39], %[wr] offset:9216\n" \
  "ds_read_b64_tr_b16 v[40:41], v65 offset:0\n" \
  "ds_read_b64_tr_b16 v[42:43], v65 offset:1024\n" \
  "ds_read_b64_tr_b16 v[44:45], v65 offset:8192\n" \
  "ds_read_b64_tr_b16 v[46:47], v65 offset:9216\n" \
  "s_waitcnt vmcnt(0)\n" \
  "s_waitcnt lgkmcnt(0)\n" \
  "s_barrier\n" \
  "ds_read_b64_tr_b16 v[48:49], %[wr] offset:16384\n" \
  "ds_read_b64_tr_b16 v[50:51], %[wr] offset:17408\n" \
  "ds_read_b64_tr_b16 v[52:53], %[wr] offset:24576\n" \
  "ds_read_b64_tr_b16 v[54:55], %[wr] offset:25600\n" \
  "ds_read_b64_tr_b16 v[56:57], v65 offset:16384\n" \
  "ds_read_b64_tr_b16 v[58:59], v65 offset:17408\n" \
  "ds_read_b64_tr_b16 v[60:61], v65 offset:24576\n" \
  "ds_read_b64_tr_b16 v[62:63], v65 offset:25600\n" \
  "s_waitcnt lgkmcnt(0)\n" \
  "s_barrier\n" \
  "s_mov_b32 s60, -2\n" \
  "s_mov_b32 s70, 0x3db504f3\n" \
  "s_mov_b32 s71, 0x3e0293ee\n" \
  "v_add_u32 v228, 0x1000, %[mo]\n" \
  "v_mov_b32 v230, 0x7fffffff\n" \
  "v_mov_b32 v232, 0xff800000\n" \
  "s_mov_b32 s78, 0\n" \
  "s_mov_b32 s79, 0\n" \
  "s_mov_b32 s80, 0\n" \
  "s_mov_b32 s65, 2.0\n" \
  "s_mov_b32 s81, -1\n" \
  "s_mov_b32 s87, 2.0\n" \
  "s_mov_b32 s93, 0\n" \
  "s_mov_b32 s94, 0\n" \
  "s_lshl_b32 s96, s60, 4\n" \
  "s_add_i32 s96, s96, %[bas]\n" \
  "v_cmp_lt_i32_e64 vcc, %[lim], 0\n" \
  "s_nop 1\n" \
  "v_cndmask_b32 v212, 0, v232, vcc\n" \
  "v_cmp_lt_i32_e64 vcc, %[lim], 1\n" \
  "s_nop 1\n" \
  "v_cndmask_b32 v213, 0, v232, vcc\n" \
  "v_cmp_lt_i32_e64 vcc, %[lim], 2\n" \
  "s_nop 1\n" \
  "v_cndmask_b32 v214, 0, v232, vcc\n" \
  "v_cmp_lt_i32_e64 vcc, %[lim], 3\n" \
  "s_nop 1\n" \
  "v_cndmask_b32 v215, 0, v232, vcc\n" \
  "v_cmp_lt_i32_e64 vcc, %[lim], 16\n" \
  "s_nop 1\n" \
  "v_cndmask_b32 v216, 0, v232, vcc\n" \
  "v_cmp_lt_i32_e64 vcc, %[lim], 17\n" \
  "s_nop 1\n" \
  "v_cndmask_b32 v217, 0, v232, vcc\n" \
  "v_cmp_lt_i32_e64 vcc, %[lim], 18\n" \
  "s_nop 1\n" \
  "v_cndmask_b32 v218, 0, v232, vcc\n" \
  "v_cmp_lt_i32_e64 vcc, %[lim], 19\n" \
  "s_nop 1\n" \
  "v_cndmask_b32 v219, 0, v232, vcc\n" \
  "s_mov_b32 s90, 0\n" \
  "s_waitcnt vmcnt(0)\n" \
  ".Lqk_top:\n" \
  "v_mfma_f32_16x16x32_bf16 v[116:119], v[164:167], a[32:35], v[116:119]\n" \
  "ds_read_b128 v[148:151], %[hA] offset:2048\n" \
  "buffer_store_dwordx2 v[232:233], %[po3], %[X], s65 offen sc1\n" \
  "v_mfma_f32_16x16x32_bf16 v[132:135], v[168:171], a[32:35], v[132:135]\n" \
  "ds_read_b128 v[152:155], %[hA] offset:20480\n" \
  "v_mfma_f32_16x16x32_bf16 v[120:123], v[164:167], a[40:43], v[120:123]\n" \
  "buffer_load_dword v251, %[cpo], %[X], s94 offen sc1\n" \
  "s_xor_b32 s76, s96, 0x2000\n" \
  "v_mfma_f32_16x16x32_bf16 v[136:139], v[168:171], a[40:43], v[136:139]\n" \
  "buffer_load_dword v255, %[cpa], %[X], s76 offen sc1\n" \
  "v_mfma_f32_16x16x32_bf16 v[124:127], a[48:51], v[164:167], v[124:127]\n" \
  "s_add_i32 s67, s60, 2\n" \
  "s_min_i32 s67, s67, 63\n" \
  "s_mul_i32 s61, s67, 36864\n" \
  "v_mfma_f32_16x16x32_bf16 v[140:143], a[48:51], v[168:171], v[140:143]\n" \
  "s_lshl_b32 s67, s60, 16\n" \
  "s_and_b32 s67, s67, 0x10000\n" \
  "s_add_i32 s62, %[hl0], s67\n" \
  "v_mfma_f32_16x16x32_bf16 v[128:131], a[56:59], v[164:167], v[128:131]\n" \
  "s_add_i32 m0, s62, 0\n" \
  "v_mfma_f32_16x16x32_bf16 v[144:147], a[56:59], v[168:171], v[144:147]\n" \
  "s_add_i32 s68, s60, 1\n" \
  "s_lshl_b32 s73, s68, 17\n" \
  "s_and_b32 s73, s73, 0x60000\n" \
  "v_mfma_f32_16x16x32_bf16 v[116:119], v[172:175], a[36:39], v[116:119]\n" \
  "ds_read_b128 v[156:159], %[hA] offset:2560\n" \
  "buffer_load_dword v250, off, %[X], s96 offset:16 sc1\n" \
  "v_mfma_f32_16x16x32_bf16 v[132:135], v[176:179], a[36:39], v[132:135]\n" \
  "s_add_i32 s63, s87, s73\n" \
  "s_add_i32 s64, s63, 0x4000\n" \
  "ds_read_b128 v[160:163], %[hA] offset:20992\n" \
  "v_mfma_f32_16x16x32_bf16 v[120:123], v[172:175], a[44:47], v[120:123]\n" \
  "buffer_load_dwordx4 %[hvo], %[HS], s61 offen offset:0 sc1 lds\n" \
  "v_mov_b64 v[100:101], 0\n" \
  "v_mfma_f32_16x16x32_bf16 v[136:139], v[176:179], a[44:47], v[136:139]\n" \
  "s_and_b32 s73, s73, 0x20000\n" \
  "s_add_i32 s65, %[pm0], s73\n" \
  "s_lshl_b32 s73, s68, 30\n" \
  "v_mfma_f32_16x16x32_bf16 v[124:127], a[52:55], v[172:175], v[124:127]\n" \
  "s_and_b32 s66, s73, 0x80000000\n" \
  "s_cmp_lt_u32 s68, 64\n" \
  "s_cselect_b32 s65, s65, 2.0\n" \
  "v_mfma_f32_16x16x32_bf16 v[140:143], a[52:55], v[176:179], v[140:143]\n" \
  "s_ashr_i32 s67, s66, 31\n" \
  "v_not_b32 v232, s67\n" \
  "v_mov_b32 v233, s90\n" \
  "v_mfma_f32_16x16x32_bf16 v[128:131], a[60:63], v[172:175], v[128:131]\n" \
  "buffer_load_dwordx4 %[hvo], %[HS], s61 offen offset:1024 sc1 lds\n" \
  "v_mov_b64 v[102:103], 0\n" \
  "v_mfma_f32_16x16x32_bf16 v[144:147], a[60:63], v[176:179], v[144:147]\n" \
  "v_mov_b64 v[104:105], 0\n" \
  "s_waitcnt lgkmcnt(2)\n" \
  "v_mfma_f32_16x16x32_bf16 v[116:119], v[148:151], a[64:67], v[116:119]\n" \
  "ds_read_b128 v[164:167], %[hA] offset:3072\n" \
  "v_mov_b64 v[106:107], 0\n" \
  "v_mfma_f32_16x16x32_bf16 v[132:135], v[152:155], a[64:67], v[132:135]\n" \
  "ds_read_b128 v[168:171], %[hA] offset:21504\n" \
  "v_mov_b64 v[108:109], 0\n" \
  "v_mfma_f32_16x16x32_bf16 v[120:123], v[148:151], a[72:75], v[120:123]\n" \
  "buffer_load_dwordx4 %[hvo], %[HS], s61 offen offset:2048 sc1 lds\n" \
  "v_mov_b64 v[110:111], 0\n" \
  "v_mfma_f32_16x16x32_bf16 v[136:139], v[152:155], a[72:75], v[136:139]\n" \
  "v_mov_b64 v[112:113], 0\n" \
  "v_mov_b64 v[114:115], 0\n" \
  "v_mfma_f32_16x16x32_bf16 v[124:127], a[80:83], v[148:151], v[124:127]\n" \
  "v_mfma_f32_16x16x32_bf16 v[140:143], a[80:83], v[152:155], v[140:143]\n" \
  "v_mfma_f32_16x16x32_bf16 v[128:131], a[88:91], v[148:151], v[128:131]\n" \
  "buffer_load_dwordx4 %[hvo], %[HS], s61 offen offset:3072 sc1 lds\n" \
  "v_mfma_f32_16x16x32_bf16 v[144:147], a[88:91], v[152:155], v[144:147]\n" \
  "s_add_i32 s61, s61, 0x1000\n" \
  "s_add_i32 m0, s62, 4096\n" \
  "s_waitcnt lgkmcnt(2)\n" \
  "v_mfma_f32_16x16x32_bf16 v[116:119], v[156:159], a[68:71], v[116:119]\n" \
  "ds_read_b128 v[172:175], %[hA] offset:3584\n" \
  "v_mfma_f32_16x16x32_bf16 v[132:135], v[160:163], a[68:71], v[132:135]\n" \
  "ds_read_b128 v[176:179], %[hA] offset:22016\n" \
  "v_mfma_f32_16x16x32_bf16 v[120:123], v[156:159], a[76:79], v[120:123]\n" \
  "buffer_load_dwordx4 %[hvo], %[HS], s61 offen offset:0 sc1 lds\n" \
  "v_mfma_f32_16x16x32_bf16 v[136:139], v[160:163], a[76:79], v[136:139]\n" \
  "v_mfma_f32_16x16x32_bf16 v[124:127], a[84:87], v[156:159], v[124:127]\n" \
  "v_mfma_f32_16x16x32_bf16 v[140:143], a[84:87], v[160:163], v[140:143]\n" \
  "v_mfma_f32_16x16x32_bf16 v[128:131], a[92:95], v[156:159], v[128:131]\n" \
  "buffer_load_dwordx4 %[hvo], %[HS], s61 offen offset:1024 sc1 lds\n" \
  "v_mfma_f32_16x16x32_bf16 v[144:147], a[92:95], v[160:163], v[144:147]\n" \
  "s_waitcnt lgkmcnt(2)\n" \
  "v_mfma_f32_16x16x32_bf16 v[116:119], v[164:167], a[96:99], v[116:119]\n" \
  "ds_read_b128 v[148:151], %[hA] offset:4096\n" \
  "v_mfma_f32_16x16x32_bf16 v[132:135], v[168:171], a[96:99], v[132:135]\n" \
  "ds_read_b128 v[152:155], %[hA] offset:22528\n" \
  "v_mfma_f32_16x16x32_bf16 v[120:123], v[164:167], a[104:107], v[120:123]\n" \
  "buffer_load_dwordx4 %[hvo], %[HS], s61 offen offset:2048 sc1 lds\n" \
  "v_mfma_f32_16x16x32_bf16 v[136:139], v[168:171], a[104:107], v[136:139]\n" \
  "v_mfma_f32_16x16x32_bf16 v[124:127], a[112:115], v[164:167], v[124:127]\n" \
  "v_mfma_f32_16x16x32_bf16 v[140:143], a[112:115], v[168:171], v[140:143]\n" \
  "v_mfma_f32_16x16x32_bf16 v[128:131], a[120:123], v[164:167], v[128:131]\n" \
  "buffer_load_dwordx4 %[hvo], %[HS], s61 offen offset:3072 sc1 lds\n" \
  "v_mfma_f32_16x16x32_bf16 v[144:147], a[120:123], v[168:171], v[144:147]\n" \
  "s_add_i32 s61, s61, 0x1000\n" \
  "s_add_i32 m0, s62, 8192\n" \
  "s_waitcnt lgkmcnt(2)\n" \
  "v_mfma_f32_16x16x32_bf16 v[116:119], v[172:175], a[100:103], v[116:119]\n" \
  "ds_read_b128 v[156:159], %[hA] offset:4608\n" \
  "v_mfma_f32_16x16x32_bf16 v[132:135], v[176:179], a[100:103], v[132:135]\n" \
  "ds_read_b128 v[160:163], %[hA] offset:23040\n" \
  "v_mfma_f32_16x16x32_bf16 v[120:123], v[172:175], a[108:111], v[120:123]\n" \
  "buffer_load_dwordx4 %[hvo], %[HS], s61 offen offset:0 sc1 lds\n" \
  "v_mfma_f32_16x16x32_bf16 v[136:139], v[176:179], a[108:111], v[136:139]\n" \
  "v_mfma_f32_16x16x32_bf16 v[124:127], a[116:119], v[172:175], v[124:127]\n" \
  "v_mfma_f32_16x16x32_bf16 v[140:143], a[116:119], v[176:179], v[140:143]\n" \
  "buffer_store_dword v232, %[pcl], %[X], s65 offen sc1\n" \
  "v_mfma_f32_16x16x32_bf16 v[128:131], a[124:127], v[172:175], v[128:131]\n" \
  "v_mfma_f32_16x16x32_bf16 v[144:147], a[124:127], v[176:179], v[144:147]\n" \
  "buffer_atomic_umax v233, %[rpo], %[X], s93 offen sc1\n" \
  "s_waitcnt lgkmcnt(2)\n" \
  "v_mfma_f32_16x16x32_bf16 v[116:119], v[148:151], a[128:131], v[116:119]\n" \
  "ds_read_b128 v[164:167], %[hA] offset:5120\n" \
  "v_mfma_f32_16x16x32_bf16 v[132:135], v[152:155], a[128:131], v[132:135]\n" \
  "ds_read_b128 v[168:171], %[hA] offset:23552\n" \
  "v_mfma_f32_16x16x32_bf16 v[120:123], v[148:151], a[136:139], v[120:123]\n" \
  "v_mfma_f32_16x16x32_bf16 v[136:139], v[152:155], a[136:139], v[136:139]\n" \
  "v_mfma_f32_16x16x32_bf16 v[124:127], a[144:147], v[148:151], v[124:127]\n" \
  "v_mfma_f32_16x16x32_bf16 v[140:143], a[144:147], v[152:155], v[140:143]\n" \
  "v_mfma_f32_16x16x32_bf16 v[128:131], a[152:155], v[148:151], v[128:131]\n" \
  "v_mfma_f32_16x16x32_bf16 v[144:147], a[152:155], v[152:155], v[144:147]\n" \
  "s_waitcnt lgkmcnt(2)\n" \
  "v_mfma_f32_16x16x32_bf16 v[116:119], v[156:159], a[132:135], v[116:119]\n" \
  "ds_read_b128 v[172:175], %[hA] offset:5632\n" \
  "v_mfma_f32_16x16x32_bf16 v[132:135], v[160:163], a[132:135], v[132:135]\n" \
  "ds_read_b128 v[176:179], %[hA] offset:24064\n" \
  "v_mfma_f32_16x16x32_bf16 v[120:123], v[156:159], a[140:143], v[120:123]\n" \
  "v_mfma_f32_16x16x32_bf16 v[136:139], v[160:163], a[140:143], v[136:139]\n" \
  "v_mfma_f32_16x16x32_bf16 v[124:127], a[148:151], v[156:159], v[124:127]\n" \
  "s_waitcnt vmcnt(11)\n" \
  "v_readfirstlane_b32 s72, v250\n" \
  "v_mfma_f32_16x16x32_bf16 v[140:143], a[148:151], v[160:163], v[140:143]\n" \
  "v_max_u32_dpp v251, v251, v251 quad_perm:[1,0,3,2] row_mask:0xf bank_mask:0xf\n" \
  "v_mfma_f32_16x16x32_bf16 v[128:131], a[156:159], v[156:159], v[128:131]\n" \
  "v_mfma_f32_16x16x32_bf16 v[144:147], a[156:159], v[160:163], v[144:147]\n" \
  "v_max_u32_dpp v251, v251, v251 quad_perm:[2,3,0,1] row_mask:0xf bank_mask:0xf\n" \
  "s_waitcnt lgkmcnt(2)\n" \
  "v_mfma_f32_16x16x32_bf16 v[116:119], v[164:167], a[160:163], v[116:119]\n" \
  "ds_read_b128 v[148:151], %[hA] offset:6144\n" \
  "v_mfma_f32_16x16x32_bf16 v[132:135], v[168:171], a[160:163], v[132:135]\n" \
  "ds_read_b128 v[152:155], %[hA] offset:24576\n" \
  "v_max_u32_dpp v251, v251, v251 row_half_mirror row_mask:0xf bank_mask:0xf\n" \
  "v_mfma_f32_16x16x32_bf16 v[120:123], v[164:167], a[168:171], v[120:123]\n" \
  "v_mfma_f32_16x16x32_bf16 v[136:139], v[168:171], a[168:171], v[136:139]\n" \
  "v_readfirstlane_b32 s92, v251\n" \
  "v_readfirstlane_b32 s73, v255\n" \
  "v_mfma_f32_16x16x32_bf16 v[124:127], a[176:179], v[164:167], v[124:127]\n" \
  "s_sub_i32 s92, s92, s95\n" \
  "s_add_i32 s94, s92, 1\n" \
  "s_ashr_i32 s94, s94, 2\n" \
  "v_mfma_f32_16x16x32_bf16 v[140:143], a[176:179], v[168:171], v[140:143]\n" \
  "s_max_i32 s92, s92, s94\n" \
  "s_max_i32 s92, s92, %[dn]\n" \
  "s_min_i32 s92, s92, %[up]\n" \
  "v_mfma_f32_16x16x32_bf16 v[128:131], a[184:187], v[164:167], v[128:131]\n" \
  "s_cmp_lt_i32 s60, s88\n" \
  "s_cselect_b32 s92, 0, s92\n" \
  "s_add_i32 s95, s95, s92\n" \
  "v_mfma_f32_16x16x32_bf16 v[144:147], a[184:187], v[168:171], v[144:147]\n" \
  "s_waitcnt lgkmcnt(2)\n" \
  "v_mfma_f32_16x16x32_bf16 v[116:119], v[172:175], a[164:167], v[116:119]\n" \
  "ds_read_b128 v[156:159], %[hA] offset:6656\n" \
  "v_mfma_f32_16x16x32_bf16 v[132:135], v[176:179], a[164:167], v[132:135]\n" \
  "ds_read_b128 v[160:163], %[hA] offset:25088\n" \
  "v_mfma_f32_16x16x32_bf16 v[120:123], v[172:175], a[172:175], v[120:123]\n" \
  "s_mul_i32 s76, s95, 209\n" \
  "s_lshr_b32 s76, s76, 8\n" \
  "s_add_i32 s76, s76, %[c2]\n" \
  "v_mfma_f32_16x16x32_bf16 v[136:139], v[176:179], a[172:175], v[136:139]\n" \
  "s_add_i32 s92, s95, %[mrg]\n" \
  "s_max_i32 s92, s92, s76\n" \
  "s_add_i32 s94, s72, s92\n" \
  "v_mfma_f32_16x16x32_bf16 v[124:127], a[180:183], v[172:175], v[124:127]\n" \
  "s_sub_i32 s76, s92, s76\n" \
  "s_lshr_b32 s76, s76, 1\n" \
  "s_add_i32 s76, s76, %[c4]\n" \
  "v_mfma_f32_16x16x32_bf16 v[140:143], a[180:183], v[176:179], v[140:143]\n" \
  "s_mul_i32 s92, s95, 135\n" \
  "s_lshr_b32 s92, s92, 8\n" \
  "s_add_i32 s76, s76, s92\n" \
  "v_mfma_f32_16x16x32_bf16 v[128:131], a[188:191], v[172:175], v[128:131]\n" \
  "s_add_i32 s92, s73, %[lag]\n" \
  "s_sub_i32 s92, s92, s94\n" \
  "s_max_i32 s92, s92, 0\n" \
  "v_mfma_f32_16x16x32_bf16 v[144:147], a[188:191], v[176:179], v[144:147]\n" \
  "s_cmp_lt_i32 s60, 59\n" \
  "s_cselect_b32 s77, %[gg], 0\n" \
  "s_waitcnt lgkmcnt(2)\n" \
  "v_mfma_f32_16x16x32_bf16 v[116:119], v[148:151], a[192:195], v[116:119]\n" \
  "ds_read_b128 v[164:167], %[hA] offset:7168\n" \
  "v_mfma_f32_16x16x32_bf16 v[132:135], v[152:155], a[192:195], v[132:135]\n" \
  "ds_read_b128 v[168:171], %[hA] offset:25600\n" \
  "v_mfma_f32_16x16x32_bf16 v[120:123], v[148:151], a[200:203], v[120:123]\n" \
  "s_cmp_lg_u32 s73, 0\n" \
  "s_cselect_b32 s92, s92, s77\n" \
  "s_add_i32 s86, s94, s92\n" \
  "v_mfma_f32_16x16x32_bf16 v[136:139], v[152:155], a[200:203], v[136:139]\n" \
  "s_add_i32 s76, s76, s86\n" \
  "v_or_b32_e64 v252, s86, 1\n" \
  "v_mov_b32 v253, s76\n" \
  "v_mfma_f32_16x16x32_bf16 v[124:127], a[208:211], v[148:151], v[124:127]\n" \
  "s_cmp_lt_i32 s60, 0\n" \
  "s_cselect_b32 s73, 2.0, s96\n" \
  "v_mfma_f32_16x16x32_bf16 v[140:143], a[208:211], v[152:155], v[140:143]\n" \
  "v_mfma_f32_16x16x32_bf16 v[128:131], a[216:219], v[148:151], v[128:131]\n" \
  "v_mfma_f32_16x16x32_bf16 v[144:147], a[216:219], v[152:155], v[144:147]\n" \
  "s_waitcnt lgkmcnt(2)\n" \
  "v_mfma_f32_16x16x32_bf16 v[116:119], v[156:159], a[196:199], v[116:119]\n" \
  "ds_read_b128 v[172:175], %[hA] offset:7680\n" \
  "v_mfma_f32_16x16x32_bf16 v[132:135], v[160:163], a[196:199], v[132:135]\n" \
  "ds_read_b128 v[176:179], %[hA] offset:26112\n" \
  "v_mfma_f32_16x16x32_bf16 v[120:123], v[156:159], a[204:207], v[120:123]\n" \
  "ds_read_b128 v[148:151], %[hA] offset:8192\n" \
  "v_mfma_f32_16x16x32_bf16 v[136:139], v[160:163], a[204:207], v[136:139]\n" \
  "ds_read_b128 v[152:155], %[hA] offset:26624\n" \
  "v_mfma_f32_16x16x32_bf16 v[124:127], a[212:215], v[156:159], v[124:127]\n" \
  "v_mfma_f32_16x16x32_bf16 v[140:143], a[212:215], v[160:163], v[140:143]\n" \
  "v_mfma_f32_16x16x32_bf16 v[128:131], a[220:223], v[156:159], v[128:131]\n" \
  "v_mfma_f32_16x16x32_bf16 v[144:147], a[220:223], v[160:163], v[144:147]\n" \
  "s_memrealtime s[74:75]\n" \
  "s_waitcnt lgkmcnt(4)\n" \
  "v_mfma_f32_16x16x32_bf16 v[124:127], a[240:243], v[164:167], v[124:127]\n" \
  "ds_read_b128 v[156:159], %[hA] offset:8704\n" \
  "v_mfma_f32_16x16x32_bf16 v[140:143], a[240:243], v[168:171], v[140:143]\n" \
  "ds_read_b128 v[160:163], %[hA] offset:27136\n" \
  "v_mfma_f32_16x16x32_bf16 v[128:131], a[248:251], v[164:167], v[128:131]\n" \
  "v_mfma_f32_16x16x32_bf16 v[144:147], a[248:251], v[168:171], v[144:147]\n" \
  "s_waitcnt lgkmcnt(0)\n" \
  "s_sub_i32 s91, s74, s72\n" \
  "s_cmp_gt_u32 s91, 14\n" \
  "v_mfma_f32_16x16x32_bf16 v[124:127], a[244:247], v[172:175], v[124:127]\n" \
  "s_cbranch_scc1 .Lqk_late\n" \
  ".Lqk_al_ok:\n" \
  "v_mfma_f32_16x16x32_bf16 v[140:143], a[244:247], v[176:179], v[140:143]\n" \
  "buffer_load_dwordx4 v[64:67], %[mo], %[X], s63 offen offset:0 sc1\n" \
  "v_mfma_f32_16x16x32_bf16 v[128:131], a[252:255], v[172:175], v[128:131]\n" \
  "v_mfma_f32_16x16x32_bf16 v[144:147], a[252:255], v[176:179], v[144:147]\n" \
  "v_mfma_f32_16x16x32_bf16 v[124:127], v[48:51], v[148:151], v[124:127]\n" \
  "buffer_load_dwordx4 v[68:71], %[mo], %[X], s63 offen offset:1024 sc1\n" \
  "v_mfma_f32_16x16x32_bf16 v[140:143], v[48:51], v[152:155], v[140:143]\n" \
  "v_mfma_f32_16x16x32_bf16 v[128:131], v[56:59], v[148:151], v[128:131]\n" \
  "v_mfma_f32_16x16x32_bf16 v[144:147], v[56:59], v[152:155], v[144:147]\n" \
  "buffer_load_dwordx4 v[72:75], %[mo], %[X], s63 offen offset:2048 sc1\n" \
  "v_mfma_f32_16x16x32_bf16 v[124:127], v[52:55], v[156:159], v[124:127]\n" \
  "v_mfma_f32_16x16x32_bf16 v[140:143], v[52:55], v[160:163], v[140:143]\n" \
  "v_mfma_f32_16x16x32_bf16 v[128:131], v[60:63], v[156:159], v[128:131]\n" \
  "buffer_load_dwordx4 v[76:79], %[mo], %[X], s63 offen offset:3072 sc1\n" \
  "v_mfma_f32_16x16x32_bf16 v[144:147], v[60:63], v[160:163], v[144:147]\n" \
  "v_mfma_f32_16x16x32_bf16 v[116:119], v[164:167], a[224:227], v[116:119]\n" \
  "v_mfma_f32_16x16x32_bf16 v[132:135], v[168:171], a[224:227], v[132:135]\n" \
  "buffer_load_dwordx4 v[80:83], v228, %[X], s63 offen offset:0 sc1\n" \
  "v_mfma_f32_16x16x32_bf16 v[120:123], v[164:167], a[232:235], v[120:123]\n" \
  "v_cvt_pk_bf16_f32 v220, v124, v125\n" \
  "v_cvt_pk_bf16_f32 v221, v126, v127\n" \
  "v_mfma_f32_16x16x32_bf16 v[136:139], v[168:171], a[232:235], v[136:139]\n" \
  "v_cvt_pk_bf16_f32 v222, v128, v129\n" \
  "v_cvt_pk_bf16_f32 v223, v130, v131\n" \
  "v_mfma_f32_16x16x32_bf16 v[116:119], v[172:175], a[228:231], v[116:119]\n" \
  "buffer_load_dwordx4 v[84:87], v228, %[X], s63 offen offset:1024 sc1\n" \
  "v_cvt_pk_bf16_f32 v224, v140, v141\n" \
  "v_mfma_f32_16x16x32_bf16 v[132:135], v[176:179], a[228:231], v[132:135]\n" \
  "ds_write_b128 %[ktW], v[220:223] offset:0\n" \
  "v_mfma_f32_16x16x32_bf16 v[120:123], v[172:175], a[236:239], v[120:123]\n" \
  "v_cvt_pk_bf16_f32 v225, v142, v143\n" \
  "v_cvt_pk_bf16_f32 v226, v144, v145\n" \
  "v_mfma_f32_16x16x32_bf16 v[136:139], v[176:179], a[236:239], v[136:139]\n" \
  "buffer_load_dwordx4 v[88:91], v228, %[X], s63 offen offset:2048 sc1\n" \
  "v_cvt_pk_bf16_f32 v227, v146, v147\n" \
  "v_mfma_f32_16x16x32_bf16 v[116:119], v[148:151], v[32:35], v[116:119]\n" \
  "ds_write_b128 %[ktW], v[224:227] offset:4096\n" \
  "v_mfma_f32_16x16x32_bf16 v[132:135], v[152:155], v[32:35], v[132:135]\n" \
  "v_add_f32 v231, v124, v140\n" \
  "v_xor_b32 %[hA], 0x10000, %[hA]\n" \
  "v_mfma_f32_16x16x32_bf16 v[120:123], v[148:151], v[40:43], v[120:123]\n" \
  "buffer_load_dwordx4 v[92:95], v228, %[X], s63 offen offset:3072 sc1\n" \
  "v_mfma_f32_16x16x32_bf16 v[136:139], v[152:155], v[40:43], v[136:139]\n" \
  "v_mfma_f32_16x16x32_bf16 v[116:119], v[156:159], v[36:39], v[116:119]\n" \
  "s_waitcnt vmcnt(10)\n" \
  "s_waitcnt lgkmcnt(0)\n" \
  "s_barrier\n" \
  "v_mfma_f32_16x16x32_bf16 v[132:135], v[160:163], v[36:39], v[132:135]\n" \
  "buffer_load_dwordx4 v[96:99], %[mo], %[X], s64 offen offset:0 sc1\n" \
  "ds_read_b128 v[180:183], %[ktR] offset:0\n" \
  "v_mfma_f32_16x16x32_bf16 v[120:123], v[156:159], v[44:47], v[120:123]\n" \
  "ds_read_b128 v[184:187], %[ktR] offset:1024\n" \
  "v_mfma_f32_16x16x32_bf16 v[136:139], v[160:163], v[44:47], v[136:139]\n" \
  "ds_read_b128 v[188:191], %[ktR] offset:2048\n" \
  ".Lqk_m:\n" \
  "s_waitcnt vmcnt(8)\n" \
  "v_smfmac_f32_16x16x64_bf16 v[100:103], v[64:67], a[0:7], %[idx]\n" \
  "ds_read_b128 v[192:195], %[ktR] offset:3072\n" \
  "v_smfmac_f32_16x16x64_bf16 v[104:107], v[64:67], a[8:15], %[idx]\n" \
  "ds_read_b128 v[196:199], %[ktR] offset:4096\n" \
  "v_smfmac_f32_16x16x64_bf16 v[108:111], v[64:67], a[16:23], %[idx]\n" \
  "ds_read_b128 v[200:203], %[ktR] offset:5120\n" \
  "v_cvt_pk_bf16_f32 v220, v116, v117\n" \
  "v_smfmac_f32_16x16x64_bf16 v[112:115], v[64:67], a[24:31], %[idx]\n" \
  "ds_read_b128 v[204:207], %[ktR] offset:6144\n" \
  "s_waitcnt vmcnt(7)\n" \
  "v_smfmac_f32_16x16x64_bf16 v[100:103], v[68:71], a[32:39], %[idx]\n" \
  "ds_read_b128 v[208:211], %[ktR] offset:7168\n" \
  "v_cvt_pk_bf16_f32 v221, v118, v119\n" \
  "v_smfmac_f32_16x16x64_bf16 v[104:107], v[68:71], a[40:47], %[idx]\n" \
  "ds_read_b128 v[164:167], %[hA] offset:1024\n" \
  "v_cvt_pk_bf16_f32 v222, v120, v121\n" \
  "v_smfmac_f32_16x16x64_bf16 v[108:111], v[68:71], a[48:55], %[idx]\n" \
  "ds_read_b128 v[168:171], %[hA] offset:19456\n" \
  "v_cvt_pk_bf16_f32 v223, v122, v123\n" \
  "v_smfmac_f32_16x16x64_bf16 v[112:115], v[68:71], a[56:63], %[idx]\n" \
  "ds_read_b128 v[172:175], %[hA] offset:1536\n" \
  "s_waitcnt vmcnt(6)\n" \
  "v_smfmac_f32_16x16x64_bf16 v[100:103], v[72:75], a[64:71], %[idx]\n" \
  "ds_read_b128 v[176:179], %[hA] offset:19968\n" \
  "v_cvt_pk_bf16_f32 v224, v132, v133\n" \
  "v_smfmac_f32_16x16x64_bf16 v[104:107], v[72:75], a[72:79], %[idx]\n" \
  "ds_read_b128 v[148:151], %[hA] offset:0\n" \
  "v_cvt_pk_bf16_f32 v225, v134, v135\n" \
  "v_smfmac_f32_16x16x64_bf16 v[108:111], v[72:75], a[80:87], %[idx]\n" \
  "ds_read_b128 v[152:155], %[hA] offset:18432\n" \
  "v_cvt_pk_bf16_f32 v226, v136, v137\n" \
  "v_smfmac_f32_16x16x64_bf16 v[112:115], v[72:75], a[88:95], %[idx]\n" \
  "ds_read_b128 v[156:159], %[hA] offset:512\n" \
  "s_waitcnt vmcnt(5)\n" \
  "v_smfmac_f32_16x16x64_bf16 v[100:103], v[76:79], a[96:103], %[idx]\n" \
  "ds_read_b128 v[160:163], %[hA] offset:18944\n" \
  "v_cvt_pk_bf16_f32 v227, v138, v139\n" \
  "v_smfmac_f32_16x16x64_bf16 v[104:107], v[76:79], a[104:111], %[idx]\n" \
  "ds_write_b64 %[qtW], v[220:221] offset:0\n" \
  "v_smfmac_f32_16x16x64_bf16 v[108:111], v[76:79], a[112:119], %[idx]\n" \
  "ds_write_b64 %[qtW], v[222:223] offset:2816\n" \
  "v_smfmac_f32_16x16x64_bf16 v[112:115], v[76:79], a[120:127], %[idx]\n" \
  "s_waitcnt vmcnt(4)\n" \
  "v_smfmac_f32_16x16x64_bf16 v[100:103], v[80:83], a[128:135], %[idx]\n" \
  "ds_write_b64 %[qtW], v[224:225] offset:64\n" \
  "v_smfmac_f32_16x16x64_bf16 v[104:107], v[80:83], a[136:143], %[idx]\n" \
  "ds_write_b64 %[qtW], v[226:227] offset:2880\n" \
  "v_smfmac_f32_16x16x64_bf16 v[108:111], v[80:83], a[144:151], %[idx]\n" \
  "s_add_i32 s60, s60, 1\n" \
  "s_add_i32 s96, s96, 16\n" \
  "s_mov_b32 s94, s93\n" \
  "v_smfmac_f32_16x16x64_bf16 v[112:115], v[80:83], a[152:159], %[idx]\n" \
  "s_waitcnt vmcnt(3)\n" \
  "v_smfmac_f32_16x16x64_bf16 v[100:103], v[84:87], a[160:167], %[idx]\n" \
  "s_sub_i32 s90, s74, s89\n" \
  "s_max_i32 s92, s91, -2\n" \
  "s_add_i32 s89, s72, s92\n" \
  "v_smfmac_f32_16x16x64_bf16 v[104:107], v[84:87], a[168:175], %[idx]\n" \
  "s_ashr_i32 s92, s92, 1\n" \
  "s_add_i32 s90, s90, s92\n" \
  "v_smfmac_f32_16x16x64_bf16 v[108:111], v[84:87], a[176:183], %[idx]\n" \
  "s_mul_i32 s93, s60, 130\n" \
  "s_and_b32 s93, s93, 0xfc\n" \
  "v_smfmac_f32_16x16x64_bf16 v[112:115], v[84:87], a[184:191], %[idx]\n" \
  "s_waitcnt vmcnt(2)\n" \
  "v_smfmac_f32_16x16x64_bf16 v[100:103], v[88:91], a[192:199], %[idx]\n" \
  "v_smfmac_f32_16x16x64_bf16 v[104:107], v[88:91], a[200:207], %[idx]\n" \
  "v_smfmac_f32_16x16x64_bf16 v[108:111], v[88:91], a[208:215], %[idx]\n" \
  "v_smfmac_f32_16x16x64_bf16 v[112:115], v[88:91], a[216:223], %[idx]\n" \
  "s_waitcnt vmcnt(1)\n" \
  "v_smfmac_f32_16x16x64_bf16 v[100:103], v[92:95], a[224:231], %[idx]\n" \
  "v_smfmac_f32_16x16x64_bf16 v[104:107], v[92:95], a[232:239], %[idx]\n" \
  "s_waitcnt vmcnt(0)\n" \
  "v_smfmac_f32_16x16x64_bf16 v[100:103], v[96:99], v[32:39], %[idx]\n" \
  "v_smfmac_f32_16x16x64_bf16 v[104:107], v[96:99], v[40:47], %[idx]\n" \
  "s_nop 1\n" \
  "v_smfmac_f32_16x16x64_bf16 v[108:111], v[92:95], a[240:247], %[idx]\n" \
  "s_nop 1\n" \
  "v_smfmac_f32_16x16x64_bf16 v[112:115], v[92:95], a[248:255], %[idx]\n" \
  "s_waitcnt lgkmcnt(4)\n" \
  "s_nop 0\n" \
  "v_add_f32 v232, v100, v102\n" \
  "v_smfmac_f32_16x16x64_bf16 v[108:111], v[96:99], v[48:55], %[idx]\n" \
  "v_add_f32 v233, v101, v103\n" \
  "v_smfmac_f32_16x16x64_bf16 v[112:115], v[96:99], v[56:63], %[idx]\n" \
  "v_add_f32 v234, v104, v106\n" \
  "v_add_f32 v235, v105, v107\n" \
  "v_cvt_pk_bf16_f32 v236, v232, v233\n" \
  "v_cvt_pk_bf16_f32 v237, v234, v235\n" \
  "ds_write2st64_b32 %[qW], v236, v237 offset1:11\n" \
  "s_nop 0\n" \
  "v_pk_add_f32 v[232:233], v[108:109], v[110:111]\n" \
  "s_nop 0\n" \
  "v_pk_add_f32 v[234:235], v[112:113], v[114:115]\n" \
  "v_cvt_pk_bf16_f32 v236, v232, v233\n" \
  "v_cvt_pk_bf16_f32 v237, v234, v235\n" \
  "ds_write2st64_b32 %[kW], v236, v237 offset1:11\n" \
  "v_fma_f32 v232, v232, v233, v231\n" \
  "v_cmp_u_f32 vcc, v232, v232\n" \
  "v_mfma_f32_16x16x32_bf16 v[116:119], v[148:151], a[0:3], 0\n" \
  "v_mfma_f32_16x16x32_bf16 v[132:135], v[152:155], a[0:3], 0\n" \
  "s_cbranch_vccnz .Lqk_sm\n" \
  ".Lqk_sm_back:\n" \
  "s_waitcnt lgkmcnt(0)\n" \
  "s_barrier\n" \
  "ds_read_b64_tr_b16 v[64:65], %[qR] offset:0\n" \
  "ds_read_b64_tr_b16 v[66:67], %[qR] offset:2816\n" \
  "v_mfma_f32_16x16x32_bf16 v[120:123], v[148:151], a[8:11], 0\n" \
  "ds_read_b64_tr_b16 v[68:69], %[qR] offset:5632\n" \
  "ds_read_b64_tr_b16 v[70:71], %[qR] offset:8448\n" \
  "v_mfma_f32_16x16x32_bf16 v[136:139], v[152:155], a[8:11], 0\n" \
  "ds_read_b64_tr_b16 v[72:73], %[qR] offset:11264\n" \
  "ds_read_b64_tr_b16 v[74:75], %[qR] offset:14080\n" \
  "v_mfma_f32_16x16x32_bf16 v[124:127], a[16:19], v[148:151], 0\n" \
  "ds_read_b64_tr_b16 v[76:77], %[qR] offset:16896\n" \
  "ds_read_b64_tr_b16 v[78:79], %[qR] offset:19712\n" \
  "s_waitcnt lgkmcnt(6)\n" \
  "v_mfma_f32_16x16x32_bf16 v[100:103], v[180:183], v[64:67], v[212:215]\n" \
  "ds_read_b64_tr_b16 v[80:81], %[kR] offset:0\n" \
  "ds_read_b64_tr_b16 v[82:83], %[kR] offset:2816\n" \
  "s_waitcnt lgkmcnt(6)\n" \
  "v_mfma_f32_16x16x32_bf16 v[100:103], v[184:187], v[68:71], v[100:103]\n" \
  "ds_read_b64_tr_b16 v[84:85], %[kR] offset:5632\n" \
  "ds_read_b64_tr_b16 v[86:87], %[kR] offset:8448\n" \
  "s_waitcnt lgkmcnt(6)\n" \
  "v_mfma_f32_16x16x32_bf16 v[100:103], v[188:191], v[72:75], v[100:103]\n" \
  "ds_read_b64_tr_b16 v[88:89], %[kR] offset:11264\n" \
  "ds_read_b64_tr_b16 v[90:91], %[kR] offset:14080\n" \
  "s_waitcnt lgkmcnt(6)\n" \
  "v_mfma_f32_16x16x32_bf16 v[100:103], v[192:195], v[76:79], v[100:103]\n" \
  "ds_read_b64_tr_b16 v[92:93], %[kR] offset:16896\n" \
  "ds_read_b64_tr_b16 v[94:95], %[kR] offset:19712\n" \
  "v_mfma_f32_16x16x32_bf16 v[104:107], v[196:199], v[64:67], v[216:219]\n" \
  "buffer_store_dwordx2 v[252:253], %[cpw], %[X], s73 offen offset:32 sc1\n" \
  "v_mfma_f32_16x16x32_bf16 v[104:107], v[200:203], v[68:71], v[104:107]\n" \
  "v_mfma_f32_16x16x32_bf16 v[104:107], v[204:207], v[72:75], v[104:107]\n" \
  "v_mfma_f32_16x16x32_bf16 v[104:107], v[208:211], v[76:79], v[104:107]\n" \
  "s_waitcnt lgkmcnt(6)\n" \
  "v_mfma_f32_16x16x32_bf16 v[108:111], v[80:83], v[64:67], 0\n" \
  "s_waitcnt lgkmcnt(4)\n" \
  "v_mfma_f32_16x16x32_bf16 v[108:111], v[84:87], v[68:71], v[108:111]\n" \
  "v_max3_f32 v248, v100, v101, v102\n" \
  "s_waitcnt lgkmcnt(2)\n" \
  "v_mfma_f32_16x16x32_bf16 v[108:111], v[88:91], v[72:75], v[108:111]\n" \
  "s_waitcnt lgkmcnt(0)\n" \
  "v_mfma_f32_16x16x32_bf16 v[108:111], v[92:95], v[76:79], v[108:111]\n" \
  "v_max3_f32 v249, v103, v104, v105\n" \
  "v_max3_f32 v248, v248, v106, v107\n" \
  "v_mfma_f32_16x16x32_bf16 v[140:143], a[16:19], v[152:155], 0\n" \
  "s_nop 4\n" \
  "v_max3_f32 v249, v249, v108, v109\n" \
  "v_max_f32 v232, v248, v249\n" \
  "v_max_f32 v233, v248, v249\n" \
  "v_mfma_f32_16x16x32_bf16 v[128:131], a[24:27], v[148:151], 0\n" \
  "s_nop 0\n" \
  "v_permlane16_swap_b32 v232, v233\n" \
  "v_max_f32 v248, v232, v233\n" \
  "v_max_f32 v249, v232, v233\n" \
  "v_mfma_f32_16x16x32_bf16 v[144:147], a[24:27], v[152:155], 0\n" \
  "s_nop 0\n" \
  "v_permlane32_swap_b32 v248, v249\n" \
  "v_max_f32 v248, v248, v249\n" \
  "v_mul_f32_e64 v248, -s71, v248\n" \
  "v_pk_fma_f32 v[100:101], v[100:101], s[70:71], v[248:249] op_sel:[0,1,0] op_sel_hi:[1,1,0]\n" \
  "v_pk_fma_f32 v[102:103], v[102:103], s[70:71], v[248:249] op_sel:[0,1,0] op_sel_hi:[1,1,0]\n" \
  "v_pk_fma_f32 v[104:105], v[104:105], s[70:71], v[248:249] op_sel:[0,1,0] op_sel_hi:[1,1,0]\n" \
  "v_pk_fma_f32 v[106:107], v[106:107], s[70:71], v[248:249] op_sel:[0,1,0] op_sel_hi:[1,1,0]\n" \
  "v_pk_fma_f32 v[108:109], v[108:109], s[70:71], v[248:249] op_sel:[0,1,0] op_sel_hi:[1,1,0]\n" \
  "v_exp_f32 v100, v100\n" \
  "v_exp_f32 v101, v101\n" \
  "v_exp_f32 v102, v102\n" \
  "v_exp_f32 v103, v103\n" \
  "v_exp_f32 v104, v104\n" \
  "v_exp_f32 v105, v105\n" \
  "v_exp_f32 v106, v106\n" \
  "v_exp_f32 v107, v107\n" \
  "v_exp_f32 v108, v108\n" \
  "v_exp_f32 v109, v109\n" \
  "v_pk_add_f32 v[248:249], v[100:101], v[102:103]\n" \
  "v_pk_add_f32 v[232:233], v[104:105], v[106:107]\n" \
  "v_pk_add_f32 v[248:249], v[248:249], v[108:109]\n" \
  "v_pk_add_f32 v[248:249], v[248:249], v[232:233]\n" \
  "v_add_f32 v232, v248, v249\n" \
  "v_add_f32 v233, v248, v249\n" \
  "v_mfma_f32_16x16x32_bf16 v[116:119], v[156:159], a[4:7], v[116:119]\n" \
  "s_nop 0\n" \
  "v_permlane16_swap_b32 v232, v233\n" \
  "v_add_f32 v248, v232, v233\n" \
  "v_add_f32 v249, v232, v233\n" \
  "v_mfma_f32_16x16x32_bf16 v[132:135], v[160:163], a[4:7], v[132:135]\n" \
  "s_nop 0\n" \
  "v_permlane32_swap_b32 v248, v249\n" \
  "v_add_f32 v248, v248, v249\n" \
  "v_rcp_f32 v248, v248\n" \
  "s_cmp_lg_u32 s60, s81\n" \
  "v_bfi_b32 v248, v230, v248, s66\n" \
  "v_pk_mul_f32 v[100:101], v[248:249], v[100:101] op_sel_hi:[0,1]\n" \
  "v_pk_mul_f32 v[102:103], v[248:249], v[102:103] op_sel_hi:[0,1]\n" \
  "v_pk_mul_f32 v[104:105], v[248:249], v[104:105] op_sel_hi:[0,1]\n" \
  "v_pk_mul_f32 v[106:107], v[248:249], v[106:107] op_sel_hi:[0,1]\n" \
  "v_pk_mul_f32 v[108:109], v[248:249], v[108:109] op_sel_hi:[0,1]\n" \
  "v_cvt_pk_bf16_f32 v232, v100, v101\n" \
  "v_cvt_pk_bf16_f32 v233, v102, v103\n" \
  "v_cvt_pk_bf16_f32 v234, v104, v105\n" \
  "v_cvt_pk_bf16_f32 v235, v106, v107\n" \
  "v_cvt_pk_bf16_f32 v236, v108, v109\n" \
  "buffer_store_dwordx4 v[232:235], %[po1], %[X], s65 offen sc1\n" \
  "v_mfma_f32_16x16x32_bf16 v[120:123], v[156:159], a[12:15], v[120:123]\n" \
  "v_mfma_f32_16x16x32_bf16 v[136:139], v[160:163], a[12:15], v[136:139]\n" \
  "buffer_store_dword v236, %[po2], %[X], s65 offen sc1\n" \
  "v_mfma_f32_16x16x32_bf16 v[124:127], a[20:23], v[156:159], v[124:127]\n" \
  "v_mfma_f32_16x16x32_bf16 v[140:143], a[20:23], v[160:163], v[140:143]\n" \
  "v_mfma_f32_16x16x32_bf16 v[128:131], a[28:31], v[156:159], v[128:131]\n" \
  "v_mfma_f32_16x16x32_bf16 v[144:147], a[28:31], v[160:163], v[144:147]\n" \
  "s_cbranch_scc1 .Lqk_top\n" \
  "s_cmp_eq_u32 s60, -1\n" \
  "s_cbranch_scc1 .Lqk_entry\n" \
  "buffer_store_dwordx2 v[232:233], %[po3], %[X], s65 offen sc1\n" \
  "s_branch .Lqk_end\n" \
  ".Lqk_sm:\n" \
  "s_cmp_lt_i32 s60, 0\n" \
  "s_cbranch_scc1 .Lqk_sm_back\n" \
  "v_cmp_u_f32 vcc, v231, v231\n" \
  "s_cbranch_vccnz .Lqk_sh\n" \
  "s_mov_b32 s69, 0\n" \
  ".Lqk_smw:\n" \
  "buffer_load_dwordx4 v[64:67], %[mo], %[X], s63 offen offset:0 sc1\n" \
  "buffer_load_dwordx4 v[68:71], %[mo], %[X], s63 offen offset:1024 sc1\n" \
  "buffer_load_dwordx4 v[72:75], %[mo], %[X], s63 offen offset:2048 sc1\n" \
  "buffer_load_dwordx4 v[76:79], %[mo], %[X], s63 offen offset:3072 sc1\n" \
  "buffer_load_dwordx4 v[80:83], v228, %[X], s63 offen offset:0 sc1\n" \
  "buffer_load_dwordx4 v[84:87], v228, %[X], s63 offen offset:1024 sc1\n" \
  "buffer_load_dwordx4 v[88:91], v228, %[X], s63 offen offset:2048 sc1\n" \
  "buffer_load_dwordx4 v[92:95], v228, %[X], s63 offen offset:3072 sc1\n" \
  "buffer_load_dwordx4 v[96:99], %[mo], %[X], s64 offen offset:0 sc1\n" \
  "s_waitcnt vmcnt(0)\n" \
  "v_max3_u32 v229, v64, v65, v66\n" \
  "v_max3_u32 v229, v229, v67, v68\n" \
  "v_max3_u32 v229, v229, v69, v70\n" \
  "v_max3_u32 v229, v229, v71, v72\n" \
  "v_max3_u32 v229, v229, v73, v74\n" \
  "v_max3_u32 v229, v229, v75, v76\n" \
  "v_max3_u32 v229, v229, v77, v78\n" \
  "v_max3_u32 v229, v229, v79, v80\n" \
  "v_max3_u32 v229, v229, v81, v82\n" \
  "v_max3_u32 v229, v229, v83, v84\n" \
  "v_max3_u32 v229, v229, v85, v86\n" \
  "v_max3_u32 v229, v229, v87, v88\n" \
  "v_max3_u32 v229, v229, v89, v90\n" \
  "v_max3_u32 v229, v229, v91, v92\n" \
  "v_max3_u32 v229, v229, v93, v94\n" \
  "v_max3_u32 v229, v229, v95, v96\n" \
  "v_max3_u32 v229, v229, v97, v98\n" \
  "v_max_u32 v229, v229, v99\n" \
  "v_cmp_eq_u32 vcc, -1, v229\n" \
  "s_cbranch_vccz .Lqk_smw_ok\n" \
  "s_add_i32 s69, s69, 1\n" \
  "s_and_b32 s67, s69, 1023\n" \
  "s_cbranch_scc1 .Lqk_smw\n" \
  "buffer_load_dword v229, off, %[F], 0 offset:1024 sc1\n" \
  "s_waitcnt vmcnt(0)\n" \
  "v_readfirstlane_b32 s67, v229\n" \
  "s_cmp_lg_u32 s67, 0\n" \
  "s_cbranch_scc1 .Lqk_smw_ok\n" \
  "s_cmp_lt_u32 s69, 400000\n" \
  "s_cbranch_scc1 .Lqk_smw\n" \
  "v_mov_b32 v229, 1\n" \
  "buffer_store_dword v229, off, %[F], 0 offset:1024 sc1\n" \
  ".Lqk_smw_ok:\n" \
  "s_memrealtime s[76:77]\n" \
  "s_waitcnt lgkmcnt(0)\n" \
  "s_add_i32 s76, s76, s95\n" \
  "s_add_i32 s76, s76, %[sh1]\n" \
  "s_sub_i32 s76, s76, s86\n" \
  "s_cmp_gt_u32 s76, -3000\n" \
  "s_cselect_b32 s76, 0, s76\n" \
  "s_add_i32 s86, s86, s76\n" \
  "s_add_i32 s88, s60, 3\n" \
  "v_or_b32_e64 v252, s86, 1\n" \
  "v_add_u32 v253, s76, v253\n" \
  "v_mov_b64 v[100:101], 0\n" \
  "v_mov_b64 v[102:103], 0\n" \
  "v_mov_b64 v[104:105], 0\n" \
  "v_mov_b64 v[106:107], 0\n" \
  "v_mov_b64 v[108:109], 0\n" \
  "v_mov_b64 v[110:111], 0\n" \
  "v_mov_b64 v[112:113], 0\n" \
  "v_mov_b64 v[114:115], 0\n" \
  "s_nop 1\n" \
  "v_smfmac_f32_16x16x64_bf16 v[100:103], v[64:67], a[0:7], %[idx]\n" \
  "v_smfmac_f32_16x16x64_bf16 v[104:107], v[64:67], a[8:15], %[idx]\n" \
  "v_smfmac_f32_16x16x64_bf16 v[108:111], v[64:67], a[16:23], %[idx]\n" \
  "v_smfmac_f32_16x16x64_bf16 v[112:115], v[64:67], a[24:31], %[idx]\n" \
  "v_smfmac_f32_16x16x64_bf16 v[100:103], v[68:71], a[32:39], %[idx]\n" \
  "v_smfmac_f32_16x16x64_bf16 v[104:107], v[68:71], a[40:47], %[idx]\n" \
  "v_smfmac_f32_16x16x64_bf16 v[108:111], v[68:71], a[48:55], %[idx]\n" \
  "v_smfmac_f32_16x16x64_bf16 v[112:115], v[68:71], a[56:63], %[idx]\n" \
  "v_smfmac_f32_16x16x64_bf16 v[100:103], v[72:75], a[64:71], %[idx]\n" \
  "v_smfmac_f32_16x16x64_bf16 v[104:107], v[72:75], a[72:79], %[idx]\n" \
  "v_smfmac_f32_16x16x64_bf16 v[108:111], v[72:75], a[80:87], %[idx]\n" \
  "v_smfmac_f32_16x16x64_bf16 v[112:115], v[72:75], a[88:95], %[idx]\n" \
  "v_smfmac_f32_16x16x64_bf16 v[100:103], v[76:79], a[96:103], %[idx]\n" \
  "v_smfmac_f32_16x16x64_bf16 v[104:107], v[76:79], a[104:111], %[idx]\n" \
  "v_smfmac_f32_16x16x64_bf16 v[108:111], v[76:79], a[112:119], %[idx]\n" \
  "v_smfmac_f32_16x16x64_bf16 v[112:115], v[76:79], a[120:127], %[idx]\n" \
  "v_smfmac_f32_16x16x64_bf16 v[100:103], v[80:83], a[128:135], %[idx]\n" \
  "v_smfmac_f32_16x16x64_bf16 v[104:107], v[80:83], a[136:143], %[idx]\n" \
  "v_smfmac_f32_16x16x64_bf16 v[108:111], v[80:83], a[144:151], %[idx]\n" \
  "v_smfmac_f32_16x16x64_bf16 v[112:115], v[80:83], a[152:159], %[idx]\n" \
  "v_smfmac_f32_16x16x64_bf16 v[100:103], v[84:87], a[160:167], %[idx]\n" \
  "v_smfmac_f32_16x16x64_bf16 v[104:107], v[84:87], a[168:175], %[idx]\n" \
  "v_smfmac_f32_16x16x64_bf16 v[108:111], v[84:87], a[176:183], %[idx]\n" \
  "v_smfmac_f32_16x16x64_bf16 v[112:115], v[84:87], a[184:191], %[idx]\n" \
  "v_smfmac_f32_16x16x64_bf16 v[100:103], v[88:91], a[192:199], %[idx]\n" \
  "v_smfmac_f32_16x16x64_bf16 v[104:107], v[88:91], a[200:207], %[idx]\n" \
  "v_smfmac_f32_16x16x64_bf16 v[108:111], v[88:91], a[208:215], %[idx]\n" \
  "v_smfmac_f32_16x16x64_bf16 v[112:115], v[88:91], a[216:223], %[idx]\n" \
  "v_smfmac_f32_16x16x64_bf16 v[100:103], v[92:95], a[224:231], %[idx]\n" \
  "v_smfmac_f32_16x16x64_bf16 v[104:107], v[92:95], a[232:239], %[idx]\n" \
  "v_smfmac_f32_16x16x64_bf16 v[108:111], v[92:95], a[240:247], %[idx]\n" \
  "v_smfmac_f32_16x16x64_bf16 v[112:115], v[92:95], a[248:255], %[idx]\n" \
  "v_smfmac_f32_16x16x64_bf16 v[100:103], v[96:99], v[32:39], %[idx]\n" \
  "v_smfmac_f32_16x16x64_bf16 v[104:107], v[96:99], v[40:47], %[idx]\n" \
  "v_smfmac_f32_16x16x64_bf16 v[108:111], v[96:99], v[48:55], %[idx]\n" \
  "v_smfmac_f32_16x16x64_bf16 v[112:115], v[96:99], v[56:63], %[idx]\n" \
  "s_nop 7\n" \
  "v_pk_add_f32 v[232:233], v[100:101], v[102:103]\n" \
  "v_pk_add_f32 v[234:235], v[104:105], v[106:107]\n" \
  "v_cvt_pk_bf16_f32 v236, v232, v233\n" \
  "v_cvt_pk_bf16_f32 v237, v234, v235\n" \
  "ds_write2st64_b32 %[qW], v236, v237 offset1:11\n" \
  "v_pk_add_f32 v[232:233], v[108:109], v[110:111]\n" \
  "v_pk_add_f32 v[234:235], v[112:113], v[114:115]\n" \
  "v_cvt_pk_bf16_f32 v236, v232, v233\n" \
  "v_cvt_pk_bf16_f32 v237, v234, v235\n" \
  "ds_write2st64_b32 %[kW], v236, v237 offset1:11\n" \
  "s_branch .Lqk_sm_back\n" \
  ".Lqk_late:\n" \
  "s_cmp_lt_i32 s60, 0\n" \
  "s_cbranch_scc1 .Lqk_al_ok\n" \
  "s_cmp_gt_u32 s91, -3000\n" \
  "s_cbranch_scc0 .Lqk_late2\n" \
  ".Lqk_wt:\n" \
  "s_memrealtime s[76:77]\n" \
  "s_waitcnt lgkmcnt(0)\n" \
  "s_sub_i32 s76, s76, s72\n" \
  "s_cmp_lt_i32 s76, 0\n" \
  "s_cbranch_scc1 .Lqk_wt\n" \
  "s_branch .Lqk_al_ok\n" \
  ".Lqk_late2:\n" \
  "s_cmp_lg_u32 s72, 0\n" \
  "s_cbranch_scc1 .Lqk_late3\n" \
  "s_mov_b32 s69, 0\n" \
  ".Lqk_tw:\n" \
  "buffer_load_dword v229, off, %[X], s96 offset:16 sc1\n" \
  "s_waitcnt vmcnt(0)\n" \
  "v_readfirstlane_b32 s72, v229\n" \
  "s_cmp_lg_u32 s72, 0\n" \
  "s_cbranch_scc1 .Lqk_tw_ok\n" \
  "s_sleep 1\n" \
  "s_add_i32 s69, s69, 1\n" \
  "s_cmp_lt_u32 s69, 200\n" \
  "s_cbranch_scc1 .Lqk_tw\n" \
  ".Lqk_tw_ok:\n" \
  "s_add_i32 s76, s72, s95\n" \
  "s_add_i32 s76, s76, 8\n" \
  "s_sub_i32 s76, s76, s86\n" \
  "s_cmp_gt_u32 s76, -3000\n" \
  "s_cselect_b32 s76, 0, s76\n" \
  "s_add_i32 s86, s86, s76\n" \
  "s_add_i32 s88, s60, 3\n" \
  "v_or_b32_e64 v252, s86, 1\n" \
  "v_add_u32 v253, s76, v253\n" \
  "s_sub_i32 s91, s74, s72\n" \
  "s_cmp_gt_u32 s91, -3000\n" \
  "s_cbranch_scc1 .Lqk_wt\n" \
  ".Lqk_late3:\n" \
  "s_memrealtime s[76:77]\n" \
  "s_waitcnt lgkmcnt(0)\n" \
  "s_add_i32 s76, s76, s95\n" \
  "s_add_i32 s76, s76, 90\n" \
  "s_sub_i32 s76, s76, s86\n" \
  "s_cmp_gt_u32 s76, -3000\n" \
  "s_cselect_b32 s76, 0, s76\n" \
  "s_add_i32 s86, s86, s76\n" \
  "s_add_i32 s88, s60, 3\n" \
  "v_or_b32_e64 v252, s86, 1\n" \
  "v_add_u32 v253, s76, v253\n" \
  "s_branch .Lqk_al_ok\n" \
  ".Lqk_sh:\n" \
  "s_mul_i32 s75, s60, 36864\n" \
  "s_add_i32 s76, s75, 0x1000\n" \
  "s_add_i32 s77, s75, 0x2000\n" \
  "s_add_i32 s60, s60, -1\n" \
  "s_add_i32 s96, s96, -16\n" \
  "s_mov_b32 s93, s94\n" \
  "v_xor_b32 %[hA], 0x10000, %[hA]\n" \
  "s_waitcnt vmcnt(0) lgkmcnt(0)\n" \
  "s_barrier\n" \
  "s_mov_b32 s69, 0\n" \
  ".Lqk_shw:\n" \
  "buffer_load_dwordx4 v[64:67], %[hvo], %[HS], s75 offen offset:0 sc1\n" \
  "buffer_load_dwordx4 v[68:71], %[hvo], %[HS], s75 offen offset:1024 sc1\n" \
  "buffer_load_dwordx4 v[72:75], %[hvo], %[HS], s75 offen offset:2048 sc1\n" \
  "buffer_load_dwordx4 v[76:79], %[hvo], %[HS], s75 offen offset:3072 sc1\n" \
  "buffer_load_dwordx4 v[80:83], %[hvo], %[HS], s76 offen offset:0 sc1\n" \
  "buffer_load_dwordx4 v[84:87], %[hvo], %[HS], s76 offen offset:1024 sc1\n" \
  "buffer_load_dwordx4 v[88:91], %[hvo], %[HS], s76 offen offset:2048 sc1\n" \
  "buffer_load_dwordx4 v[92:95], %[hvo], %[HS], s76 offen offset:3072 sc1\n" \
  "buffer_load_dwordx4 v[96:99], %[hvo], %[HS], s77 offen offset:0 sc1\n" \
  "s_waitcnt vmcnt(0)\n" \
  "v_max3_u32 v229, v64, v65, v66\n" \
  "v_max3_u32 v229, v229, v67, v68\n" \
  "v_max3_u32 v229, v229, v69, v70\n" \
  "v_max3_u32 v229, v229, v71, v72\n" \
  "v_max3_u32 v229, v229, v73, v74\n" \
  "v_max3_u32 v229, v229, v75, v76\n" \
  "v_max3_u32 v229, v229, v77, v78\n" \
  "v_max3_u32 v229, v229, v79, v80\n" \
  "v_max3_u32 v229, v229, v81, v82\n" \
  "v_max3_u32 v229, v229, v83, v84\n" \
  "v_max3_u32 v229, v229, v85, v86\n" \
  "v_max3_u32 v229, v229, v87, v88\n" \
  "v_max3_u32 v229, v229, v89, v90\n" \
  "v_max3_u32 v229, v229, v91, v92\n" \
  "v_max3_u32 v229, v229, v93, v94\n" \
  "v_max3_u32 v229, v229, v95, v96\n" \
  "v_max3_u32 v229, v229, v97, v98\n" \
  "v_max_u32 v229, v229, v99\n" \
  "v_cmp_eq_u32 vcc, -1, v229\n" \
  "s_cbranch_vccz .Lqk_shw_ok\n" \
  "s_sleep 4\n" \
  "s_add_i32 s69, s69, 1\n" \
  "s_and_b32 s67, s69, 1023\n" \
  "s_cbranch_scc1 .Lqk_shw\n" \
  "buffer_load_dword v229, off, %[F], 0 offset:1024 sc1\n" \
  "s_waitcnt vmcnt(0)\n" \
  "v_readfirstlane_b32 s67, v229\n" \
  "s_cmp_lg_u32 s67, 0\n" \
  "s_cbranch_scc1 .Lqk_shw_ok\n" \
  "s_cmp_lt_u32 s69, 400000\n" \
  "s_cbranch_scc1 .Lqk_shw\n" \
  "v_mov_b32 v229, 1\n" \
  "buffer_store_dword v229, off, %[F], 0 offset:1024 sc1\n" \
  ".Lqk_shw_ok:\n" \
  "s_xor_b32 s67, s62, 0x10000\n" \
  "v_add_u32 v229, s67, %[mo]\n" \
  "ds_write_b128 v229, v[64:67] offset:0\n" \
  "ds_write_b128 v229, v[68:71] offset:1024\n" \
  "ds_write_b128 v229, v[72:75] offset:2048\n" \
  "ds_write_b128 v229, v[76:79] offset:3072\n" \
  "ds_write_b128 v229, v[80:83] offset:4096\n" \
  "ds_write_b128 v229, v[84:87] offset:5120\n" \
  "ds_write_b128 v229, v[88:91] offset:6144\n" \
  "ds_write_b128 v229, v[92:95] offset:7168\n" \
  "ds_write_b128 v229, v[96:99] offset:8192\n" \
  "s_waitcnt lgkmcnt(0)\n" \
  "s_barrier\n" \
  "ds_read_b128 v[148:151], %[hA] offset:0\n" \
  "ds_read_b128 v[152:155], %[hA] offset:18432\n" \
  "ds_read_b128 v[156:159], %[hA] offset:512\n" \
  "ds_read_b128 v[160:163], %[hA] offset:18944\n" \
  "ds_read_b128 v[164:167], %[hA] offset:1024\n" \
  "ds_read_b128 v[168:171], %[hA] offset:19456\n" \
  "ds_read_b128 v[172:175], %[hA] offset:1536\n" \
  "ds_read_b128 v[176:179], %[hA] offset:19968\n" \
  "s_waitcnt lgkmcnt(0)\n" \
  "v_mfma_f32_16x16x32_bf16 v[116:119], v[148:151], a[0:3], 0\n" \
  "v_mfma_f32_16x16x32_bf16 v[132:135], v[152:155], a[0:3], 0\n" \
  "v_mfma_f32_16x16x32_bf16 v[120:123], v[148:151], a[8:11], 0\n" \
  "v_mfma_f32_16x16x32_bf16 v[136:139], v[152:155], a[8:11], 0\n" \
  "v_mfma_f32_16x16x32_bf16 v[124:127], a[16:19], v[148:151], 0\n" \
  "v_mfma_f32_16x16x32_bf16 v[140:143], a[16:19], v[152:155], 0\n" \
  "v_mfma_f32_16x16x32_bf16 v[128:131], a[24:27], v[148:151], 0\n" \
  "v_mfma_f32_16x16x32_bf16 v[144:147], a[24:27], v[152:155], 0\n" \
  "v_mfma_f32_16x16x32_bf16 v[116:119], v[156:159], a[4:7], v[116:119]\n" \
  "v_mfma_f32_16x16x32_bf16 v[132:135], v[160:163], a[4:7], v[132:135]\n" \
  "v_mfma_f32_16x16x32_bf16 v[120:123], v[156:159], a[12:15], v[120:123]\n" \
  "v_mfma_f32_16x16x32_bf16 v[136:139], v[160:163], a[12:15], v[136:139]\n" \
  "v_mfma_f32_16x16x32_bf16 v[124:127], a[20:23], v[156:159], v[124:127]\n" \
  "v_mfma_f32_16x16x32_bf16 v[140:143], a[20:23], v[160:163], v[140:143]\n" \
  "v_mfma_f32_16x16x32_bf16 v[128:131], a[28:31], v[156:159], v[128:131]\n" \
  "v_mfma_f32_16x16x32_bf16 v[144:147], a[28:31], v[160:163], v[144:147]\n" \
  "v_mfma_f32_16x16x32_bf16 v[116:119], v[164:167], a[32:35], v[116:119]\n" \
  "ds_read_b128 v[148:151], %[hA] offset:2048\n" \
  "v_mfma_f32_16x16x32_bf16 v[132:135], v[168:171], a[32:35], v[132:135]\n" \
  "ds_read_b128 v[152:155], %[hA] offset:20480\n" \
  "v_mfma_f32_16x16x32_bf16 v[120:123], v[164:167], a[40:43], v[120:123]\n" \
  "v_mfma_f32_16x16x32_bf16 v[136:139], v[168:171], a[40:43], v[136:139]\n" \
  "v_mfma_f32_16x16x32_bf16 v[124:127], a[48:51], v[164:167], v[124:127]\n" \
  "v_mfma_f32_16x16x32_bf16 v[140:143], a[48:51], v[168:171], v[140:143]\n" \
  "v_mfma_f32_16x16x32_bf16 v[128:131], a[56:59], v[164:167], v[128:131]\n" \
  "v_mfma_f32_16x16x32_bf16 v[144:147], a[56:59], v[168:171], v[144:147]\n" \
  "v_mfma_f32_16x16x32_bf16 v[116:119], v[172:175], a[36:39], v[116:119]\n" \
  "ds_read_b128 v[156:159], %[hA] offset:2560\n" \
  "v_mfma_f32_16x16x32_bf16 v[132:135], v[176:179], a[36:39], v[132:135]\n" \
  "ds_read_b128 v[160:163], %[hA] offset:20992\n" \
  "v_mfma_f32_16x16x32_bf16 v[120:123], v[172:175], a[44:47], v[120:123]\n" \
  "v_mfma_f32_16x16x32_bf16 v[136:139], v[176:179], a[44:47], v[136:139]\n" \
  "v_mfma_f32_16x16x32_bf16 v[124:127], a[52:55], v[172:175], v[124:127]\n" \
  "v_mfma_f32_16x16x32_bf16 v[140:143], a[52:55], v[176:179], v[140:143]\n" \
  "v_mfma_f32_16x16x32_bf16 v[128:131], a[60:63], v[172:175], v[128:131]\n" \
  "v_mfma_f32_16x16x32_bf16 v[144:147], a[60:63], v[176:179], v[144:147]\n" \
  "s_waitcnt lgkmcnt(2)\n" \
  "v_mfma_f32_16x16x32_bf16 v[116:119], v[148:151], a[64:67], v[116:119]\n" \
  "ds_read_b128 v[164:167], %[hA] offset:3072\n" \
  "v_mfma_f32_16x16x32_bf16 v[132:135], v[152:155], a[64:67], v[132:135]\n" \
  "ds_read_b128 v[168:171], %[hA] offset:21504\n" \
  "v_mfma_f32_16x16x32_bf16 v[120:123], v[148:151], a[72:75], v[120:123]\n" \
  "v_mfma_f32_16x16x32_bf16 v[136:139], v[152:155], a[72:75], v[136:139]\n" \
  "v_mfma_f32_16x16x32_bf16 v[124:127], a[80:83], v[148:151], v[124:127]\n" \
  "v_mfma_f32_16x16x32_bf16 v[140:143], a[80:83], v[152:155], v[140:143]\n" \
  "v_mfma_f32_16x16x32_bf16 v[128:131], a[88:91], v[148:151], v[128:131]\n" \
  "v_mfma_f32_16x16x32_bf16 v[144:147], a[88:91], v[152:155], v[144:147]\n" \
  "s_waitcnt lgkmcnt(2)\n" \
  "v_mfma_f32_16x16x32_bf16 v[116:119], v[156:159], a[68:71], v[116:119]\n" \
  "ds_read_b128 v[172:175], %[hA] offset:3584\n" \
  "v_mfma_f32_16x16x32_bf16 v[132:135], v[160:163], a[68:71], v[132:135]\n" \
  "ds_read_b128 v[176:179], %[hA] offset:22016\n" \
  "v_mfma_f32_16x16x32_bf16 v[120:123], v[156:159], a[76:79], v[120:123]\n" \
  "v_mfma_f32_16x16x32_bf16 v[136:139], v[160:163], a[76:79], v[136:139]\n" \
  "v_mfma_f32_16x16x32_bf16 v[124:127], a[84:87], v[156:159], v[124:127]\n" \
  "v_mfma_f32_16x16x32_bf16 v[140:143], a[84:87], v[160:163], v[140:143]\n" \
  "v_mfma_f32_16x16x32_bf16 v[128:131], a[92:95], v[156:159], v[128:131]\n" \
  "v_mfma_f32_16x16x32_bf16 v[144:147], a[92:95], v[160:163], v[144:147]\n" \
  "s_waitcnt lgkmcnt(2)\n" \
  "v_mfma_f32_16x16x32_bf16 v[116:119], v[164:167], a[96:99], v[116:119]\n" \
  "ds_read_b128 v[148:151], %[hA] offset:4096\n" \
  "v_mfma_f32_16x16x32_bf16 v[132:135], v[168:171], a[96:99], v[132:135]\n" \
  "ds_read_b128 v[152:155], %[hA] offset:22528\n" \
  "v_mfma_f32_16x16x32_bf16 v[120:123], v[164:167], a[104:107], v[120:123]\n" \
  "v_mfma_f32_16x16x32_bf16 v[136:139], v[168:171], a[104:107], v[136:139]\n" \
  "v_mfma_f32_16x16x32_bf16 v[124:127], a[112:115], v[164:167], v[124:127]\n" \
  "v_mfma_f32_16x16x32_bf16 v[140:143], a[112:115], v[168:171], v[140:143]\n" \
  "v_mfma_f32_16x16x32_bf16 v[128:131], a[120:123], v[164:167], v[128:131]\n" \
  "v_mfma_f32_16x16x32_bf16 v[144:147], a[120:123], v[168:171], v[144:147]\n" \
  "s_waitcnt lgkmcnt(2)\n" \
  "v_mfma_f32_16x16x32_bf16 v[116:119], v[172:175], a[100:103], v[116:119]\n" \
  "ds_read_b128 v[156:159], %[hA] offset:4608\n" \
  "v_mfma_f32_16x16x32_bf16 v[132:135], v[176:179], a[100:103], v[132:135]\n" \
  "ds_read_b128 v[160:163], %[hA] offset:23040\n" \
  "v_mfma_f32_16x16x32_bf16 v[120:123], v[172:175], a[108:111], v[120:123]\n" \
  "v_mfma_f32_16x16x32_bf16 v[136:139], v[176:179], a[108:111], v[136:139]\n" \
  "v_mfma_f32_16x16x32_bf16 v[124:127], a[116:119], v[172:175], v[124:127]\n" \
  "v_mfma_f32_16x16x32_bf16 v[140:143], a[116:119], v[176:179], v[140:143]\n" \
  "v_mfma_f32_16x16x32_bf16 v[128:131], a[124:127], v[172:175], v[128:131]\n" \
  "v_mfma_f32_16x16x32_bf16 v[144:147], a[124:127], v[176:179], v[144:147]\n" \
  "s_waitcnt lgkmcnt(2)\n" \
  "v_mfma_f32_16x16x32_bf16 v[116:119], v[148:151], a[128:131], v[116:119]\n" \
  "ds_read_b128 v[164:167], %[hA] offset:5120\n" \
  "v_mfma_f32_16x16x32_bf16 v[132:135], v[152:155], a[128:131], v[132:135]\n" \
  "ds_read_b128 v[168:171], %[hA] offset:23552\n" \
  "v_mfma_f32_16x16x32_bf16 v[120:123], v[148:151], a[136:139], v[120:123]\n" \
  "v_mfma_f32_16x16x32_bf16 v[136:139], v[152:155], a[136:139], v[136:139]\n" \
  "v_mfma_f32_16x16x32_bf16 v[124:127], a[144:147], v[148:151], v[124:127]\n" \
  "v_mfma_f32_16x16x32_bf16 v[140:143], a[144:147], v[152:155], v[140:143]\n" \
  "v_mfma_f32_16x16x32_bf16 v[128:131], a[152:155], v[148:151], v[128:131]\n" \
  "v_mfma_f32_16x16x32_bf16 v[144:147], a[152:155], v[152:155], v[144:147]\n" \
  "s_waitcnt lgkmcnt(2)\n" \
  "v_mfma_f32_16x16x32_bf16 v[116:119], v[156:159], a[132:135], v[116:119]\n" \
  "ds_read_b128 v[172:175], %[hA] offset:5632\n" \
  "v_mfma_f32_16x16x32_bf16 v[132:135], v[160:163], a[132:135], v[132:135]\n" \
  "ds_read_b128 v[176:179], %[hA] offset:24064\n" \
  "v_mfma_f32_16x16x32_bf16 v[120:123], v[156:159], a[140:143], v[120:123]\n" \
  "v_mfma_f32_16x16x32_bf16 v[136:139], v[160:163], a[140:143], v[136:139]\n" \
  "v_mfma_f32_16x16x32_bf16 v[124:127], a[148:151], v[156:159], v[124:127]\n" \
  "v_mfma_f32_16x16x32_bf16 v[140:143], a[148:151], v[160:163], v[140:143]\n" \
  "v_mfma_f32_16x16x32_bf16 v[128:131], a[156:159], v[156:159], v[128:131]\n" \
  "v_mfma_f32_16x16x32_bf16 v[144:147], a[156:159], v[160:163], v[144:147]\n" \
  "s_waitcnt lgkmcnt(2)\n" \
  "v_mfma_f32_16x16x32_bf16 v[116:119], v[164:167], a[160:163], v[116:119]\n" \
  "ds_read_b128 v[148:151], %[hA] offset:6144\n" \
  "v_mfma_f32_16x16x32_bf16 v[132:135], v[168:171], a[160:163], v[132:135]\n" \
  "ds_read_b128 v[152:155], %[hA] offset:24576\n" \
  "v_mfma_f32_16x16x32_bf16 v[120:123], v[164:167], a[168:171], v[120:123]\n" \
  "v_mfma_f32_16x16x32_bf16 v[136:139], v[168:171], a[168:171], v[136:139]\n" \
  "v_mfma_f32_16x16x32_bf16 v[124:127], a[176:179], v[164:167], v[124:127]\n" \
  "v_mfma_f32_16x16x32_bf16 v[140:143], a[176:179], v[168:171], v[140:143]\n" \
  "v_mfma_f32_16x16x32_bf16 v[128:131], a[184:187], v[164:167], v[128:131]\n" \
  "v_mfma_f32_16x16x32_bf16 v[144:147], a[184:187], v[168:171], v[144:147]\n" \
  "s_waitcnt lgkmcnt(2)\n" \
  "v_mfma_f32_16x16x32_bf16 v[116:119], v[172:175], a[164:167], v[116:119]\n" \
  "ds_read_b128 v[156:159], %[hA] offset:6656\n" \
  "v_mfma_f32_16x16x32_bf16 v[132:135], v[176:179], a[164:167], v[132:135]\n" \
  "ds_read_b128 v[160:163], %[hA] offset:25088\n" \
  "v_mfma_f32_16x16x32_bf16 v[120:123], v[172:175], a[172:175], v[120:123]\n" \
  "v_mfma_f32_16x16x32_bf16 v[136:139], v[176:179], a[172:175], v[136:139]\n" \
  "v_mfma_f32_16x16x32_bf16 v[124:127], a[180:183], v[172:175], v[124:127]\n" \
  "v_mfma_f32_16x16x32_bf16 v[140:143], a[180:183], v[176:179], v[140:143]\n" \
  "v_mfma_f32_16x16x32_bf16 v[128:131], a[188:191], v[172:175], v[128:131]\n" \
  "v_mfma_f32_16x16x32_bf16 v[144:147], a[188:191], v[176:179], v[144:147]\n" \
  "s_waitcnt lgkmcnt(2)\n" \
  "v_mfma_f32_16x16x32_bf16 v[116:119], v[148:151], a[192:195], v[116:119]\n" \
  "ds_read_b128 v[164:167], %[hA] offset:7168\n" \
  "v_mfma_f32_16x16x32_bf16 v[132:135], v[152:155], a[192:195], v[132:135]\n" \
  "ds_read_b128 v[168:171], %[hA] offset:25600\n" \
  "v_mfma_f32_16x16x32_bf16 v[120:123], v[148:151], a[200:203], v[120:123]\n" \
  "v_mfma_f32_16x16x32_bf16 v[136:139], v[152:155], a[200:203], v[136:139]\n" \
  "v_mfma_f32_16x16x32_bf16 v[124:127], a[208:211], v[148:151], v[124:127]\n" \
  "v_mfma_f32_16x16x32_bf16 v[140:143], a[208:211], v[152:155], v[140:143]\n" \
  "v_mfma_f32_16x16x32_bf16 v[128:131], a[216:219], v[148:151], v[128:131]\n" \
  "v_mfma_f32_16x16x32_bf16 v[144:147], a[216:219], v[152:155], v[144:147]\n" \
  "s_waitcnt lgkmcnt(2)\n" \
  "v_mfma_f32_16x16x32_bf16 v[116:119], v[156:159], a[196:199], v[116:119]\n" \
  "ds_read_b128 v[172:175], %[hA] offset:7680\n" \
  "v_mfma_f32_16x16x32_bf16 v[132:135], v[160:163], a[196:199], v[132:135]\n" \
  "ds_read_b128 v[176:179], %[hA] offset:26112\n" \
  "v_mfma_f32_16x16x32_bf16 v[120:123], v[156:159], a[204:207], v[120:123]\n" \
  "ds_read_b128 v[148:151], %[hA] offset:8192\n" \
  "v_mfma_f32_16x16x32_bf16 v[136:139], v[160:163], a[204:207], v[136:139]\n" \
  "ds_read_b128 v[152:155], %[hA] offset:26624\n" \
  "v_mfma_f32_16x16x32_bf16 v[124:127], a[212:215], v[156:159], v[124:127]\n" \
  "v_mfma_f32_16x16x32_bf16 v[140:143], a[212:215], v[160:163], v[140:143]\n" \
  "v_mfma_f32_16x16x32_bf16 v[128:131], a[220:223], v[156:159], v[128:131]\n" \
  "v_mfma_f32_16x16x32_bf16 v[144:147], a[220:223], v[160:163], v[144:147]\n" \
  "s_waitcnt lgkmcnt(4)\n" \
  "v_mfma_f32_16x16x32_bf16 v[124:127], a[240:243], v[164:167], v[124:127]\n" \
  "ds_read_b128 v[156:159], %[hA] offset:8704\n" \
  "v_mfma_f32_16x16x32_bf16 v[140:143], a[240:243], v[168:171], v[140:143]\n" \
  "ds_read_b128 v[160:163], %[hA] offset:27136\n" \
  "v_mfma_f32_16x16x32_bf16 v[128:131], a[248:251], v[164:167], v[128:131]\n" \
  "v_mfma_f32_16x16x32_bf16 v[144:147], a[248:251], v[168:171], v[144:147]\n" \
  "s_waitcnt lgkmcnt(4)\n" \
  "v_mfma_f32_16x16x32_bf16 v[124:127], a[244:247], v[172:175], v[124:127]\n" \
  "v_mfma_f32_16x16x32_bf16 v[140:143], a[244:247], v[176:179], v[140:143]\n" \
  "v_mfma_f32_16x16x32_bf16 v[128:131], a[252:255], v[172:175], v[128:131]\n" \
  "v_mfma_f32_16x16x32_bf16 v[144:147], a[252:255], v[176:179], v[144:147]\n" \
  "s_waitcnt lgkmcnt(2)\n" \
  "v_mfma_f32_16x16x32_bf16 v[124:127], v[48:51], v[148:151], v[124:127]\n" \
  "v_mfma_f32_16x16x32_bf16 v[140:143], v[48:51], v[152:155], v[140:143]\n" \
  "v_mfma_f32_16x16x32_bf16 v[128:131], v[56:59], v[148:151], v[128:131]\n" \
  "v_mfma_f32_16x16x32_bf16 v[144:147], v[56:59], v[152:155], v[144:147]\n" \
  "s_waitcnt lgkmcnt(0)\n" \
  "v_mfma_f32_16x16x32_bf16 v[124:127], v[52:55], v[156:159], v[124:127]\n" \
  "v_mfma_f32_16x16x32_bf16 v[140:143], v[52:55], v[160:163], v[140:143]\n" \
  "v_mfma_f32_16x16x32_bf16 v[128:131], v[60:63], v[156:159], v[128:131]\n" \
  "v_mfma_f32_16x16x32_bf16 v[144:147], v[60:63], v[160:163], v[144:147]\n" \
  "v_mfma_f32_16x16x32_bf16 v[116:119], v[164:167], a[224:227], v[116:119]\n" \
  "v_mfma_f32_16x16x32_bf16 v[132:135], v[168:171], a[224:227], v[132:135]\n" \
  "v_mfma_f32_16x16x32_bf16 v[120:123], v[164:167], a[232:235], v[120:123]\n" \
  "v_mfma_f32_16x16x32_bf16 v[136:139], v[168:171], a[232:235], v[136:139]\n" \
  "v_mfma_f32_16x16x32_bf16 v[116:119], v[172:175], a[228:231], v[116:119]\n" \
  "v_mfma_f32_16x16x32_bf16 v[132:135], v[176:179], a[228:231], v[132:135]\n" \
  "v_mfma_f32_16x16x32_bf16 v[120:123], v[172:175], a[236:239], v[120:123]\n" \
  "v_mfma_f32_16x16x32_bf16 v[136:139], v[176:179], a[236:239], v[136:139]\n" \
  "v_mfma_f32_16x16x32_bf16 v[116:119], v[148:151], v[32:35], v[116:119]\n" \
  "v_mfma_f32_16x16x32_bf16 v[132:135], v[152:155], v[32:35], v[132:135]\n" \
  "v_mfma_f32_16x16x32_bf16 v[120:123], v[148:151], v[40:43], v[120:123]\n" \
  "v_mfma_f32_16x16x32_bf16 v[136:139], v[152:155], v[40:43], v[136:139]\n" \
  "v_mfma_f32_16x16x32_bf16 v[116:119], v[156:159], v[36:39], v[116:119]\n" \
  "v_mfma_f32_16x16x32_bf16 v[132:135], v[160:163], v[36:39], v[132:135]\n" \
  "v_mfma_f32_16x16x32_bf16 v[120:123], v[156:159], v[44:47], v[120:123]\n" \
  "v_mfma_f32_16x16x32_bf16 v[136:139], v[160:163], v[44:47], v[136:139]\n" \
  "s_memrealtime s[76:77]\n" \
  "s_waitcnt lgkmcnt(0)\n" \
  "s_add_i32 s76, s76, s95\n" \
  "s_add_i32 s76, s76, 90\n" \
  "s_sub_i32 s76, s76, s86\n" \
  "s_cmp_gt_u32 s76, -3000\n" \
  "s_cselect_b32 s76, 0, s76\n" \
  "s_add_i32 s86, s86, s76\n" \
  "s_add_i32 s88, s60, 3\n" \
  "v_or_b32_e64 v252, s86, 1\n" \
  "v_add_u32 v253, s76, v253\n" \
  "buffer_load_dwordx4 v[64:67], %[mo], %[X], s63 offen offset:0 sc1\n" \
  "buffer_load_dwordx4 v[68:71], %[mo], %[X], s63 offen offset:1024 sc1\n" \
  "buffer_load_dwordx4 v[72:75], %[mo], %[X], s63 offen offset:2048 sc1\n" \
  "buffer_load_dwordx4 v[76:79], %[mo], %[X], s63 offen offset:3072 sc1\n" \
  "buffer_load_dwordx4 v[80:83], v228, %[X], s63 offen offset:0 sc1\n" \
  "buffer_load_dwordx4 v[84:87], v228, %[X], s63 offen offset:1024 sc1\n" \
  "buffer_load_dwordx4 v[88:91], v228, %[X], s63 offen offset:2048 sc1\n" \
  "buffer_load_dwordx4 v[92:95], v228, %[X], s63 offen offset:3072 sc1\n" \
  "buffer_load_dwordx4 v[96:99], %[mo], %[X], s64 offen offset:0 sc1\n" \
  "v_mov_b64 v[100:101], 0\n" \
  "v_mov_b64 v[102:103], 0\n" \
  "v_mov_b64 v[104:105], 0\n" \
  "v_mov_b64 v[106:107], 0\n" \
  "v_mov_b64 v[108:109], 0\n" \
  "v_mov_b64 v[110:111], 0\n" \
  "v_mov_b64 v[112:113], 0\n" \
  "v_mov_b64 v[114:115], 0\n" \
  "v_cvt_pk_bf16_f32 v220, v124, v125\n" \
  "v_cvt_pk_bf16_f32 v221, v126, v127\n" \
  "v_cvt_pk_bf16_f32 v222, v128, v129\n" \
  "v_cvt_pk_bf16_f32 v223, v130, v131\n" \
  "v_cvt_pk_bf16_f32 v224, v140, v141\n" \
  "v_cvt_pk_bf16_f32 v225, v142, v143\n" \
  "v_cvt_pk_bf16_f32 v226, v144, v145\n" \
  "v_cvt_pk_bf16_f32 v227, v146, v147\n" \
  "ds_write_b128 %[ktW], v[220:223] offset:0\n" \
  "ds_write_b128 %[ktW], v[224:227] offset:4096\n" \
  "v_xor_b32 %[hA], 0x10000, %[hA]\n" \
  "v_mov_b32 v231, 0\n" \
  "s_waitcnt lgkmcnt(0)\n" \
  "s_barrier\n" \
  "ds_read_b128 v[180:183], %[ktR] offset:0\n" \
  "ds_read_b128 v[184:187], %[ktR] offset:1024\n" \
  "ds_read_b128 v[188:191], %[ktR] offset:2048\n" \
  "ds_read_b128 v[192:195], %[ktR] offset:3072\n" \
  "ds_read_b128 v[196:199], %[ktR] offset:4096\n" \
  "ds_read_b128 v[200:203], %[ktR] offset:5120\n" \
  "ds_read_b128 v[204:207], %[ktR] offset:6144\n" \
  "ds_read_b128 v[208:211], %[ktR] offset:7168\n" \
  "s_waitcnt lgkmcnt(0)\n" \
  "s_branch .Lqk_m\n" \
  ".Lqk_entry:\n" \
  "s_mov_b32 s81, 63\n" \
  "s_mov_b32 s87, %[mr0]\n" \
  "s_cmp_eq_u32 %[grp], 0\n" \
  "s_cbranch_scc1 .Lqk_go\n" \
  "s_mov_b32 s69, 0\n" \
  "s_xor_b32 s75, %[bas], 0x2000\n" \
  ".Lqk_g1:\n" \
  "buffer_load_dword v229, off, %[X], s75 offset:48 sc1\n" \
  "s_waitcnt vmcnt(0)\n" \
  "v_readfirstlane_b32 s74, v229\n" \
  "s_cmp_lg_u32 s74, 0\n" \
  "s_cbranch_scc1 .Lqk_g2\n" \
  "s_sleep 8\n" \
  "s_add_i32 s69, s69, 1\n" \
  "s_cmp_lt_u32 s69, 400\n" \
  "s_cbranch_scc1 .Lqk_g1\n" \
  ".Lqk_g2:\n" \
  "s_add_i32 s74, s74, %[g1t]\n" \
  ".Lqk_g3:\n" \
  "s_memrealtime s[76:77]\n" \
  "s_waitcnt lgkmcnt(0)\n" \
  "s_sub_i32 s76, s76, s74\n" \
  "s_cmp_gt_u32 s76, -3000\n" \
  "s_cbranch_scc1 .Lqk_g3\n" \
  ".Lqk_go:\n" \
  "s_waitcnt vmcnt(0) lgkmcnt(0)\n" \
  "s_barrier\n" \
  "s_mov_b32 s75, 0\n" \
  "s_movk_i32 s76, 0x1000\n" \
  "s_movk_i32 s77, 0x2000\n" \
  "v_mov_b32 v236, 1\n" \
  "buffer_atomic_add v236, %[rdy], %[X], %[bas] offen offset:1536 sc0 sc1\n" \
  "s_mov_b32 s69, 0\n" \
  ".Lqk_enw:\n" \
  "buffer_load_dwordx4 v[64:67], %[hvo], %[HS], s75 offen offset:0 sc1\n" \
  "buffer_load_dwordx4 v[68:71], %[hvo], %[HS], s75 offen offset:1024 sc1\n" \
  "buffer_load_dwordx4 v[72:75], %[hvo], %[HS], s75 offen offset:2048 sc1\n" \
  "buffer_load_dwordx4 v[76:79], %[hvo], %[HS], s75 offen offset:3072 sc1\n" \
  "buffer_load_dwordx4 v[80:83], %[hvo], %[HS], s76 offen offset:0 sc1\n" \
  "buffer_load_dwordx4 v[84:87], %[hvo], %[HS], s76 offen offset:1024 sc1\n" \
  "buffer_load_dwordx4 v[88:91], %[hvo], %[HS], s76 offen offset:2048 sc1\n" \
  "buffer_load_dwordx4 v[92:95], %[hvo], %[HS], s76 offen offset:3072 sc1\n" \
  "buffer_load_dwordx4 v[96:99], %[hvo], %[HS], s77 offen offset:0 sc1\n" \
  "s_waitcnt vmcnt(0)\n" \
  "v_max3_u32 v229, v64, v65, v66\n" \
  "v_max3_u32 v229, v229, v67, v68\n" \
  "v_max3_u32 v229, v229, v69, v70\n" \
  "v_max3_u32 v229, v229, v71, v72\n" \
  "v_max3_u32 v229, v229, v73, v74\n" \
  "v_max3_u32 v229, v229, v75, v76\n" \
  "v_max3_u32 v229, v229, v77, v78\n" \
  "v_max3_u32 v229, v229, v79, v80\n" \
  "v_max3_u32 v229, v229, v81, v82\n" \
  "v_max3_u32 v229, v229, v83, v84\n" \
  "v_max3_u32 v229, v229, v85, v86\n" \
  "v_max3_u32 v229, v229, v87, v88\n" \
  "v_max3_u32 v229, v229, v89, v90\n" \
  "v_max3_u32 v229, v229, v91, v92\n" \
  "v_max3_u32 v229, v229, v93, v94\n" \
  "v_max3_u32 v229, v229, v95, v96\n" \
  "v_max3_u32 v229, v229, v97, v98\n" \
  "v_max_u32 v229, v229, v99\n" \
  "v_cmp_eq_u32 vcc, -1, v229\n" \
  "s_cbranch_vccz .Lqk_enw_ok\n" \
  "s_sleep 4\n" \
  "s_add_i32 s69, s69, 1\n" \
  "s_and_b32 s67, s69, 1023\n" \
  "s_cbranch_scc1 .Lqk_enw\n" \
  "buffer_load_dword v229, off, %[F], 0 offset:1024 sc1\n" \
  "s_waitcnt vmcnt(0)\n" \
  "v_readfirstlane_b32 s67, v229\n" \
  "s_cmp_lg_u32 s67, 0\n" \
  "s_cbranch_scc1 .Lqk_enw_ok\n" \
  "s_cmp_lt_u32 s69, 400000\n" \
  "s_cbranch_scc1 .Lqk_enw\n" \
  "v_mov_b32 v229, 1\n" \
  "buffer_store_dword v229, off, %[F], 0 offset:1024 sc1\n" \
  ".Lqk_enw_ok:\n" \
  "ds_write_b128 %[hvo], v[64:67] offset:0\n" \
  "ds_write_b128 %[hvo], v[68:71] offset:1024\n" \
  "ds_write_b128 %[hvo], v[72:75] offset:2048\n" \
  "ds_write_b128 %[hvo], v[76:79] offset:3072\n" \
  "ds_write_b128 %[hvo], v[80:83] offset:4096\n" \
  "ds_write_b128 %[hvo], v[84:87] offset:5120\n" \
  "ds_write_b128 %[hvo], v[88:91] offset:6144\n" \
  "ds_write_b128 %[hvo], v[92:95] offset:7168\n" \
  "ds_write_b128 %[hvo], v[96:99] offset:8192\n" \
  "v_readfirstlane_b32 s92, v236\n" \
  "s_cmp_lg_u32 s92, 63\n" \
  "s_cbranch_scc1 .Lqk_nrdy\n" \
  "s_memrealtime s[74:75]\n" \
  "s_waitcnt lgkmcnt(0)\n" \
  "s_add_i32 s74, s74, %[x0]\n" \
  "s_or_b32 s74, s74, 1\n" \
  "s_mul_i32 s75, %[tau], 162\n" \
  "s_lshr_b32 s75, s75, 8\n" \
  "s_add_i32 s75, s75, s74\n" \
  "v_mov_b32 v236, s74\n" \
  "v_mov_b32 v237, s75\n" \
  "buffer_store_dwordx2 v[236:237], %[rdy], %[X], %[bas] offen offset:0 sc1\n" \
  "s_add_i32 s74, s74, %[tau]\n" \
  "s_or_b32 s74, s74, 1\n" \
  "s_add_i32 s75, s75, %[tau]\n" \
  "v_mov_b32 v236, s74\n" \
  "v_mov_b32 v237, s75\n" \
  "buffer_store_dwordx2 v[236:237], %[rdy], %[X], %[bas] offen offset:16 sc1\n" \
  ".Lqk_nrdy:\n" \
  "s_mov_b32 s95, %[tau]\n" \
  "s_mov_b32 s88, 3\n" \
  "s_mov_b32 s90, 0\n" \
  "s_mov_b32 s78, 0\n" \
  "s_waitcnt lgkmcnt(0)\n" \
  "s_barrier\n" \
  "ds_read_b128 v[148:151], %[hA] offset:0\n" \
  "ds_read_b128 v[152:155], %[hA] offset:18432\n" \
  "ds_read_b128 v[156:159], %[hA] offset:512\n" \
  "ds_read_b128 v[160:163], %[hA] offset:18944\n" \
  "ds_read_b128 v[164:167], %[hA] offset:1024\n" \
  "ds_read_b128 v[168:171], %[hA] offset:19456\n" \
  "ds_read_b128 v[172:175], %[hA] offset:1536\n" \
  "ds_read_b128 v[176:179], %[hA] offset:19968\n" \
  "s_waitcnt lgkmcnt(0)\n" \
  "v_mfma_f32_16x16x32_bf16 v[116:119], v[148:151], a[0:3], 0\n" \
  "v_mfma_f32_16x16x32_bf16 v[132:135], v[152:155], a[0:3], 0\n" \
  "v_mfma_f32_16x16x32_bf16 v[120:123], v[148:151], a[8:11], 0\n" \
  "v_mfma_f32_16x16x32_bf16 v[136:139], v[152:155], a[8:11], 0\n" \
  "v_mfma_f32_16x16x32_bf16 v[124:127], a[16:19], v[148:151], 0\n" \
  "v_mfma_f32_16x16x32_bf16 v[140:143], a[16:19], v[152:155], 0\n" \
  "v_mfma_f32_16x16x32_bf16 v[128:131], a[24:27], v[148:151], 0\n" \
  "v_mfma_f32_16x16x32_bf16 v[144:147], a[24:27], v[152:155], 0\n" \
  "v_mfma_f32_16x16x32_bf16 v[116:119], v[156:159], a[4:7], v[116:119]\n" \
  "v_mfma_f32_16x16x32_bf16 v[132:135], v[160:163], a[4:7], v[132:135]\n" \
  "v_mfma_f32_16x16x32_bf16 v[120:123], v[156:159], a[12:15], v[120:123]\n" \
  "v_mfma_f32_16x16x32_bf16 v[136:139], v[160:163], a[12:15], v[136:139]\n" \
  "v_mfma_f32_16x16x32_bf16 v[124:127], a[20:23], v[156:159], v[124:127]\n" \
  "v_mfma_f32_16x16x32_bf16 v[140:143], a[20:23], v[160:163], v[140:143]\n" \
  "v_mfma_f32_16x16x32_bf16 v[128:131], a[28:31], v[156:159], v[128:131]\n" \
  "v_mfma_f32_16x16x32_bf16 v[144:147], a[28:31], v[160:163], v[144:147]\n" \
  "s_waitcnt lgkmcnt(0)\n" \
  "s_branch .Lqk_top\n" \
  ".Lqk_end:\n"
#define QK_CLOB "v32", "v33", "v34", "v35", "v36", "v37", "v38", "v39", "v40", "v41", "v42", "v43", "v44", "v45", "v46", "v47", "v48", "v49", "v50", "v51", "v52", "v53", "v54", "v55", "v56", "v57", "v58", "v59", "v60", "v61", "v62", "v63", "v64", "v65", "v66", "v67", "v68", "v69", "v70", "v71", "v72", "v73", "v74", "v75", "v76", "v77", "v78", "v79", "v80", "v81", "v82", "v83", "v84", "v85", "v86", "v87", "v88", "v89", "v90", "v91", "v92", "v93", "v94", "v95", "v96", "v97", "v98", "v99", "v100", "v101", "v102", "v103", "v104", "v105", "v106", "v107", "v108", "v109", "v110", "v111", "v112", "v113", "v114", "v115", "v116", "v117", "v118", "v119", "v120", "v121", "v122", "v123", "v124", "v125", "v126", "v127", "v128", "v129", "v130", "v131", "v132", "v133", "v134", "v135", "v136", "v137", "v138", "v139", "v140", "v141", "v142", "v143", "v144", "v145", "v146", "v147", "v148", "v149", "v150", "v151", "v152", "v153", "v154", "v155", "v156", "v157", "v158", "v159", "v160", "v161", "v162", "v163", "v164", "v165", "v166", "v167", "v168", "v169", "v170", "v171", "v172", "v173", "v174", "v175", "v176", "v177", "v178", "v179", "v180", "v181", "v182", "v183", "v184", "v185", "v186", "v187", "v188", "v189", "v190", "v191", "v192", "v193", "v194", "v195", "v196", "v197", "v198", "v199", "v200", "v201", "v202", "v203", "v204", "v205", "v206", "v207", "v208", "v209", "v210", "v211", "v212", "v213", "v214", "v215", "v216", "v217", "v218", "v219", "v220", "v221", "v222", "v223", "v224", "v225", "v226", "v227", "v228", "v229", "v230", "v231", "v232", "v233", "v234", "v235", "v236", "v237", "v238", "v239", "v240", "v241", "v242", "v243", "v244", "v245", "v246", "v247", "v248", "v249", "v250", "v251", "v252", "v253", "v254", "v255", "s60", "s61", "s62", "s63", "s64", "s65", "s66", "s67", "s68", "s69", "s70", "s71", "s72", "s73", "s74", "s75", "s76", "s77", "s78", "s79", "s80", "s81", "s82", "s83", "s84", "s85", "s86", "s87", "s88", "s89", "s90", "s91", "s92", "s93", "s94", "s95", "s96", "a0", "a1", "a2", "a3", "a4", "a5", "a6", "a7", "a8", "a9", "a10", "a11", "a12", "a13", "a14", "a15", "a16", "a17", "a18", "a19", "a20", "a21", "a22", "a23", "a24", "a25", "a26", "a27", "a28", "a29", "a30", "a31", "a32", "a33", "a34", "a35", "a36", "a37", "a38", "a39", "a40", "a41", "a42", "a43", "a44", "a45", "a46", "a47", "a48", "a49", "a50", "a51", "a52", "a53", "a54", "a55", "a56", "a57", "a58", "a59", "a60", "a61", "a62", "a63", "a64", "a65", "a66", "a67", "a68", "a69", "a70", "a71", "a72", "a73", "a74", "a75", "a76", "a77", "a78", "a79", "a80", "a81", "a82", "a83", "a84", "a85", "a86", "a87", "a88", "a89", "a90", "a91", "a92", "a93", "a94", "a95", "a96", "a97", "a98", "a99", "a100", "a101", "a102", "a103", "a104", "a105", "a106", "a107", "a108", "a109", "a110", "a111", "a112", "a113", "a114", "a115", "a116", "a117", "a118", "a119", "a120", "a121", "a122", "a123", "a124", "a125", "a126", "a127", "a128", "a129", "a130", "a131", "a132", "a133", "a134", "a135", "a136", "a137", "a138", "a139", "a140", "a141", "a142", "a143", "a144", "a145", "a146", "a147", "a148", "a149", "a150", "a151", "a152", "a153", "a154", "a155", "a156", "a157", "a158", "a159", "a160", "a161", "a162", "a163", "a164", "a165", "a166", "a167", "a168", "a169", "a170", "a171", "a172", "a173", "a174", "a175", "a176", "a177", "a178", "a179", "a180", "a181", "a182", "a183", "a184", "a185", "a186", "a187", "a188", "a189", "a190", "a191", "a192", "a193", "a194", "a195", "a196", "a197", "a198", "a199", "a200", "a201", "a202", "a203", "a204", "a205", "a206", "a207", "a208", "a209", "a210", "a211", "a212", "a213", "a214", "a215", "a216", "a217", "a218", "a219", "a220", "a221", "a222", "a223", "a224", "a225", "a226", "a227", "a228", "a229", "a230", "a231", "a232", "a233", "a234", "a235", "a236", "a237", "a238", "a239", "a240", "a241", "a242", "a243", "a244", "a245", "a246", "a247", "a248", "a249", "a250", "a251", "a252", "a253", "a254", "a255", "vcc", "scc", "m0", "memory"
#define VT_ASM \
  "s_nop 4\n" \
  "s_mov_b32 s60, -1\n" \
  "s_mov_b32 s81, 0\n" \
  "s_mov_b32 s87, 2.0\n" \
  "s_mov_b32 s78, 0\n" \
  "s_mov_b32 s79, 0\n" \
  "s_mov_b32 s80, 0\n" \
  "s_add_i32 s67, %[bas], -48\n" \
  "s_mov_b32 s66, 0\n" \
  ".Lvt_top:\n" \
  "s_memrealtime s[74:75]\n" \
  "s_waitcnt lgkmcnt(0)\n" \
  "s_sub_i32 s91, s74, s66\n" \
  "s_cmp_gt_u32 s91, 14\n" \
  "s_cbranch_scc1 .Lvt_late\n" \
  ".Lvt_al_ok:\n" \
  "v_mfma_f32_16x16x32_bf16 v[64:67], a[0:3], v[80:83], 0\n" \
  "ds_read_b128 v[104:107], %[hA] offset:1536\n" \
  "v_mfma_f32_16x16x32_bf16 v[72:75], a[0:3], v[84:87], 0\n" \
  "ds_read_b128 v[108:111], %[hA] offset:19968\n" \
  "v_mfma_f32_16x16x32_bf16 v[68:71], a[8:11], v[80:83], 0\n" \
  "v_mov_b32 v225, s60\n" \
  "s_add_i32 s67, s67, 16\n" \
  "v_mfma_f32_16x16x32_bf16 v[76:79], a[8:11], v[84:87], 0\n" \
  "v_mfma_f32_16x16x32_bf16 v[64:67], a[4:7], v[88:91], v[64:67]\n" \
  "ds_read_b128 v[80:83], %[hA] offset:2048\n" \
  "v_mfma_f32_16x16x32_bf16 v[72:75], a[4:7], v[92:95], v[72:75]\n" \
  "ds_read_b128 v[84:87], %[hA] offset:20480\n" \
  "v_mfma_f32_16x16x32_bf16 v[68:71], a[12:15], v[88:91], v[68:71]\n" \
  "s_sub_i32 s90, s74, s89\n" \
  "s_max_i32 s92, s91, -2\n" \
  "s_add_i32 s89, s66, s92\n" \
  "v_mfma_f32_16x16x32_bf16 v[76:79], a[12:15], v[92:95], v[76:79]\n" \
  "s_ashr_i32 s92, s92, 1\n" \
  "s_add_i32 s90, s90, s92\n" \
  "v_mfma_f32_16x16x32_bf16 v[64:67], a[16:19], v[96:99], v[64:67]\n" \
  "s_mul_i32 s93, s60, 130\n" \
  "s_and_b32 s93, s93, 0xfc\n" \
  "ds_read_b128 v[88:91], %[hA] offset:2560\n" \
  "v_mfma_f32_16x16x32_bf16 v[72:75], a[16:19], v[100:103], v[72:75]\n" \
  "ds_read_b128 v[92:95], %[hA] offset:20992\n" \
  "v_mov_b32 v227, s90\n" \
  "v_mfma_f32_16x16x32_bf16 v[68:71], a[24:27], v[96:99], v[68:71]\n" \
  "v_mfma_f32_16x16x32_bf16 v[76:79], a[24:27], v[100:103], v[76:79]\n" \
  "s_waitcnt lgkmcnt(4)\n" \
  "v_mfma_f32_16x16x32_bf16 v[64:67], a[20:23], v[104:107], v[64:67]\n" \
  "ds_read_b128 v[96:99], %[hA] offset:3072\n" \
  "v_mfma_f32_16x16x32_bf16 v[72:75], a[20:23], v[108:111], v[72:75]\n" \
  "ds_read_b128 v[100:103], %[hA] offset:21504\n" \
  "v_mfma_f32_16x16x32_bf16 v[68:71], a[28:31], v[104:107], v[68:71]\n" \
  "v_mfma_f32_16x16x32_bf16 v[76:79], a[28:31], v[108:111], v[76:79]\n" \
  "s_waitcnt lgkmcnt(4)\n" \
  "v_mfma_f32_16x16x32_bf16 v[64:67], a[32:35], v[80:83], v[64:67]\n" \
  "ds_read_b128 v[104:107], %[hA] offset:3584\n" \
  "v_mfma_f32_16x16x32_bf16 v[72:75], a[32:35], v[84:87], v[72:75]\n" \
  "ds_read_b128 v[108:111], %[hA] offset:22016\n" \
  "v_mfma_f32_16x16x32_bf16 v[68:71], a[40:43], v[80:83], v[68:71]\n" \
  "v_mfma_f32_16x16x32_bf16 v[76:79], a[40:43], v[84:87], v[76:79]\n" \
  "s_waitcnt lgkmcnt(4)\n" \
  "v_mfma_f32_16x16x32_bf16 v[64:67], a[36:39], v[88:91], v[64:67]\n" \
  "ds_read_b128 v[80:83], %[hA] offset:4096\n" \
  "v_mfma_f32_16x16x32_bf16 v[72:75], a[36:39], v[92:95], v[72:75]\n" \
  "ds_read_b128 v[84:87], %[hA] offset:22528\n" \
  "v_mfma_f32_16x16x32_bf16 v[68:71], a[44:47], v[88:91], v[68:71]\n" \
  "s_waitcnt vmcnt(0)\n" \
  "s_barrier\n" \
  "buffer_store_dword v225, %[flo], %[X], s87 offen sc1\n" \
  "v_mfma_f32_16x16x32_bf16 v[76:79], a[44:47], v[92:95], v[76:79]\n" \
  "s_waitcnt lgkmcnt(4)\n" \
  "v_mfma_f32_16x16x32_bf16 v[64:67], a[48:51], v[96:99], v[64:67]\n" \
  "ds_read_b128 v[88:91], %[hA] offset:4608\n" \
  "v_mfma_f32_16x16x32_bf16 v[72:75], a[48:51], v[100:103], v[72:75]\n" \
  "ds_read_b128 v[92:95], %[hA] offset:23040\n" \
  "v_mfma_f32_16x16x32_bf16 v[68:71], a[56:59], v[96:99], v[68:71]\n" \
  "buffer_load_dwordx2 v[228:229], off, %[X], s67 sc1\n" \
  "v_mfma_f32_16x16x32_bf16 v[76:79], a[56:59], v[100:103], v[76:79]\n" \
  "s_waitcnt lgkmcnt(4)\n" \
  "v_mfma_f32_16x16x32_bf16 v[64:67], a[52:55], v[104:107], v[64:67]\n" \
  "ds_read_b128 v[96:99], %[hA] offset:5120\n" \
  "v_mfma_f32_16x16x32_bf16 v[72:75], a[52:55], v[108:111], v[72:75]\n" \
  "ds_read_b128 v[100:103], %[hA] offset:23552\n" \
  "v_mfma_f32_16x16x32_bf16 v[68:71], a[60:63], v[104:107], v[68:71]\n" \
  "buffer_atomic_umax v227, %[rpo], %[X], s93 offen sc1\n" \
  "v_mfma_f32_16x16x32_bf16 v[76:79], a[60:63], v[108:111], v[76:79]\n" \
  "s_waitcnt lgkmcnt(4)\n" \
  "v_mfma_f32_16x16x32_bf16 v[64:67], a[64:67], v[80:83], v[64:67]\n" \
  "ds_read_b128 v[104:107], %[hA] offset:5632\n" \
  "v_mfma_f32_16x16x32_bf16 v[72:75], a[64:67], v[84:87], v[72:75]\n" \
  "ds_read_b128 v[108:111], %[hA] offset:24064\n" \
  "v_mfma_f32_16x16x32_bf16 v[68:71], a[72:75], v[80:83], v[68:71]\n" \
  "s_add_i32 s64, s60, 1\n" \
  "s_min_i32 s61, s64, 63\n" \
  "s_mul_i32 s61, s61, 36864\n" \
  "v_mfma_f32_16x16x32_bf16 v[76:79], a[72:75], v[84:87], v[76:79]\n" \
  "s_waitcnt lgkmcnt(4)\n" \
  "v_mfma_f32_16x16x32_bf16 v[64:67], a[68:71], v[88:91], v[64:67]\n" \
  "ds_read_b128 v[80:83], %[hA] offset:6144\n" \
  "v_mfma_f32_16x16x32_bf16 v[72:75], a[68:71], v[92:95], v[72:75]\n" \
  "ds_read_b128 v[84:87], %[hA] offset:24576\n" \
  "v_mfma_f32_16x16x32_bf16 v[68:71], a[76:79], v[88:91], v[68:71]\n" \
  "s_lshl_b32 s62, s64, 16\n" \
  "s_and_b32 s62, s62, 0x10000\n" \
  "s_add_i32 s62, %[hl0], s62\n" \
  "v_mfma_f32_16x16x32_bf16 v[76:79], a[76:79], v[92:95], v[76:79]\n" \
  "s_waitcnt lgkmcnt(4)\n" \
  "v_mfma_f32_16x16x32_bf16 v[64:67], a[80:83], v[96:99], v[64:67]\n" \
  "ds_read_b128 v[88:91], %[hA] offset:6656\n" \
  "v_mfma_f32_16x16x32_bf16 v[72:75], a[80:83], v[100:103], v[72:75]\n" \
  "ds_read_b128 v[92:95], %[hA] offset:25088\n" \
  "v_mfma_f32_16x16x32_bf16 v[68:71], a[88:91], v[96:99], v[68:71]\n" \
  "s_and_b32 s63, s60, 7\n" \
  "s_lshl_b32 s63, s63, 22\n" \
  "s_add_i32 m0, s62, 0\n" \
  "v_mfma_f32_16x16x32_bf16 v[76:79], a[88:91], v[100:103], v[76:79]\n" \
  "s_waitcnt lgkmcnt(4)\n" \
  "v_mfma_f32_16x16x32_bf16 v[64:67], a[84:87], v[104:107], v[64:67]\n" \
  "ds_read_b128 v[96:99], %[hA] offset:7168\n" \
  "v_mfma_f32_16x16x32_bf16 v[72:75], a[84:87], v[108:111], v[72:75]\n" \
  "ds_read_b128 v[100:103], %[hA] offset:25600\n" \
  "v_mfma_f32_16x16x32_bf16 v[68:71], a[92:95], v[104:107], v[68:71]\n" \
  "s_cmp_lt_i32 s60, 0\n" \
  "s_cselect_b32 s63, 2.0, s63\n" \
  "s_cselect_b32 s61, 2.0, s61\n" \
  "v_mfma_f32_16x16x32_bf16 v[76:79], a[92:95], v[108:111], v[76:79]\n" \
  "s_waitcnt lgkmcnt(4)\n" \
  "v_mfma_f32_16x16x32_bf16 v[64:67], a[96:99], v[80:83], v[64:67]\n" \
  "ds_read_b128 v[104:107], %[hA] offset:7680\n" \
  "v_mfma_f32_16x16x32_bf16 v[72:75], a[96:99], v[84:87], v[72:75]\n" \
  "ds_read_b128 v[108:111], %[hA] offset:26112\n" \
  "v_mfma_f32_16x16x32_bf16 v[68:71], a[104:107], v[80:83], v[68:71]\n" \
  "v_mfma_f32_16x16x32_bf16 v[76:79], a[104:107], v[84:87], v[76:79]\n" \
  "s_waitcnt lgkmcnt(4)\n" \
  "v_mfma_f32_16x16x32_bf16 v[64:67], a[100:103], v[88:91], v[64:67]\n" \
  "ds_read_b128 v[80:83], %[hA] offset:8192\n" \
  "v_mfma_f32_16x16x32_bf16 v[72:75], a[100:103], v[92:95], v[72:75]\n" \
  "ds_read_b128 v[84:87], %[hA] offset:26624\n" \
  "v_mfma_f32_16x16x32_bf16 v[68:71], a[108:111], v[88:91], v[68:71]\n" \
  "v_mfma_f32_16x16x32_bf16 v[76:79], a[108:111], v[92:95], v[76:79]\n" \
  "s_waitcnt lgkmcnt(4)\n" \
  "v_mfma_f32_16x16x32_bf16 v[64:67], a[112:115], v[96:99], v[64:67]\n" \
  "ds_read_b128 v[88:91], %[hA] offset:8704\n" \
  "v_mfma_f32_16x16x32_bf16 v[72:75], a[112:115], v[100:103], v[72:75]\n" \
  "ds_read_b128 v[92:95], %[hA] offset:27136\n" \
  "v_mfma_f32_16x16x32_bf16 v[68:71], a[120:123], v[96:99], v[68:71]\n" \
  "v_mfma_f32_16x16x32_bf16 v[76:79], a[120:123], v[100:103], v[76:79]\n" \
  "s_waitcnt lgkmcnt(4)\n" \
  "v_mfma_f32_16x16x32_bf16 v[64:67], a[116:119], v[104:107], v[64:67]\n" \
  "v_mfma_f32_16x16x32_bf16 v[72:75], a[116:119], v[108:111], v[72:75]\n" \
  "v_mfma_f32_16x16x32_bf16 v[68:71], a[124:127], v[104:107], v[68:71]\n" \
  "v_mfma_f32_16x16x32_bf16 v[76:79], a[124:127], v[108:111], v[76:79]\n" \
  "s_waitcnt lgkmcnt(2)\n" \
  "v_mfma_f32_16x16x32_bf16 v[64:67], a[128:131], v[80:83], v[64:67]\n" \
  "buffer_load_dwordx4 %[hvo], %[HS], s61 offen offset:0 sc1 lds\n" \
  "v_mfma_f32_16x16x32_bf16 v[72:75], a[128:131], v[84:87], v[72:75]\n" \
  "v_mfma_f32_16x16x32_bf16 v[68:71], a[136:139], v[80:83], v[68:71]\n" \
  "v_mfma_f32_16x16x32_bf16 v[76:79], a[136:139], v[84:87], v[76:79]\n" \
  "s_waitcnt lgkmcnt(0)\n" \
  "v_mfma_f32_16x16x32_bf16 v[64:67], a[132:135], v[88:91], v[64:67]\n" \
  "buffer_load_dwordx4 %[hvo], %[HS], s61 offen offset:1024 sc1 lds\n" \
  "v_mfma_f32_16x16x32_bf16 v[72:75], a[132:135], v[92:95], v[72:75]\n" \
  "v_mfma_f32_16x16x32_bf16 v[68:71], a[140:143], v[88:91], v[68:71]\n" \
  "s_nop 2\n" \
  "v_mfma_f32_16x16x32_bf16 v[76:79], a[140:143], v[92:95], v[76:79]\n" \
  "buffer_load_dwordx4 %[hvo], %[HS], s61 offen offset:2048 sc1 lds\n" \
  "v_cvt_pk_bf16_f32 v112, v64, v65\n" \
  "v_cvt_pk_bf16_f32 v113, v66, v67\n" \
  "s_nop 0\n" \
  "v_cvt_pk_bf16_f32 v114, v68, v69\n" \
  "v_cvt_pk_bf16_f32 v115, v70, v71\n" \
  "ds_write_b128 %[vW], v[112:115]\n" \
  "v_cvt_pk_bf16_f32 v116, v72, v73\n" \
  "v_cvt_pk_bf16_f32 v117, v74, v75\n" \
  "v_cvt_pk_bf16_f32 v118, v76, v77\n" \
  "v_cvt_pk_bf16_f32 v119, v78, v79\n" \
  "ds_write_b128 %[vW], v[116:119] offset:4096\n" \
  "buffer_load_dwordx4 %[hvo], %[HS], s61 offen offset:3072 sc1 lds\n" \
  "v_cmp_u_f32 vcc, v64, v72\n" \
  "s_cbranch_vccnz .Lvt_sh\n" \
  ".Lvt_sh_back:\n" \
  "s_waitcnt lgkmcnt(0)\n" \
  "s_barrier\n" \
  "s_add_i32 s61, s61, 0x1000\n" \
  "s_add_i32 m0, s62, 4096\n" \
  "s_nop 0\n" \
  "buffer_load_dwordx4 %[hvo], %[HS], s61 offen offset:0 sc1 lds\n" \
  "ds_read_b128 v[120:123], %[vR] offset:0\n" \
  "ds_read_b128 v[136:139], %[vR] offset:4096\n" \
  "ds_read_b128 v[124:127], %[vR] offset:1024\n" \
  "ds_read_b128 v[140:143], %[vR] offset:5120\n" \
  "ds_read_b128 v[128:131], %[vR] offset:2048\n" \
  "ds_read_b128 v[144:147], %[vR] offset:6144\n" \
  "ds_read_b128 v[132:135], %[vR] offset:3072\n" \
  "ds_read_b128 v[148:151], %[vR] offset:7168\n" \
  "buffer_load_dwordx4 %[hvo], %[HS], s61 offen offset:1024 sc1 lds\n" \
  "s_waitcnt lgkmcnt(6)\n" \
  "v_mfma_f32_16x16x32_bf16 v[152:155], v[120:123], a[144:147], 0\n" \
  "buffer_load_dwordx4 %[hvo], %[HS], s61 offen offset:2048 sc1 lds\n" \
  "v_mfma_f32_16x16x32_bf16 v[156:159], v[136:139], a[144:147], 0\n" \
  "s_waitcnt lgkmcnt(4)\n" \
  "v_mfma_f32_16x16x32_bf16 v[152:155], v[124:127], a[148:151], v[152:155]\n" \
  "v_mfma_f32_16x16x32_bf16 v[156:159], v[140:143], a[148:151], v[156:159]\n" \
  "s_waitcnt lgkmcnt(2)\n" \
  "v_mfma_f32_16x16x32_bf16 v[152:155], v[128:131], a[152:155], v[152:155]\n" \
  "buffer_load_dwordx4 %[hvo], %[HS], s61 offen offset:3072 sc1 lds\n" \
  "v_mfma_f32_16x16x32_bf16 v[156:159], v[144:147], a[152:155], v[156:159]\n" \
  "s_waitcnt lgkmcnt(0)\n" \
  "v_mfma_f32_16x16x32_bf16 v[152:155], v[132:135], a[156:159], v[152:155]\n" \
  "v_mfma_f32_16x16x32_bf16 v[156:159], v[148:151], a[156:159], v[156:159]\n" \
  "v_mfma_f32_16x16x32_bf16 v[160:163], v[120:123], a[160:163], 0\n" \
  "s_add_i32 s61, s61, 0x1000\n" \
  "s_add_i32 m0, s62, 8192\n" \
  "s_nop 0\n" \
  "buffer_load_dwordx4 %[hvo], %[HS], s61 offen offset:0 sc1 lds\n" \
  "v_mfma_f32_16x16x32_bf16 v[164:167], v[136:139], a[160:163], 0\n" \
  "v_mfma_f32_16x16x32_bf16 v[160:163], v[124:127], a[164:167], v[160:163]\n" \
  "v_cvt_pk_bf16_f32 v176, v152, v153\n" \
  "v_cvt_pk_bf16_f32 v177, v154, v155\n" \
  "v_mfma_f32_16x16x32_bf16 v[164:167], v[140:143], a[164:167], v[164:167]\n" \
  "v_cvt_pk_bf16_f32 v178, v156, v157\n" \
  "v_cvt_pk_bf16_f32 v179, v158, v159\n" \
  "v_mfma_f32_16x16x32_bf16 v[160:163], v[128:131], a[168:171], v[160:163]\n" \
  "buffer_store_dwordx4 v[176:179], %[sto], %[X], s63 offen sc1\n" \
  "s_add_i32 s63, s63, 0x10000\n" \
  "v_mfma_f32_16x16x32_bf16 v[164:167], v[144:147], a[168:171], v[164:167]\n" \
  "v_mfma_f32_16x16x32_bf16 v[160:163], v[132:135], a[172:175], v[160:163]\n" \
  "v_mfma_f32_16x16x32_bf16 v[164:167], v[148:151], a[172:175], v[164:167]\n" \
  "v_mfma_f32_16x16x32_bf16 v[168:171], v[120:123], a[176:179], 0\n" \
  "s_nop 0\n" \
  "v_mfma_f32_16x16x32_bf16 v[172:175], v[136:139], a[176:179], 0\n" \
  "s_nop 2\n" \
  "v_mfma_f32_16x16x32_bf16 v[168:171], v[124:127], a[180:183], v[168:171]\n" \
  "v_cvt_pk_bf16_f32 v180, v160, v161\n" \
  "v_cvt_pk_bf16_f32 v181, v162, v163\n" \
  "v_mfma_f32_16x16x32_bf16 v[172:175], v[140:143], a[180:183], v[172:175]\n" \
  "v_cvt_pk_bf16_f32 v182, v164, v165\n" \
  "v_cvt_pk_bf16_f32 v183, v166, v167\n" \
  "v_mfma_f32_16x16x32_bf16 v[168:171], v[128:131], a[184:187], v[168:171]\n" \
  "buffer_store_dwordx4 v[180:183], %[sto], %[X], s63 offen sc1\n" \
  "s_add_i32 s63, s63, 0x10000\n" \
  "v_mfma_f32_16x16x32_bf16 v[172:175], v[144:147], a[184:187], v[172:175]\n" \
  "v_mfma_f32_16x16x32_bf16 v[168:171], v[132:135], a[188:191], v[168:171]\n" \
  "v_mfma_f32_16x16x32_bf16 v[172:175], v[148:151], a[188:191], v[172:175]\n" \
  "v_mfma_f32_16x16x32_bf16 v[152:155], v[120:123], a[192:195], 0\n" \
  "s_nop 0\n" \
  "v_mfma_f32_16x16x32_bf16 v[156:159], v[136:139], a[192:195], 0\n" \
  "s_nop 2\n" \
  "v_mfma_f32_16x16x32_bf16 v[152:155], v[124:127], a[196:199], v[152:155]\n" \
  "v_cvt_pk_bf16_f32 v184, v168, v169\n" \
  "v_cvt_pk_bf16_f32 v185, v170, v171\n" \
  "v_mfma_f32_16x16x32_bf16 v[156:159], v[140:143], a[196:199], v[156:159]\n" \
  "v_cvt_pk_bf16_f32 v186, v172, v173\n" \
  "v_cvt_pk_bf16_f32 v187, v174, v175\n" \
  "v_mfma_f32_16x16x32_bf16 v[152:155], v[128:131], a[200:203], v[152:155]\n" \
  "buffer_store_dwordx4 v[184:187], %[sto], %[X], s63 offen sc1\n" \
  "s_add_i32 s63, s63, 0x10000\n" \
  "v_mfma_f32_16x16x32_bf16 v[156:159], v[144:147], a[200:203], v[156:159]\n" \
  "v_mfma_f32_16x16x32_bf16 v[152:155], v[132:135], a[204:207], v[152:155]\n" \
  "v_mfma_f32_16x16x32_bf16 v[156:159], v[148:151], a[204:207], v[156:159]\n" \
  "v_mfma_f32_16x16x32_bf16 v[160:163], v[120:123], a[208:211], 0\n" \
  "s_nop 0\n" \
  "v_mfma_f32_16x16x32_bf16 v[164:167], v[136:139], a[208:211], 0\n" \
  "s_nop 2\n" \
  "v_mfma_f32_16x16x32_bf16 v[160:163], v[124:127], a[212:215], v[160:163]\n" \
  "v_cvt_pk_bf16_f32 v176, v152, v153\n" \
  "v_cvt_pk_bf16_f32 v177, v154, v155\n" \
  "v_mfma_f32_16x16x32_bf16 v[164:167], v[140:143], a[212:215], v[164:167]\n" \
  "v_cvt_pk_bf16_f32 v178, v156, v157\n" \
  "v_cvt_pk_bf16_f32 v179, v158, v159\n" \
  "v_mfma_f32_16x16x32_bf16 v[160:163], v[128:131], a[216:219], v[160:163]\n" \
  "buffer_store_dwordx4 v[176:179], %[sto], %[X], s63 offen sc1\n" \
  "s_add_i32 s63, s63, 0x10000\n" \
  "v_mfma_f32_16x16x32_bf16 v[164:167], v[144:147], a[216:219], v[164:167]\n" \
  "v_mfma_f32_16x16x32_bf16 v[160:163], v[132:135], a[220:223], v[160:163]\n" \
  "v_mfma_f32_16x16x32_bf16 v[164:167], v[148:151], a[220:223], v[164:167]\n" \
  "v_mfma_f32_16x16x32_bf16 v[168:171], v[120:123], a[224:227], 0\n" \
  "s_nop 0\n" \
  "v_mfma_f32_16x16x32_bf16 v[172:175], v[136:139], a[224:227], 0\n" \
  "s_nop 2\n" \
  "v_mfma_f32_16x16x32_bf16 v[168:171], v[124:127], a[228:231], v[168:171]\n" \
  "v_cvt_pk_bf16_f32 v180, v160, v161\n" \
  "v_cvt_pk_bf16_f32 v181, v162, v163\n" \
  "v_mfma_f32_16x16x32_bf16 v[172:175], v[140:143], a[228:231], v[172:175]\n" \
  "v_cvt_pk_bf16_f32 v182, v164, v165\n" \
  "v_cvt_pk_bf16_f32 v183, v166, v167\n" \
  "v_mfma_f32_16x16x32_bf16 v[168:171], v[128:131], a[232:235], v[168:171]\n" \
  "buffer_store_dwordx4 v[180:183], %[sto], %[X], s63 offen sc1\n" \
  "s_add_i32 s63, s63, %[px]\n" \
  "v_mfma_f32_16x16x32_bf16 v[172:175], v[144:147], a[232:235], v[172:175]\n" \
  "v_mfma_f32_16x16x32_bf16 v[168:171], v[132:135], a[236:239], v[168:171]\n" \
  "v_mfma_f32_16x16x32_bf16 v[172:175], v[148:151], a[236:239], v[172:175]\n" \
  "v_mfma_f32_16x16x32_bf16 v[152:155], v[120:123], a[240:243], 0\n" \
  "s_nop 0\n" \
  "v_mfma_f32_16x16x32_bf16 v[156:159], v[136:139], a[240:243], 0\n" \
  "s_nop 2\n" \
  "v_mfma_f32_16x16x32_bf16 v[152:155], v[124:127], a[244:247], v[152:155]\n" \
  "v_cvt_pk_bf16_f32 v184, v168, v169\n" \
  "v_cvt_pk_bf16_f32 v185, v170, v171\n" \
  "v_mfma_f32_16x16x32_bf16 v[156:159], v[140:143], a[244:247], v[156:159]\n" \
  "v_cvt_pk_bf16_f32 v186, v172, v173\n" \
  "v_cvt_pk_bf16_f32 v187, v174, v175\n" \
  "v_mfma_f32_16x16x32_bf16 v[152:155], v[128:131], a[248:251], v[152:155]\n" \
  "buffer_store_dwordx4 v[184:187], %[sto], %[X], s63 offen sc1\n" \
  "s_add_i32 s63, s63, 0x10000\n" \
  "v_mfma_f32_16x16x32_bf16 v[156:159], v[144:147], a[248:251], v[156:159]\n" \
  "v_mfma_f32_16x16x32_bf16 v[152:155], v[132:135], a[252:255], v[152:155]\n" \
  "v_mfma_f32_16x16x32_bf16 v[156:159], v[148:151], a[252:255], v[156:159]\n" \
  "v_mfma_f32_16x16x32_bf16 v[160:163], v[120:123], v[32:35], 0\n" \
  "s_nop 0\n" \
  "v_mfma_f32_16x16x32_bf16 v[164:167], v[136:139], v[32:35], 0\n" \
  "s_nop 2\n" \
  "v_mfma_f32_16x16x32_bf16 v[160:163], v[124:127], v[36:39], v[160:163]\n" \
  "v_cvt_pk_bf16_f32 v176, v152, v153\n" \
  "v_cvt_pk_bf16_f32 v177, v154, v155\n" \
  "v_mfma_f32_16x16x32_bf16 v[164:167], v[140:143], v[36:39], v[164:167]\n" \
  "v_cvt_pk_bf16_f32 v178, v156, v157\n" \
  "v_cvt_pk_bf16_f32 v179, v158, v159\n" \
  "v_mfma_f32_16x16x32_bf16 v[160:163], v[128:131], v[40:43], v[160:163]\n" \
  "buffer_store_dwordx4 v[176:179], %[sto], %[X], s63 offen sc1\n" \
  "s_add_i32 s63, s63, 0x10000\n" \
  "v_mfma_f32_16x16x32_bf16 v[164:167], v[144:147], v[40:43], v[164:167]\n" \
  "v_mfma_f32_16x16x32_bf16 v[160:163], v[132:135], v[44:47], v[160:163]\n" \
  "v_mfma_f32_16x16x32_bf16 v[164:167], v[148:151], v[44:47], v[164:167]\n" \
  "v_mfma_f32_16x16x32_bf16 v[168:171], v[120:123], v[48:51], 0\n" \
  "s_nop 0\n" \
  "v_mfma_f32_16x16x32_bf16 v[172:175], v[136:139], v[48:51], 0\n" \
  "s_nop 2\n" \
  "v_mfma_f32_16x16x32_bf16 v[168:171], v[124:127], v[52:55], v[168:171]\n" \
  "v_cvt_pk_bf16_f32 v180, v160, v161\n" \
  "v_cvt_pk_bf16_f32 v181, v162, v163\n" \
  "v_mfma_f32_16x16x32_bf16 v[172:175], v[140:143], v[52:55], v[172:175]\n" \
  "v_cvt_pk_bf16_f32 v182, v164, v165\n" \
  "v_cvt_pk_bf16_f32 v183, v166, v167\n" \
  "v_mfma_f32_16x16x32_bf16 v[168:171], v[128:131], v[56:59], v[168:171]\n" \
  "buffer_store_dwordx4 v[180:183], %[sto], %[X], s63 offen sc1\n" \
  "s_add_i32 s63, s63, 0x10000\n" \
  "v_mfma_f32_16x16x32_bf16 v[172:175], v[144:147], v[56:59], v[172:175]\n" \
  "v_mfma_f32_16x16x32_bf16 v[168:171], v[132:135], v[60:63], v[168:171]\n" \
  "s_nop 2\n" \
  "v_mfma_f32_16x16x32_bf16 v[172:175], v[148:151], v[60:63], v[172:175]\n" \
  "s_nop 3\n" \
  "v_cvt_pk_bf16_f32 v184, v168, v169\n" \
  "v_cvt_pk_bf16_f32 v185, v170, v171\n" \
  "s_nop 1\n" \
  "v_cvt_pk_bf16_f32 v186, v172, v173\n" \
  "v_cvt_pk_bf16_f32 v187, v174, v175\n" \
  "buffer_store_dwordx4 v[184:187], %[sto], %[X], s63 offen sc1\n" \
  "s_add_i32 s63, s63, 0x10000\n" \
  "v_xor_b32 %[hA], 0x10000, %[hA]\n" \
  "s_waitcnt vmcnt(9)\n" \
  "s_barrier\n" \
  "ds_read_b128 v[80:83], %[hA] offset:0\n" \
  "ds_read_b128 v[84:87], %[hA] offset:18432\n" \
  "ds_read_b128 v[88:91], %[hA] offset:512\n" \
  "ds_read_b128 v[92:95], %[hA] offset:18944\n" \
  "ds_read_b128 v[96:99], %[hA] offset:1024\n" \
  "ds_read_b128 v[100:103], %[hA] offset:19456\n" \
  "v_readfirstlane_b32 s64, v228\n" \
  "s_cmp_eq_u32 s64, 0\n" \
  "s_cbranch_scc1 .Lvt_w\n" \
  ".Lvt_w_ok:\n" \
  "v_readfirstlane_b32 s66, v229\n" \
  "s_add_i32 s66, s66, %[vtc]\n" \
  "s_add_i32 s60, s60, 1\n" \
  "s_cmp_lg_u32 s60, s81\n" \
  "s_cbranch_scc1 .Lvt_top\n" \
  "s_cmp_eq_u32 s60, 0\n" \
  "s_cbranch_scc1 .Lvt_entry\n" \
  "v_mov_b32 v225, 64\n" \
  "s_waitcnt vmcnt(0)\n" \
  "s_barrier\n" \
  "buffer_store_dword v225, %[flo], %[X], s87 offen sc1\n" \
  "s_branch .Lvt_end\n" \
  ".Lvt_entry:\n" \
  "s_mov_b32 s81, 64\n" \
  "s_mov_b32 s87, 0\n" \
  "s_waitcnt vmcnt(0) lgkmcnt(0)\n" \
  "s_barrier\n" \
  "v_xor_b32 v188, 0x80, %[wv]\n" \
  "v_xor_b32 v189, 32, %[wr]\n" \
  "v_add_u32 v190, 0x10000, %[wr]\n" \
  "v_add_u32 v191, 0x10000, v189\n" \
  "s_cmp_eq_u32 %[grp], 0\n" \
  "s_cbranch_scc1 .Lvt_ws1\n" \
  "s_sleep 8\n" \
  ".Lvt_ws1:\n" \
  "s_add_i32 m0, %[wl], 0\n" \
  "s_mov_b32 s76, 0\n" \
  "buffer_load_dwordx4 %[wv], %[WA], s76 offen lds\n" \
  "s_add_i32 m0, %[wl], 1024\n" \
  "s_mov_b32 s76, 32768\n" \
  "buffer_load_dwordx4 %[wv], %[WA], s76 offen lds\n" \
  "s_add_i32 m0, %[wl], 2048\n" \
  "s_mov_b32 s76, 65536\n" \
  "buffer_load_dwordx4 v188, %[WA], s76 offen lds\n" \
  "s_add_i32 m0, %[wl], 3072\n" \
  "s_mov_b32 s76, 98304\n" \
  "buffer_load_dwordx4 v188, %[WA], s76 offen lds\n" \
  "s_cmp_eq_u32 %[grp], 0\n" \
  "s_cbranch_scc1 .Lvt_ws2\n" \
  "s_sleep 8\n" \
  ".Lvt_ws2:\n" \
  "s_add_i32 m0, %[wl], 16384\n" \
  "s_mov_b32 s76, 524288\n" \
  "buffer_load_dwordx4 %[wv], %[WA], s76 offen lds\n" \
  "s_add_i32 m0, %[wl], 17408\n" \
  "s_mov_b32 s76, 557056\n" \
  "buffer_load_dwordx4 %[wv], %[WA], s76 offen lds\n" \
  "s_add_i32 m0, %[wl], 18432\n" \
  "s_mov_b32 s76, 589824\n" \
  "buffer_load_dwordx4 v188, %[WA], s76 offen lds\n" \
  "s_add_i32 m0, %[wl], 19456\n" \
  "s_mov_b32 s76, 622592\n" \
  "buffer_load_dwordx4 v188, %[WA], s76 offen lds\n" \
  "s_cmp_eq_u32 %[grp], 0\n" \
  "s_cbranch_scc1 .Lvt_ws3\n" \
  "s_sleep 8\n" \
  ".Lvt_ws3:\n" \
  "s_add_i32 m0, %[wl], 32768\n" \
  "s_mov_b32 s76, 1048576\n" \
  "buffer_load_dwordx4 %[wv], %[WA], s76 offen lds\n" \
  "s_add_i32 m0, %[wl], 33792\n" \
  "s_mov_b32 s76, 1081344\n" \
  "buffer_load_dwordx4 %[wv], %[WA], s76 offen lds\n" \
  "s_add_i32 m0, %[wl], 34816\n" \
  "s_mov_b32 s76, 1114112\n" \
  "buffer_load_dwordx4 v188, %[WA], s76 offen lds\n" \
  "s_add_i32 m0, %[wl], 35840\n" \
  "s_mov_b32 s76, 1146880\n" \
  "buffer_load_dwordx4 v188, %[WA], s76 offen lds\n" \
  "s_cmp_eq_u32 %[grp], 0\n" \
  "s_cbranch_scc1 .Lvt_ws4\n" \
  "s_sleep 8\n" \
  ".Lvt_ws4:\n" \
  "s_add_i32 m0, %[wl], 49152\n" \
  "s_mov_b32 s76, 1572864\n" \
  "buffer_load_dwordx4 %[wv], %[WA], s76 offen lds\n" \
  "s_add_i32 m0, %[wl], 50176\n" \
  "s_mov_b32 s76, 1605632\n" \
  "buffer_load_dwordx4 %[wv], %[WA], s76 offen lds\n" \
  "s_add_i32 m0, %[wl], 51200\n" \
  "s_mov_b32 s76, 1638400\n" \
  "buffer_load_dwordx4 v188, %[WA], s76 offen lds\n" \
  "s_add_i32 m0, %[wl], 52224\n" \
  "s_mov_b32 s76, 1671168\n" \
  "buffer_load_dwordx4 v188, %[WA], s76 offen lds\n" \
  "s_cmp_eq_u32 %[grp], 0\n" \
  "s_cbranch_scc1 .Lvt_ws5\n" \
  "s_sleep 8\n" \
  ".Lvt_ws5:\n" \
  "s_add_i32 m0, %[wl], 65536\n" \
  "s_mov_b32 s76, 2097152\n" \
  "buffer_load_dwordx4 %[wv], %[WA], s76 offen lds\n" \
  "s_add_i32 m0, %[wl], 66560\n" \
  "s_mov_b32 s76, 2129920\n" \
  "buffer_load_dwordx4 %[wv], %[WA], s76 offen lds\n" \
  "s_add_i32 m0, %[wl], 67584\n" \
  "s_mov_b32 s76, 2162688\n" \
  "buffer_load_dwordx4 v188, %[WA], s76 offen lds\n" \
  "s_add_i32 m0, %[wl], 68608\n" \
  "s_mov_b32 s76, 2195456\n" \
  "buffer_load_dwordx4 v188, %[WA], s76 offen lds\n" \
  "s_cmp_eq_u32 %[grp], 0\n" \
  "s_cbranch_scc1 .Lvt_ws6\n" \
  "s_sleep 8\n" \
  ".Lvt_ws6:\n" \
  "s_add_i32 m0, %[wl], 81920\n" \
  "s_mov_b32 s76, 2621440\n" \
  "buffer_load_dwordx4 %[wv], %[WA], s76 offen lds\n" \
  "s_add_i32 m0, %[wl], 82944\n" \
  "s_mov_b32 s76, 2654208\n" \
  "buffer_load_dwordx4 %[wv], %[WA], s76 offen lds\n" \
  "s_add_i32 m0, %[wl], 83968\n" \
  "s_mov_b32 s76, 2686976\n" \
  "buffer_load_dwordx4 v188, %[WA], s76 offen lds\n" \
  "s_add_i32 m0, %[wl], 84992\n" \
  "s_mov_b32 s76, 2719744\n" \
  "buffer_load_dwordx4 v188, %[WA], s76 offen lds\n" \
  "s_cmp_eq_u32 %[grp], 0\n" \
  "s_cbranch_scc1 .Lvt_ws7\n" \
  "s_sleep 8\n" \
  ".Lvt_ws7:\n" \
  "s_add_i32 m0, %[wl], 98304\n" \
  "s_mov_b32 s76, 3145728\n" \
  "buffer_load_dwordx4 %[wv], %[WA], s76 offen lds\n" \
  "s_add_i32 m0, %[wl], 99328\n" \
  "s_mov_b32 s76, 3178496\n" \
  "buffer_load_dwordx4 %[wv], %[WA], s76 offen lds\n" \
  "s_add_i32 m0, %[wl], 100352\n" \
  "s_mov_b32 s76, 3211264\n" \
  "buffer_load_dwordx4 v188, %[WA], s76 offen lds\n" \
  "s_add_i32 m0, %[wl], 101376\n" \
  "s_mov_b32 s76, 3244032\n" \
  "buffer_load_dwordx4 v188, %[WA], s76 offen lds\n" \
  "s_cmp_eq_u32 %[grp], 0\n" \
  "s_cbranch_scc1 .Lvt_ws8\n" \
  "s_sleep 8\n" \
  ".Lvt_ws8:\n" \
  "s_add_i32 m0, %[wl], 114688\n" \
  "s_mov_b32 s76, 3670016\n" \
  "buffer_load_dwordx4 %[wv], %[WA], s76 offen lds\n" \
  "s_add_i32 m0, %[wl], 115712\n" \
  "s_mov_b32 s76, 3702784\n" \
  "buffer_load_dwordx4 %[wv], %[WA], s76 offen lds\n" \
  "s_add_i32 m0, %[wl], 116736\n" \
  "s_mov_b32 s76, 3735552\n" \
  "buffer_load_dwordx4 v188, %[WA], s76 offen lds\n" \
  "s_add_i32 m0, %[wl], 117760\n" \
  "s_mov_b32 s76, 3768320\n" \
  "buffer_load_dwordx4 v188, %[WA], s76 offen lds\n" \
  "s_waitcnt vmcnt(28)\n" \
  "s_waitcnt lgkmcnt(0)\n" \
  "s_barrier\n" \
  "ds_read_b64_tr_b16 a[0:1], %[wr] offset:0\n" \
  "ds_read_b64_tr_b16 a[2:3], %[wr] offset:1024\n" \
  "ds_read_b64_tr_b16 a[4:5], %[wr] offset:8192\n" \
  "ds_read_b64_tr_b16 a[6:7], %[wr] offset:9216\n" \
  "ds_read_b64_tr_b16 a[8:9], v189 offset:0\n" \
  "ds_read_b64_tr_b16 a[10:11], v189 offset:1024\n" \
  "ds_read_b64_tr_b16 a[12:13], v189 offset:8192\n" \
  "ds_read_b64_tr_b16 a[14:15], v189 offset:9216\n" \
  "s_waitcnt vmcnt(24)\n" \
  "s_waitcnt lgkmcnt(0)\n" \
  "s_barrier\n" \
  "s_cmp_eq_u32 %[grp], 0\n" \
  "s_cbranch_scc1 .Lvt_ws9\n" \
  "s_sleep 8\n" \
  ".Lvt_ws9:\n" \
  "s_add_i32 m0, %[wl], 0\n" \
  "s_mov_b32 s76, 4194304\n" \
  "buffer_load_dwordx4 %[wv], %[WA], s76 offen lds\n" \
  "s_add_i32 m0, %[wl], 1024\n" \
  "s_mov_b32 s76, 4227072\n" \
  "buffer_load_dwordx4 %[wv], %[WA], s76 offen lds\n" \
  "s_add_i32 m0, %[wl], 2048\n" \
  "s_mov_b32 s76, 4259840\n" \
  "buffer_load_dwordx4 v188, %[WA], s76 offen lds\n" \
  "s_add_i32 m0, %[wl], 3072\n" \
  "s_mov_b32 s76, 4292608\n" \
  "buffer_load_dwordx4 v188, %[WA], s76 offen lds\n" \
  "ds_read_b64_tr_b16 a[16:17], %[wr] offset:16384\n" \
  "ds_read_b64_tr_b16 a[18:19], %[wr] offset:17408\n" \
  "ds_read_b64_tr_b16 a[20:21], %[wr] offset:24576\n" \
  "ds_read_b64_tr_b16 a[22:23], %[wr] offset:25600\n" \
  "ds_read_b64_tr_b16 a[24:25], v189 offset:16384\n" \
  "ds_read_b64_tr_b16 a[26:27], v189 offset:17408\n" \
  "ds_read_b64_tr_b16 a[28:29], v189 offset:24576\n" \
  "ds_read_b64_tr_b16 a[30:31], v189 offset:25600\n" \
  "s_waitcnt vmcnt(24)\n" \
  "s_waitcnt lgkmcnt(0)\n" \
  "s_barrier\n" \
  "ds_read_b64_tr_b16 a[32:33], %[wr] offset:32768\n" \
  "ds_read_b64_tr_b16 a[34:35], %[wr] offset:33792\n" \
  "ds_read_b64_tr_b16 a[36:37], %[wr] offset:40960\n" \
  "ds_read_b64_tr_b16 a[38:39], %[wr] offset:41984\n" \
  "ds_read_b64_tr_b16 a[40:41], v189 offset:32768\n" \
  "ds_read_b64_tr_b16 a[42:43], v189 offset:33792\n" \
  "ds_read_b64_tr_b16 a[44:45], v189 offset:40960\n" \
  "ds_read_b64_tr_b16 a[46:47], v189 offset:41984\n" \
  "s_waitcnt vmcnt(20)\n" \
  "s_waitcnt lgkmcnt(0)\n" \
  "s_barrier\n" \
  "ds_read_b64_tr_b16 a[48:49], %[wr] offset:49152\n" \
  "ds_read_b64_tr_b16 a[50:51], %[wr] offset:50176\n" \
  "ds_read_b64_tr_b16 a[52:53], %[wr] offset:57344\n" \
  "ds_read_b64_tr_b16 a[54:55], %[wr] offset:58368\n" \
  "ds_read_b64_tr_b16 a[56:57], v189 offset:49152\n" \
  "ds_read_b64_tr_b16 a[58:59], v189 offset:50176\n" \
  "ds_read_b64_tr_b16 a[60:61], v189 offset:57344\n" \
  "ds_read_b64_tr_b16 a[62:63], v189 offset:58368\n" \
  "s_waitcnt vmcnt(16)\n" \
  "s_waitcnt lgkmcnt(0)\n" \
  "s_barrier\n" \
  "s_cmp_eq_u32 %[grp], 0\n" \
  "s_cbranch_scc1 .Lvt_ws10\n" \
  "s_sleep 16\n" \
  ".Lvt_ws10:\n" \
  "s_add_i32 m0, %[hl0], 16384\n" \
  "s_mov_b32 s76, 0\n" \
  "buffer_load_dwordx4 %[hvo], %[WB], s76 offen offset:0 lds\n" \
  "buffer_load_dwordx4 %[hvo], %[WB], s76 offen offset:1024 lds\n" \
  "buffer_load_dwordx4 %[hvo], %[WB], s76 offen offset:2048 lds\n" \
  "buffer_load_dwordx4 %[hvo], %[WB], s76 offen offset:3072 lds\n" \
  "s_add_i32 m0, %[hl0], 20480\n" \
  "s_mov_b32 s76, 4096\n" \
  "buffer_load_dwordx4 %[hvo], %[WB], s76 offen offset:0 lds\n" \
  "buffer_load_dwordx4 %[hvo], %[WB], s76 offen offset:1024 lds\n" \
  "buffer_load_dwordx4 %[hvo], %[WB], s76 offen offset:2048 lds\n" \
  "buffer_load_dwordx4 %[hvo], %[WB], s76 offen offset:3072 lds\n" \
  "s_add_i32 m0, %[hl0], 24576\n" \
  "s_mov_b32 s76, 8192\n" \
  "buffer_load_dwordx4 %[hvo], %[WB], s76 offen offset:0 lds\n" \
  "ds_read_b64_tr_b16 a[64:65], v190 offset:0\n" \
  "ds_read_b64_tr_b16 a[66:67], v190 offset:1024\n" \
  "ds_read_b64_tr_b16 a[68:69], v190 offset:8192\n" \
  "ds_read_b64_tr_b16 a[70:71], v190 offset:9216\n" \
  "ds_read_b64_tr_b16 a[72:73], v191 offset:0\n" \
  "ds_read_b64_tr_b16 a[74:75], v191 offset:1024\n" \
  "ds_read_b64_tr_b16 a[76:77], v191 offset:8192\n" \
  "ds_read_b64_tr_b16 a[78:79], v191 offset:9216\n" \
  "s_waitcnt vmcnt(21)\n" \
  "s_waitcnt lgkmcnt(0)\n" \
  "s_barrier\n" \
  "ds_read_b64_tr_b16 a[80:81], v190 offset:16384\n" \
  "ds_read_b64_tr_b16 a[82:83], v190 offset:17408\n" \
  "ds_read_b64_tr_b16 a[84:85], v190 offset:24576\n" \
  "ds_read_b64_tr_b16 a[86:87], v190 offset:25600\n" \
  "ds_read_b64_tr_b16 a[88:89], v191 offset:16384\n" \
  "ds_read_b64_tr_b16 a[90:91], v191 offset:17408\n" \
  "ds_read_b64_tr_b16 a[92:93], v191 offset:24576\n" \
  "ds_read_b64_tr_b16 a[94:95], v191 offset:25600\n" \
  "s_waitcnt vmcnt(17)\n" \
  "s_waitcnt lgkmcnt(0)\n" \
  "s_barrier\n" \
  "s_cmp_eq_u32 %[grp], 0\n" \
  "s_cbranch_scc1 .Lvt_ws11\n" \
  "s_sleep 16\n" \
  ".Lvt_ws11:\n" \
  "s_add_i32 m0, %[hl0], 53248\n" \
  "s_mov_b32 s76, 36864\n" \
  "buffer_load_dwordx4 %[hvo], %[WB], s76 offen offset:0 lds\n" \
  "buffer_load_dwordx4 %[hvo], %[WB], s76 offen offset:1024 lds\n" \
  "buffer_load_dwordx4 %[hvo], %[WB], s76 offen offset:2048 lds\n" \
  "buffer_load_dwordx4 %[hvo], %[WB], s76 offen offset:3072 lds\n" \
  "s_add_i32 m0, %[hl0], 57344\n" \
  "s_mov_b32 s76, 40960\n" \
  "buffer_load_dwordx4 %[hvo], %[WB], s76 offen offset:0 lds\n" \
  "buffer_load_dwordx4 %[hvo], %[WB], s76 offen offset:1024 lds\n" \
  "buffer_load_dwordx4 %[hvo], %[WB], s76 offen offset:2048 lds\n" \
  "buffer_load_dwordx4 %[hvo], %[WB], s76 offen offset:3072 lds\n" \
  "s_add_i32 m0, %[hl0], 61440\n" \
  "s_mov_b32 s76, 45056\n" \
  "buffer_load_dwordx4 %[hvo], %[WB], s76 offen offset:0 lds\n" \
  "ds_read_b64_tr_b16 a[96:97], v190 offset:32768\n" \
  "ds_read_b64_tr_b16 a[98:99], v190 offset:33792\n" \
  "ds_read_b64_tr_b16 a[100:101], v190 offset:40960\n" \
  "ds_read_b64_tr_b16 a[102:103], v190 offset:41984\n" \
  "ds_read_b64_tr_b16 a[104:105], v191 offset:32768\n" \
  "ds_read_b64_tr_b16 a[106:107], v191 offset:33792\n" \
  "ds_read_b64_tr_b16 a[108:109], v191 offset:40960\n" \
  "ds_read_b64_tr_b16 a[110:111], v191 offset:41984\n" \
  "s_waitcnt vmcnt(22)\n" \
  "s_waitcnt lgkmcnt(0)\n" \
  "s_barrier\n" \
  "ds_read_b64_tr_b16 a[112:113], v190 offset:49152\n" \
  "ds_read_b64_tr_b16 a[114:115], v190 offset:50176\n" \
  "ds_read_b64_tr_b16 a[116:117], v190 offset:57344\n" \
  "ds_read_b64_tr_b16 a[118:119], v190 offset:58368\n" \
  "ds_read_b64_tr_b16 a[120:121], v191 offset:49152\n" \
  "ds_read_b64_tr_b16 a[122:123], v191 offset:50176\n" \
  "ds_read_b64_tr_b16 a[124:125], v191 offset:57344\n" \
  "ds_read_b64_tr_b16 a[126:127], v191 offset:58368\n" \
  "s_waitcnt vmcnt(18)\n" \
  "s_waitcnt lgkmcnt(0)\n" \
  "s_barrier\n" \
  "s_cmp_eq_u32 %[grp], 0\n" \
  "s_cbranch_scc1 .Lvt_ws12\n" \
  "s_sleep 16\n" \
  ".Lvt_ws12:\n" \
  "s_add_i32 m0, %[hl0], 90112\n" \
  "s_mov_b32 s76, 73728\n" \
  "buffer_load_dwordx4 %[hvo], %[WB], s76 offen offset:0 lds\n" \
  "buffer_load_dwordx4 %[hvo], %[WB], s76 offen offset:1024 lds\n" \
  "buffer_load_dwordx4 %[hvo], %[WB], s76 offen offset:2048 lds\n" \
  "buffer_load_dwordx4 %[hvo], %[WB], s76 offen offset:3072 lds\n" \
  "s_add_i32 m0, %[hl0], 94208\n" \
  "s_mov_b32 s76, 77824\n" \
  "buffer_load_dwordx4 %[hvo], %[WB], s76 offen offset:0 lds\n" \
  "buffer_load_dwordx4 %[hvo], %[WB], s76 offen offset:1024 lds\n" \
  "buffer_load_dwordx4 %[hvo], %[WB], s76 offen offset:2048 lds\n" \
  "buffer_load_dwordx4 %[hvo], %[WB], s76 offen offset:3072 lds\n" \
  "s_add_i32 m0, %[hl0], 98304\n" \
  "s_mov_b32 s76, 81920\n" \
  "buffer_load_dwordx4 %[hvo], %[WB], s76 offen offset:0 lds\n" \
  "ds_read_b64_tr_b16 a[128:129], %[wr] offset:0\n" \
  "ds_read_b64_tr_b16 a[130:131], %[wr] offset:1024\n" \
  "ds_read_b64_tr_b16 a[132:133], %[wr] offset:8192\n" \
  "ds_read_b64_tr_b16 a[134:135], %[wr] offset:9216\n" \
  "ds_read_b64_tr_b16 a[136:137], v189 offset:0\n" \
  "ds_read_b64_tr_b16 a[138:139], v189 offset:1024\n" \
  "ds_read_b64_tr_b16 a[140:141], v189 offset:8192\n" \
  "ds_read_b64_tr_b16 a[142:143], v189 offset:9216\n" \
  "s_waitcnt vmcnt(18)\n" \
  "s_waitcnt lgkmcnt(0)\n" \
  "s_barrier\n" \
  "v_add_u32 v192, 16384, %[wr2]\n" \
  "ds_read_b64_tr_b16 a[144:145], v192 offset:0\n" \
  "ds_read_b64_tr_b16 a[146:147], v192 offset:18432\n" \
  "ds_read_b64_tr_b16 a[160:161], v192 offset:32\n" \
  "ds_read_b64_tr_b16 a[162:163], v192 offset:18464\n" \
  "ds_read_b64_tr_b16 a[176:177], v192 offset:64\n" \
  "ds_read_b64_tr_b16 a[178:179], v192 offset:18496\n" \
  "ds_read_b64_tr_b16 a[192:193], v192 offset:96\n" \
  "ds_read_b64_tr_b16 a[194:195], v192 offset:18528\n" \
  "ds_read_b64_tr_b16 a[208:209], v192 offset:128\n" \
  "ds_read_b64_tr_b16 a[210:211], v192 offset:18560\n" \
  "ds_read_b64_tr_b16 a[224:225], v192 offset:160\n" \
  "ds_read_b64_tr_b16 a[226:227], v192 offset:18592\n" \
  "ds_read_b64_tr_b16 a[240:241], v192 offset:192\n" \
  "ds_read_b64_tr_b16 a[242:243], v192 offset:18624\n" \
  "ds_read_b64_tr_b16 v[32:33], v192 offset:224\n" \
  "ds_read_b64_tr_b16 v[34:35], v192 offset:18656\n" \
  "ds_read_b64_tr_b16 v[48:49], v192 offset:256\n" \
  "ds_read_b64_tr_b16 v[50:51], v192 offset:18688\n" \
  "s_waitcnt vmcnt(9)\n" \
  "s_waitcnt lgkmcnt(0)\n" \
  "s_barrier\n" \
  "s_cmp_eq_u32 %[grp], 0\n" \
  "s_cbranch_scc1 .Lvt_ws13\n" \
  "s_sleep 16\n" \
  ".Lvt_ws13:\n" \
  "s_add_i32 m0, %[hl0], 0\n" \
  "s_mov_b32 s76, 110592\n" \
  "buffer_load_dwordx4 %[hvo], %[WB], s76 offen offset:0 lds\n" \
  "buffer_load_dwordx4 %[hvo], %[WB], s76 offen offset:1024 lds\n" \
  "buffer_load_dwordx4 %[hvo], %[WB], s76 offen offset:2048 lds\n" \
  "buffer_load_dwordx4 %[hvo], %[WB], s76 offen offset:3072 lds\n" \
  "s_add_i32 m0, %[hl0], 4096\n" \
  "s_mov_b32 s76, 114688\n" \
  "buffer_load_dwordx4 %[hvo], %[WB], s76 offen offset:0 lds\n" \
  "buffer_load_dwordx4 %[hvo], %[WB], s76 offen offset:1024 lds\n" \
  "buffer_load_dwordx4 %[hvo], %[WB], s76 offen offset:2048 lds\n" \
  "buffer_load_dwordx4 %[hvo], %[WB], s76 offen offset:3072 lds\n" \
  "s_add_i32 m0, %[hl0], 8192\n" \
  "s_mov_b32 s76, 118784\n" \
  "buffer_load_dwordx4 %[hvo], %[WB], s76 offen offset:0 lds\n" \
  "v_add_u32 v192, 53248, %[wr2]\n" \
  "ds_read_b64_tr_b16 a[148:149], v192 offset:0\n" \
  "ds_read_b64_tr_b16 a[150:151], v192 offset:18432\n" \
  "ds_read_b64_tr_b16 a[164:165], v192 offset:32\n" \
  "ds_read_b64_tr_b16 a[166:167], v192 offset:18464\n" \
  "ds_read_b64_tr_b16 a[180:181], v192 offset:64\n" \
  "ds_read_b64_tr_b16 a[182:183], v192 offset:18496\n" \
  "ds_read_b64_tr_b16 a[196:197], v192 offset:96\n" \
  "ds_read_b64_tr_b16 a[198:199], v192 offset:18528\n" \
  "ds_read_b64_tr_b16 a[212:213], v192 offset:128\n" \
  "ds_read_b64_tr_b16 a[214:215], v192 offset:18560\n" \
  "ds_read_b64_tr_b16 a[228:229], v192 offset:160\n" \
  "ds_read_b64_tr_b16 a[230:231], v192 offset:18592\n" \
  "ds_read_b64_tr_b16 a[244:245], v192 offset:192\n" \
  "ds_read_b64_tr_b16 a[246:247], v192 offset:18624\n" \
  "ds_read_b64_tr_b16 v[36:37], v192 offset:224\n" \
  "ds_read_b64_tr_b16 v[38:39], v192 offset:18656\n" \
  "ds_read_b64_tr_b16 v[52:53], v192 offset:256\n" \
  "ds_read_b64_tr_b16 v[54:55], v192 offset:18688\n" \
  "s_waitcnt vmcnt(9)\n" \
  "s_waitcnt lgkmcnt(0)\n" \
  "s_barrier\n" \
  "v_add_u32 v192, 90112, %[wr2]\n" \
  "ds_read_b64_tr_b16 a[152:153], v192 offset:0\n" \
  "ds_read_b64_tr_b16 a[154:155], v192 offset:18432\n" \
  "ds_read_b64_tr_b16 a[168:169], v192 offset:32\n" \
  "ds_read_b64_tr_b16 a[170:171], v192 offset:18464\n" \
  "ds_read_b64_tr_b16 a[184:185], v192 offset:64\n" \
  "ds_read_b64_tr_b16 a[186:187], v192 offset:18496\n" \
  "ds_read_b64_tr_b16 a[200:201], v192 offset:96\n" \
  "ds_read_b64_tr_b16 a[202:203], v192 offset:18528\n" \
  "ds_read_b64_tr_b16 a[216:217], v192 offset:128\n" \
  "ds_read_b64_tr_b16 a[218:219], v192 offset:18560\n" \
  "ds_read_b64_tr_b16 a[232:233], v192 offset:160\n" \
  "ds_read_b64_tr_b16 a[234:235], v192 offset:18592\n" \
  "ds_read_b64_tr_b16 a[248:249], v192 offset:192\n" \
  "ds_read_b64_tr_b16 a[250:251], v192 offset:18624\n" \
  "ds_read_b64_tr_b16 v[40:41], v192 offset:224\n" \
  "ds_read_b64_tr_b16 v[42:43], v192 offset:18656\n" \
  "ds_read_b64_tr_b16 v[56:57], v192 offset:256\n" \
  "ds_read_b64_tr_b16 v[58:59], v192 offset:18688\n" \
  "s_waitcnt vmcnt(0)\n" \
  "s_waitcnt lgkmcnt(0)\n" \
  "s_barrier\n" \
  "v_add_u32 v192, 0, %[wr2]\n" \
  "ds_read_b64_tr_b16 a[156:157], v192 offset:0\n" \
  "ds_read_b64_tr_b16 a[158:159], v192 offset:18432\n" \
  "ds_read_b64_tr_b16 a[172:173], v192 offset:32\n" \
  "ds_read_b64_tr_b16 a[174:175], v192 offset:18464\n" \
  "ds_read_b64_tr_b16 a[188:189], v192 offset:64\n" \
  "ds_read_b64_tr_b16 a[190:191], v192 offset:18496\n" \
  "ds_read_b64_tr_b16 a[204:205], v192 offset:96\n" \
  "ds_read_b64_tr_b16 a[206:207], v192 offset:18528\n" \
  "ds_read_b64_tr_b16 a[220:221], v192 offset:128\n" \
  "ds_read_b64_tr_b16 a[222:223], v192 offset:18560\n" \
  "ds_read_b64_tr_b16 a[236:237], v192 offset:160\n" \
  "ds_read_b64_tr_b16 a[238:239], v192 offset:18592\n" \
  "ds_read_b64_tr_b16 a[252:253], v192 offset:192\n" \
  "ds_read_b64_tr_b16 a[254:255], v192 offset:18624\n" \
  "ds_read_b64_tr_b16 v[44:45], v192 offset:224\n" \
  "ds_read_b64_tr_b16 v[46:47], v192 offset:18656\n" \
  "ds_read_b64_tr_b16 v[60:61], v192 offset:256\n" \
  "ds_read_b64_tr_b16 v[62:63], v192 offset:18688\n" \
  "s_waitcnt lgkmcnt(0)\n" \
  "s_barrier\n" \
  "s_cmp_eq_u32 %[grp], 0\n" \
  "s_cbranch_scc1 .Lvt_go\n" \
  "s_mov_b32 s65, 0\n" \
  "s_xor_b32 s75, %[bas], 0x2000\n" \
  ".Lvt_g1:\n" \
  "buffer_load_dword v224, off, %[X], s75 offset:48 sc1\n" \
  "s_waitcnt vmcnt(0)\n" \
  "v_readfirstlane_b32 s74, v224\n" \
  "s_cmp_lg_u32 s74, 0\n" \
  "s_cbranch_scc1 .Lvt_g2\n" \
  "s_sleep 8\n" \
  "s_add_i32 s65, s65, 1\n" \
  "s_cmp_lt_u32 s65, 400\n" \
  "s_cbranch_scc1 .Lvt_g1\n" \
  ".Lvt_g2:\n" \
  "s_add_i32 s74, s74, %[g1t]\n" \
  ".Lvt_g3:\n" \
  "s_memrealtime s[76:77]\n" \
  "s_waitcnt lgkmcnt(0)\n" \
  "s_sub_i32 s76, s76, s74\n" \
  "s_cmp_gt_u32 s76, -3000\n" \
  "s_cbranch_scc1 .Lvt_g3\n" \
  ".Lvt_go:\n" \
  "s_waitcnt vmcnt(0) lgkmcnt(0)\n" \
  "s_barrier\n" \
  "s_mov_b32 s75, 0\n" \
  "s_movk_i32 s76, 0x1000\n" \
  "s_movk_i32 s77, 0x2000\n" \
  "v_mov_b32 v226, 1\n" \
  "buffer_atomic_add v226, %[rdy], %[X], %[bas] offen offset:1536 sc0 sc1\n" \
  "s_mov_b32 s65, 0\n" \
  ".Lvt_enw:\n" \
  "buffer_load_dwordx4 v[188:191], %[hvo], %[HS], s75 offen offset:0 sc1\n" \
  "buffer_load_dwordx4 v[192:195], %[hvo], %[HS], s75 offen offset:1024 sc1\n" \
  "buffer_load_dwordx4 v[196:199], %[hvo], %[HS], s75 offen offset:2048 sc1\n" \
  "buffer_load_dwordx4 v[200:203], %[hvo], %[HS], s75 offen offset:3072 sc1\n" \
  "buffer_load_dwordx4 v[204:207], %[hvo], %[HS], s76 offen offset:0 sc1\n" \
  "buffer_load_dwordx4 v[208:211], %[hvo], %[HS], s76 offen offset:1024 sc1\n" \
  "buffer_load_dwordx4 v[212:215], %[hvo], %[HS], s76 offen offset:2048 sc1\n" \
  "buffer_load_dwordx4 v[216:219], %[hvo], %[HS], s76 offen offset:3072 sc1\n" \
  "buffer_load_dwordx4 v[220:223], %[hvo], %[HS], s77 offen offset:0 sc1\n" \
  "s_waitcnt vmcnt(0)\n" \
  "v_max3_u32 v224, v188, v189, v190\n" \
  "v_max3_u32 v224, v224, v191, v192\n" \
  "v_max3_u32 v224, v224, v193, v194\n" \
  "v_max3_u32 v224, v224, v195, v196\n" \
  "v_max3_u32 v224, v224, v197, v198\n" \
  "v_max3_u32 v224, v224, v199, v200\n" \
  "v_max3_u32 v224, v224, v201, v202\n" \
  "v_max3_u32 v224, v224, v203, v204\n" \
  "v_max3_u32 v224, v224, v205, v206\n" \
  "v_max3_u32 v224, v224, v207, v208\n" \
  "v_max3_u32 v224, v224, v209, v210\n" \
  "v_max3_u32 v224, v224, v211, v212\n" \
  "v_max3_u32 v224, v224, v213, v214\n" \
  "v_max3_u32 v224, v224, v215, v216\n" \
  "v_max3_u32 v224, v224, v217, v218\n" \
  "v_max3_u32 v224, v224, v219, v220\n" \
  "v_max3_u32 v224, v224, v221, v222\n" \
  "v_max_u32 v224, v224, v223\n" \
  "v_cmp_eq_u32 vcc, -1, v224\n" \
  "s_cbranch_vccz .Lvt_enw_ok\n" \
  "s_sleep 4\n" \
  "s_add_i32 s65, s65, 1\n" \
  "s_and_b32 s64, s65, 1023\n" \
  "s_cbranch_scc1 .Lvt_enw\n" \
  "buffer_load_dword v224, off, %[F], 0 offset:1024 sc1\n" \
  "s_waitcnt vmcnt(0)\n" \
  "v_readfirstlane_b32 s64, v224\n" \
  "s_cmp_lg_u32 s64, 0\n" \
  "s_cbranch_scc1 .Lvt_enw_ok\n" \
  "s_cmp_lt_u32 s65, 400000\n" \
  "s_cbranch_scc1 .Lvt_enw\n" \
  "v_mov_b32 v224, 1\n" \
  "buffer_store_dword v224, off, %[F], 0 offset:1024 sc1\n" \
  ".Lvt_enw_ok:\n" \
  "ds_write_b128 %[hvo], v[188:191] offset:0\n" \
  "ds_write_b128 %[hvo], v[192:195] offset:1024\n" \
  "ds_write_b128 %[hvo], v[196:199] offset:2048\n" \
  "ds_write_b128 %[hvo], v[200:203] offset:3072\n" \
  "ds_write_b128 %[hvo], v[204:207] offset:4096\n" \
  "ds_write_b128 %[hvo], v[208:211] offset:5120\n" \
  "ds_write_b128 %[hvo], v[212:215] offset:6144\n" \
  "ds_write_b128 %[hvo], v[216:219] offset:7168\n" \
  "ds_write_b128 %[hvo], v[220:223] offset:8192\n" \
  "v_readfirstlane_b32 s92, v226\n" \
  "s_cmp_lg_u32 s92, 63\n" \
  "s_cbranch_scc1 .Lvt_nrdy\n" \
  "s_memrealtime s[74:75]\n" \
  "s_waitcnt lgkmcnt(0)\n" \
  "s_add_i32 s74, s74, %[x0]\n" \
  "s_or_b32 s74, s74, 1\n" \
  "s_mul_i32 s75, %[tau], 162\n" \
  "s_lshr_b32 s75, s75, 8\n" \
  "s_add_i32 s75, s75, s74\n" \
  "v_mov_b32 v226, s74\n" \
  "v_mov_b32 v227, s75\n" \
  "buffer_store_dwordx2 v[226:227], %[rdy], %[X], %[bas] offen offset:0 sc1\n" \
  "s_add_i32 s74, s74, %[tau]\n" \
  "s_or_b32 s74, s74, 1\n" \
  "s_add_i32 s75, s75, %[tau]\n" \
  "v_mov_b32 v226, s74\n" \
  "v_mov_b32 v227, s75\n" \
  "buffer_store_dwordx2 v[226:227], %[rdy], %[X], %[bas] offen offset:16 sc1\n" \
  ".Lvt_nrdy:\n" \
  "s_waitcnt lgkmcnt(0)\n" \
  "s_barrier\n" \
  "ds_read_b128 v[80:83], %[hA] offset:0\n" \
  "ds_read_b128 v[84:87], %[hA] offset:18432\n" \
  "ds_read_b128 v[88:91], %[hA] offset:512\n" \
  "ds_read_b128 v[92:95], %[hA] offset:18944\n" \
  "ds_read_b128 v[96:99], %[hA] offset:1024\n" \
  "ds_read_b128 v[100:103], %[hA] offset:19456\n" \
  "s_waitcnt lgkmcnt(0)\n" \
  "s_branch .Lvt_al_ok\n" \
  ".Lvt_w:\n" \
  "s_cmp_lt_i32 s60, 1\n" \
  "s_cbranch_scc1 .Lvt_w_ok\n" \
  "s_mov_b32 s65, 0\n" \
  ".Lvt_ww:\n" \
  "buffer_load_dwordx2 v[228:229], off, %[X], s67 sc1\n" \
  "s_waitcnt vmcnt(0)\n" \
  "v_readfirstlane_b32 s64, v228\n" \
  "s_cmp_lg_u32 s64, 0\n" \
  "s_cbranch_scc1 .Lvt_w_ok\n" \
  "s_sleep 2\n" \
  "s_add_i32 s65, s65, 1\n" \
  "s_and_b32 s64, s65, 1023\n" \
  "s_cbranch_scc1 .Lvt_ww\n" \
  "buffer_load_dword v224, off, %[F], 0 offset:1024 sc1\n" \
  "s_waitcnt vmcnt(0)\n" \
  "v_readfirstlane_b32 s64, v224\n" \
  "s_cmp_lg_u32 s64, 0\n" \
  "s_cbranch_scc1 .Lvt_ww_ok\n" \
  "s_cmp_lt_u32 s65, 400000\n" \
  "s_cbranch_scc1 .Lvt_ww\n" \
  "v_mov_b32 v224, 1\n" \
  "buffer_store_dword v224, off, %[F], 0 offset:1024 sc1\n" \
  ".Lvt_ww_ok:\n" \
  "s_branch .Lvt_w_ok\n" \
  ".Lvt_sh:\n" \
  "s_cmp_lt_i32 s60, 0\n" \
  "s_cbranch_scc1 .Lvt_sh_back\n" \
  "s_mul_i32 s75, s60, 36864\n" \
  "s_add_i32 s76, s75, 0x1000\n" \
  "s_add_i32 s77, s75, 0x2000\n" \
  "s_waitcnt lgkmcnt(0)\n" \
  "s_barrier\n" \
  "s_mov_b32 s65, 0\n" \
  ".Lvt_shw:\n" \
  "buffer_load_dwordx4 v[188:191], %[hvo], %[HS], s75 offen offset:0 sc1\n" \
  "buffer_load_dwordx4 v[192:195], %[hvo], %[HS], s75 offen offset:1024 sc1\n" \
  "buffer_load_dwordx4 v[196:199], %[hvo], %[HS], s75 offen offset:2048 sc1\n" \
  "buffer_load_dwordx4 v[200:203], %[hvo], %[HS], s75 offen offset:3072 sc1\n" \
  "buffer_load_dwordx4 v[204:207], %[hvo], %[HS], s76 offen offset:0 sc1\n" \
  "buffer_load_dwordx4 v[208:211], %[hvo], %[HS], s76 offen offset:1024 sc1\n" \
  "buffer_load_dwordx4 v[212:215], %[hvo], %[HS], s76 offen offset:2048 sc1\n" \
  "buffer_load_dwordx4 v[216:219], %[hvo], %[HS], s76 offen offset:3072 sc1\n" \
  "buffer_load_dwordx4 v[220:223], %[hvo], %[HS], s77 offen offset:0 sc1\n" \
  "s_waitcnt vmcnt(0)\n" \
  "v_max3_u32 v224, v188, v189, v190\n" \
  "v_max3_u32 v224, v224, v191, v192\n" \
  "v_max3_u32 v224, v224, v193, v194\n" \
  "v_max3_u32 v224, v224, v195, v196\n" \
  "v_max3_u32 v224, v224, v197, v198\n" \
  "v_max3_u32 v224, v224, v199, v200\n" \
  "v_max3_u32 v224, v224, v201, v202\n" \
  "v_max3_u32 v224, v224, v203, v204\n" \
  "v_max3_u32 v224, v224, v205, v206\n" \
  "v_max3_u32 v224, v224, v207, v208\n" \
  "v_max3_u32 v224, v224, v209, v210\n" \
  "v_max3_u32 v224, v224, v211, v212\n" \
  "v_max3_u32 v224, v224, v213, v214\n" \
  "v_max3_u32 v224, v224, v215, v216\n" \
  "v_max3_u32 v224, v224, v217, v218\n" \
  "v_max3_u32 v224, v224, v219, v220\n" \
  "v_max3_u32 v224, v224, v221, v222\n" \
  "v_max_u32 v224, v224, v223\n" \
  "v_cmp_eq_u32 vcc, -1, v224\n" \
  "s_cbranch_vccz .Lvt_shw_ok\n" \
  "s_sleep 4\n" \
  "s_add_i32 s65, s65, 1\n" \
  "s_and_b32 s64, s65, 1023\n" \
  "s_cbranch_scc1 .Lvt_shw\n" \
  "buffer_load_dword v224, off, %[F], 0 offset:1024 sc1\n" \
  "s_waitcnt vmcnt(0)\n" \
  "v_readfirstlane_b32 s64, v224\n" \
  "s_cmp_lg_u32 s64, 0\n" \
  "s_cbranch_scc1 .Lvt_shw_ok\n" \
  "s_cmp_lt_u32 s65, 400000\n" \
  "s_cbranch_scc1 .Lvt_shw\n" \
  "v_mov_b32 v224, 1\n" \
  "buffer_store_dword v224, off, %[F], 0 offset:1024 sc1\n" \
  ".Lvt_shw_ok:\n" \
  "s_xor_b32 s64, s62, 0x10000\n" \
  "v_add_u32 v224, s64, %[la]\n" \
  "ds_write_b128 v224, v[188:191] offset:0\n" \
  "ds_write_b128 v224, v[192:195] offset:1024\n" \
  "ds_write_b128 v224, v[196:199] offset:2048\n" \
  "ds_write_b128 v224, v[200:203] offset:3072\n" \
  "ds_write_b128 v224, v[204:207] offset:4096\n" \
  "ds_write_b128 v224, v[208:211] offset:5120\n" \
  "ds_write_b128 v224, v[212:215] offset:6144\n" \
  "ds_write_b128 v224, v[216:219] offset:7168\n" \
  "ds_write_b128 v224, v[220:223] offset:8192\n" \
  "s_waitcnt lgkmcnt(0)\n" \
  "s_barrier\n" \
  "ds_read_b128 v[80:83], %[hA] offset:0\n" \
  "ds_read_b128 v[84:87], %[hA] offset:18432\n" \
  "ds_read_b128 v[88:91], %[hA] offset:512\n" \
  "ds_read_b128 v[92:95], %[hA] offset:18944\n" \
  "ds_read_b128 v[96:99], %[hA] offset:1024\n" \
  "ds_read_b128 v[100:103], %[hA] offset:19456\n" \
  "s_waitcnt lgkmcnt(4)\n" \
  "v_mfma_f32_16x16x32_bf16 v[64:67], a[0:3], v[80:83], 0\n" \
  "ds_read_b128 v[104:107], %[hA] offset:1536\n" \
  "v_mfma_f32_16x16x32_bf16 v[72:75], a[0:3], v[84:87], 0\n" \
  "ds_read_b128 v[108:111], %[hA] offset:19968\n" \
  "v_mfma_f32_16x16x32_bf16 v[68:71], a[8:11], v[80:83], 0\n" \
  "v_mfma_f32_16x16x32_bf16 v[76:79], a[8:11], v[84:87], 0\n" \
  "s_waitcnt lgkmcnt(4)\n" \
  "v_mfma_f32_16x16x32_bf16 v[64:67], a[4:7], v[88:91], v[64:67]\n" \
  "ds_read_b128 v[80:83], %[hA] offset:2048\n" \
  "v_mfma_f32_16x16x32_bf16 v[72:75], a[4:7], v[92:95], v[72:75]\n" \
  "ds_read_b128 v[84:87], %[hA] offset:20480\n" \
  "v_mfma_f32_16x16x32_bf16 v[68:71], a[12:15], v[88:91], v[68:71]\n" \
  "v_mfma_f32_16x16x32_bf16 v[76:79], a[12:15], v[92:95], v[76:79]\n" \
  "s_waitcnt lgkmcnt(4)\n" \
  "v_mfma_f32_16x16x32_bf16 v[64:67], a[16:19], v[96:99], v[64:67]\n" \
  "ds_read_b128 v[88:91], %[hA] offset:2560\n" \
  "v_mfma_f32_16x16x32_bf16 v[72:75], a[16:19], v[100:103], v[72:75]\n" \
  "ds_read_b128 v[92:95], %[hA] offset:20992\n" \
  "v_mfma_f32_16x16x32_bf16 v[68:71], a[24:27], v[96:99], v[68:71]\n" \
  "v_mfma_f32_16x16x32_bf16 v[76:79], a[24:27], v[100:103], v[76:79]\n" \
  "s_waitcnt lgkmcnt(4)\n" \
  "v_mfma_f32_16x16x32_bf16 v[64:67], a[20:23], v[104:107], v[64:67]\n" \
  "ds_read_b128 v[96:99], %[hA] offset:3072\n" \
  "v_mfma_f32_16x16x32_bf16 v[72:75], a[20:23], v[108:111], v[72:75]\n" \
  "ds_read_b128 v[100:103], %[hA] offset:21504\n" \
  "v_mfma_f32_16x16x32_bf16 v[68:71], a[28:31], v[104:107], v[68:71]\n" \
  "v_mfma_f32_16x16x32_bf16 v[76:79], a[28:31], v[108:111], v[76:79]\n" \
  "s_waitcnt lgkmcnt(4)\n" \
  "v_mfma_f32_16x16x32_bf16 v[64:67], a[32:35], v[80:83], v[64:67]\n" \
  "ds_read_b128 v[104:107], %[hA] offset:3584\n" \
  "v_mfma_f32_16x16x32_bf16 v[72:75], a[32:35], v[84:87], v[72:75]\n" \
  "ds_read_b128 v[108:111], %[hA] offset:22016\n" \
  "v_mfma_f32_16x16x32_bf16 v[68:71], a[40:43], v[80:83], v[68:71]\n" \
  "v_mfma_f32_16x16x32_bf16 v[76:79], a[40:43], v[84:87], v[76:79]\n" \
  "s_waitcnt lgkmcnt(4)\n" \
  "v_mfma_f32_16x16x32_bf16 v[64:67], a[36:39], v[88:91], v[64:67]\n" \
  "ds_read_b128 v[80:83], %[hA] offset:4096\n" \
  "v_mfma_f32_16x16x32_bf16 v[72:75], a[36:39], v[92:95], v[72:75]\n" \
  "ds_read_b128 v[84:87], %[hA] offset:22528\n" \
  "v_mfma_f32_16x16x32_bf16 v[68:71], a[44:47], v[88:91], v[68:71]\n" \
  "v_mfma_f32_16x16x32_bf16 v[76:79], a[44:47], v[92:95], v[76:79]\n" \
  "s_waitcnt lgkmcnt(4)\n" \
  "v_mfma_f32_16x16x32_bf16 v[64:67], a[48:51], v[96:99], v[64:67]\n" \
  "ds_read_b128 v[88:91], %[hA] offset:4608\n" \
  "v_mfma_f32_16x16x32_bf16 v[72:75], a[48:51], v[100:103], v[72:75]\n" \
  "ds_read_b128 v[92:95], %[hA] offset:23040\n" \
  "v_mfma_f32_16x16x32_bf16 v[68:71], a[56:59], v[96:99], v[68:71]\n" \
  "v_mfma_f32_16x16x32_bf16 v[76:79], a[56:59], v[100:103], v[76:79]\n" \
  "s_waitcnt lgkmcnt(4)\n" \
  "v_mfma_f32_16x16x32_bf16 v[64:67], a[52:55], v[104:107], v[64:67]\n" \
  "ds_read_b128 v[96:99], %[hA] offset:5120\n" \
  "v_mfma_f32_16x16x32_bf16 v[72:75], a[52:55], v[108:111], v[72:75]\n" \
  "ds_read_b128 v[100:103], %[hA] offset:23552\n" \
  "v_mfma_f32_16x16x32_bf16 v[68:71], a[60:63], v[104:107], v[68:71]\n" \
  "v_mfma_f32_16x16x32_bf16 v[76:79], a[60:63], v[108:111], v[76:79]\n" \
  "s_waitcnt lgkmcnt(4)\n" \
  "v_mfma_f32_16x16x32_bf16 v[64:67], a[64:67], v[80:83], v[64:67]\n" \
  "ds_read_b128 v[104:107], %[hA] offset:5632\n" \
  "v_mfma_f32_16x16x32_bf16 v[72:75], a[64:67], v[84:87], v[72:75]\n" \
  "ds_read_b128 v[108:111], %[hA] offset:24064\n" \
  "v_mfma_f32_16x16x32_bf16 v[68:71], a[72:75], v[80:83], v[68:71]\n" \
  "v_mfma_f32_16x16x32_bf16 v[76:79], a[72:75], v[84:87], v[76:79]\n" \
  "s_waitcnt lgkmcnt(4)\n" \
  "v_mfma_f32_16x16x32_bf16 v[64:67], a[68:71], v[88:91], v[64:67]\n" \
  "ds_read_b128 v[80:83], %[hA] offset:6144\n" \
  "v_mfma_f32_16x16x32_bf16 v[72:75], a[68:71], v[92:95], v[72:75]\n" \
  "ds_read_b128 v[84:87], %[hA] offset:24576\n" \
  "v_mfma_f32_16x16x32_bf16 v[68:71], a[76:79], v[88:91], v[68:71]\n" \
  "v_mfma_f32_16x16x32_bf16 v[76:79], a[76:79], v[92:95], v[76:79]\n" \
  "s_waitcnt lgkmcnt(4)\n" \
  "v_mfma_f32_16x16x32_bf16 v[64:67], a[80:83], v[96:99], v[64:67]\n" \
  "ds_read_b128 v[88:91], %[hA] offset:6656\n" \
  "v_mfma_f32_16x16x32_bf16 v[72:75], a[80:83], v[100:103], v[72:75]\n" \
  "ds_read_b128 v[92:95], %[hA] offset:25088\n" \
  "v_mfma_f32_16x16x32_bf16 v[68:71], a[88:91], v[96:99], v[68:71]\n" \
  "v_mfma_f32_16x16x32_bf16 v[76:79], a[88:91], v[100:103], v[76:79]\n" \
  "s_waitcnt lgkmcnt(4)\n" \
  "v_mfma_f32_16x16x32_bf16 v[64:67], a[84:87], v[104:107], v[64:67]\n" \
  "ds_read_b128 v[96:99], %[hA] offset:7168\n" \
  "v_mfma_f32_16x16x32_bf16 v[72:75], a[84:87], v[108:111], v[72:75]\n" \
  "ds_read_b128 v[100:103], %[hA] offset:25600\n" \
  "v_mfma_f32_16x16x32_bf16 v[68:71], a[92:95], v[104:107], v[68:71]\n" \
  "v_mfma_f32_16x16x32_bf16 v[76:79], a[92:95], v[108:111], v[76:79]\n" \
  "s_waitcnt lgkmcnt(4)\n" \
  "v_mfma_f32_16x16x32_bf16 v[64:67], a[96:99], v[80:83], v[64:67]\n" \
  "ds_read_b128 v[104:107], %[hA] offset:7680\n" \
  "v_mfma_f32_16x16x32_bf16 v[72:75], a[96:99], v[84:87], v[72:75]\n" \
  "ds_read_b128 v[108:111], %[hA] offset:26112\n" \
  "v_mfma_f32_16x16x32_bf16 v[68:71], a[104:107], v[80:83], v[68:71]\n" \
  "v_mfma_f32_16x16x32_bf16 v[76:79], a[104:107], v[84:87], v[76:79]\n" \
  "s_waitcnt lgkmcnt(4)\n" \
  "v_mfma_f32_16x16x32_bf16 v[64:67], a[100:103], v[88:91], v[64:67]\n" \
  "ds_read_b128 v[80:83], %[hA] offset:8192\n" \
  "v_mfma_f32_16x16x32_bf16 v[72:75], a[100:103], v[92:95], v[72:75]\n" \
  "ds_read_b128 v[84:87], %[hA] offset:26624\n" \
  "v_mfma_f32_16x16x32_bf16 v[68:71], a[108:111], v[88:91], v[68:71]\n" \
  "v_mfma_f32_16x16x32_bf16 v[76:79], a[108:111], v[92:95], v[76:79]\n" \
  "s_waitcnt lgkmcnt(4)\n" \
  "v_mfma_f32_16x16x32_bf16 v[64:67], a[112:115], v[96:99], v[64:67]\n" \
  "ds_read_b128 v[88:91], %[hA] offset:8704\n" \
  "v_mfma_f32_16x16x32_bf16 v[72:75], a[112:115], v[100:103], v[72:75]\n" \
  "ds_read_b128 v[92:95], %[hA] offset:27136\n" \
  "v_mfma_f32_16x16x32_bf16 v[68:71], a[120:123], v[96:99], v[68:71]\n" \
  "v_mfma_f32_16x16x32_bf16 v[76:79], a[120:123], v[100:103], v[76:79]\n" \
  "s_waitcnt lgkmcnt(4)\n" \
  "v_mfma_f32_16x16x32_bf16 v[64:67], a[116:119], v[104:107], v[64:67]\n" \
  "v_mfma_f32_16x16x32_bf16 v[72:75], a[116:119], v[108:111], v[72:75]\n" \
  "v_mfma_f32_16x16x32_bf16 v[68:71], a[124:127], v[104:107], v[68:71]\n" \
  "v_mfma_f32_16x16x32_bf16 v[76:79], a[124:127], v[108:111], v[76:79]\n" \
  "s_waitcnt lgkmcnt(2)\n" \
  "v_mfma_f32_16x16x32_bf16 v[64:67], a[128:131], v[80:83], v[64:67]\n" \
  "v_mfma_f32_16x16x32_bf16 v[72:75], a[128:131], v[84:87], v[72:75]\n" \
  "v_mfma_f32_16x16x32_bf16 v[68:71], a[136:139], v[80:83], v[68:71]\n" \
  "v_mfma_f32_16x16x32_bf16 v[76:79], a[136:139], v[84:87], v[76:79]\n" \
  "s_waitcnt lgkmcnt(0)\n" \
  "v_mfma_f32_16x16x32_bf16 v[64:67], a[132:135], v[88:91], v[64:67]\n" \
  "v_mfma_f32_16x16x32_bf16 v[72:75], a[132:135], v[92:95], v[72:75]\n" \
  "v_mfma_f32_16x16x32_bf16 v[68:71], a[140:143], v[88:91], v[68:71]\n" \
  "v_mfma_f32_16x16x32_bf16 v[76:79], a[140:143], v[92:95], v[76:79]\n" \
  "s_nop 7\n" \
  "v_cvt_pk_bf16_f32 v112, v64, v65\n" \
  "v_cvt_pk_bf16_f32 v113, v66, v67\n" \
  "v_cvt_pk_bf16_f32 v114, v68, v69\n" \
  "v_cvt_pk_bf16_f32 v115, v70, v71\n" \
  "ds_write_b128 %[vW], v[112:115]\n" \
  "v_cvt_pk_bf16_f32 v116, v72, v73\n" \
  "v_cvt_pk_bf16_f32 v117, v74, v75\n" \
  "v_cvt_pk_bf16_f32 v118, v76, v77\n" \
  "v_cvt_pk_bf16_f32 v119, v78, v79\n" \
  "ds_write_b128 %[vW], v[116:119] offset:4096\n" \
  "s_branch .Lvt_sh_back\n" \
  ".Lvt_late:\n" \
  "s_cmp_lt_i32 s60, 0\n" \
  "s_cbranch_scc1 .Lvt_al_ok\n" \
  "s_cmp_gt_u32 s91, -3000\n" \
  "s_cbranch_scc0 .Lvt_late2\n" \
  ".Lvt_wt:\n" \
  "s_memrealtime s[76:77]\n" \
  "s_waitcnt lgkmcnt(0)\n" \
  "s_sub_i32 s76, s76, s66\n" \
  "s_cmp_lt_i32 s76, 0\n" \
  "s_cbranch_scc1 .Lvt_wt\n" \
  "s_branch .Lvt_al_ok\n" \
  ".Lvt_late2:\n" \
  "s_cmp_lg_u32 s66, 0\n" \
  "s_cbranch_scc1 .Lvt_late3\n" \
  "s_mov_b32 s65, 0\n" \
  ".Lvt_tw:\n" \
  "buffer_load_dword v224, off, %[X], s67 sc1\n" \
  "s_waitcnt vmcnt(0)\n" \
  "v_readfirstlane_b32 s66, v224\n" \
  "s_cmp_lg_u32 s66, 0\n" \
  "s_cbranch_scc1 .Lvt_tw_ok\n" \
  "s_sleep 1\n" \
  "s_add_i32 s65, s65, 1\n" \
  "s_cmp_lt_u32 s65, 200\n" \
  "s_cbranch_scc1 .Lvt_tw\n" \
  ".Lvt_tw_ok:\n" \
  "s_sub_i32 s91, s74, s66\n" \
  "s_cmp_gt_u32 s91, -3000\n" \
  "s_cbranch_scc1 .Lvt_wt\n" \
  ".Lvt_late3:\n" \
  "s_branch .Lvt_al_ok\n" \
  ".Lvt_end:\n"
#define VT_CLOB "v32", "v33", "v34", "v35", "v36", "v37", "v38", "v39", "v40", "v41", "v42", "v43", "v44", "v45", "v46", "v47", "v48", "v49", "v50", "v51", "v52", "v53", "v54", "v55", "v56", "v57", "v58", "v59", "v60", "v61", "v62", "v63", "v64", "v65", "v66", "v67", "v68", "v69", "v70", "v71", "v72", "v73", "v74", "v75", "v76", "v77", "v78", "v79", "v80", "v81", "v82", "v83", "v84", "v85", "v86", "v87", "v88", "v89", "v90", "v91", "v92", "v93", "v94", "v95", "v96", "v97", "v98", "v99", "v100", "v101", "v102", "v103", "v104", "v105", "v106", "v107", "v108", "v109", "v110", "v111", "v112", "v113", "v114", "v115", "v116", "v117", "v118", "v119", "v120", "v121", "v122", "v123", "v124", "v125", "v126", "v127", "v128", "v129", "v130", "v131", "v132", "v133", "v134", "v135", "v136", "v137", "v138", "v139", "v140", "v141", "v142", "v143", "v144", "v145", "v146", "v147", "v148", "v149", "v150", "v151", "v152", "v153", "v154", "v155", "v156", "v157", "v158", "v159", "v160", "v161", "v162", "v163", "v164", "v165", "v166", "v167", "v168", "v169", "v170", "v171", "v172", "v173", "v174", "v175", "v176", "v177", "v178", "v179", "v180", "v181", "v182", "v183", "v184", "v185", "v186", "v187", "v188", "v189", "v190", "v191", "v192", "v193", "v194", "v195", "v196", "v197", "v198", "v199", "v200", "v201", "v202", "v203", "v204", "v205", "v206", "v207", "v208", "v209", "v210", "v211", "v212", "v213", "v214", "v215", "v216", "v217", "v218", "v219", "v220", "v221", "v222", "v223", "v224", "v225", "v226", "v227", "v228", "v229", "v230", "v231", "v232", "v233", "v234", "v235", "v236", "v237", "v238", "v239", "v240", "v241", "v242", "v243", "v244", "v245", "v246", "v247", "v248", "v249", "v250", "v251", "v252", "v253", "v254", "v255", "s60", "s61", "s62", "s63", "s64", "s65", "s66", "s67", "s68", "s69", "s70", "s71", "s72", "s73", "s74", "s75", "s76", "s77", "s78", "s79", "s80", "s81", "s82", "s83", "s84", "s85", "s86", "s87", "s88", "s89", "s90", "s91", "s92", "s93", "a0", "a1", "a2", "a3", "a4", "a5", "a6", "a7", "a8", "a9", "a10", "a11", "a12", "a13", "a14", "a15", "a16", "a17", "a18", "a19", "a20", "a21", "a22", "a23", "a24", "a25", "a26", "a27", "a28", "a29", "a30", "a31", "a32", "a33", "a34", "a35", "a36", "a37", "a38", "a39", "a40", "a41", "a42", "a43", "a44", "a45", "a46", "a47", "a48", "a49", "a50", "a51", "a52", "a53", "a54", "a55", "a56", "a57", "a58", "a59", "a60", "a61", "a62", "a63", "a64", "a65", "a66", "a67", "a68", "a69", "a70", "a71", "a72", "a73", "a74", "a75", "a76", "a77", "a78", "a79", "a80", "a81", "a82", "a83", "a84", "a85", "a86", "a87", "a88", "a89", "a90", "a91", "a92", "a93", "a94", "a95", "a96", "a97", "a98", "a99", "a100", "a101", "a102", "a103", "a104", "a105", "a106", "a107", "a108", "a109", "a110", "a111", "a112", "a113", "a114", "a115", "a116", "a117", "a118", "a119", "a120", "a121", "a122", "a123", "a124", "a125", "a126", "a127", "a128", "a129", "a130", "a131", "a132", "a133", "a134", "a135", "a136", "a137", "a138", "a139", "a140", "a141", "a142", "a143", "a144", "a145", "a146", "a147", "a148", "a149", "a150", "a151", "a152", "a153", "a154", "a155", "a156", "a157", "a158", "a159", "a160", "a161", "a162", "a163", "a164", "a165", "a166", "a167", "a168", "a169", "a170", "a171", "a172", "a173", "a174", "a175", "a176", "a177", "a178", "a179", "a180", "a181", "a182", "a183", "a184", "a185", "a186", "a187", "a188", "a189", "a190", "a191", "a192", "a193", "a194", "a195", "a196", "a197", "a198", "a199", "a200", "a201", "a202", "a203", "a204", "a205", "a206", "a207", "a208", "a209", "a210", "a211", "a212", "a213", "a214", "a215", "a216", "a217", "a218", "a219", "a220", "a221", "a222", "a223", "a224", "a225", "a226", "a227", "a228", "a229", "a230", "a231", "a232", "a233", "a234", "a235", "a236", "a237", "a238", "a239", "a240", "a241", "a242", "a243", "a244", "a245", "a246", "a247", "a248", "a249", "a250", "a251", "a252", "a253", "a254", "a255", "vcc", "scc", "m0", "memory"
#define O_ASM \
  "s_nop 4\n" \
  "s_mov_b32 s56, -2\n" \
  "v_add_u32 v242, 0x1000, %[la]\n" \
  "s_mov_b32 s78, 0\n" \
  "s_mov_b32 s79, 0\n" \
  "s_mov_b32 s80, 0\n" \
  "s_mov_b32 s87, 2.0\n" \
  "s_mov_b32 s88, 0x7fff\n" \
  "v_mov_b64 v[246:247], -1\n" \
  "v_mov_b64 v[248:249], -1\n" \
  "s_mov_b32 s68, 2.0\n" \
  "s_mov_b32 s70, 2.0\n" \
  "v_mov_b32 v244, 0\n" \
  "s_mov_b32 s67, 589824\n" \
  "s_add_i32 s94, %[bas], -32\n" \
  ".Lo_top:\n" \
  "s_memrealtime s[74:75]\n" \
  "s_add_i32 s56, s56, 1\n" \
  "s_add_i32 s94, s94, 16\n" \
  "s_lshl_b32 s62, s56, 17\n" \
  "s_and_b32 s63, s62, 0x60000\n" \
  "s_add_i32 s57, s87, s63\n" \
  "s_mov_b32 m0, %[ml0]\n" \
  "v_readfirstlane_b32 s65, v240\n" \
  "v_readfirstlane_b32 s86, v241\n" \
  "s_cmp_lg_u32 s65, 0\n" \
  "s_cselect_b32 s76, %[oc1], 0\n" \
  "s_add_i32 s65, s65, s76\n" \
  "s_waitcnt lgkmcnt(0)\n" \
  "s_sub_i32 s91, s74, s65\n" \
  "s_cmp_gt_u32 s91, 14\n" \
  "s_cbranch_scc1 .Loa_late\n" \
  ".Loa_al_ok:\n" \
  "buffer_load_dwordx4 %[c0], %[X], s57 offen sc1 lds\n" \
  "s_add_i32 s57, s57, 0x1000\n" \
  "s_add_i32 m0, %[ml0], 0x1000\n" \
  "s_nop 1\n" \
  "buffer_load_dwordx4 %[c0], %[X], s57 offen sc1 lds\n" \
  "s_add_i32 s57, s57, 0x3000\n" \
  "s_add_i32 m0, %[ml0], 0x2000\n" \
  "s_nop 1\n" \
  "buffer_load_dwordx4 %[c2], %[X], s57 offen sc1 lds\n" \
  "buffer_load_dword v244, %[flo], %[X], 0 offen sc1\n" \
  "s_bfe_i32 s63, s56, 0x10001\n" \
  "s_and_b32 s60, s63, 0x80008000\n" \
  "s_add_i32 s63, s62, 0x40000\n" \
  "s_and_b32 s63, s63, 0x60000\n" \
  "s_add_i32 s58, %[mw0], s63\n" \
  "s_cmp_gt_i32 s56, 1\n" \
  "s_cselect_b32 s58, s58, 2.0\n" \
  "s_mov_b32 s71, s70\n" \
  "s_mov_b32 s70, s68\n" \
  "s_add_i32 s67, s67, 589824\n" \
  "s_cmp_eq_u32 s67, 1769472\n" \
  "s_cselect_b32 s67, 0, s67\n" \
  "s_add_i32 s68, %[vm0], s67\n" \
  "s_add_i32 s63, s62, 0x20000\n" \
  "s_and_b32 s63, s63, 0x60000\n" \
  "s_add_i32 s61, %[mw0], s63\n" \
  "s_cmp_lt_u32 s56, 63\n" \
  "s_cselect_b32 s61, s61, 2.0\n" \
  "s_and_b32 s62, s62, 0x20000\n" \
  "s_add_i32 s59, %[pm0], s62\n" \
  "s_and_b32 s63, s56, 7\n" \
  "s_lshl_b32 s69, s63, 22\n" \
  "s_add_i32 s69, s69, %[vt0]\n" \
  "s_cmp_lt_i32 s56, 0\n" \
  "s_cselect_b32 s66, 2.0, s67\n" \
  "s_cselect_b32 s68, 2.0, s68\n" \
  "s_cselect_b32 s59, 2.0, s59\n" \
  "s_cselect_b32 s69, 2.0, s69\n" \
  "s_add_i32 s63, s56, 1\n" \
  "v_mov_b64 v[196:197], 0\n" \
  "v_mov_b64 v[198:199], 0\n" \
  "v_mov_b64 v[200:201], 0\n" \
  "v_mov_b64 v[202:203], 0\n" \
  "v_mov_b64 v[160:161], 0\n" \
  "v_mov_b64 v[162:163], 0\n" \
  "v_mov_b64 v[164:165], 0\n" \
  "v_mov_b64 v[166:167], 0\n" \
  "v_mov_b64 v[168:169], 0\n" \
  "v_mov_b64 v[170:171], 0\n" \
  "v_mov_b64 v[172:173], 0\n" \
  "v_mov_b64 v[174:175], 0\n" \
  "v_mov_b64 v[176:177], 0\n" \
  "v_mov_b64 v[178:179], 0\n" \
  "v_mov_b64 v[180:181], 0\n" \
  "v_mov_b64 v[182:183], 0\n" \
  "v_mov_b64 v[184:185], 0\n" \
  "v_mov_b64 v[186:187], 0\n" \
  "v_mov_b64 v[188:189], 0\n" \
  "v_mov_b64 v[190:191], 0\n" \
  "v_mov_b64 v[192:193], 0\n" \
  "v_mov_b64 v[194:195], 0\n" \
  "buffer_store_dwordx4 v[246:249], %[la], %[X], s71 offen sc1\n" \
  "buffer_store_dwordx4 v[246:249], %[la], %[X], s71 offen offset:1024 sc1\n" \
  "s_waitcnt vmcnt(2)\n" \
  "v_cmp_gt_i32 vcc, s63, v244\n" \
  "s_barrier\n" \
  "ds_read_b128 v[104:107], %[mA] offset:0\n" \
  "ds_read_b128 v[108:111], %[mA] offset:1024\n" \
  "ds_read_b128 v[112:115], %[mA] offset:2048\n" \
  "ds_read_b128 v[116:119], %[mA] offset:3072\n" \
  "ds_read_b128 v[120:123], %[mA] offset:4096\n" \
  "buffer_store_dword v246, %[lm], %[X], s58 offen sc1\n" \
  "s_cbranch_vccnz .Lo_sf\n" \
  ".Lo_sf_back:\n" \
  "s_waitcnt lgkmcnt(4)\n" \
  "v_smfmac_f32_16x16x64_bf16 v[196:199], v[104:107], a[0:7], %[idx]\n" \
  "ds_read_b128 v[124:127], %[mA] offset:5120\n" \
  "v_smfmac_f32_16x16x64_bf16 v[200:203], v[104:107], a[8:15], %[idx]\n" \
  "s_waitcnt lgkmcnt(4)\n" \
  "v_smfmac_f32_16x16x64_bf16 v[196:199], v[108:111], a[16:23], %[idx]\n" \
  "ds_read_b128 v[128:131], %[mA] offset:6144\n" \
  "v_smfmac_f32_16x16x64_bf16 v[200:203], v[108:111], a[24:31], %[idx]\n" \
  "s_waitcnt lgkmcnt(4)\n" \
  "v_smfmac_f32_16x16x64_bf16 v[196:199], v[112:115], a[32:39], %[idx]\n" \
  "ds_read_b128 v[132:135], %[mA] offset:7168\n" \
  "v_smfmac_f32_16x16x64_bf16 v[200:203], v[112:115], a[40:47], %[idx]\n" \
  "s_waitcnt lgkmcnt(4)\n" \
  "v_smfmac_f32_16x16x64_bf16 v[196:199], v[116:119], a[48:55], %[idx]\n" \
  "ds_read_b128 v[136:139], %[mA8] offset:0\n" \
  "v_smfmac_f32_16x16x64_bf16 v[200:203], v[116:119], a[56:63], %[idx]\n" \
  "buffer_load_dwordx4 v[64:67], %[la], %[X], s69 offen offset:0 sc1\n" \
  "s_waitcnt lgkmcnt(4)\n" \
  "v_smfmac_f32_16x16x64_bf16 v[196:199], v[120:123], a[64:71], %[idx]\n" \
  "v_smfmac_f32_16x16x64_bf16 v[200:203], v[120:123], a[72:79], %[idx]\n" \
  "s_waitcnt lgkmcnt(3)\n" \
  "v_smfmac_f32_16x16x64_bf16 v[196:199], v[124:127], a[80:87], %[idx]\n" \
  "v_smfmac_f32_16x16x64_bf16 v[200:203], v[124:127], a[88:95], %[idx]\n" \
  "buffer_load_dwordx4 v[68:71], %[la], %[X], s69 offen offset:1024 sc1\n" \
  "s_waitcnt lgkmcnt(2)\n" \
  "v_smfmac_f32_16x16x64_bf16 v[196:199], v[128:131], a[96:103], %[idx]\n" \
  "v_smfmac_f32_16x16x64_bf16 v[200:203], v[128:131], a[104:111], %[idx]\n" \
  "s_waitcnt lgkmcnt(1)\n" \
  "v_smfmac_f32_16x16x64_bf16 v[196:199], v[132:135], a[112:119], %[idx]\n" \
  "v_smfmac_f32_16x16x64_bf16 v[200:203], v[132:135], a[120:127], %[idx]\n" \
  "buffer_load_dwordx4 v[72:75], %[la], %[X], s69 offen offset:2048 sc1\n" \
  "s_waitcnt lgkmcnt(0)\n" \
  "v_smfmac_f32_16x16x64_bf16 v[196:199], v[136:139], a[128:135], %[idx]\n" \
  "s_nop 2\n" \
  "v_smfmac_f32_16x16x64_bf16 v[200:203], v[136:139], a[136:143], %[idx]\n" \
  "s_nop 3\n" \
  "v_pk_add_f32 v[196:197], v[196:197], v[198:199]\n" \
  "s_nop 2\n" \
  "v_pk_add_f32 v[200:201], v[200:201], v[202:203]\n" \
  "v_cvt_pk_bf16_f32 v198, v196, v197\n" \
  "v_cvt_pk_bf16_f32 v199, v200, v201\n" \
  "ds_write2_b32 %[aW], v198, v199 offset1:16\n" \
  "v_cmp_u_f32 vcc, v196, v197\n" \
  "s_cbranch_vccnz .Lo_sm\n" \
  ".Lo_sm_back:\n" \
  "s_waitcnt lgkmcnt(0)\n" \
  "s_barrier\n" \
  "ds_read_b64_tr_b16 v[212:213], %[aR] offset:0\n" \
  "ds_read_b64_tr_b16 v[214:215], %[aR] offset:128\n" \
  "ds_read_b64_tr_b16 v[216:217], %[aR] offset:1024\n" \
  "ds_read_b64_tr_b16 v[218:219], %[aR] offset:1152\n" \
  "buffer_load_dwordx4 v[76:79], %[la], %[X], s69 offen offset:3072 sc1\n" \
  "s_waitcnt lgkmcnt(2)\n" \
  "v_smfmac_f32_16x16x64_bf16 v[160:163], v[212:215], a[144:151], %[idx]\n" \
  "buffer_load_dwordx4 v[80:83], v242, %[X], s69 offen offset:0 sc1\n" \
  "s_waitcnt lgkmcnt(0)\n" \
  "v_smfmac_f32_16x16x64_bf16 v[160:163], v[216:219], a[152:159], %[idx]\n" \
  "s_nop 1\n" \
  "v_smfmac_f32_16x16x64_bf16 v[164:167], v[212:215], a[160:167], %[idx]\n" \
  "s_nop 1\n" \
  "v_smfmac_f32_16x16x64_bf16 v[164:167], v[216:219], a[168:175], %[idx]\n" \
  "s_nop 1\n" \
  "v_smfmac_f32_16x16x64_bf16 v[168:171], v[212:215], a[176:183], %[idx]\n" \
  "buffer_load_dwordx4 v[84:87], v242, %[X], s69 offen offset:1024 sc1\n" \
  "v_add_f32 v160, v160, v162\n" \
  "v_smfmac_f32_16x16x64_bf16 v[168:171], v[216:219], a[184:191], %[idx]\n" \
  "v_add_f32 v161, v161, v163\n" \
  "v_smfmac_f32_16x16x64_bf16 v[172:175], v[212:215], a[192:199], %[idx]\n" \
  "v_cvt_pk_bf16_f32 v220, v160, v161\n" \
  "v_smfmac_f32_16x16x64_bf16 v[172:175], v[216:219], a[200:207], %[idx]\n" \
  "v_add_f32 v164, v164, v166\n" \
  "v_smfmac_f32_16x16x64_bf16 v[176:179], v[212:215], a[208:215], %[idx]\n" \
  "buffer_load_dwordx4 v[88:91], v242, %[X], s69 offen offset:2048 sc1\n" \
  "v_add_f32 v165, v165, v167\n" \
  "v_smfmac_f32_16x16x64_bf16 v[176:179], v[216:219], a[216:223], %[idx]\n" \
  "v_cvt_pk_bf16_f32 v221, v164, v165\n" \
  "v_smfmac_f32_16x16x64_bf16 v[180:183], v[212:215], a[224:231], %[idx]\n" \
  "v_add_f32 v168, v168, v170\n" \
  "v_smfmac_f32_16x16x64_bf16 v[180:183], v[216:219], a[232:239], %[idx]\n" \
  "v_add_f32 v169, v169, v171\n" \
  "v_smfmac_f32_16x16x64_bf16 v[184:187], v[212:215], a[240:247], %[idx]\n" \
  "buffer_load_dwordx4 v[92:95], v242, %[X], s69 offen offset:3072 sc1\n" \
  "v_cvt_pk_bf16_f32 v222, v168, v169\n" \
  "v_smfmac_f32_16x16x64_bf16 v[184:187], v[216:219], a[248:255], %[idx]\n" \
  "v_add_f32 v172, v172, v174\n" \
  "v_smfmac_f32_16x16x64_bf16 v[188:191], v[212:215], v[32:39], %[idx]\n" \
  "v_add_f32 v173, v173, v175\n" \
  "v_smfmac_f32_16x16x64_bf16 v[188:191], v[216:219], v[40:47], %[idx]\n" \
  "v_cvt_pk_bf16_f32 v223, v172, v173\n" \
  "v_smfmac_f32_16x16x64_bf16 v[192:195], v[212:215], v[48:55], %[idx]\n" \
  "v_add_f32 v176, v176, v178\n" \
  "v_smfmac_f32_16x16x64_bf16 v[192:195], v[216:219], v[56:63], %[idx]\n" \
  "v_add_f32 v177, v177, v179\n" \
  "v_permlane32_swap_b32 v220, v222\n" \
  "v_permlane32_swap_b32 v221, v223\n" \
  "v_cvt_pk_bf16_f32 v224, v176, v177\n" \
  "v_pk_add_f32 v[180:181], v[180:181], v[182:183]\n" \
  "v_cvt_pk_bf16_f32 v225, v180, v181\n" \
  "v_permlane16_swap_b32 v220, v221\n" \
  "v_permlane16_swap_b32 v222, v223\n" \
  "v_min_u32 v220, -2, v220\n" \
  "buffer_store_dwordx4 v[220:223], %[vmo], %[X], s66 offen sc1\n" \
  "s_add_i32 s66, s66, 0x10000\n" \
  "v_pk_add_f32 v[184:185], v[184:185], v[186:187]\n" \
  "v_cvt_pk_bf16_f32 v226, v184, v185\n" \
  "v_pk_add_f32 v[188:189], v[188:189], v[190:191]\n" \
  "v_cvt_pk_bf16_f32 v227, v188, v189\n" \
  "v_pk_add_f32 v[192:193], v[192:193], v[194:195]\n" \
  "v_cvt_pk_bf16_f32 v228, v192, v193\n" \
  "v_permlane32_swap_b32 v224, v226\n" \
  "v_permlane32_swap_b32 v225, v227\n" \
  "v_min_u32 v228, -2, v228\n" \
  "buffer_store_dword v228, %[vmo1], %[X], s66 offen sc1\n" \
  "v_permlane16_swap_b32 v224, v225\n" \
  "v_permlane16_swap_b32 v226, v227\n" \
  "v_min_u32 v224, -2, v224\n" \
  "buffer_store_dwordx4 v[224:227], %[vmo], %[X], s66 offen sc1\n" \
  "s_cmp_eq_u32 s56, 63\n" \
  "s_cbranch_scc1 .Lo_end\n" \
  "buffer_load_dword v245, off, %[X], %[ufo] sc1\n" \
  "s_memrealtime s[74:75]\n" \
  "s_sub_i32 s76, s88, s86\n" \
  "s_cmp_lt_u32 s76, 1000\n" \
  "s_cselect_b32 s86, s88, s86\n" \
  "s_waitcnt lgkmcnt(0)\n" \
  "s_sub_i32 s91, s74, s86\n" \
  "s_cmp_gt_u32 s91, 14\n" \
  "s_cbranch_scc1 .Lob_late\n" \
  ".Lob_al_ok:\n" \
  "buffer_load_dwordx4 v[96:99], %[la], %[X], s68 offen offset:0 sc1\n" \
  "s_nop 7\n" \
  "buffer_load_dwordx4 v[100:103], %[la], %[X], s68 offen offset:1024 sc1\n" \
  "s_nop 7\n" \
  "buffer_load_dwordx4 v[140:143], %[la], %[X], s59 offen offset:0 sc1\n" \
  "s_nop 7\n" \
  "buffer_load_dwordx4 v[144:147], %[la], %[X], s59 offen offset:1024 sc1\n" \
  "s_nop 7\n" \
  "buffer_load_dwordx4 v[148:151], %[la], %[X], s59 offen offset:2048 sc1\n" \
  "s_nop 7\n" \
  "buffer_load_dwordx4 v[152:155], %[la], %[X], s59 offen offset:3072 sc1\n" \
  "s_nop 7\n" \
  "buffer_load_dwordx4 v[156:159], v242, %[X], s59 offen offset:0 sc1\n" \
  "s_nop 7\n" \
  "s_nop 15\n" \
  "buffer_load_dwordx2 v[240:241], off, %[X], s94 offset:16 sc1\n" \
  "s_waitcnt vmcnt(8)\n" \
  "v_subrev_u32 v243, s56, v245\n" \
  "s_waitcnt vmcnt(5)\n" \
  "v_mfma_f32_16x16x32_bf16 v[204:207], v[64:67], v[140:143], 0\n" \
  "v_mfma_f32_16x16x32_bf16 v[208:211], v[68:71], v[140:143], 0\n" \
  "v_bitop3_b32 v243, v243, v140, s60 bitop3:0xf6\n" \
  "s_waitcnt vmcnt(4)\n" \
  "v_mfma_f32_16x16x32_bf16 v[204:207], v[72:75], v[144:147], v[204:207]\n" \
  "v_mfma_f32_16x16x32_bf16 v[208:211], v[76:79], v[144:147], v[208:211]\n" \
  "v_bitop3_b32 v243, v243, v144, s60 bitop3:0xf6\n" \
  "s_waitcnt vmcnt(3)\n" \
  "v_mfma_f32_16x16x32_bf16 v[204:207], v[80:83], v[148:151], v[204:207]\n" \
  "v_mfma_f32_16x16x32_bf16 v[208:211], v[84:87], v[148:151], v[208:211]\n" \
  "v_bitop3_b32 v243, v243, v148, s60 bitop3:0xf6\n" \
  "s_waitcnt vmcnt(2)\n" \
  "v_mfma_f32_16x16x32_bf16 v[204:207], v[88:91], v[152:155], v[204:207]\n" \
  "v_mfma_f32_16x16x32_bf16 v[208:211], v[92:95], v[152:155], v[208:211]\n" \
  "v_bitop3_b32 v243, v243, v152, s60 bitop3:0xf6\n" \
  "s_waitcnt vmcnt(1)\n" \
  "v_mfma_f32_16x16x32_bf16 v[204:207], v[96:99], v[156:159], v[204:207]\n" \
  "v_mfma_f32_16x16x32_bf16 v[208:211], v[100:103], v[156:159], v[208:211]\n" \
  "v_bitop3_b32 v238, v156, v157, s60 bitop3:0x7e\n" \
  "v_bitop3_b32 v239, v158, v159, s60 bitop3:0x7e\n" \
  "v_or3_b32 v243, v243, v238, v239\n" \
  "v_ashrrev_i32 v238, 31, v243\n" \
  "s_nop 3\n" \
  "v_add_f32_dpp v204, v208, v204 row_ror:8 row_mask:0xf bank_mask:0xf\n" \
  "v_add_f32_dpp v205, v209, v205 row_ror:8 row_mask:0xf bank_mask:0xf\n" \
  "v_add_f32_dpp v206, v210, v206 row_ror:8 row_mask:0xf bank_mask:0xf\n" \
  "v_add_f32_dpp v207, v211, v207 row_ror:8 row_mask:0xf bank_mask:0xf\n" \
  "ds_write_b128 %[rmW], v[204:207]\n" \
  "v_fma_f32 v238, v204, v205, v238\n" \
  "v_add_f32 v239, v206, v207\n" \
  "v_cmp_u_f32 vcc, v238, v239\n" \
  "s_cbranch_vccnz .Lo_sq\n" \
  ".Lo_sq_back:\n" \
  "s_waitcnt lgkmcnt(0)\n" \
  "s_barrier\n" \
  "ds_read2st64_b64 v[230:233], %[rmA] offset0:0 offset1:2\n" \
  "ds_read2st64_b64 v[234:237], %[rmA] offset0:4 offset1:6\n" \
  "s_waitcnt lgkmcnt(1)\n" \
  "v_pk_add_f32 v[230:231], v[230:231], v[232:233]\n" \
  "s_waitcnt lgkmcnt(0)\n" \
  "v_pk_add_f32 v[234:235], v[234:235], v[236:237]\n" \
  "v_pk_add_f32 v[230:231], v[230:231], v[234:235]\n" \
  "v_cvt_pk_bf16_f32 v230, v230, v231\n" \
  "v_xor_b32 v230, s60, v230\n" \
  "v_min_u32 v230, -2, v230\n" \
  "buffer_store_dword v230, %[lm], %[X], s61 offen sc1\n" \
  "s_waitcnt vmcnt(1)\n" \
  "s_cmp_lg_u32 s56, -1\n" \
  "s_cbranch_scc1 .Lo_top\n" \
  "s_mov_b32 s87, %[mr0]\n" \
  "s_waitcnt vmcnt(0) lgkmcnt(0)\n" \
  "s_barrier\n" \
  "v_xor_b32 v104, 0x80, %[wv]\n" \
  "v_xor_b32 v105, 32, %[wr]\n" \
  "v_add_u32 v106, 0x10000, %[wr]\n" \
  "v_add_u32 v107, 0x10000, v105\n" \
  "s_cmp_eq_u32 %[grp], 0\n" \
  "s_cbranch_scc1 .Lo_ws1\n" \
  "s_sleep 8\n" \
  ".Lo_ws1:\n" \
  "s_add_i32 m0, %[wl], 0\n" \
  "s_mov_b32 s76, 0\n" \
  "buffer_load_dwordx4 %[wv], %[WA], s76 offen lds\n" \
  "s_add_i32 m0, %[wl], 1024\n" \
  "s_mov_b32 s76, 32768\n" \
  "buffer_load_dwordx4 %[wv], %[WA], s76 offen lds\n" \
  "s_add_i32 m0, %[wl], 2048\n" \
  "s_mov_b32 s76, 65536\n" \
  "buffer_load_dwordx4 v104, %[WA], s76 offen lds\n" \
  "s_add_i32 m0, %[wl], 3072\n" \
  "s_mov_b32 s76, 98304\n" \
  "buffer_load_dwordx4 v104, %[WA], s76 offen lds\n" \
  "s_cmp_eq_u32 %[grp], 0\n" \
  "s_cbranch_scc1 .Lo_ws2\n" \
  "s_sleep 8\n" \
  ".Lo_ws2:\n" \
  "s_add_i32 m0, %[wl], 16384\n" \
  "s_mov_b32 s76, 524288\n" \
  "buffer_load_dwordx4 %[wv], %[WA], s76 offen lds\n" \
  "s_add_i32 m0, %[wl], 17408\n" \
  "s_mov_b32 s76, 557056\n" \
  "buffer_load_dwordx4 %[wv], %[WA], s76 offen lds\n" \
  "s_add_i32 m0, %[wl], 18432\n" \
  "s_mov_b32 s76, 589824\n" \
  "buffer_load_dwordx4 v104, %[WA], s76 offen lds\n" \
  "s_add_i32 m0, %[wl], 19456\n" \
  "s_mov_b32 s76, 622592\n" \
  "buffer_load_dwordx4 v104, %[WA], s76 offen lds\n" \
  "s_cmp_eq_u32 %[grp], 0\n" \
  "s_cbranch_scc1 .Lo_ws3\n" \
  "s_sleep 8\n" \
  ".Lo_ws3:\n" \
  "s_add_i32 m0, %[wl], 32768\n" \
  "s_mov_b32 s76, 1048576\n" \
  "buffer_load_dwordx4 %[wv], %[WA], s76 offen lds\n" \
  "s_add_i32 m0, %[wl], 33792\n" \
  "s_mov_b32 s76, 1081344\n" \
  "buffer_load_dwordx4 %[wv], %[WA], s76 offen lds\n" \
  "s_add_i32 m0, %[wl], 34816\n" \
  "s_mov_b32 s76, 1114112\n" \
  "buffer_load_dwordx4 v104, %[WA], s76 offen lds\n" \
  "s_add_i32 m0, %[wl], 35840\n" \
  "s_mov_b32 s76, 1146880\n" \
  "buffer_load_dwordx4 v104, %[WA], s76 offen lds\n" \
  "s_cmp_eq_u32 %[grp], 0\n" \
  "s_cbranch_scc1 .Lo_ws4\n" \
  "s_sleep 8\n" \
  ".Lo_ws4:\n" \
  "s_add_i32 m0, %[wl], 49152\n" \
  "s_mov_b32 s76, 1572864\n" \
  "buffer_load_dwordx4 %[wv], %[WA], s76 offen lds\n" \
  "s_add_i32 m0, %[wl], 50176\n" \
  "s_mov_b32 s76, 1605632\n" \
  "buffer_load_dwordx4 %[wv], %[WA], s76 offen lds\n" \
  "s_add_i32 m0, %[wl], 51200\n" \
  "s_mov_b32 s76, 1638400\n" \
  "buffer_load_dwordx4 v104, %[WA], s76 offen lds\n" \
  "s_add_i32 m0, %[wl], 52224\n" \
  "s_mov_b32 s76, 1671168\n" \
  "buffer_load_dwordx4 v104, %[WA], s76 offen lds\n" \
  "s_cmp_eq_u32 %[grp], 0\n" \
  "s_cbranch_scc1 .Lo_ws5\n" \
  "s_sleep 8\n" \
  ".Lo_ws5:\n" \
  "s_add_i32 m0, %[wl], 65536\n" \
  "s_mov_b32 s76, 2097152\n" \
  "buffer_load_dwordx4 %[wv], %[WA], s76 offen lds\n" \
  "s_add_i32 m0, %[wl], 66560\n" \
  "s_mov_b32 s76, 2129920\n" \
  "buffer_load_dwordx4 %[wv], %[WA], s76 offen lds\n" \
  "s_add_i32 m0, %[wl], 67584\n" \
  "s_mov_b32 s76, 2162688\n" \
  "buffer_load_dwordx4 v104, %[WA], s76 offen lds\n" \
  "s_add_i32 m0, %[wl], 68608\n" \
  "s_mov_b32 s76, 2195456\n" \
  "buffer_load_dwordx4 v104, %[WA], s76 offen lds\n" \
  "s_cmp_eq_u32 %[grp], 0\n" \
  "s_cbranch_scc1 .Lo_ws6\n" \
  "s_sleep 8\n" \
  ".Lo_ws6:\n" \
  "s_add_i32 m0, %[wl], 81920\n" \
  "s_mov_b32 s76, 2621440\n" \
  "buffer_load_dwordx4 %[wv], %[WA], s76 offen lds\n" \
  "s_add_i32 m0, %[wl], 82944\n" \
  "s_mov_b32 s76, 2654208\n" \
  "buffer_load_dwordx4 %[wv], %[WA], s76 offen lds\n" \
  "s_add_i32 m0, %[wl], 83968\n" \
  "s_mov_b32 s76, 2686976\n" \
  "buffer_load_dwordx4 v104, %[WA], s76 offen lds\n" \
  "s_add_i32 m0, %[wl], 84992\n" \
  "s_mov_b32 s76, 2719744\n" \
  "buffer_load_dwordx4 v104, %[WA], s76 offen lds\n" \
  "s_cmp_eq_u32 %[grp], 0\n" \
  "s_cbranch_scc1 .Lo_ws7\n" \
  "s_sleep 8\n" \
  ".Lo_ws7:\n" \
  "s_add_i32 m0, %[wl], 98304\n" \
  "s_mov_b32 s76, 3145728\n" \
  "buffer_load_dwordx4 %[wv], %[WA], s76 offen lds\n" \
  "s_add_i32 m0, %[wl], 99328\n" \
  "s_mov_b32 s76, 3178496\n" \
  "buffer_load_dwordx4 %[wv], %[WA], s76 offen lds\n" \
  "s_add_i32 m0, %[wl], 100352\n" \
  "s_mov_b32 s76, 3211264\n" \
  "buffer_load_dwordx4 v104, %[WA], s76 offen lds\n" \
  "s_add_i32 m0, %[wl], 101376\n" \
  "s_mov_b32 s76, 3244032\n" \
  "buffer_load_dwordx4 v104, %[WA], s76 offen lds\n" \
  "s_cmp_eq_u32 %[grp], 0\n" \
  "s_cbranch_scc1 .Lo_ws8\n" \
  "s_sleep 8\n" \
  ".Lo_ws8:\n" \
  "s_add_i32 m0, %[wl], 114688\n" \
  "s_mov_b32 s76, 3670016\n" \
  "buffer_load_dwordx4 %[wv], %[WA], s76 offen lds\n" \
  "s_add_i32 m0, %[wl], 115712\n" \
  "s_mov_b32 s76, 3702784\n" \
  "buffer_load_dwordx4 %[wv], %[WA], s76 offen lds\n" \
  "s_add_i32 m0, %[wl], 116736\n" \
  "s_mov_b32 s76, 3735552\n" \
  "buffer_load_dwordx4 v104, %[WA], s76 offen lds\n" \
  "s_add_i32 m0, %[wl], 117760\n" \
  "s_mov_b32 s76, 3768320\n" \
  "buffer_load_dwordx4 v104, %[WA], s76 offen lds\n" \
  "s_waitcnt vmcnt(28)\n" \
  "s_waitcnt lgkmcnt(0)\n" \
  "s_barrier\n" \
  "ds_read_b64_tr_b16 a[0:1], %[wr] offset:0\n" \
  "ds_read_b64_tr_b16 a[2:3], %[wr] offset:1024\n" \
  "ds_read_b64_tr_b16 a[4:5], %[wr] offset:8192\n" \
  "ds_read_b64_tr_b16 a[6:7], %[wr] offset:9216\n" \
  "ds_read_b64_tr_b16 a[8:9], v105 offset:0\n" \
  "ds_read_b64_tr_b16 a[10:11], v105 offset:1024\n" \
  "ds_read_b64_tr_b16 a[12:13], v105 offset:8192\n" \
  "ds_read_b64_tr_b16 a[14:15], v105 offset:9216\n" \
  "s_waitcnt vmcnt(24)\n" \
  "s_waitcnt lgkmcnt(0)\n" \
  "s_barrier\n" \
  "s_cmp_eq_u32 %[grp], 0\n" \
  "s_cbranch_scc1 .Lo_ws9\n" \
  "s_sleep 8\n" \
  ".Lo_ws9:\n" \
  "s_add_i32 m0, %[wl], 0\n" \
  "s_mov_b32 s76, 4194304\n" \
  "buffer_load_dwordx4 %[wv], %[WA], s76 offen lds\n" \
  "s_add_i32 m0, %[wl], 1024\n" \
  "s_mov_b32 s76, 4227072\n" \
  "buffer_load_dwordx4 %[wv], %[WA], s76 offen lds\n" \
  "s_add_i32 m0, %[wl], 2048\n" \
  "s_mov_b32 s76, 4259840\n" \
  "buffer_load_dwordx4 v104, %[WA], s76 offen lds\n" \
  "s_add_i32 m0, %[wl], 3072\n" \
  "s_mov_b32 s76, 4292608\n" \
  "buffer_load_dwordx4 v104, %[WA], s76 offen lds\n" \
  "ds_read_b64_tr_b16 a[16:17], %[wr] offset:16384\n" \
  "ds_read_b64_tr_b16 a[18:19], %[wr] offset:17408\n" \
  "ds_read_b64_tr_b16 a[20:21], %[wr] offset:24576\n" \
  "ds_read_b64_tr_b16 a[22:23], %[wr] offset:25600\n" \
  "ds_read_b64_tr_b16 a[24:25], v105 offset:16384\n" \
  "ds_read_b64_tr_b16 a[26:27], v105 offset:17408\n" \
  "ds_read_b64_tr_b16 a[28:29], v105 offset:24576\n" \
  "ds_read_b64_tr_b16 a[30:31], v105 offset:25600\n" \
  "s_waitcnt vmcnt(24)\n" \
  "s_waitcnt lgkmcnt(0)\n" \
  "s_barrier\n" \
  "ds_read_b64_tr_b16 a[32:33], %[wr] offset:32768\n" \
  "ds_read_b64_tr_b16 a[34:35], %[wr] offset:33792\n" \
  "ds_read_b64_tr_b16 a[36:37], %[wr] offset:40960\n" \
  "ds_read_b64_tr_b16 a[38:39], %[wr] offset:41984\n" \
  "ds_read_b64_tr_b16 a[40:41], v105 offset:32768\n" \
  "ds_read_b64_tr_b16 a[42:43], v105 offset:33792\n" \
  "ds_read_b64_tr_b16 a[44:45], v105 offset:40960\n" \
  "ds_read_b64_tr_b16 a[46:47], v105 offset:41984\n" \
  "s_waitcnt vmcnt(20)\n" \
  "s_waitcnt lgkmcnt(0)\n" \
  "s_barrier\n" \
  "ds_read_b64_tr_b16 a[48:49], %[wr] offset:49152\n" \
  "ds_read_b64_tr_b16 a[50:51], %[wr] offset:50176\n" \
  "ds_read_b64_tr_b16 a[52:53], %[wr] offset:57344\n" \
  "ds_read_b64_tr_b16 a[54:55], %[wr] offset:58368\n" \
  "ds_read_b64_tr_b16 a[56:57], v105 offset:49152\n" \
  "ds_read_b64_tr_b16 a[58:59], v105 offset:50176\n" \
  "ds_read_b64_tr_b16 a[60:61], v105 offset:57344\n" \
  "ds_read_b64_tr_b16 a[62:63], v105 offset:58368\n" \
  "s_waitcnt vmcnt(16)\n" \
  "s_waitcnt lgkmcnt(0)\n" \
  "s_barrier\n" \
  "s_cmp_eq_u32 %[grp], 0\n" \
  "s_cbranch_scc1 .Lo_ws10\n" \
  "s_sleep 16\n" \
  ".Lo_ws10:\n" \
  "s_add_i32 m0, %[hl0], 16384\n" \
  "s_mov_b32 s76, 0\n" \
  "buffer_load_dwordx4 %[hvo], %[WB], s76 offen offset:0 lds\n" \
  "buffer_load_dwordx4 %[hvo], %[WB], s76 offen offset:1024 lds\n" \
  "buffer_load_dwordx4 %[hvo], %[WB], s76 offen offset:2048 lds\n" \
  "buffer_load_dwordx4 %[hvo], %[WB], s76 offen offset:3072 lds\n" \
  "s_add_i32 m0, %[hl0], 20480\n" \
  "s_mov_b32 s76, 4096\n" \
  "buffer_load_dwordx4 %[hvo], %[WB], s76 offen offset:0 lds\n" \
  "buffer_load_dwordx4 %[hvo], %[WB], s76 offen offset:1024 lds\n" \
  "buffer_load_dwordx4 %[hvo], %[WB], s76 offen offset:2048 lds\n" \
  "buffer_load_dwordx4 %[hvo], %[WB], s76 offen offset:3072 lds\n" \
  "s_add_i32 m0, %[hl0], 24576\n" \
  "s_mov_b32 s76, 8192\n" \
  "buffer_load_dwordx4 %[hvo], %[WB], s76 offen offset:0 lds\n" \
  "ds_read_b64_tr_b16 a[64:65], v106 offset:0\n" \
  "ds_read_b64_tr_b16 a[66:67], v106 offset:1024\n" \
  "ds_read_b64_tr_b16 a[68:69], v106 offset:8192\n" \
  "ds_read_b64_tr_b16 a[70:71], v106 offset:9216\n" \
  "ds_read_b64_tr_b16 a[72:73], v107 offset:0\n" \
  "ds_read_b64_tr_b16 a[74:75], v107 offset:1024\n" \
  "ds_read_b64_tr_b16 a[76:77], v107 offset:8192\n" \
  "ds_read_b64_tr_b16 a[78:79], v107 offset:9216\n" \
  "s_waitcnt vmcnt(21)\n" \
  "s_waitcnt lgkmcnt(0)\n" \
  "s_barrier\n" \
  "ds_read_b64_tr_b16 a[80:81], v106 offset:16384\n" \
  "ds_read_b64_tr_b16 a[82:83], v106 offset:17408\n" \
  "ds_read_b64_tr_b16 a[84:85], v106 offset:24576\n" \
  "ds_read_b64_tr_b16 a[86:87], v106 offset:25600\n" \
  "ds_read_b64_tr_b16 a[88:89], v107 offset:16384\n" \
  "ds_read_b64_tr_b16 a[90:91], v107 offset:17408\n" \
  "ds_read_b64_tr_b16 a[92:93], v107 offset:24576\n" \
  "ds_read_b64_tr_b16 a[94:95], v107 offset:25600\n" \
  "s_waitcnt vmcnt(17)\n" \
  "s_waitcnt lgkmcnt(0)\n" \
  "s_barrier\n" \
  "s_cmp_eq_u32 %[grp], 0\n" \
  "s_cbranch_scc1 .Lo_ws11\n" \
  "s_sleep 16\n" \
  ".Lo_ws11:\n" \
  "s_add_i32 m0, %[hl0], 53248\n" \
  "s_mov_b32 s76, 36864\n" \
  "buffer_load_dwordx4 %[hvo], %[WB], s76 offen offset:0 lds\n" \
  "buffer_load_dwordx4 %[hvo], %[WB], s76 offen offset:1024 lds\n" \
  "buffer_load_dwordx4 %[hvo], %[WB], s76 offen offset:2048 lds\n" \
  "buffer_load_dwordx4 %[hvo], %[WB], s76 offen offset:3072 lds\n" \
  "s_add_i32 m0, %[hl0], 57344\n" \
  "s_mov_b32 s76, 40960\n" \
  "buffer_load_dwordx4 %[hvo], %[WB], s76 offen offset:0 lds\n" \
  "buffer_load_dwordx4 %[hvo], %[WB], s76 offen offset:1024 lds\n" \
  "buffer_load_dwordx4 %[hvo], %[WB], s76 offen offset:2048 lds\n" \
  "buffer_load_dwordx4 %[hvo], %[WB], s76 offen offset:3072 lds\n" \
  "s_add_i32 m0, %[hl0], 61440\n" \
  "s_mov_b32 s76, 45056\n" \
  "buffer_load_dwordx4 %[hvo], %[WB], s76 offen offset:0 lds\n" \
  "ds_read_b64_tr_b16 a[96:97], v106 offset:32768\n" \
  "ds_read_b64_tr_b16 a[98:99], v106 offset:33792\n" \
  "ds_read_b64_tr_b16 a[100:101], v106 offset:40960\n" \
  "ds_read_b64_tr_b16 a[102:103], v106 offset:41984\n" \
  "ds_read_b64_tr_b16 a[104:105], v107 offset:32768\n" \
  "ds_read_b64_tr_b16 a[106:107], v107 offset:33792\n" \
  "ds_read_b64_tr_b16 a[108:109], v107 offset:40960\n" \
  "ds_read_b64_tr_b16 a[110:111], v107 offset:41984\n" \
  "s_waitcnt vmcnt(22)\n" \
  "s_waitcnt lgkmcnt(0)\n" \
  "s_barrier\n" \
  "ds_read_b64_tr_b16 a[112:113], v106 offset:49152\n" \
  "ds_read_b64_tr_b16 a[114:115], v106 offset:50176\n" \
  "ds_read_b64_tr_b16 a[116:117], v106 offset:57344\n" \
  "ds_read_b64_tr_b16 a[118:119], v106 offset:58368\n" \
  "ds_read_b64_tr_b16 a[120:121], v107 offset:49152\n" \
  "ds_read_b64_tr_b16 a[122:123], v107 offset:50176\n" \
  "ds_read_b64_tr_b16 a[124:125], v107 offset:57344\n" \
  "ds_read_b64_tr_b16 a[126:127], v107 offset:58368\n" \
  "s_waitcnt vmcnt(18)\n" \
  "s_waitcnt lgkmcnt(0)\n" \
  "s_barrier\n" \
  "s_cmp_eq_u32 %[grp], 0\n" \
  "s_cbranch_scc1 .Lo_ws12\n" \
  "s_sleep 16\n" \
  ".Lo_ws12:\n" \
  "s_add_i32 m0, %[hl0], 90112\n" \
  "s_mov_b32 s76, 73728\n" \
  "buffer_load_dwordx4 %[hvo], %[WB], s76 offen offset:0 lds\n" \
  "buffer_load_dwordx4 %[hvo], %[WB], s76 offen offset:1024 lds\n" \
  "buffer_load_dwordx4 %[hvo], %[WB], s76 offen offset:2048 lds\n" \
  "buffer_load_dwordx4 %[hvo], %[WB], s76 offen offset:3072 lds\n" \
  "s_add_i32 m0, %[hl0], 94208\n" \
  "s_mov_b32 s76, 77824\n" \
  "buffer_load_dwordx4 %[hvo], %[WB], s76 offen offset:0 lds\n" \
  "buffer_load_dwordx4 %[hvo], %[WB], s76 offen offset:1024 lds\n" \
  "buffer_load_dwordx4 %[hvo], %[WB], s76 offen offset:2048 lds\n" \
  "buffer_load_dwordx4 %[hvo], %[WB], s76 offen offset:3072 lds\n" \
  "s_add_i32 m0, %[hl0], 98304\n" \
  "s_mov_b32 s76, 81920\n" \
  "buffer_load_dwordx4 %[hvo], %[WB], s76 offen offset:0 lds\n" \
  "ds_read_b64_tr_b16 a[128:129], %[wr] offset:0\n" \
  "ds_read_b64_tr_b16 a[130:131], %[wr] offset:1024\n" \
  "ds_read_b64_tr_b16 a[132:133], %[wr] offset:8192\n" \
  "ds_read_b64_tr_b16 a[134:135], %[wr] offset:9216\n" \
  "ds_read_b64_tr_b16 a[136:137], v105 offset:0\n" \
  "ds_read_b64_tr_b16 a[138:139], v105 offset:1024\n" \
  "ds_read_b64_tr_b16 a[140:141], v105 offset:8192\n" \
  "ds_read_b64_tr_b16 a[142:143], v105 offset:9216\n" \
  "s_waitcnt vmcnt(18)\n" \
  "s_waitcnt lgkmcnt(0)\n" \
  "s_barrier\n" \
  "v_add_u32 v108, 16384, %[wr2]\n" \
  "ds_read_b64_tr_b16 a[144:145], v108 offset:0\n" \
  "ds_read_b64_tr_b16 a[146:147], v108 offset:18432\n" \
  "ds_read_b64_tr_b16 a[160:161], v108 offset:32\n" \
  "ds_read_b64_tr_b16 a[162:163], v108 offset:18464\n" \
  "ds_read_b64_tr_b16 a[176:177], v108 offset:64\n" \
  "ds_read_b64_tr_b16 a[178:179], v108 offset:18496\n" \
  "ds_read_b64_tr_b16 a[192:193], v108 offset:96\n" \
  "ds_read_b64_tr_b16 a[194:195], v108 offset:18528\n" \
  "ds_read_b64_tr_b16 a[208:209], v108 offset:128\n" \
  "ds_read_b64_tr_b16 a[210:211], v108 offset:18560\n" \
  "ds_read_b64_tr_b16 a[224:225], v108 offset:160\n" \
  "ds_read_b64_tr_b16 a[226:227], v108 offset:18592\n" \
  "ds_read_b64_tr_b16 a[240:241], v108 offset:192\n" \
  "ds_read_b64_tr_b16 a[242:243], v108 offset:18624\n" \
  "ds_read_b64_tr_b16 v[32:33], v108 offset:224\n" \
  "ds_read_b64_tr_b16 v[34:35], v108 offset:18656\n" \
  "ds_read_b64_tr_b16 v[48:49], v108 offset:256\n" \
  "ds_read_b64_tr_b16 v[50:51], v108 offset:18688\n" \
  "s_waitcnt vmcnt(9)\n" \
  "s_waitcnt lgkmcnt(0)\n" \
  "s_barrier\n" \
  "s_cmp_eq_u32 %[grp], 0\n" \
  "s_cbranch_scc1 .Lo_ws13\n" \
  "s_sleep 16\n" \
  ".Lo_ws13:\n" \
  "s_add_i32 m0, %[hl0], 0\n" \
  "s_mov_b32 s76, 110592\n" \
  "buffer_load_dwordx4 %[hvo], %[WB], s76 offen offset:0 lds\n" \
  "buffer_load_dwordx4 %[hvo], %[WB], s76 offen offset:1024 lds\n" \
  "buffer_load_dwordx4 %[hvo], %[WB], s76 offen offset:2048 lds\n" \
  "buffer_load_dwordx4 %[hvo], %[WB], s76 offen offset:3072 lds\n" \
  "s_add_i32 m0, %[hl0], 4096\n" \
  "s_mov_b32 s76, 114688\n" \
  "buffer_load_dwordx4 %[hvo], %[WB], s76 offen offset:0 lds\n" \
  "buffer_load_dwordx4 %[hvo], %[WB], s76 offen offset:1024 lds\n" \
  "buffer_load_dwordx4 %[hvo], %[WB], s76 offen offset:2048 lds\n" \
  "buffer_load_dwordx4 %[hvo], %[WB], s76 offen offset:3072 lds\n" \
  "s_add_i32 m0, %[hl0], 8192\n" \
  "s_mov_b32 s76, 118784\n" \
  "buffer_load_dwordx4 %[hvo], %[WB], s76 offen offset:0 lds\n" \
  "v_add_u32 v108, 53248, %[wr2]\n" \
  "ds_read_b64_tr_b16 a[148:149], v108 offset:0\n" \
  "ds_read_b64_tr_b16 a[150:151], v108 offset:18432\n" \
  "ds_read_b64_tr_b16 a[164:165], v108 offset:32\n" \
  "ds_read_b64_tr_b16 a[166:167], v108 offset:18464\n" \
  "ds_read_b64_tr_b16 a[180:181], v108 offset:64\n" \
  "ds_read_b64_tr_b16 a[182:183], v108 offset:18496\n" \
  "ds_read_b64_tr_b16 a[196:197], v108 offset:96\n" \
  "ds_read_b64_tr_b16 a[198:199], v108 offset:18528\n" \
  "ds_read_b64_tr_b16 a[212:213], v108 offset:128\n" \
  "ds_read_b64_tr_b16 a[214:215], v108 offset:18560\n" \
  "ds_read_b64_tr_b16 a[228:229], v108 offset:160\n" \
  "ds_read_b64_tr_b16 a[230:231], v108 offset:18592\n" \
  "ds_read_b64_tr_b16 a[244:245], v108 offset:192\n" \
  "ds_read_b64_tr_b16 a[246:247], v108 offset:18624\n" \
  "ds_read_b64_tr_b16 v[36:37], v108 offset:224\n" \
  "ds_read_b64_tr_b16 v[38:39], v108 offset:18656\n" \
  "ds_read_b64_tr_b16 v[52:53], v108 offset:256\n" \
  "ds_read_b64_tr_b16 v[54:55], v108 offset:18688\n" \
  "s_waitcnt vmcnt(9)\n" \
  "s_waitcnt lgkmcnt(0)\n" \
  "s_barrier\n" \
  "v_add_u32 v108, 90112, %[wr2]\n" \
  "ds_read_b64_tr_b16 a[152:153], v108 offset:0\n" \
  "ds_read_b64_tr_b16 a[154:155], v108 offset:18432\n" \
  "ds_read_b64_tr_b16 a[168:169], v108 offset:32\n" \
  "ds_read_b64_tr_b16 a[170:171], v108 offset:18464\n" \
  "ds_read_b64_tr_b16 a[184:185], v108 offset:64\n" \
  "ds_read_b64_tr_b16 a[186:187], v108 offset:18496\n" \
  "ds_read_b64_tr_b16 a[200:201], v108 offset:96\n" \
  "ds_read_b64_tr_b16 a[202:203], v108 offset:18528\n" \
  "ds_read_b64_tr_b16 a[216:217], v108 offset:128\n" \
  "ds_read_b64_tr_b16 a[218:219], v108 offset:18560\n" \
  "ds_read_b64_tr_b16 a[232:233], v108 offset:160\n" \
  "ds_read_b64_tr_b16 a[234:235], v108 offset:18592\n" \
  "ds_read_b64_tr_b16 a[248:249], v108 offset:192\n" \
  "ds_read_b64_tr_b16 a[250:251], v108 offset:18624\n" \
  "ds_read_b64_tr_b16 v[40:41], v108 offset:224\n" \
  "ds_read_b64_tr_b16 v[42:43], v108 offset:18656\n" \
  "ds_read_b64_tr_b16 v[56:57], v108 offset:256\n" \
  "ds_read_b64_tr_b16 v[58:59], v108 offset:18688\n" \
  "s_waitcnt vmcnt(0)\n" \
  "s_waitcnt lgkmcnt(0)\n" \
  "s_barrier\n" \
  "v_add_u32 v108, 0, %[wr2]\n" \
  "ds_read_b64_tr_b16 a[156:157], v108 offset:0\n" \
  "ds_read_b64_tr_b16 a[158:159], v108 offset:18432\n" \
  "ds_read_b64_tr_b16 a[172:173], v108 offset:32\n" \
  "ds_read_b64_tr_b16 a[174:175], v108 offset:18464\n" \
  "ds_read_b64_tr_b16 a[188:189], v108 offset:64\n" \
  "ds_read_b64_tr_b16 a[190:191], v108 offset:18496\n" \
  "ds_read_b64_tr_b16 a[204:205], v108 offset:96\n" \
  "ds_read_b64_tr_b16 a[206:207], v108 offset:18528\n" \
  "ds_read_b64_tr_b16 a[220:221], v108 offset:128\n" \
  "ds_read_b64_tr_b16 a[222:223], v108 offset:18560\n" \
  "ds_read_b64_tr_b16 a[236:237], v108 offset:160\n" \
  "ds_read_b64_tr_b16 a[238:239], v108 offset:18592\n" \
  "ds_read_b64_tr_b16 a[252:253], v108 offset:192\n" \
  "ds_read_b64_tr_b16 a[254:255], v108 offset:18624\n" \
  "ds_read_b64_tr_b16 v[44:45], v108 offset:224\n" \
  "ds_read_b64_tr_b16 v[46:47], v108 offset:18656\n" \
  "ds_read_b64_tr_b16 v[60:61], v108 offset:256\n" \
  "ds_read_b64_tr_b16 v[62:63], v108 offset:18688\n" \
  "s_waitcnt lgkmcnt(0)\n" \
  "s_barrier\n" \
  "s_cmp_eq_u32 %[grp], 0\n" \
  "s_cbranch_scc1 .Loa_go\n" \
  "s_mov_b32 s64, 0\n" \
  "s_xor_b32 s75, %[bas], 0x2000\n" \
  ".Loa_g1:\n" \
  "buffer_load_dword v238, off, %[X], s75 offset:48 sc1\n" \
  "s_waitcnt vmcnt(0)\n" \
  "v_readfirstlane_b32 s74, v238\n" \
  "s_cmp_lg_u32 s74, 0\n" \
  "s_cbranch_scc1 .Loa_g2\n" \
  "s_sleep 8\n" \
  "s_add_i32 s64, s64, 1\n" \
  "s_cmp_lt_u32 s64, 400\n" \
  "s_cbranch_scc1 .Loa_g1\n" \
  ".Loa_g2:\n" \
  "s_add_i32 s74, s74, %[g1t]\n" \
  ".Loa_g3:\n" \
  "s_memrealtime s[76:77]\n" \
  "s_waitcnt lgkmcnt(0)\n" \
  "s_sub_i32 s76, s76, s74\n" \
  "s_cmp_gt_u32 s76, -3000\n" \
  "s_cbranch_scc1 .Loa_g3\n" \
  ".Loa_go:\n" \
  "s_waitcnt vmcnt(0)\n" \
  "v_mov_b64 v[240:241], 0\n" \
  "s_branch .Lo_top\n" \
  ".Lo_sq:\n" \
  "s_cmp_lt_i32 s56, 0\n" \
  "s_cbranch_scc1 .Lo_sq_back\n" \
  "s_mov_b32 s64, 0\n" \
  ".Lo_sqw:\n" \
  "buffer_load_dword v245, off, %[X], %[ufo] sc1\n" \
  "buffer_load_dwordx4 v[96:99], %[la], %[X], s68 offen offset:0 sc1\n" \
  "buffer_load_dwordx4 v[100:103], %[la], %[X], s68 offen offset:1024 sc1\n" \
  "buffer_load_dwordx4 v[140:143], %[la], %[X], s59 offen offset:0 sc1\n" \
  "buffer_load_dwordx4 v[144:147], %[la], %[X], s59 offen offset:1024 sc1\n" \
  "buffer_load_dwordx4 v[148:151], %[la], %[X], s59 offen offset:2048 sc1\n" \
  "buffer_load_dwordx4 v[152:155], %[la], %[X], s59 offen offset:3072 sc1\n" \
  "buffer_load_dwordx4 v[156:159], v242, %[X], s59 offen offset:0 sc1\n" \
  "s_waitcnt vmcnt(4)\n" \
  "v_subrev_u32 v243, s56, v245\n" \
  "s_waitcnt vmcnt(4)\n" \
  "v_mfma_f32_16x16x32_bf16 v[204:207], v[64:67], v[140:143], 0\n" \
  "v_mfma_f32_16x16x32_bf16 v[208:211], v[68:71], v[140:143], 0\n" \
  "v_bitop3_b32 v243, v243, v140, s60 bitop3:0xf6\n" \
  "s_waitcnt vmcnt(3)\n" \
  "v_mfma_f32_16x16x32_bf16 v[204:207], v[72:75], v[144:147], v[204:207]\n" \
  "v_mfma_f32_16x16x32_bf16 v[208:211], v[76:79], v[144:147], v[208:211]\n" \
  "v_bitop3_b32 v243, v243, v144, s60 bitop3:0xf6\n" \
  "s_waitcnt vmcnt(2)\n" \
  "v_mfma_f32_16x16x32_bf16 v[204:207], v[80:83], v[148:151], v[204:207]\n" \
  "v_mfma_f32_16x16x32_bf16 v[208:211], v[84:87], v[148:151], v[208:211]\n" \
  "v_bitop3_b32 v243, v243, v148, s60 bitop3:0xf6\n" \
  "s_waitcnt vmcnt(1)\n" \
  "v_mfma_f32_16x16x32_bf16 v[204:207], v[88:91], v[152:155], v[204:207]\n" \
  "v_mfma_f32_16x16x32_bf16 v[208:211], v[92:95], v[152:155], v[208:211]\n" \
  "v_bitop3_b32 v243, v243, v152, s60 bitop3:0xf6\n" \
  "s_waitcnt vmcnt(0)\n" \
  "v_mfma_f32_16x16x32_bf16 v[204:207], v[96:99], v[156:159], v[204:207]\n" \
  "v_mfma_f32_16x16x32_bf16 v[208:211], v[100:103], v[156:159], v[208:211]\n" \
  "v_bitop3_b32 v238, v156, v157, s60 bitop3:0x7e\n" \
  "v_bitop3_b32 v239, v158, v159, s60 bitop3:0x7e\n" \
  "v_or3_b32 v243, v243, v238, v239\n" \
  "v_max3_u32 v238, v97, v98, v99\n" \
  "v_max3_u32 v238, v238, v101, v102\n" \
  "v_max_u32 v238, v238, v103\n" \
  "v_and_b32 v238, %[w4], v238\n" \
  "v_ashrrev_i32 v243, 31, v243\n" \
  "v_max3_u32 v238, v238, v96, v100\n" \
  "v_max_u32 v238, v238, v243\n" \
  "v_cmp_eq_u32 vcc, -1, v238\n" \
  "s_cbranch_vccz .Lo_sqw_ok\n" \
  "s_add_i32 s64, s64, 1\n" \
  "s_and_b32 s77, s64, 1023\n" \
  "s_cbranch_scc1 .Lo_sqw\n" \
  "buffer_load_dword v239, off, %[F], 0 offset:1024 sc1\n" \
  "s_waitcnt vmcnt(0)\n" \
  "v_readfirstlane_b32 s77, v239\n" \
  "s_cmp_lg_u32 s77, 0\n" \
  "s_cbranch_scc1 .Lo_sqw_ok\n" \
  "s_cmp_lt_u32 s64, 400000\n" \
  "s_cbranch_scc1 .Lo_sqw\n" \
  "v_mov_b32 v239, 1\n" \
  "buffer_store_dword v239, off, %[F], 0 offset:1024 sc1\n" \
  ".Lo_sqw_ok:\n" \
  "v_add_f32_dpp v204, v208, v204 row_ror:8 row_mask:0xf bank_mask:0xf\n" \
  "v_add_f32_dpp v205, v209, v205 row_ror:8 row_mask:0xf bank_mask:0xf\n" \
  "v_add_f32_dpp v206, v210, v206 row_ror:8 row_mask:0xf bank_mask:0xf\n" \
  "v_add_f32_dpp v207, v211, v207 row_ror:8 row_mask:0xf bank_mask:0xf\n" \
  "ds_write_b128 %[rmW], v[204:207]\n" \
  "s_memrealtime s[88:89]\n" \
  "s_waitcnt lgkmcnt(0)\n" \
  "s_add_i32 s88, s88, %[od1]\n" \
  "s_add_i32 s88, s88, 40\n" \
  "s_branch .Lo_sq_back\n" \
  ".Lo_sm:\n" \
  "s_cmp_lt_i32 s56, 0\n" \
  "s_cbranch_scc1 .Lo_sm_back\n" \
  "s_add_i32 s76, s57, -0x4000\n" \
  "v_mov_b64 v[196:197], 0\n" \
  "v_mov_b64 v[198:199], 0\n" \
  "v_mov_b64 v[200:201], 0\n" \
  "v_mov_b64 v[202:203], 0\n" \
  "s_mov_b32 s64, 0\n" \
  ".Lo_smw:\n" \
  "buffer_load_dwordx4 v[104:107], %[la], %[X], s76 offen offset:0 sc1\n" \
  "buffer_load_dwordx4 v[108:111], %[la], %[X], s76 offen offset:1024 sc1\n" \
  "buffer_load_dwordx4 v[112:115], %[la], %[X], s76 offen offset:2048 sc1\n" \
  "buffer_load_dwordx4 v[116:119], %[la], %[X], s76 offen offset:3072 sc1\n" \
  "buffer_load_dwordx4 v[120:123], v242, %[X], s76 offen offset:0 sc1\n" \
  "buffer_load_dwordx4 v[124:127], v242, %[X], s76 offen offset:1024 sc1\n" \
  "buffer_load_dwordx4 v[128:131], v242, %[X], s76 offen offset:2048 sc1\n" \
  "buffer_load_dwordx4 v[132:135], v242, %[X], s76 offen offset:3072 sc1\n" \
  "buffer_load_dwordx4 v[136:139], %[la], %[X], s57 offen offset:0 sc1\n" \
  "s_waitcnt vmcnt(0)\n" \
  "v_max3_u32 v239, v104, v105, v106\n" \
  "v_max3_u32 v239, v239, v107, v108\n" \
  "v_max3_u32 v239, v239, v109, v110\n" \
  "v_max3_u32 v239, v239, v111, v112\n" \
  "v_max3_u32 v239, v239, v113, v114\n" \
  "v_max3_u32 v239, v239, v115, v116\n" \
  "v_max3_u32 v239, v239, v117, v118\n" \
  "v_max3_u32 v239, v239, v119, v120\n" \
  "v_max3_u32 v239, v239, v121, v122\n" \
  "v_max3_u32 v239, v239, v123, v124\n" \
  "v_max3_u32 v239, v239, v125, v126\n" \
  "v_max3_u32 v239, v239, v127, v128\n" \
  "v_max3_u32 v239, v239, v129, v130\n" \
  "v_max3_u32 v239, v239, v131, v132\n" \
  "v_max3_u32 v239, v239, v133, v134\n" \
  "v_max3_u32 v239, v239, v135, v136\n" \
  "v_max3_u32 v239, v239, v137, v138\n" \
  "v_max_u32 v239, v239, v139\n" \
  "v_cmp_eq_u32 vcc, -1, v239\n" \
  "s_cbranch_vccz .Lo_smw_ok\n" \
  "s_add_i32 s64, s64, 1\n" \
  "s_and_b32 s77, s64, 1023\n" \
  "s_cbranch_scc1 .Lo_smw\n" \
  "buffer_load_dword v239, off, %[F], 0 offset:1024 sc1\n" \
  "s_waitcnt vmcnt(0)\n" \
  "v_readfirstlane_b32 s77, v239\n" \
  "s_cmp_lg_u32 s77, 0\n" \
  "s_cbranch_scc1 .Lo_smw_ok\n" \
  "s_cmp_lt_u32 s64, 400000\n" \
  "s_cbranch_scc1 .Lo_smw\n" \
  "v_mov_b32 v239, 1\n" \
  "buffer_store_dword v239, off, %[F], 0 offset:1024 sc1\n" \
  ".Lo_smw_ok:\n" \
  "s_memrealtime s[88:89]\n" \
  "v_smfmac_f32_16x16x64_bf16 v[196:199], v[104:107], a[0:7], %[idx]\n" \
  "v_smfmac_f32_16x16x64_bf16 v[200:203], v[104:107], a[8:15], %[idx]\n" \
  "v_smfmac_f32_16x16x64_bf16 v[196:199], v[108:111], a[16:23], %[idx]\n" \
  "v_smfmac_f32_16x16x64_bf16 v[200:203], v[108:111], a[24:31], %[idx]\n" \
  "v_smfmac_f32_16x16x64_bf16 v[196:199], v[112:115], a[32:39], %[idx]\n" \
  "v_smfmac_f32_16x16x64_bf16 v[200:203], v[112:115], a[40:47], %[idx]\n" \
  "v_smfmac_f32_16x16x64_bf16 v[196:199], v[116:119], a[48:55], %[idx]\n" \
  "v_smfmac_f32_16x16x64_bf16 v[200:203], v[116:119], a[56:63], %[idx]\n" \
  "v_smfmac_f32_16x16x64_bf16 v[196:199], v[120:123], a[64:71], %[idx]\n" \
  "v_smfmac_f32_16x16x64_bf16 v[200:203], v[120:123], a[72:79], %[idx]\n" \
  "v_smfmac_f32_16x16x64_bf16 v[196:199], v[124:127], a[80:87], %[idx]\n" \
  "v_smfmac_f32_16x16x64_bf16 v[200:203], v[124:127], a[88:95], %[idx]\n" \
  "v_smfmac_f32_16x16x64_bf16 v[196:199], v[128:131], a[96:103], %[idx]\n" \
  "v_smfmac_f32_16x16x64_bf16 v[200:203], v[128:131], a[104:111], %[idx]\n" \
  "v_smfmac_f32_16x16x64_bf16 v[196:199], v[132:135], a[112:119], %[idx]\n" \
  "v_smfmac_f32_16x16x64_bf16 v[200:203], v[132:135], a[120:127], %[idx]\n" \
  "v_smfmac_f32_16x16x64_bf16 v[196:199], v[136:139], a[128:135], %[idx]\n" \
  "v_smfmac_f32_16x16x64_bf16 v[200:203], v[136:139], a[136:143], %[idx]\n" \
  "s_waitcnt lgkmcnt(0)\n" \
  "s_add_i32 s88, s88, %[od1]\n" \
  "s_nop 5\n" \
  "v_pk_add_f32 v[196:197], v[196:197], v[198:199]\n" \
  "v_pk_add_f32 v[200:201], v[200:201], v[202:203]\n" \
  "v_cvt_pk_bf16_f32 v198, v196, v197\n" \
  "v_cvt_pk_bf16_f32 v199, v200, v201\n" \
  "ds_write2_b32 %[aW], v198, v199 offset1:16\n" \
  "s_branch .Lo_sm_back\n" \
  ".Lo_sf:\n" \
  "s_mov_b32 s64, 0\n" \
  ".Lo_sfw:\n" \
  "buffer_load_dword v244, %[flo], %[X], 0 offen sc1\n" \
  "s_waitcnt vmcnt(0)\n" \
  "v_cmp_gt_i32 vcc, s63, v244\n" \
  "s_cbranch_vccz .Lo_sfw_ok\n" \
  "s_sleep 1\n" \
  "s_add_i32 s64, s64, 1\n" \
  "s_and_b32 s77, s64, 1023\n" \
  "s_cbranch_scc1 .Lo_sfw\n" \
  "buffer_load_dword v239, off, %[F], 0 offset:1024 sc1\n" \
  "s_waitcnt vmcnt(0)\n" \
  "v_readfirstlane_b32 s77, v239\n" \
  "s_cmp_lg_u32 s77, 0\n" \
  "s_cbranch_scc1 .Lo_sfw_ok\n" \
  "s_cmp_lt_u32 s64, 400000\n" \
  "s_cbranch_scc1 .Lo_sfw\n" \
  "v_mov_b32 v239, 1\n" \
  "buffer_store_dword v239, off, %[F], 0 offset:1024 sc1\n" \
  ".Lo_sfw_ok:\n" \
  "s_branch .Lo_sf_back\n" \
  ".Loa_late:\n" \
  "s_cmp_lt_i32 s56, 0\n" \
  "s_cbranch_scc1 .Loa_al_ok\n" \
  "s_cmp_gt_u32 s91, -3000\n" \
  "s_cbranch_scc0 .Loa_late2\n" \
  ".Loa_wt:\n" \
  "s_memrealtime s[76:77]\n" \
  "s_waitcnt lgkmcnt(0)\n" \
  "s_sub_i32 s76, s76, s65\n" \
  "s_cmp_lt_i32 s76, 0\n" \
  "s_cbranch_scc1 .Loa_wt\n" \
  "s_branch .Loa_al_ok\n" \
  ".Loa_late2:\n" \
  "s_cmp_lg_u32 s65, 0\n" \
  "s_cbranch_scc1 .Loa_late3\n" \
  "s_mov_b32 s64, 0\n" \
  ".Loa_tw:\n" \
  "buffer_load_dword v238, off, %[X], s94 sc1\n" \
  "s_waitcnt vmcnt(0)\n" \
  "v_readfirstlane_b32 s65, v238\n" \
  "s_cmp_lg_u32 s65, 0\n" \
  "s_cbranch_scc1 .Loa_tw_ok\n" \
  "s_sleep 1\n" \
  "s_add_i32 s64, s64, 1\n" \
  "s_cmp_lt_u32 s64, 200\n" \
  "s_cbranch_scc1 .Loa_tw\n" \
  ".Loa_tw_ok:\n" \
  "s_sub_i32 s91, s74, s65\n" \
  "s_cmp_gt_u32 s91, -3000\n" \
  "s_cbranch_scc1 .Loa_wt\n" \
  ".Loa_late3:\n" \
  "s_branch .Loa_al_ok\n" \
  ".Lob_late:\n" \
  "s_cmp_lt_i32 s56, 0\n" \
  "s_cbranch_scc1 .Lob_al_ok\n" \
  "s_cmp_gt_u32 s91, -3000\n" \
  "s_cbranch_scc0 .Lob_late2\n" \
  ".Lob_wt:\n" \
  "s_memrealtime s[76:77]\n" \
  "s_waitcnt lgkmcnt(0)\n" \
  "s_sub_i32 s76, s76, s86\n" \
  "s_cmp_lt_i32 s76, 0\n" \
  "s_cbranch_scc1 .Lob_wt\n" \
  "s_branch .Lob_al_ok\n" \
  ".Lob_late2:\n" \
  "s_cmp_lg_u32 s86, 0\n" \
  "s_cbranch_scc1 .Lob_late3\n" \
  "s_mov_b32 s64, 0\n" \
  ".Lob_tw:\n" \
  "buffer_load_dword v238, off, %[X], s94 offset:4 sc1\n" \
  "s_waitcnt vmcnt(0)\n" \
  "v_readfirstlane_b32 s86, v238\n" \
  "s_cmp_lg_u32 s86, 0\n" \
  "s_cbranch_scc1 .Lob_tw_ok\n" \
  "s_sleep 1\n" \
  "s_add_i32 s64, s64, 1\n" \
  "s_cmp_lt_u32 s64, 200\n" \
  "s_cbranch_scc1 .Lob_tw\n" \
  ".Lob_tw_ok:\n" \
  "s_sub_i32 s91, s74, s86\n" \
  "s_cmp_gt_u32 s91, -3000\n" \
  "s_cbranch_scc1 .Lob_wt\n" \
  ".Lob_late3:\n" \
  "s_branch .Lob_al_ok\n" \
  ".Lo_end:\n"
#define O_CLOB "v32", "v33", "v34", "v35", "v36", "v37", "v38", "v39", "v40", "v41", "v42", "v43", "v44", "v45", "v46", "v47", "v48", "v49", "v50", "v51", "v52", "v53", "v54", "v55", "v56", "v57", "v58", "v59", "v60", "v61", "v62", "v63", "v64", "v65", "v66", "v67", "v68", "v69", "v70", "v71", "v72", "v73", "v74", "v75", "v76", "v77", "v78", "v79", "v80", "v81", "v82", "v83", "v84", "v85", "v86", "v87", "v88", "v89", "v90", "v91", "v92", "v93", "v94", "v95", "v96", "v97", "v98", "v99", "v100", "v101", "v102", "v103", "v104", "v105", "v106", "v107", "v108", "v109", "v110", "v111", "v112", "v113", "v114", "v115", "v116", "v117", "v118", "v119", "v120", "v121", "v122", "v123", "v124", "v125", "v126", "v127", "v128", "v129", "v130", "v131", "v132", "v133", "v134", "v135", "v136", "v137", "v138", "v139", "v140", "v141", "v142", "v143", "v144", "v145", "v146", "v147", "v148", "v149", "v150", "v151", "v152", "v153", "v154", "v155", "v156", "v157", "v158", "v159", "v160", "v161", "v162", "v163", "v164", "v165", "v166", "v167", "v168", "v169", "v170", "v171", "v172", "v173", "v174", "v175", "v176", "v177", "v178", "v179", "v180", "v181", "v182", "v183", "v184", "v185", "v186", "v187", "v188", "v189", "v190", "v191", "v192", "v193", "v194", "v195", "v196", "v197", "v198", "v199", "v200", "v201", "v202", "v203", "v204", "v205", "v206", "v207", "v208", "v209", "v210", "v211", "v212", "v213", "v214", "v215", "v216", "v217", "v218", "v219", "v220", "v221", "v222", "v223", "v224", "v225", "v226", "v227", "v228", "v229", "v230", "v231", "v232", "v233", "v234", "v235", "v236", "v237", "v238", "v239", "v240", "v241", "v242", "v243", "v244", "v245", "v246", "v247", "v248", "v249", "v250", "v251", "v252", "v253", "v254", "v255", "s56", "s57", "s58", "s59", "s60", "s61", "s62", "s63", "s64", "s65", "s66", "s67", "s68", "s69", "s70", "s71", "s72", "s73", "s74", "s75", "s76", "s77", "s78", "s79", "s80", "s81", "s82", "s83", "s84", "s85", "s86", "s87", "s88", "s89", "s90", "s91", "s92", "s93", "s94", "a0", "a1", "a2", "a3", "a4", "a5", "a6", "a7", "a8", "a9", "a10", "a11", "a12", "a13", "a14", "a15", "a16", "a17", "a18", "a19", "a20", "a21", "a22", "a23", "a24", "a25", "a26", "a27", "a28", "a29", "a30", "a31", "a32", "a33", "a34", "a35", "a36", "a37", "a38", "a39", "a40", "a41", "a42", "a43", "a44", "a45", "a46", "a47", "a48", "a49", "a50", "a51", "a52", "a53", "a54", "a55", "a56", "a57", "a58", "a59", "a60", "a61", "a62", "a63", "a64", "a65", "a66", "a67", "a68", "a69", "a70", "a71", "a72", "a73", "a74", "a75", "a76", "a77", "a78", "a79", "a80", "a81", "a82", "a83", "a84", "a85", "a86", "a87", "a88", "a89", "a90", "a91", "a92", "a93", "a94", "a95", "a96", "a97", "a98", "a99", "a100", "a101", "a102", "a103", "a104", "a105", "a106", "a107", "a108", "a109", "a110", "a111", "a112", "a113", "a114", "a115", "a116", "a117", "a118", "a119", "a120", "a121", "a122", "a123", "a124", "a125", "a126", "a127", "a128", "a129", "a130", "a131", "a132", "a133", "a134", "a135", "a136", "a137", "a138", "a139", "a140", "a141", "a142", "a143", "a144", "a145", "a146", "a147", "a148", "a149", "a150", "a151", "a152", "a153", "a154", "a155", "a156", "a157", "a158", "a159", "a160", "a161", "a162", "a163", "a164", "a165", "a166", "a167", "a168", "a169", "a170", "a171", "a172", "a173", "a174", "a175", "a176", "a177", "a178", "a179", "a180", "a181", "a182", "a183", "a184", "a185", "a186", "a187", "a188", "a189", "a190", "a191", "a192", "a193", "a194", "a195", "a196", "a197", "a198", "a199", "a200", "a201", "a202", "a203", "a204", "a205", "a206", "a207", "a208", "a209", "a210", "a211", "a212", "a213", "a214", "a215", "a216", "a217", "a218", "a219", "a220", "a221", "a222", "a223", "a224", "a225", "a226", "a227", "a228", "a229", "a230", "a231", "a232", "a233", "a234", "a235", "a236", "a237", "a238", "a239", "a240", "a241", "a242", "a243", "a244", "a245", "a246", "a247", "a248", "a249", "a250", "a251", "a252", "a253", "a254", "a255", "vcc", "scc", "m0", "memory"
#define U_ASM \
  "s_nop 4\n" \
  "s_mov_b32 s56, -2\n" \
  "v_add_u32 v183, 0x1000, %[la]\n" \
  "s_mov_b32 s78, 0\n" \
  "s_mov_b32 s79, 0\n" \
  "s_mov_b32 s80, 0\n" \
  "s_mov_b32 s81, -1\n" \
  "s_mov_b32 s87, 2.0\n" \
  "s_mov_b32 s88, 2.0\n" \
  "s_mov_b32 s66, 589824\n" \
  "s_add_i32 s94, %[bas], -32\n" \
  "s_waitcnt vmcnt(0)\n" \
  ".Lu_top:\n" \
  "s_add_i32 s56, s56, 1\n" \
  "s_add_i32 s94, s94, 16\n" \
  "v_readfirstlane_b32 s64, v182\n" \
  "s_bfe_i32 s62, s56, 0x10001\n" \
  "s_and_b32 s60, s62, 0x80008000\n" \
  "s_mul_i32 s59, s56, 36864\n" \
  "s_add_i32 s59, s59, %[ow]\n" \
  "s_lshl_b32 s61, s56, 17\n" \
  "s_and_b32 s61, s61, 0x20000\n" \
  "s_add_i32 s57, %[pt0], s61\n" \
  "s_add_i32 s66, s66, 589824\n" \
  "s_cmp_eq_u32 s66, 1769472\n" \
  "s_cselect_b32 s66, 0, s66\n" \
  "s_add_i32 s70, %[vm0], s66\n" \
  "s_cmp_lt_i32 s56, 0\n" \
  "s_cselect_b32 s59, 2.0, s59\n" \
  "s_cselect_b32 s57, 2.0, s57\n" \
  "s_cselect_b32 s70, 2.0, s70\n" \
  "s_add_i32 s58, s57, 0x4000\n" \
  "s_add_i32 s71, s70, 0x4000\n" \
  "s_cmp_lg_u32 s64, 0\n" \
  "s_cselect_b32 s76, %[uc2], 0\n" \
  "s_add_i32 s64, s64, s76\n" \
  "s_memrealtime s[74:75]\n" \
  "s_waitcnt lgkmcnt(0)\n" \
  "s_sub_i32 s91, s74, s64\n" \
  "s_cmp_gt_u32 s91, 14\n" \
  "s_cbranch_scc1 .Lub_late\n" \
  ".Lub_al_ok:\n" \
  "buffer_load_dwordx4 v[128:131], %[la], %[X], s70 offen offset:0 sc1\n" \
  "s_add_i32 s62, s56, 1\n" \
  "s_and_b32 s65, s62, 7\n" \
  "s_lshl_b32 s65, s65, 22\n" \
  "s_add_i32 s65, s65, s88\n" \
  "s_cmp_lt_i32 s56, 63\n" \
  "s_cselect_b32 s65, s65, 2.0\n" \
  "s_min_i32 s61, s62, 63\n" \
  "s_add_i32 s61, s61, 1\n" \
  "v_mov_b32 v185, s62\n" \
  "buffer_load_dwordx4 v[132:135], %[la], %[X], s70 offen offset:1024 sc1\n" \
  "s_nop 7\n" \
  "buffer_load_dwordx4 v[136:139], %[la], %[X], s71 offen offset:0 sc1\n" \
  "s_nop 7\n" \
  "buffer_load_dwordx4 v[140:143], %[la], %[X], s71 offen offset:1024 sc1\n" \
  "s_nop 7\n" \
  "buffer_load_dwordx4 v[40:43], %[la], %[X], s57 offen offset:2048 sc1\n" \
  "s_nop 7\n" \
  "buffer_load_dwordx4 v[44:47], %[la], %[X], s57 offen offset:3072 sc1\n" \
  "s_nop 7\n" \
  "buffer_load_dwordx4 v[48:51], v183, %[X], s57 offen offset:0 sc1\n" \
  "s_nop 7\n" \
  "buffer_load_dwordx4 v[52:55], v183, %[X], s57 offen offset:1024 sc1\n" \
  "s_waitcnt vmcnt(8)\n" \
  "v_accvgpr_read_b32 v96, a0\n" \
  "v_accvgpr_read_b32 v97, a1\n" \
  "v_accvgpr_read_b32 v98, a8\n" \
  "v_accvgpr_read_b32 v99, a9\n" \
  "v_accvgpr_read_b32 v100, a4\n" \
  "v_accvgpr_read_b32 v101, a5\n" \
  "v_accvgpr_read_b32 v102, a12\n" \
  "v_accvgpr_read_b32 v103, a13\n" \
  "buffer_load_dwordx4 v[56:59], v183, %[X], s57 offen offset:2048 sc1\n" \
  "v_accvgpr_read_b32 v104, a16\n" \
  "v_accvgpr_read_b32 v105, a17\n" \
  "v_accvgpr_read_b32 v106, a24\n" \
  "v_accvgpr_read_b32 v107, a25\n" \
  "v_accvgpr_read_b32 v108, a20\n" \
  "v_accvgpr_read_b32 v109, a21\n" \
  "v_accvgpr_read_b32 v110, a28\n" \
  "v_accvgpr_read_b32 v111, a29\n" \
  "buffer_load_dwordx4 v[60:63], v183, %[X], s57 offen offset:3072 sc1\n" \
  "v_accvgpr_read_b32 v112, a32\n" \
  "v_accvgpr_read_b32 v113, a33\n" \
  "v_accvgpr_read_b32 v114, a40\n" \
  "v_accvgpr_read_b32 v115, a41\n" \
  "v_accvgpr_read_b32 v116, a36\n" \
  "v_accvgpr_read_b32 v117, a37\n" \
  "v_accvgpr_read_b32 v118, a44\n" \
  "v_accvgpr_read_b32 v119, a45\n" \
  "buffer_load_dwordx4 v[64:67], %[la], %[X], s58 offen offset:0 sc1\n" \
  "v_accvgpr_read_b32 v120, a48\n" \
  "v_accvgpr_read_b32 v121, a49\n" \
  "v_accvgpr_read_b32 v122, a56\n" \
  "v_accvgpr_read_b32 v123, a57\n" \
  "v_accvgpr_read_b32 v124, a52\n" \
  "v_accvgpr_read_b32 v125, a53\n" \
  "v_accvgpr_read_b32 v126, a60\n" \
  "v_accvgpr_read_b32 v127, a61\n" \
  "buffer_load_dwordx4 v[68:71], %[la], %[X], s58 offen offset:1024 sc1\n" \
  "s_nop 7\n" \
  "buffer_load_dwordx4 v[72:75], %[la], %[X], s58 offen offset:2048 sc1\n" \
  "s_nop 7\n" \
  "buffer_load_dwordx4 v[76:79], %[la], %[X], s58 offen offset:3072 sc1\n" \
  "s_nop 7\n" \
  "buffer_load_dword v184, %[flo], %[X], 0 offen sc1\n" \
  "s_nop 7\n" \
  "s_waitcnt vmcnt(10)\n" \
  "v_mfma_f32_16x16x32_bf16 v[148:151], v[128:131], v[40:43], 0\n" \
  "v_bitop3_b32 v176, v40, v41, s60 bitop3:0x7e\n" \
  "v_bitop3_b32 v177, v42, v43, s60 bitop3:0x7e\n" \
  "v_mfma_f32_16x16x32_bf16 v[156:159], v[136:139], v[40:43], 0\n" \
  "buffer_load_dwordx4 v[32:35], %[la], %[X], s57 offen offset:0 sc1\n" \
  "s_waitcnt vmcnt(10)\n" \
  "v_mfma_f32_16x16x32_bf16 v[148:151], v[132:135], v[44:47], v[148:151]\n" \
  "v_bitop3_b32 v178, v44, v45, s60 bitop3:0x7e\n" \
  "v_or3_b32 v176, v176, v177, v178\n" \
  "v_mfma_f32_16x16x32_bf16 v[156:159], v[140:143], v[44:47], v[156:159]\n" \
  "v_bitop3_b32 v177, v46, v47, s60 bitop3:0x7e\n" \
  "buffer_load_dwordx4 v[36:39], %[la], %[X], s57 offen offset:1024 sc1\n" \
  "s_waitcnt vmcnt(10)\n" \
  "v_mfma_f32_16x16x32_bf16 v[148:151], a[0:3], v[48:51], v[148:151]\n" \
  "v_mfma_f32_16x16x32_bf16 v[156:159], a[32:35], v[48:51], v[156:159]\n" \
  "buffer_load_dwordx4 v[80:83], v183, %[X], s58 offen offset:0 sc1\n" \
  "v_max3_u32 v180, v129, v130, v131\n" \
  "v_max3_u32 v180, v180, v133, v134\n" \
  "v_max_u32 v180, v180, v135\n" \
  "v_and_b32 v180, %[w40], v180\n" \
  "v_max3_u32 v180, v180, v128, v132\n" \
  "v_max3_u32 v181, v137, v138, v139\n" \
  "v_max3_u32 v181, v181, v141, v142\n" \
  "v_max_u32 v181, v181, v143\n" \
  "v_and_b32 v181, %[w41], v181\n" \
  "v_max3_u32 v181, v181, v136, v140\n" \
  "s_waitcnt vmcnt(10)\n" \
  "v_mfma_f32_16x16x32_bf16 v[148:151], a[4:7], v[52:55], v[148:151]\n" \
  "v_bitop3_b32 v178, v48, v52, s60 bitop3:0x7e\n" \
  "v_or3_b32 v176, v176, v177, v178\n" \
  "v_mfma_f32_16x16x32_bf16 v[156:159], a[36:39], v[52:55], v[156:159]\n" \
  "buffer_load_dwordx4 v[84:87], v183, %[X], s58 offen offset:1024 sc1\n" \
  "s_waitcnt vmcnt(10)\n" \
  "v_mfma_f32_16x16x32_bf16 v[148:151], a[8:11], v[56:59], v[148:151]\n" \
  "v_mfma_f32_16x16x32_bf16 v[156:159], a[40:43], v[56:59], v[156:159]\n" \
  "buffer_load_dwordx4 v[88:91], v183, %[X], s58 offen offset:2048 sc1\n" \
  "s_waitcnt vmcnt(10)\n" \
  "v_mfma_f32_16x16x32_bf16 v[148:151], a[12:15], v[60:63], v[148:151]\n" \
  "v_bitop3_b32 v177, v56, v60, s60 bitop3:0x7e\n" \
  "v_mfma_f32_16x16x32_bf16 v[156:159], a[44:47], v[60:63], v[156:159]\n" \
  "buffer_load_dwordx4 v[92:95], v183, %[X], s58 offen offset:3072 sc1\n" \
  "s_waitcnt vmcnt(10)\n" \
  "v_mfma_f32_16x16x32_bf16 v[148:151], a[16:19], v[64:67], v[148:151]\n" \
  "v_mfma_f32_16x16x32_bf16 v[156:159], a[48:51], v[64:67], v[156:159]\n" \
  "buffer_load_dword v182, off, %[X], s94 offset:20 sc1\n" \
  "s_waitcnt vmcnt(10)\n" \
  "v_mfma_f32_16x16x32_bf16 v[148:151], a[20:23], v[68:71], v[148:151]\n" \
  "v_bitop3_b32 v178, v64, v68, s60 bitop3:0x7e\n" \
  "v_or3_b32 v176, v176, v177, v178\n" \
  "v_mfma_f32_16x16x32_bf16 v[156:159], a[52:55], v[68:71], v[156:159]\n" \
  "s_waitcnt vmcnt(9)\n" \
  "v_mfma_f32_16x16x32_bf16 v[148:151], a[24:27], v[72:75], v[148:151]\n" \
  "v_bitop3_b32 v176, v176, v72, s60 bitop3:0xf6\n" \
  "v_mfma_f32_16x16x32_bf16 v[156:159], a[56:59], v[72:75], v[156:159]\n" \
  "s_waitcnt vmcnt(8)\n" \
  "v_mfma_f32_16x16x32_bf16 v[148:151], a[28:31], v[76:79], v[148:151]\n" \
  "v_bitop3_b32 v176, v176, v76, s60 bitop3:0xf6\n" \
  "v_mfma_f32_16x16x32_bf16 v[156:159], a[60:63], v[76:79], v[156:159]\n" \
  "s_waitcnt vmcnt(7)\n" \
  "v_subrev_u32 v184, s61, v184\n" \
  "v_or_b32 v176, v176, v184\n" \
  "v_ashrrev_i32 v176, 31, v176\n" \
  "v_max3_u32 v180, v180, v181, v176\n" \
  "v_cmp_eq_u32 vcc, -1, v180\n" \
  "s_cbranch_vccnz .Lu_sp\n" \
  ".Lu_sp_back:\n" \
  "s_waitcnt vmcnt(6)\n" \
  "v_mfma_f32_16x16x32_bf16 v[144:147], v[128:131], v[32:35], 0\n" \
  "v_bitop3_b32 v176, v32, v33, s60 bitop3:0x7e\n" \
  "v_bitop3_b32 v177, v34, v35, s60 bitop3:0x7e\n" \
  "v_mfma_f32_16x16x32_bf16 v[152:155], v[136:139], v[32:35], 0\n" \
  "ds_write_b128 %[rtW], v[148:151] offset:1024\n" \
  "s_waitcnt vmcnt(5)\n" \
  "v_mfma_f32_16x16x32_bf16 v[144:147], v[132:135], v[36:39], v[144:147]\n" \
  "v_bitop3_b32 v178, v36, v37, s60 bitop3:0x7e\n" \
  "v_or3_b32 v176, v176, v177, v178\n" \
  "v_mfma_f32_16x16x32_bf16 v[152:155], v[140:143], v[36:39], v[152:155]\n" \
  "v_bitop3_b32 v177, v38, v39, s60 bitop3:0x7e\n" \
  "ds_write_b128 %[rtW], v[156:159] offset:9216\n" \
  "s_waitcnt vmcnt(4)\n" \
  "v_mfma_f32_16x16x32_bf16 v[144:147], v[96:99], v[80:83], v[144:147]\n" \
  "v_bitop3_b32 v178, v80, v82, s60 bitop3:0x7e\n" \
  "v_or3_b32 v176, v176, v177, v178\n" \
  "v_mfma_f32_16x16x32_bf16 v[152:155], v[112:115], v[80:83], v[152:155]\n" \
  "s_waitcnt vmcnt(3)\n" \
  "v_mfma_f32_16x16x32_bf16 v[144:147], v[100:103], v[84:87], v[144:147]\n" \
  "v_bitop3_b32 v177, v84, v86, s60 bitop3:0x7e\n" \
  "v_mfma_f32_16x16x32_bf16 v[152:155], v[116:119], v[84:87], v[152:155]\n" \
  "buffer_load_dwordx4 a[0:3], %[la], %[X], s65 offen offset:0 sc1\n" \
  "s_waitcnt vmcnt(3)\n" \
  "v_mfma_f32_16x16x32_bf16 v[144:147], v[104:107], v[88:91], v[144:147]\n" \
  "v_bitop3_b32 v178, v88, v90, s60 bitop3:0x7e\n" \
  "v_or3_b32 v176, v176, v177, v178\n" \
  "v_mfma_f32_16x16x32_bf16 v[152:155], v[120:123], v[88:91], v[152:155]\n" \
  "buffer_load_dwordx4 a[4:7], %[la], %[X], s65 offen offset:1024 sc1\n" \
  "s_waitcnt vmcnt(3)\n" \
  "v_mfma_f32_16x16x32_bf16 v[144:147], v[108:111], v[92:95], v[144:147]\n" \
  "v_bitop3_b32 v176, v176, v92, s60 bitop3:0xf6\n" \
  "v_bitop3_b32 v176, v176, v94, s60 bitop3:0xf6\n" \
  "v_mfma_f32_16x16x32_bf16 v[152:155], v[124:127], v[92:95], v[152:155]\n" \
  "buffer_load_dwordx4 a[8:11], %[la], %[X], s65 offen offset:2048 sc1\n" \
  "v_cmp_gt_i32 vcc, 0, v176\n" \
  "s_cbranch_vccnz .Lu_sr\n" \
  ".Lu_sr_back:\n" \
  "s_nop 1\n" \
  "ds_write_b128 %[rtW], v[144:147] offset:0\n" \
  "buffer_load_dwordx4 a[12:15], %[la], %[X], s65 offen offset:3072 sc1\n" \
  "s_nop 0\n" \
  "ds_write_b128 %[rtW], v[152:155] offset:8192\n" \
  "buffer_load_dwordx4 a[16:19], v183, %[X], s65 offen offset:0 sc1\n" \
  "s_waitcnt lgkmcnt(0)\n" \
  "s_barrier\n" \
  "ds_read2st64_b64 v[160:163], %[rtA] offset0:0 offset1:4\n" \
  "ds_read2st64_b64 v[164:167], %[rtA] offset0:8 offset1:12\n" \
  "ds_read2st64_b64 v[168:171], %[rtA] offset0:16 offset1:20\n" \
  "ds_read2st64_b64 v[172:175], %[rtA] offset0:24 offset1:28\n" \
  "buffer_store_dword v185, %[ufo], %[X], s87 offen sc1\n" \
  "buffer_load_dwordx4 a[20:23], v183, %[X], s65 offen offset:1024 sc1\n" \
  "s_waitcnt lgkmcnt(2)\n" \
  "v_pk_add_f32 v[160:161], v[160:161], v[162:163]\n" \
  "v_pk_add_f32 v[164:165], v[164:165], v[166:167]\n" \
  "v_pk_add_f32 v[160:161], v[160:161], v[164:165]\n" \
  "v_cvt_pk_bf16_f32 v160, v160, v161\n" \
  "v_xor_b32 v160, s60, v160\n" \
  "v_min_u32 v160, -2, v160\n" \
  "buffer_store_dword v160, %[lo], %[OS], s59 offen offset:0 sc1\n" \
  "buffer_load_dwordx4 a[24:27], v183, %[X], s65 offen offset:2048 sc1\n" \
  "s_waitcnt lgkmcnt(0)\n" \
  "v_pk_add_f32 v[168:169], v[168:169], v[170:171]\n" \
  "v_pk_add_f32 v[172:173], v[172:173], v[174:175]\n" \
  "v_pk_add_f32 v[168:169], v[168:169], v[172:173]\n" \
  "v_cvt_pk_bf16_f32 v168, v168, v169\n" \
  "v_xor_b32 v168, s60, v168\n" \
  "v_min_u32 v168, -2, v168\n" \
  "buffer_store_dword v168, %[lo], %[OS], s59 offen offset:256 sc1\n" \
  "s_barrier\n" \
  "s_waitcnt vmcnt(10)\n" \
  "buffer_load_dwordx4 a[28:31], v183, %[X], s65 offen offset:3072 sc1\n" \
  "s_add_i32 s65, s65, 0x10000\n" \
  "s_nop 7\n" \
  "buffer_load_dwordx4 a[32:35], %[la], %[X], s65 offen offset:0 sc1\n" \
  "s_nop 7\n" \
  "buffer_load_dwordx4 a[36:39], %[la], %[X], s65 offen offset:1024 sc1\n" \
  "s_nop 7\n" \
  "buffer_load_dwordx4 a[40:43], %[la], %[X], s65 offen offset:2048 sc1\n" \
  "s_nop 7\n" \
  "buffer_load_dwordx4 a[44:47], %[la], %[X], s65 offen offset:3072 sc1\n" \
  "s_nop 7\n" \
  "buffer_load_dwordx4 a[48:51], v183, %[X], s65 offen offset:0 sc1\n" \
  "s_nop 7\n" \
  "buffer_load_dwordx4 a[52:55], v183, %[X], s65 offen offset:1024 sc1\n" \
  "s_nop 7\n" \
  "buffer_load_dwordx4 a[56:59], v183, %[X], s65 offen offset:2048 sc1\n" \
  "s_nop 7\n" \
  "buffer_load_dwordx4 a[60:63], v183, %[X], s65 offen offset:3072 sc1\n" \
  "s_add_i32 s65, s65, 0x10000\n" \
  "s_nop 7\n" \
  "s_cmp_lg_u32 s56, s81\n" \
  "s_cbranch_scc1 .Lu_top\n" \
  "s_cmp_eq_u32 s56, -1\n" \
  "s_cbranch_scc0 .Lu_end\n" \
  "s_mov_b32 s81, 63\n" \
  "s_mov_b32 s87, 0\n" \
  "s_mov_b32 s88, %[vt0]\n" \
  "s_cmp_eq_u32 %[grp], 0\n" \
  "s_cbranch_scc1 .Lub_go\n" \
  "s_mov_b32 s63, 0\n" \
  "s_xor_b32 s75, %[bas], 0x2000\n" \
  ".Lub_g1:\n" \
  "buffer_load_dword v180, off, %[X], s75 offset:48 sc1\n" \
  "s_waitcnt vmcnt(0)\n" \
  "v_readfirstlane_b32 s74, v180\n" \
  "s_cmp_lg_u32 s74, 0\n" \
  "s_cbranch_scc1 .Lub_g2\n" \
  "s_sleep 8\n" \
  "s_add_i32 s63, s63, 1\n" \
  "s_cmp_lt_u32 s63, 400\n" \
  "s_cbranch_scc1 .Lub_g1\n" \
  ".Lub_g2:\n" \
  "s_add_i32 s74, s74, %[g1t]\n" \
  ".Lub_g3:\n" \
  "s_memrealtime s[76:77]\n" \
  "s_waitcnt lgkmcnt(0)\n" \
  "s_sub_i32 s76, s76, s74\n" \
  "s_cmp_gt_u32 s76, -3000\n" \
  "s_cbranch_scc1 .Lub_g3\n" \
  ".Lub_go:\n" \
  "s_add_i32 s62, s56, 1\n" \
  "s_and_b32 s65, s62, 7\n" \
  "s_lshl_b32 s65, s65, 22\n" \
  "s_add_i32 s65, s65, s88\n" \
  "s_cmp_lt_i32 s56, 63\n" \
  "s_cselect_b32 s65, s65, 2.0\n" \
  "s_min_i32 s61, s62, 63\n" \
  "s_add_i32 s61, s61, 1\n" \
  "s_mov_b32 s63, 0\n" \
  ".Lu_enw:\n" \
  "buffer_load_dword v184, %[flo], %[X], 0 offen sc1\n" \
  "s_waitcnt vmcnt(0)\n" \
  "v_cmp_gt_i32 vcc, s61, v184\n" \
  "s_cbranch_vccz .Lu_enw_ok\n" \
  "s_sleep 4\n" \
  "s_add_i32 s63, s63, 1\n" \
  "s_and_b32 s77, s63, 1023\n" \
  "s_cbranch_scc1 .Lu_enw\n" \
  "buffer_load_dword v181, off, %[F], 0 offset:1024 sc1\n" \
  "s_waitcnt vmcnt(0)\n" \
  "v_readfirstlane_b32 s77, v181\n" \
  "s_cmp_lg_u32 s77, 0\n" \
  "s_cbranch_scc1 .Lu_enw_ok\n" \
  "s_cmp_lt_u32 s63, 400000\n" \
  "s_cbranch_scc1 .Lu_enw\n" \
  "v_mov_b32 v181, 1\n" \
  "buffer_store_dword v181, off, %[F], 0 offset:1024 sc1\n" \
  ".Lu_enw_ok:\n" \
  "buffer_load_dwordx4 a[0:3], %[la], %[X], s65 offen offset:0 sc1\n" \
  "buffer_load_dwordx4 a[4:7], %[la], %[X], s65 offen offset:1024 sc1\n" \
  "buffer_load_dwordx4 a[8:11], %[la], %[X], s65 offen offset:2048 sc1\n" \
  "buffer_load_dwordx4 a[12:15], %[la], %[X], s65 offen offset:3072 sc1\n" \
  "buffer_load_dwordx4 a[16:19], v183, %[X], s65 offen offset:0 sc1\n" \
  "buffer_load_dwordx4 a[20:23], v183, %[X], s65 offen offset:1024 sc1\n" \
  "buffer_load_dwordx4 a[24:27], v183, %[X], s65 offen offset:2048 sc1\n" \
  "buffer_load_dwordx4 a[28:31], v183, %[X], s65 offen offset:3072 sc1\n" \
  "s_add_i32 s65, s65, 0x10000\n" \
  "buffer_load_dwordx4 a[32:35], %[la], %[X], s65 offen offset:0 sc1\n" \
  "buffer_load_dwordx4 a[36:39], %[la], %[X], s65 offen offset:1024 sc1\n" \
  "buffer_load_dwordx4 a[40:43], %[la], %[X], s65 offen offset:2048 sc1\n" \
  "buffer_load_dwordx4 a[44:47], %[la], %[X], s65 offen offset:3072 sc1\n" \
  "buffer_load_dwordx4 a[48:51], v183, %[X], s65 offen offset:0 sc1\n" \
  "buffer_load_dwordx4 a[52:55], v183, %[X], s65 offen offset:1024 sc1\n" \
  "buffer_load_dwordx4 a[56:59], v183, %[X], s65 offen offset:2048 sc1\n" \
  "buffer_load_dwordx4 a[60:63], v183, %[X], s65 offen offset:3072 sc1\n" \
  "s_add_i32 s65, s65, 0x10000\n" \
  "v_mov_b32 v182, 0\n" \
  "s_branch .Lu_top\n" \
  ".Lu_sp:\n" \
  "s_cmp_lt_i32 s56, 0\n" \
  "s_cbranch_scc1 .Lu_sp_back\n" \
  "s_mov_b32 s63, 0\n" \
  ".Lu_spw:\n" \
  "buffer_load_dwordx4 v[128:131], %[la], %[X], s70 offen offset:0 sc1\n" \
  "buffer_load_dwordx4 v[132:135], %[la], %[X], s70 offen offset:1024 sc1\n" \
  "buffer_load_dwordx4 v[136:139], %[la], %[X], s71 offen offset:0 sc1\n" \
  "buffer_load_dwordx4 v[140:143], %[la], %[X], s71 offen offset:1024 sc1\n" \
  "buffer_load_dwordx4 v[40:43], %[la], %[X], s57 offen offset:2048 sc1\n" \
  "buffer_load_dwordx4 v[44:47], %[la], %[X], s57 offen offset:3072 sc1\n" \
  "buffer_load_dwordx4 v[48:51], v183, %[X], s57 offen offset:0 sc1\n" \
  "buffer_load_dwordx4 v[52:55], v183, %[X], s57 offen offset:1024 sc1\n" \
  "buffer_load_dwordx4 v[56:59], v183, %[X], s57 offen offset:2048 sc1\n" \
  "buffer_load_dwordx4 v[60:63], v183, %[X], s57 offen offset:3072 sc1\n" \
  "buffer_load_dwordx4 v[64:67], %[la], %[X], s58 offen offset:0 sc1\n" \
  "buffer_load_dwordx4 v[68:71], %[la], %[X], s58 offen offset:1024 sc1\n" \
  "buffer_load_dwordx4 v[72:75], %[la], %[X], s58 offen offset:2048 sc1\n" \
  "buffer_load_dwordx4 v[76:79], %[la], %[X], s58 offen offset:3072 sc1\n" \
  "buffer_load_dword v184, %[flo], %[X], 0 offen sc1\n" \
  "s_waitcnt vmcnt(0)\n" \
  "v_max3_u32 v180, v129, v130, v131\n" \
  "v_max3_u32 v180, v180, v133, v134\n" \
  "v_max_u32 v180, v180, v135\n" \
  "v_and_b32 v180, %[w40], v180\n" \
  "v_max3_u32 v180, v180, v128, v132\n" \
  "v_max3_u32 v181, v137, v138, v139\n" \
  "v_max3_u32 v181, v181, v141, v142\n" \
  "v_max_u32 v181, v181, v143\n" \
  "v_and_b32 v181, %[w41], v181\n" \
  "v_max3_u32 v181, v181, v136, v140\n" \
  "v_bitop3_b32 v176, v40, v41, s60 bitop3:0x7e\n" \
  "v_bitop3_b32 v177, v42, v43, s60 bitop3:0x7e\n" \
  "v_bitop3_b32 v178, v44, v45, s60 bitop3:0x7e\n" \
  "v_or3_b32 v176, v176, v177, v178\n" \
  "v_bitop3_b32 v177, v46, v47, s60 bitop3:0x7e\n" \
  "v_bitop3_b32 v178, v48, v52, s60 bitop3:0x7e\n" \
  "v_or3_b32 v176, v176, v177, v178\n" \
  "v_bitop3_b32 v177, v56, v60, s60 bitop3:0x7e\n" \
  "v_bitop3_b32 v178, v64, v68, s60 bitop3:0x7e\n" \
  "v_or3_b32 v176, v176, v177, v178\n" \
  "v_bitop3_b32 v176, v176, v72, s60 bitop3:0xf6\n" \
  "v_bitop3_b32 v176, v176, v76, s60 bitop3:0xf6\n" \
  "v_subrev_u32 v184, s61, v184\n" \
  "v_or_b32 v176, v176, v184\n" \
  "v_ashrrev_i32 v176, 31, v176\n" \
  "v_max3_u32 v180, v180, v181, v176\n" \
  "v_cmp_eq_u32 vcc, -1, v180\n" \
  "s_cbranch_vccz .Lu_spw_ok\n" \
  "s_sleep 2\n" \
  "s_add_i32 s63, s63, 1\n" \
  "s_and_b32 s77, s63, 1023\n" \
  "s_cbranch_scc1 .Lu_spw\n" \
  "buffer_load_dword v181, off, %[F], 0 offset:1024 sc1\n" \
  "s_waitcnt vmcnt(0)\n" \
  "v_readfirstlane_b32 s77, v181\n" \
  "s_cmp_lg_u32 s77, 0\n" \
  "s_cbranch_scc1 .Lu_spw_ok\n" \
  "s_cmp_lt_u32 s63, 400000\n" \
  "s_cbranch_scc1 .Lu_spw\n" \
  "v_mov_b32 v181, 1\n" \
  "buffer_store_dword v181, off, %[F], 0 offset:1024 sc1\n" \
  ".Lu_spw_ok:\n" \
  "buffer_load_dwordx4 v[32:35], %[la], %[X], s57 offen offset:0 sc1\n" \
  "buffer_load_dwordx4 v[36:39], %[la], %[X], s57 offen offset:1024 sc1\n" \
  "buffer_load_dwordx4 v[80:83], v183, %[X], s58 offen offset:0 sc1\n" \
  "buffer_load_dwordx4 v[84:87], v183, %[X], s58 offen offset:1024 sc1\n" \
  "buffer_load_dwordx4 v[88:91], v183, %[X], s58 offen offset:2048 sc1\n" \
  "buffer_load_dwordx4 v[92:95], v183, %[X], s58 offen offset:3072 sc1\n" \
  "v_mfma_f32_16x16x32_bf16 v[148:151], v[128:131], v[40:43], 0\n" \
  "v_mfma_f32_16x16x32_bf16 v[156:159], v[136:139], v[40:43], 0\n" \
  "v_mfma_f32_16x16x32_bf16 v[148:151], v[132:135], v[44:47], v[148:151]\n" \
  "v_mfma_f32_16x16x32_bf16 v[156:159], v[140:143], v[44:47], v[156:159]\n" \
  "v_mfma_f32_16x16x32_bf16 v[148:151], a[0:3], v[48:51], v[148:151]\n" \
  "v_mfma_f32_16x16x32_bf16 v[156:159], a[32:35], v[48:51], v[156:159]\n" \
  "v_mfma_f32_16x16x32_bf16 v[148:151], a[4:7], v[52:55], v[148:151]\n" \
  "v_mfma_f32_16x16x32_bf16 v[156:159], a[36:39], v[52:55], v[156:159]\n" \
  "v_mfma_f32_16x16x32_bf16 v[148:151], a[8:11], v[56:59], v[148:151]\n" \
  "v_mfma_f32_16x16x32_bf16 v[156:159], a[40:43], v[56:59], v[156:159]\n" \
  "v_mfma_f32_16x16x32_bf16 v[148:151], a[12:15], v[60:63], v[148:151]\n" \
  "v_mfma_f32_16x16x32_bf16 v[156:159], a[44:47], v[60:63], v[156:159]\n" \
  "v_mfma_f32_16x16x32_bf16 v[148:151], a[16:19], v[64:67], v[148:151]\n" \
  "v_mfma_f32_16x16x32_bf16 v[156:159], a[48:51], v[64:67], v[156:159]\n" \
  "v_mfma_f32_16x16x32_bf16 v[148:151], a[20:23], v[68:71], v[148:151]\n" \
  "v_mfma_f32_16x16x32_bf16 v[156:159], a[52:55], v[68:71], v[156:159]\n" \
  "v_mfma_f32_16x16x32_bf16 v[148:151], a[24:27], v[72:75], v[148:151]\n" \
  "v_mfma_f32_16x16x32_bf16 v[156:159], a[56:59], v[72:75], v[156:159]\n" \
  "v_mfma_f32_16x16x32_bf16 v[148:151], a[28:31], v[76:79], v[148:151]\n" \
  "v_mfma_f32_16x16x32_bf16 v[156:159], a[60:63], v[76:79], v[156:159]\n" \
  "s_waitcnt vmcnt(0)\n" \
  "s_branch .Lu_sp_back\n" \
  ".Lu_sr:\n" \
  "s_cmp_lt_i32 s56, 0\n" \
  "s_cbranch_scc1 .Lu_sr_back\n" \
  "s_mov_b32 s63, 0\n" \
  ".Lu_srw:\n" \
  "buffer_load_dwordx4 v[32:35], %[la], %[X], s57 offen offset:0 sc1\n" \
  "buffer_load_dwordx4 v[36:39], %[la], %[X], s57 offen offset:1024 sc1\n" \
  "buffer_load_dwordx4 v[80:83], v183, %[X], s58 offen offset:0 sc1\n" \
  "buffer_load_dwordx4 v[84:87], v183, %[X], s58 offen offset:1024 sc1\n" \
  "buffer_load_dwordx4 v[88:91], v183, %[X], s58 offen offset:2048 sc1\n" \
  "buffer_load_dwordx4 v[92:95], v183, %[X], s58 offen offset:3072 sc1\n" \
  "s_waitcnt vmcnt(0)\n" \
  "v_bitop3_b32 v176, v32, v33, s60 bitop3:0x7e\n" \
  "v_bitop3_b32 v177, v34, v35, s60 bitop3:0x7e\n" \
  "v_bitop3_b32 v178, v36, v37, s60 bitop3:0x7e\n" \
  "v_or3_b32 v176, v176, v177, v178\n" \
  "v_bitop3_b32 v177, v38, v39, s60 bitop3:0x7e\n" \
  "v_bitop3_b32 v178, v80, v82, s60 bitop3:0x7e\n" \
  "v_or3_b32 v176, v176, v177, v178\n" \
  "v_bitop3_b32 v177, v84, v86, s60 bitop3:0x7e\n" \
  "v_bitop3_b32 v178, v88, v90, s60 bitop3:0x7e\n" \
  "v_or3_b32 v176, v176, v177, v178\n" \
  "v_bitop3_b32 v176, v176, v92, s60 bitop3:0xf6\n" \
  "v_bitop3_b32 v176, v176, v94, s60 bitop3:0xf6\n" \
  "v_cmp_gt_i32 vcc, 0, v176\n" \
  "s_cbranch_vccz .Lu_srw_ok\n" \
  "s_sleep 2\n" \
  "s_add_i32 s63, s63, 1\n" \
  "s_and_b32 s77, s63, 1023\n" \
  "s_cbranch_scc1 .Lu_srw\n" \
  "buffer_load_dword v181, off, %[F], 0 offset:1024 sc1\n" \
  "s_waitcnt vmcnt(0)\n" \
  "v_readfirstlane_b32 s77, v181\n" \
  "s_cmp_lg_u32 s77, 0\n" \
  "s_cbranch_scc1 .Lu_srw_ok\n" \
  "s_cmp_lt_u32 s63, 400000\n" \
  "s_cbranch_scc1 .Lu_srw\n" \
  "v_mov_b32 v181, 1\n" \
  "buffer_store_dword v181, off, %[F], 0 offset:1024 sc1\n" \
  ".Lu_srw_ok:\n" \
  "v_mfma_f32_16x16x32_bf16 v[144:147], v[128:131], v[32:35], 0\n" \
  "v_mfma_f32_16x16x32_bf16 v[152:155], v[136:139], v[32:35], 0\n" \
  "v_mfma_f32_16x16x32_bf16 v[144:147], v[132:135], v[36:39], v[144:147]\n" \
  "v_mfma_f32_16x16x32_bf16 v[152:155], v[140:143], v[36:39], v[152:155]\n" \
  "v_mfma_f32_16x16x32_bf16 v[144:147], v[96:99], v[80:83], v[144:147]\n" \
  "v_mfma_f32_16x16x32_bf16 v[152:155], v[112:115], v[80:83], v[152:155]\n" \
  "v_mfma_f32_16x16x32_bf16 v[144:147], v[100:103], v[84:87], v[144:147]\n" \
  "v_mfma_f32_16x16x32_bf16 v[152:155], v[116:119], v[84:87], v[152:155]\n" \
  "v_mfma_f32_16x16x32_bf16 v[144:147], v[104:107], v[88:91], v[144:147]\n" \
  "v_mfma_f32_16x16x32_bf16 v[152:155], v[120:123], v[88:91], v[152:155]\n" \
  "v_mfma_f32_16x16x32_bf16 v[144:147], v[108:111], v[92:95], v[144:147]\n" \
  "v_mfma_f32_16x16x32_bf16 v[152:155], v[124:127], v[92:95], v[152:155]\n" \
  "s_nop 7\n" \
  "s_branch .Lu_sr_back\n" \
  ".Lub_late:\n" \
  "s_cmp_lt_i32 s56, 0\n" \
  "s_cbranch_scc1 .Lub_al_ok\n" \
  "s_cmp_gt_u32 s91, -3000\n" \
  "s_cbranch_scc0 .Lub_late2\n" \
  ".Lub_wt:\n" \
  "s_memrealtime s[76:77]\n" \
  "s_waitcnt lgkmcnt(0)\n" \
  "s_sub_i32 s76, s76, s64\n" \
  "s_cmp_lt_i32 s76, 0\n" \
  "s_cbranch_scc1 .Lub_wt\n" \
  "s_branch .Lub_al_ok\n" \
  ".Lub_late2:\n" \
  "s_cmp_lg_u32 s64, 0\n" \
  "s_cbranch_scc1 .Lub_late3\n" \
  "s_mov_b32 s63, 0\n" \
  ".Lub_tw:\n" \
  "buffer_load_dword v180, off, %[X], s94 offset:4 sc1\n" \
  "s_waitcnt vmcnt(0)\n" \
  "v_readfirstlane_b32 s64, v180\n" \
  "s_cmp_lg_u32 s64, 0\n" \
  "s_cbranch_scc1 .Lub_tw_ok\n" \
  "s_sleep 1\n" \
  "s_add_i32 s63, s63, 1\n" \
  "s_cmp_lt_u32 s63, 200\n" \
  "s_cbranch_scc1 .Lub_tw\n" \
  ".Lub_tw_ok:\n" \
  "s_sub_i32 s91, s74, s64\n" \
  "s_cmp_gt_u32 s91, -3000\n" \
  "s_cbranch_scc1 .Lub_wt\n" \
  ".Lub_late3:\n" \
  "s_branch .Lub_al_ok\n" \
  ".Lu_end:\n" \
  "s_waitcnt vmcnt(0)\n"
#define U_CLOB "v32", "v33", "v34", "v35", "v36", "v37", "v38", "v39", "v40", "v41", "v42", "v43", "v44", "v45", "v46", "v47", "v48", "v49", "v50", "v51", "v52", "v53", "v54", "v55", "v56", "v57", "v58", "v59", "v60", "v61", "v62", "v63", "v64", "v65", "v66", "v67", "v68", "v69", "v70", "v71", "v72", "v73", "v74", "v75", "v76", "v77", "v78", "v79", "v80", "v81", "v82", "v83", "v84", "v85", "v86", "v87", "v88", "v89", "v90", "v91", "v92", "v93", "v94", "v95", "v96", "v97", "v98", "v99", "v100", "v101", "v102", "v103", "v104", "v105", "v106", "v107", "v108", "v109", "v110", "v111", "v112", "v113", "v114", "v115", "v116", "v117", "v118", "v119", "v120", "v121", "v122", "v123", "v124", "v125", "v126", "v127", "v128", "v129", "v130", "v131", "v132", "v133", "v134", "v135", "v136", "v137", "v138", "v139", "v140", "v141", "v142", "v143", "v144", "v145", "v146", "v147", "v148", "v149", "v150", "v151", "v152", "v153", "v154", "v155", "v156", "v157", "v158", "v159", "v160", "v161", "v162", "v163", "v164", "v165", "v166", "v167", "v168", "v169", "v170", "v171", "v172", "v173", "v174", "v175", "v176", "v177", "v178", "v179", "v180", "v181", "v182", "v183", "v184", "v185", "v186", "v187", "v188", "v189", "v190", "v191", "v192", "v193", "v194", "v195", "v196", "v197", "v198", "v199", "v200", "v201", "v202", "v203", "v204", "v205", "v206", "v207", "v208", "v209", "v210", "v211", "v212", "v213", "v214", "v215", "v216", "v217", "v218", "v219", "v220", "v221", "v222", "v223", "v224", "v225", "v226", "v227", "v228", "v229", "v230", "v231", "v232", "v233", "v234", "v235", "v236", "v237", "v238", "v239", "v240", "v241", "v242", "v243", "v244", "v245", "v246", "v247", "v248", "v249", "v250", "v251", "v252", "v253", "v254", "v255", "s56", "s57", "s58", "s59", "s60", "s61", "s62", "s63", "s64", "s65", "s66", "s67", "s68", "s69", "s70", "s71", "s72", "s73", "s74", "s75", "s76", "s77", "s78", "s79", "s80", "s81", "s82", "s83", "s84", "s85", "s86", "s87", "s88", "s89", "s90", "s91", "s92", "s93", "s94", "a0", "a1", "a2", "a3", "a4", "a5", "a6", "a7", "a8", "a9", "a10", "a11", "a12", "a13", "a14", "a15", "a16", "a17", "a18", "a19", "a20", "a21", "a22", "a23", "a24", "a25", "a26", "a27", "a28", "a29", "a30", "a31", "a32", "a33", "a34", "a35", "a36", "a37", "a38", "a39", "a40", "a41", "a42", "a43", "a44", "a45", "a46", "a47", "a48", "a49", "a50", "a51", "a52", "a53", "a54", "a55", "a56", "a57", "a58", "a59", "a60", "a61", "a62", "a63", "a64", "a65", "a66", "a67", "a68", "a69", "a70", "a71", "a72", "a73", "a74", "a75", "a76", "a77", "a78", "a79", "a80", "a81", "a82", "a83", "a84", "a85", "a86", "a87", "a88", "a89", "a90", "a91", "a92", "a93", "a94", "a95", "a96", "a97", "a98", "a99", "a100", "a101", "a102", "a103", "a104", "a105", "a106", "a107", "a108", "a109", "a110", "a111", "a112", "a113", "a114", "a115", "a116", "a117", "a118", "a119", "a120", "a121", "a122", "a123", "a124", "a125", "a126", "a127", "a128", "a129", "a130", "a131", "a132", "a133", "a134", "a135", "a136", "a137", "a138", "a139", "a140", "a141", "a142", "a143", "a144", "a145", "a146", "a147", "a148", "a149", "a150", "a151", "a152", "a153", "a154", "a155", "a156", "a157", "a158", "a159", "a160", "a161", "a162", "a163", "a164", "a165", "a166", "a167", "a168", "a169", "a170", "a171", "a172", "a173", "a174", "a175", "a176", "a177", "a178", "a179", "a180", "a181", "a182", "a183", "a184", "a185", "a186", "a187", "a188", "a189", "a190", "a191", "a192", "a193", "a194", "a195", "a196", "a197", "a198", "a199", "a200", "a201", "a202", "a203", "a204", "a205", "a206", "a207", "a208", "a209", "a210", "a211", "a212", "a213", "a214", "a215", "a216", "a217", "a218", "a219", "a220", "a221", "a222", "a223", "a224", "a225", "a226", "a227", "a228", "a229", "a230", "a231", "a232", "a233", "a234", "a235", "a236", "a237", "a238", "a239", "a240", "a241", "a242", "a243", "a244", "a245", "a246", "a247", "a248", "a249", "a250", "a251", "a252", "a253", "a254", "a255", "vcc", "scc", "m0", "memory"
#define TAU 236
#define SH1 40
#define MRG 2
#define XST 150
#define G1T 100
#define VTC -8
#define UC2 5
#define OC1 3
#define OD1 110
#define LAG 355
#define WST(A, B, nb) [WA] "s"(mkd(A, (DM * HD - h * DH) * 2u)), [WB] "s"(mkd(B, nb)), [wl] "s"(US(w * 4096)), \
  [wv] "v"((unsigned)((16 * w + g) * 8192 + (fr ^ (2 * g)) * 16)), [wr] "v"((unsigned)((8 * g + (fr >> 2)) * 256 + ((4 * w + ((fr & 3) >> 1)) ^ (2 * (fr >> 2) + 8 * (g & 1))) * 16 + (fr & 1) * 8))
#define WST2 WST(p.Wv + h * DH, p.Wo + (size_t)h * (DH * DM), DH * DM * 2u), [wr2] "v"((unsigned)((4 * g + (fr >> 2)) * 1152 + 288 * w + 8 * (fr & 3)))
#define SCHED [tau] "s"((220)), [g1t] "s"((70)), [bas] "s"(US(gb + CTL_CH * CH + 64u)), [rdy] "v"(tid == 0 ? 0u : DROP)
__device__ __forceinline__ unsigned qrow(int r) { return (unsigned)(L_QI + (r >> 4) * 2816 + (r & 15) * 160 + ((r >> 2) & 3) * 8); }
__device__ __forceinline__ void qk_wg(const MP& p, int role, int grp, int h, int rep, unsigned gb) {
  const int tid = threadIdx.x, lane = tid & 63, w = __builtin_amdgcn_readfirstlane(tid >> 6);
  const int fr = lane & 15, g = lane >> 4;
  const int jj = (h >> 1) & 3, hh = h & 1;
  const unsigned pb = w < NPR ? (unsigned)(16 * (w >> 1) + 4 * (w & 1) + (h >> 3)) * CH + (unsigned)(((h >> 3) & 1) * 1024) : DROP;
  const unsigned ptb = (unsigned)(PT_CH - PM_CH) * CH + (unsigned)(h >> 3) * 2u * CH + 8192u - 2u * gb;
  const int qc = 8 * (w & 1) + (fr & 7), T = w >> 1;
  const unsigned po1 = fr < 8 ? pb + (unsigned)(jj * 1024 + (g * 16 + hh * 8 + fr) * 16) : T ? ptb + phys((unsigned)((4 + (h & 7)) * 1024 + (g * 16 + qc) * 16)) : DROP;
  const unsigned po2 = fr < 8 ? pb + (unsigned)(4096 + (jj * 16 + hh * 8 + fr) * 16 + g * 4) : ptb + (unsigned)((T * 2 + hh) * 1024 + (jj * 16 + qc) * 16 + g * 4);
  const unsigned po3 = fr < 8 || T ? DROP : ptb + phys((unsigned)((12 + (jj & 2) + hh) * 1024 + (g * 16 + qc) * 16 + (jj & 1) * 8));
  unsigned hA = (unsigned)(65536 + (fr >> 3) * RGB + g * 128 + (lane & 7) * 16);
  asm volatile(QK_ASM
    : [hA] "+v"(hA)
    : [X] "s"(mkd(p.win, WIN_BYTES)), [HS] "s"(mkd(p.H + (size_t)grp * (NSEG * SEGB / 2), NSEG * SEGB)), [F] "s"(mkd(p.ct, CT_TOTAL * 4u)),
      [mr0] "s"(US(gb + (unsigned)(MEM_CH + 2 * rep) * CH)), [pm0] "s"(US(gb + PM_CH * CH)), [hl0] "s"(US((unsigned)w * RGB)), WST(p.Wq + h * DH, p.Wk + h * DH, (DM * HD - h * DH) * 2u), [grp] "s"(US(grp)),
      SCHED, [x0] "s"((230)), [mrg] "s"((2)), [rpo] "v"(tid == 0 ? gb + CTL_CH * CH + (unsigned)(2048 + 256 * rep) : DROP),
      [cpo] "v"(h == 0 ? gb + CTL_CH * CH + (unsigned)(2048 + 256 * (lane & 7)) : DROP), [cpw] "v"(h == 0 && tid == 0 ? 0u : DROP),
      [cpa] "v"(h == 0 && grp ? 5u * 16u : DROP), [lag] "s"((340)), [dn] "s"(-(4)), [up] "s"((8)), [sh1] "s"((20)), [gg] "s"(US(grp * 130)), [c2] "s"((42)), [c4] "s"((22)),
      [mo] "v"((unsigned)lane * 16u), [idx] "v"((lane & 2) ? 0xEEEEu : 0x4444u),
      [qW] "v"(qrow(32 * w + fr) + 4 * g), [kW] "v"(qrow(32 * w + fr) + 128 + 8 * g),
      [qR] "v"(qrow(4 * g + (fr >> 2)) + ((fr & 2) ? 16 + 32 * w : 0) + 8 * (fr & 1)), [kR] "v"(qrow(4 * g + (fr >> 2)) + 128 + 8 * (fr & 3)),
      [qtW] "v"(qrow(32 * w + fr) + 16 + 32 * (g >> 1) + 8 * (g & 1)), [ktW] "v"(L_KT + (unsigned)(w * 1024 + lane * 16)), [ktR] "v"(L_KT + (unsigned)lane * 16u),
      [hvo] "v"((unsigned)w * RGB + (unsigned)lane * 16u), [po1] "v"(po1), [po2] "v"(po2), [po3] "v"(po3), [pcl] "v"(fr == 0 ? po1 : lane == 1 ? po2 : DROP),
      [lim] "v"(fr >= 8 ? 16 * T + qc - 4 * g : 1000)
    : QK_CLOB);
}

__device__ __forceinline__ void vt_wg(const MP& p, int role, int grp, int h, int rep, unsigned gb) {
  const int tid = threadIdx.x, lane = tid & 63, w = __builtin_amdgcn_readfirstlane(tid >> 6);
  const int fr = lane & 15, g = lane >> 4;
  unsigned hA = (unsigned)(65536 + (fr >> 3) * RGB + g * 128 + (lane & 7) * 16);
  asm volatile(VT_ASM
    : [hA] "+v"(hA)
    : [X] "s"(mkd(p.win, WIN_BYTES)), [HS] "s"(mkd(p.H + (size_t)grp * (NSEG * SEGB / 2), NSEG * SEGB)),
      [F] "s"(mkd(p.ct, CT_TOTAL * 4u)), [hl0] "s"(US((unsigned)w * RGB)), WST2, [grp] "s"(US(grp)),
      SCHED, [x0] "s"((230)), [vtc] "s"((-14)), [rpo] "v"(tid == 0 ? gb + CTL_CH * CH + (unsigned)(3072 + 256 * rep) : DROP),
      [la] "v"((unsigned)lane * 16u), [vW] "v"(L_VI + (unsigned)(w * 1024 + lane * 16)), [vR] "v"(L_VI + (unsigned)lane * 16u), [hvo] "v"((unsigned)w * RGB + (unsigned)lane * 16u),
      [px] "s"(US(4u * CH + (w == 3 ? 8192u - 2u * gb : 0u))), [sto] "v"(gb + (unsigned)(VT_CH + 36 * w + (h >> 3)) * CH + (unsigned)((h & 7) * 1024 + lane * 16)), [flo] "v"(tid == 0 ? gb + CTL_CH * CH + (unsigned)(4096 + (h >> 3) * 128 + (h & 7) * 4) : DROP)
    : VT_CLOB);
}

__device__ __forceinline__ int o_head(int j) { return 16 * (j / 18) + ((j % 18) & 15); }
__device__ __forceinline__ void o_wg(const MP& p, int role, int grp, int j, int rep, unsigned gb) {
  const int tid = threadIdx.x, lane = tid & 63, w = __builtin_amdgcn_readfirstlane(tid >> 6);
  const int fr = lane & 15, g = lane >> 4;
  const int wsl = (fr * 4 + (g ^ ((fr >> 1) & 3))) * 16;
  const int mr = 2 * g + ((lane >> 2) & 1);
  const int h = o_head(j);
  const unsigned vb = j % 18 < 16 ? (gb ^ 8192u) + (unsigned)(VM_CH + 9 * w) * CH + (unsigned)((h >> 3) * 2048 + (h & 1) * 1024 + ((h >> 1) & 3) * 256 + fr * 16) : DROP;
  unsigned la = (unsigned)lane * 16u;
  asm volatile(O_ASM
    : [la] "+v"(la)
    : [X] "s"(mkd(p.win, WIN_BYTES)),
      [F] "s"(mkd(p.ct, CT_TOTAL * 4u)), [mw0] "s"(US(gb + (unsigned)(MEM_CH + 2 * w) * CH)), [mr0] "s"(US(gb + (unsigned)(MEM_CH + 2 * rep) * CH)), [ufo] "s"(US(gb + CTL_CH * CH + (unsigned)(5120 + (j >> 1) * 128))),
      [pm0] "s"(US(gb + (unsigned)(PM_CH + (NPR == 4 ? 16 * (rep >> 1) + 4 * (rep & 1) : NPR == 2 ? 4 * (rep >> 1) : 0) + w) * CH + (unsigned)(w & 1) * 1024u)), [vm0] "s"(US((gb ^ 8192u) + (unsigned)(VM_CH + j) * CH + (unsigned)w * 2048u)), [vt0] "s"(US((gb ^ (j < 32 ? 0u : 8192u)) + (unsigned)(VT_CH + 4 * j + w) * CH)),
      [ml0] "s"(US(LO_MEM + (unsigned)w * 1024u)), [hl0] "s"(US((unsigned)w * RGB)), [hvo] "v"((unsigned)w * RGB + (unsigned)lane * 16u), WST2, [grp] "s"(US(grp)),
      [g1t] "s"((70)), [bas] "s"(US(gb + CTL_CH * CH + 64u)), [oc1] "s"((3)), [od1] "s"((110)), [w4] "s"(US(j % 9 == 8 ? -1 : 0)),
      [idx] "v"((lane & 2) ? 0xEEEEu : 0x4444u), [mA] "v"(LO_MEM + (unsigned)lane * 16u), [mA8] "v"(LO_MEM + (unsigned)(8192 + g * 1024 + fr * 16)),
      [c0] "v"((unsigned)(w * 1024 + lane * 16)), [c2] "v"(lane < 16 ? (unsigned)(w * 256 + lane * 16) : DROP),
      [aW] "v"(LO_A + (unsigned)((w * 16 + (fr >> 2) * 4 + (fr & 1)) * 32 + g * 8 + (fr & 2) * 2)), [aR] "v"(LO_A + (unsigned)((g * 8 + (fr >> 2)) * 32 + (fr & 3) * 8)),
      [vmo] "v"(vb + (unsigned)g * CH), [vmo1] "v"(vb + 4u * CH + (unsigned)g * 4u), [flo] "v"(gb + CTL_CH * CH + (unsigned)(4096 + w * 128 + (lane & 7) * 4)),
      [rmW] "v"(LO_RM + (unsigned)(w * 1024 + wsl)), [rmA] "v"(LO_RM + (unsigned)((mr * 4 + ((lane & 3) ^ ((mr >> 1) & 3))) * 16 + (lane & 8))),
      [lm] "v"(phys((unsigned)(j * 256 + lane * 4)))
    : O_CLOB);
}

__device__ __forceinline__ void u_wg(const MP& p, int role, int grp, int u, unsigned gb) {
  const int tid = threadIdx.x, lane = tid & 63, w = __builtin_amdgcn_readfirstlane(tid >> 6);
  const int fr = lane & 15, g = lane >> 4, tr = (lane >> 2) & 7;
  unsigned la = (unsigned)lane * 16u;
  asm volatile(U_ASM
    : [la] "+v"(la)
    : [X] "s"(mkd(p.win, WIN_BYTES)), [OS] "s"(mkd(p.OT + (size_t)grp * (NSEG * SEGB / 2), NSEG * SEGB)), [F] "s"(mkd(p.ct, CT_TOTAL * 4u)),
      [pt0] "s"(US((gb ^ 8192u) + (unsigned)(PT_CH + 2 * w) * CH)), [vm0] "s"(US((gb ^ 8192u) + (unsigned)(VM_CH + 2 * u) * CH + (unsigned)w * 2048u)),
      [vt0] "s"(US((gb ^ (u < 16 ? 0u : 8192u)) + (unsigned)(VT_CH + 8 * u + w) * CH)),
      [ow] "s"(US((unsigned)w * RGB)), [grp] "s"(US(grp)), [g1t] "s"((70)), [bas] "s"(US(gb + CTL_CH * CH + 64u)), [w40] "s"(US(2 * u % 9 == 8 ? -1 : 0)), [w41] "s"(US((2 * u + 1) % 9 == 8 ? -1 : 0)),
      [uc2] "s"((5)),
      [flo] "v"(gb + CTL_CH * CH + (unsigned)(4096 + w * 128 + (lane & 7) * 4)), [ufo] "v"(tid == 0 ? gb + CTL_CH * CH + (unsigned)(5120 + u * 128) : DROP),
      [rtW] "v"((unsigned)(w * 2048 + (fr * 4 + (g ^ ((fr >> 1) & 3))) * 16)),
      [rtA] "v"((unsigned)((w >> 1) * 1024 + ((8 * (w & 1) + tr) * 4 + ((2 * (lane >> 5) + ((lane >> 1) & 1)) ^ ((tr >> 1) & 3))) * 16 + (lane & 1) * 8)),
      [lo] "v"((unsigned)(u * 512 + lane * 4))
    : U_CLOB);
}

#define YSLP 2
struct Row9 { unsigned u[9]; };
__device__ __forceinline__ float rnd(float a) { return __uint_as_float(pk2(0.f, a)); }
__device__ __forceinline__ void ln18(const f32x2 (&x)[9], f32x2 (&o)[9], float* st, int lane, int w) {
  f32x2 s2 = x[0];
#pragma unroll
  for (int i = 1; i < 9; ++i) s2 += x[i];
  const float mw = i_red8add(s2[0] + s2[1]) * (1.0f / 144.0f);
  f32x2 q2 = {0.f, 0.f};
#pragma unroll
  for (int i = 0; i < 9; ++i) { const f32x2 d = x[i] - mw; q2 += d * d; }
  const float qw = i_red8add(q2[0] + q2[1]);
  if (lane < 8) *(f32x2*)(st + (lane * 4 + w) * 2) = (f32x2){mw, qw};
  BAR();
  const f32x4 a = *(const f32x4*)(st + (lane & 7) * 8), b = *(const f32x4*)(st + (lane & 7) * 8 + 4);
  const float mean = ((a[0] + a[2]) + (b[0] + b[2])) * 0.25f;
  const float d0 = a[0] - mean, d1 = a[2] - mean, d2 = b[0] - mean, d3 = b[2] - mean;
  const float m2 = ((a[1] + a[3]) + (b[1] + b[3])) + 144.0f * ((d0 * d0 + d1 * d1) + (d2 * d2 + d3 * d3));
  const float var = rnd(m2 * (1.0f / DM));
  const float m = rnd(mean), rden = __builtin_amdgcn_rcpf(rnd(__builtin_amdgcn_sqrtf(rnd(var + 1e-5f))));
#pragma unroll
  for (int i = 0; i < 9; ++i) { const f32x2 d = x[i] - m; o[i] = (f32x2){rnd(d[0]), rnd(d[1])} * rden; }
}
__device__ __forceinline__ void mix_rows(Row9& xa, const Row9& ya, const rsrc_t H, unsigned so, unsigned o0, unsigned oe, float* st, int lane, int w) {
  f32x2 x[9], y[9], ln[9];
#pragma unroll
  for (int i = 0; i < 9; ++i) { x[i] = (f32x2){lo2f(xa.u[i]), hi2f(xa.u[i])}; y[i] = (f32x2){lo2f(ya.u[i]), hi2f(ya.u[i])}; }
  ln18(x, ln, st, lane, w);
#pragma unroll
  for (int i = 0; i < 9; ++i) {
    const f32x2 m = (f32x2){rnd(ln[i][0]), rnd(ln[i][1])} * y[i];
    const f32x2 u = x[i] + (f32x2){rnd(m[0]), rnd(m[1])};
    x[i] = (f32x2){rnd(u[0]), rnd(u[1])};
  }
  ln18(x, ln, st + 64, lane, w);
  unsigned h[9];
#pragma unroll
  for (int i = 0; i < 9; ++i) h[i] = nosent(pk2(ln[i][0], ln[i][1]));
  st16(H, o0, so, (v4u){h[0], h[1], h[2], h[3]});
  st16(H, o0 + 4096u, so, (v4u){h[4], h[5], h[6], h[7]});
  st4(H, oe, so, h[8]);
#pragma unroll
  for (int i = 0; i < 9; ++i) xa.u[i] = pk2(x[i][0], x[i][1]);
}
template <int SLP>
__device__ __forceinline__ void wait9(const rsrc_t O, const rsrc_t F, unsigned so, unsigned o0, unsigned o1, unsigned oe, v4u (&v)[2], unsigned& e) {
  unsigned spins = 0;
#pragma nounroll
  while (true) {
    asm volatile("" ::: "memory");
    v[0] = ld16(O, o0, so); v[1] = ld16(O, o1, so); e = ld4(O, oe, so);
    if (__all(mx4(mx4(e, v[0]), v[1]) != SENT)) break;
    if (spin_fail(spins, F)) break;
    __builtin_amdgcn_s_sleep(SLP);
  }
}
__device__ __forceinline__ void add_rows(Row9& xa, const v4u (&v)[2], unsigned e) {
#pragma unroll
  for (int i = 0; i < 8; ++i) xa.u[i] = pk2(lo2f(xa.u[i]) + lo2f(v[i >> 2][i & 3]), hi2f(xa.u[i]) + hi2f(v[i >> 2][i & 3]));
  xa.u[8] = pk2(lo2f(xa.u[8]) + lo2f(e), hi2f(xa.u[8]) + hi2f(e));
}
__device__ __forceinline__ void arm_slots(const MP& p, int s, int rg) {
  const rsrc_t O = mkr(p.OT, 2u * NSEG * SEGB), H = mkr(p.H, 2u * NSEG * SEGB);
  const unsigned so = __builtin_amdgcn_readfirstlane((unsigned)s * SEGB + (unsigned)rg * RGB), o0 = threadIdx.x * 16u;
  const v4u sent = {SENT, SENT, SENT, SENT};
#pragma unroll
  for (int i = 0; i < 3; ++i) {
    const unsigned o = i < 2 || threadIdx.x < 64 ? o0 + 4096u * i : DROP;
    st16(H, o, s + 5 < NSEG ? so + 5u * SEGB : DROP, sent);
    st16(O, o, so, sent); st16(O, o, NSEG * SEGB + so, sent); st16(H, o, s + 2 < NSEG ? (NSEG + 2) * SEGB + so : DROP, sent);
  }
}
#define LR_Y 1024
#define LR_SV 10240
#define RSLP 4
__device__ __forceinline__ void row_wg(const MP& p, char* smem, int role, int rw, unsigned flip) {
  const int tid = threadIdx.x, lane = tid & 63, w = __builtin_amdgcn_readfirstlane(tid >> 6);
  const rsrc_t F = mkr(p.ct, CT_TOTAL * 4u), O = mkr(p.OT, 2u * NSEG * SEGB), H = mkr(p.H, 2u * NSEG * SEGB), Y = mkr(p.Y, DM * YS), X = mkr(p.win, WIN_BYTES);
  float* st = (float*)smem;
  const int r8 = tid & 7, pj = (tid >> 3) & 7;
  const unsigned o0 = (unsigned)tid * 16u;
  const unsigned oe = (unsigned)(8192 + pj * 128 + r8 * 16 + w * 4);
  const int rg = rw & 3, s0 = rw >> 2;
  const char* yR = smem + LR_Y + (8 * ((tid >> 4) * 2 + ((lane >> 1) & 1)) + ((lane & 15) >> 2)) * 16 + (lane & 1) * 8;
  const char* yE = smem + LR_Y + (512 + 8 * pj + 2 * w) * 16 + r8 * 2;
  Row9 xp;
#pragma unroll
  for (int i = 0; i < 9; ++i) xp.u[i] = 0u;
#pragma nounroll
  for (int job = rw < 12 ? -1 : 0; job < 30; ++job) {
    const bool isA = job <= 0 || (job & 1);
    const int k = job <= 0 ? 0 : (isA ? (job + 1) >> 1 : (job >> 1) - 1);
    const int s = s0 + 5 * k;
    if (s >= (isA ? NSEG : NSEG + 5)) continue;
    const unsigned to = CTL_CH * CH + 64u + 16u * (unsigned)s + 4u;
    const int tc = (132);
    const unsigned so = __builtin_amdgcn_readfirstlane((unsigned)s * SEGB + (unsigned)rg * RGB);
    char* sv = smem + LR_SV + (k % 3) * 2 * RGB;
    const size_t go = (size_t)(s * SEG + rg * 8 + r8) * DM;
    Row9 xa, ya;
    v4u v[2];
    unsigned e;
    if (isA) {
      const v4u x0 = *(const v4u*)(p.x0 + go + (tid >> 3) * 8), x1 = *(const v4u*)(p.x0 + go + 256 + (tid >> 3) * 8);
      xa.u[8] = *(const unsigned*)(p.x0 + go + 512 + 8 * pj + 2 * w);
#pragma unroll
      for (int i = 0; i < 4; ++i) { xa.u[i] = x0[i]; xa.u[4 + i] = x1[i]; }
      if (job > 0) arm_slots(p, s, rg);
      if (job == 0) {
        unsigned spins = 0;
#pragma nounroll
        while (!__all(ld4(F, (unsigned)(CT_YC + (lane & 7) * 32) * 4u, 0) >= 72u)) { if (spin_fail(spins, F)) break; __builtin_amdgcn_s_sleep(4); asm volatile("" ::: "memory"); }
      }
      if (job >= 0) wait9<YSLP>(Y, F, (unsigned)(s * SEG + rg * 8) * 2u, (unsigned)tid * YS, (unsigned)(tid + 256) * YS, (unsigned)(512 + (tid >> 2)) * YS + (unsigned)(tid & 3) * 4u, v, e);
      else { v[0] = x0; v[1] = x1; e = xa.u[8]; }
      VM0();
      *(v4u*)(smem + LR_Y + o0) = v[0]; *(v4u*)(smem + LR_Y + o0 + 4096) = v[1]; *(unsigned*)(smem + LR_Y + 8192 + tid * 4) = e;
      BAR();
#pragma unroll
      for (int i = 0; i < 2; ++i) {
        const v2u x = trd(yR + 4096 * i), y = trd(yR + 4096 * i + 64);
        ya.u[4 * i] = x.x; ya.u[4 * i + 1] = x.y; ya.u[4 * i + 2] = y.x; ya.u[4 * i + 3] = y.y;
      }
      ya.u[8] = (unsigned)*(const u16*)yE | ((unsigned)*(const u16*)(yE + 16) << 16);
    } else if (s < NSEG) {
#pragma unroll
      for (int i = 0; i < 2; ++i) {
        const v4u a = *(const v4u*)(sv + o0 + 4096 * i), b = *(const v4u*)(sv + RGB + o0 + 4096 * i);
#pragma unroll
        for (int q = 0; q < 4; ++q) { xa.u[4 * i + q] = a[q]; ya.u[4 * i + q] = b[q]; }
      }
      xa.u[8] = *(const unsigned*)(sv + 8192 + tid * 4); ya.u[8] = *(const unsigned*)(sv + RGB + 8192 + tid * 4);
      wait_time(X, flip * 8192u + to, tc);
      wait9<RSLP>(O, F, so, o0, o0 + 4096u, oe, v, e);
      add_rows(xa, v, e);
    }
    if (s < NSEG) mix_rows(xa, ya, H, job < 0 ? 0x40000000u : (isA ? so : NSEG * SEGB + so), o0, oe, st, lane, w);
    if (isA) {
#pragma unroll
      for (int i = 0; i < 2; ++i) {
        *(v4u*)(sv + o0 + 4096 * i) = (v4u){xa.u[4 * i], xa.u[4 * i + 1], xa.u[4 * i + 2], xa.u[4 * i + 3]};
        *(v4u*)(sv + RGB + o0 + 4096 * i) = (v4u){ya.u[4 * i], ya.u[4 * i + 1], ya.u[4 * i + 2], ya.u[4 * i + 3]};
      }
      *(unsigned*)(sv + 8192 + tid * 4) = xa.u[8]; *(unsigned*)(sv + RGB + 8192 + tid * 4) = ya.u[8];
    } else {
      if (k > 0) {
        wait_time(X, (flip ^ 1u) * 8192u + to - 80u, tc);
        wait9<RSLP>(O, F, (NSEG - 5) * SEGB + so, o0, o0 + 4096u, oe, v, e);
        add_rows(xp, v, e);
        u16* po = p.out + go - 5 * SEG * DM;
        *(v4u*)(po + (tid >> 3) * 8) = (v4u){xp.u[0], xp.u[1], xp.u[2], xp.u[3]};
        *(v4u*)(po + 256 + (tid >> 3) * 8) = (v4u){xp.u[4], xp.u[5], xp.u[6], xp.u[7]};
        *(unsigned*)(po + 512 + 8 * pj + 2 * w) = xp.u[8];
      }
      xp = xa;
    }
  }
}

__device__ __forceinline__ unsigned rot16(unsigned u) { return __builtin_amdgcn_alignbit(u, u, 16); }
__device__ __forceinline__ void crev_put(u16* dst, int stride, const v4u& c0, const v4u& c1) {
  const unsigned e[8] = {rot16(c0.w), rot16(c0.z), rot16(c0.y), rot16(c0.x), rot16(c1.w), rot16(c1.z), rot16(c1.y), rot16(c1.x)};
  unsigned u[7];
#pragma unroll
  for (int k = 0; k < 7; ++k) u[k] = __builtin_amdgcn_alignbit(e[k + 1], e[k], 16);
#pragma unroll
  for (int a = 0; a < 4; ++a) {
    *(v4u*)(dst + (2 * a) * stride) = (v4u){e[a], e[a + 1], e[a + 2], e[a + 3]};
    *(v4u*)(dst + (2 * a + 1) * stride) = (v4u){u[a], u[a + 1], u[a + 2], u[a + 3]};
  }
}
__device__ __forceinline__ unsigned y2pair(float y0, float y1, unsigned kv, float Dv) {
  const unsigned yy = pk2(y0, y1), kd = pk2(lo2f(kv) * Dv, hi2f(kv) * Dv);
  return nosent(pk2(lo2f(yy) + lo2f(kd), hi2f(yy) + hi2f(kd)));
}
#define CL_STRIDE 1056
#define LC_WAVE 27136
__device__ __forceinline__ void conv_pass(const u16* aB, const u16* bB, f32x16& acc) {
  v4u A[8], B[8];
#pragma unroll
  for (int q = 0; q < 8; ++q) { A[q] = *(const v4u*)(aB - 32 * (q >> 1) + 16 * (q & 1)); B[q] = *(const v4u*)(bB - 40 * (q >> 1) + 16 * (q & 1)); }
  asm volatile("s_nop 1" : "+v"(acc));
#pragma nounroll
  for (int m4 = 1; m4 <= 8; ++m4) {
    const u16* an = aB - 128 * (m4 & 7);
    const u16* bn = bB - 160 * (m4 & 7);
    PIN();
#pragma unroll
    for (int q = 0; q < 8; ++q) {
      asm volatile("v_mfma_f32_32x32x16_bf16 %0, %1, %2, %0" : "+v"(acc) : "v"(A[q]), "v"(B[q]));
      A[q] = *(const v4u*)(an - 32 * (q >> 1) + 16 * (q & 1)); B[q] = *(const v4u*)(bn - 40 * (q >> 1) + 16 * (q & 1));
      PIN();
    }
  }
  asm volatile("s_nop 7\n\ts_nop 7" : "+v"(acc) :: "memory");
}
template <typename DF>
__device__ __forceinline__ void conv(char* sm, const v4u (&cw)[2][3], const v4u (&kw)[2][2], float Dv, const rsrc_t Y, int d, int lane, DF&& done) {
  u16* crev = (u16*)sm;
  u16* kvs = (u16*)(sm + 16 * CL_STRIDE);
  const v4u z = {0u, 0u, 0u, 0u};
  crev_put(crev + (127 - 2 * lane) * 8, CL_STRIDE, cw[0][1], lane ? cw[0][0] : z);
  crev_put(crev + (126 - 2 * lane) * 8, CL_STRIDE, cw[0][2], cw[0][1]);
  if (lane < 32) *(v4u*)(crev + (lane >> 2) * CL_STRIDE + (128 + (lane & 3)) * 8) = z;
  u16* kp = kvs + (lane >> 1) * 40 + (lane & 1) * 16;
  *(v4u*)kp = z; *(v4u*)(kp + 8) = z;
  *(v4u*)(kp + 1280) = kw[0][0]; *(v4u*)(kp + 1288) = kw[0][1];
  asm volatile("" ::: "memory");
  const int i = lane & 31, h = lane >> 5;
  const int rho = (1023 - i) & 7;
  const u16* aB = crev + rho * CL_STRIDE + (1023 - (i - 8 * h) - rho);
  const u16* bB = kvs + (32 + i) * 40 + 8 * h;
#pragma nounroll
  for (int u = 0; u < 2; ++u) {
    f32x16 acc;
#pragma unroll
    for (int r = 0; r < 16; ++r) acc[r] = 0.f;
    if (u) {
      *(v4u*)(kp + 2560) = kw[1][0]; *(v4u*)(kp + 2568) = kw[1][1];
      asm volatile("" ::: "memory");
      conv_pass(aB, bB + 1280, acc);
      crev_put(crev + (127 - 2 * lane) * 8, CL_STRIDE, cw[1][1], cw[1][0]);
      crev_put(crev + (126 - 2 * lane) * 8, CL_STRIDE, cw[1][2], cw[1][1]);
      if (lane >= 62) {
        crev_put(crev + (255 - 2 * lane) * 8, CL_STRIDE, cw[0][1], cw[0][0]);
        crev_put(crev + (254 - 2 * lane) * 8, CL_STRIDE, cw[0][2], cw[0][1]);
      }
      asm volatile("" ::: "memory");
    }
    conv_pass(aB, bB, acc);
#pragma unroll
    for (int r = 0; r < 4; ++r) {
      const v2u kv4 = *(const v2u*)&kvs[(32 + 32 * u + i) * 40 + 8 * r + 4 * h];
      *(v2u*)&kvs[(96 + i) * 40 + 8 * r + 4 * h] = (v2u){y2pair(acc[4 * r], acc[4 * r + 1], kv4.x, Dv), y2pair(acc[4 * r + 2], acc[4 * r + 3], kv4.y, Dv)};
    }
    asm volatile("" ::: "memory");
#pragma unroll
    for (int k = 0; k < 2; ++k) st16(Y, (unsigned)(k * 64 + lane) * 16u, (unsigned)d * YS + 2048u * u, *(const v4u*)&kvs[(96 + 16 * k + (lane >> 2)) * 40 + 8 * (lane & 3)]);
    asm volatile("" ::: "memory");
    if (!u) done();
  }
}

#define NQE 8
#define NVE 8
#define NOE 10
__global__ __launch_bounds__(256, 1) void k_main(MP p) {
  extern __shared__ __attribute__((aligned(16))) char smem[];
  const int tid = threadIdx.x;
  unsigned cpf = 0;
  {
    unsigned long long pc; unsigned len;
    asm volatile("s_getpc_b64 %0\n.Lcpf:\n\ts_mov_b32 %1, _Z5k_pre4PreP-.Lcpf" : "=s"(pc), "=s"(len));
    cpf = __builtin_amdgcn_raw_buffer_load_b32(mkr((const void*)pc, (int)len > 0 ? len & ~3u : 0u), (unsigned)(tid + 256 * ((blockIdx.x >> 3) & 3)) * 128u, 0, 0);
  }
  unsigned xcc; asm volatile("s_getreg_b32 %0, hwreg(HW_REG_XCC_ID)" : "=s"(xcc));
  xcc &= 7u;
  int* rl = (int*)(smem + L_ROLE);
  if (tid == 0) {
    unsigned b0 = ~0u, b1 = ~0u;
#pragma unroll
    for (int k = 0; k < 4; ++k) { const v4u c = *(const v4u*)&p.ct[CT_FLIP + 4 * k]; b0 = min(b0, min(c.x, c.z)); b1 = min(b1, min(c.y, c.w)); }
    int r = 0;
#pragma nounroll
    for (unsigned k = 0; k < 8; ++k) {
      const unsigned x = xcc ^ k;
      const unsigned tk = __hip_atomic_fetch_add(&p.ct[CT_TICK + x * 32], 1u, __ATOMIC_RELAXED, __HIP_MEMORY_SCOPE_AGENT);
      if (tk < 32u) { r = (int)(x * 32u + tk); break; }
    }
    rl[0] = r; rl[1] = b1 < b0;
  }
  asm volatile("" :: "v"(cpf));
  BAR();
  int role = __builtin_amdgcn_readfirstlane(rl[0]);
  {
    const int o = (role >> 5) & 1, a = (role >> 6) & 1, tk = role & 31;
    const int c1 = o ? 16 - NQE : NQE, c2 = c1 + (o ? 16 - NVE : NVE), c3 = c2 + (o ? 18 - NOE : NOE), c4 = c3 + (o ? 9 - NOE / 2 : NOE / 2);
    const int r = tk < c1 ? 16 * a + o * NQE + tk : tk < c2 ? 32 + 16 * a + o * NVE + tk - c1 : tk < c3 ? 64 + 18 * a + o * NOE + tk - c2
                : tk < c4 ? 100 + 9 * a + o * (NOE / 2) + tk - c3 : 118 + 5 * a + o * (32 - NQE - NVE - NOE - NOE / 2) + tk - c4;
    role = (role & 128) + r;
  }
  const unsigned flip = (unsigned)__builtin_amdgcn_readfirstlane(rl[1]);
  const int i = role & 127, grp = role >> 7;
  const unsigned gb = (((unsigned)grp) ^ flip) * 8192u;
  const int rep = (int)(xcc & 3u);
  const int rw = (grp ^ 1) * 10 + (i - 118);
  if (grp || i >= 100) {
    const int lane = tid & 63, w = __builtin_amdgcn_readfirstlane(tid >> 6);
    const int d1 = (i < 118 ? (grp ? i : i + 18) : rw < 12 ? 200 : 124 + rw) * 4 + w;
    if (i >= 118) arm_slots(p, rw >> 2, rw & 3);
    if (d1 < DM) {
      const unsigned dv = (unsigned)p.ssm_D[d1] << 16;
      v4u cw[2][3], kw[2][2];
#pragma unroll
      for (int u = 0; u < 2; ++u) {
        const size_t o = (size_t)d1 * S_LEN + 1024 * u + lane * 16;
        cw[u][0] = *(const v4u*)&p.ssm_k[u || lane ? o - 8 : o]; cw[u][1] = *(const v4u*)&p.ssm_k[o]; cw[u][2] = *(const v4u*)&p.ssm_k[o + 8];
        kw[u][0] = *(const v4u*)&p.kvT[o]; kw[u][1] = *(const v4u*)&p.kvT[o + 8];
      }
      conv(smem + w * LC_WAVE, cw, kw, __uint_as_float(dv), mkr(p.Y, DM * YS), d1, lane, [&]() {
        if (lane == 0) __hip_atomic_fetch_add(&p.ct[CT_YC + (d1 & 7) * 32], 1u, __ATOMIC_RELAXED, __HIP_MEMORY_SCOPE_AGENT);
      });
    }
    BAR();
  }
  if (i < 32) qk_wg(p, role, grp, i, rep, gb);
  else if (i < 64) vt_wg(p, role, grp, i - 32, rep, gb);
  else if (i < 100) o_wg(p, role, grp, i - 64, rep, gb);
  else if (i < 118) u_wg(p, role, grp, i - 100, gb);
  else row_wg(p, smem, role, rw, flip);
}

#define PRE_KV 144
#define PRE_FILL 200
#define NCAL 4
#define FAUX 16
#define STW(base, bytes, idx16, v) __builtin_amdgcn_raw_buffer_store_b128(v, mkr(base, bytes), (unsigned)(idx16) * 16u, 0, FAUX)
struct PreP {
  const u16* kk; const u16* vv; const u16* shift_k; u16* kvT;
  const u16* mem0; v4u* win; v4u* Y; v4u* H; unsigned* ct;
};
__global__ __launch_bounds__(256) void k_pre(PreP p) {
  __shared__ __attribute__((aligned(16))) u16 sh[64 * 136];
  const int tid = threadIdx.x;
  int bid = blockIdx.x;
  if (bid < 32) {
    unsigned long long pc; int len, back;
    asm volatile("s_getpc_b64 %0\n.Lcpf0:\n\ts_mov_b32 %1, code_end-.Lcpf0\n\ts_mov_b32 %2, .Lcpf0-_Z6k_main2MP" : "=s"(pc), "=s"(len), "=s"(back));
    const bool ok = len > 0 && back > 0;
    const unsigned v = __builtin_amdgcn_raw_buffer_load_b32(mkr((const void*)pc, ok ? (unsigned)len & ~3u : 0u), (unsigned)tid * 128u, 0, 0);
    const unsigned u = __builtin_amdgcn_raw_buffer_load_b32(mkr((const void*)(pc - (unsigned)back), ok ? (unsigned)back & ~3u : 0u), (unsigned)(tid + 256 * (bid >> 3)) * 128u, 0, 0);
    asm volatile("" :: "v"(v), "v"(u));
  }
  if (bid < 8) {
    if (bid == 0) for (int i = tid; i < CT_TOTAL; i += 256) if (i < CT_FLIP || i >= CT_YC) p.ct[i] = 0u;
    const rsrc_t X = mkr(p.win, WIN_BYTES);
    unsigned xcc; asm volatile("s_getreg_b32 %0, hwreg(HW_REG_XCC_ID)" : "=s"(xcc));
    int* best = (int*)sh;
    int b0 = 1 << 30, b1 = 1 << 30;
    unsigned acc = 0;
    const unsigned ob = (unsigned)(CAL_CH - bid) * CH + (unsigned)(tid >> 6) * 2048u;
    st16(X, ob + (unsigned)((tid >> 5 & 1) * 8192 + ((tid & 31) >> 3) * 256 + (tid & 7) * 16), 0, (v4u){0u, 0u, 0u, 0u});
    asm volatile("s_waitcnt vmcnt(0)" ::: "memory");
#pragma nounroll
    for (int i = 0; i < NCAL; ++i) {
      const int par = (i ^ (tid >> 6)) & 1;
      const unsigned o = ob + (unsigned)par * 8192u + (unsigned)(i >> 1) * 256u;
      long long t0 = __builtin_amdgcn_s_memtime();
      unsigned v = ld4(X, o, 0);
      asm volatile("s_waitcnt vmcnt(0)" : "+v"(v) :: "memory");
      long long t1 = __builtin_amdgcn_s_memtime();
      acc += v;
      const int d = (int)(t1 - t0);
      if (par ^ (int)((xcc >> 2) & 1u)) b1 = d < b1 ? d : b1; else b0 = d < b0 ? d : b0;
    }
    if ((tid & 63) == 0) { best[(tid >> 6) * 2] = b0; best[(tid >> 6) * 2 + 1] = b1; }
    __syncthreads();
    if (tid < 2) p.ct[CT_FLIP + 2 * bid + tid] = (unsigned)min(min(best[tid], best[2 + tid]), min(best[4 + tid], best[6 + tid])) + acc;
    return;
  }
  bid -= 8;
  if (bid < PRE_KV) {
    const int d0 = (bid % 9) * 64, s0 = (bid / 9) * 128;
    const int dq = tid & 7, sr = tid >> 3;
    const int d = d0 + dq * 8, s = s0 + 4 * sr;
    v4u kr[7], vr[4], sw[4];
#pragma unroll
    for (int a = 0; a < 7; ++a) kr[a] = (s - 3 + a >= 0) ? *(const v4u*)&p.kk[(long)(s - 3 + a) * DM + d] : (v4u){0, 0, 0, 0};
#pragma unroll
    for (int a = 0; a < 4; ++a) { vr[a] = *(const v4u*)&p.vv[(long)(s + a) * DM + d]; sw[a] = *(const v4u*)&p.shift_k[d * 4 + 8 * a]; }
#pragma unroll
    for (int jp = 0; jp < 4; ++jp) {
      const float w0[4] = {lo2f(sw[jp].x), hi2f(sw[jp].x), lo2f(sw[jp].y), hi2f(sw[jp].y)}, w1[4] = {lo2f(sw[jp].z), hi2f(sw[jp].z), lo2f(sw[jp].w), hi2f(sw[jp].w)};
      unsigned o[4];
#pragma unroll
      for (int r = 0; r < 4; ++r) {
        unsigned acc = pk2(lo2f(kr[r][jp]) * w0[3], hi2f(kr[r][jp]) * w1[3]);
#pragma unroll
        for (int a = 1; a < 4; ++a) {
          const unsigned m = pk2(lo2f(kr[r + a][jp]) * w0[3 - a], hi2f(kr[r + a][jp]) * w1[3 - a]);
          acc = pk2(lo2f(acc) + lo2f(m), hi2f(acc) + hi2f(m));
        }
        o[r] = pk2(lo2f(acc) * lo2f(vr[r][jp]), hi2f(acc) * hi2f(vr[r][jp]));
      }
      *(v2u*)&sh[(dq * 8 + 2 * jp) * 136 + 4 * sr] = (v2u){(o[0] & 0xFFFFu) | (o[1] << 16), (o[2] & 0xFFFFu) | (o[3] << 16)};
      *(v2u*)&sh[(dq * 8 + 2 * jp + 1) * 136 + 4 * sr] = (v2u){(o[0] >> 16) | (o[1] & 0xFFFF0000u), (o[2] >> 16) | (o[3] & 0xFFFF0000u)};
    }
    __syncthreads();
#pragma unroll
    for (int i = 0; i < 4; ++i) {
      const int c = tid + 256 * i, dd = c >> 4, q = c & 15;
      STW(p.kvT, DM * S_LEN * 2u, (d0 + dd) * (S_LEN / 8) + s0 / 8 + q, *(const v4u*)&sh[dd * 136 + 8 * q]);
    }
    return;
  }
  bid -= PRE_KV;
  {
    const v4u ones = {0xFFFFFFFFu, 0xFFFFFFFFu, 0xFFFFFFFFu, 0xFFFFFFFFu};
    for (unsigned i = bid * 256 + tid; i < (VM_CH + 108u) * CH / 16u; i += PRE_FILL * 256) {
      if (i >= (CTL_CH + 1u) * CH / 16u && i < PT_CH * CH / 16u) continue;
      v4u v = i / (CH / 16u) == CTL_CH ? (v4u){0u, 0u, 0u, 0u} : ones;
      const unsigned c = ((i >> 10) & 1u) * 512u + (i & 511u);
      if (i < 2u * NREP * CH / 16u && c < 576u) {
        const unsigned* s = (const unsigned*)&p.mem0[(2 * ((c >> 2) & 3) + (c & 1)) * DM + (c >> 4) * 16 + (c & 2)];
        v = (v4u){nosent(s[0]), nosent(s[2]), nosent(s[4]), nosent(s[6])};
      }
      STW(p.win, WIN_BYTES, i, v);
    }
    for (unsigned i = bid * 256 + tid; i < DM * 256; i += PRE_FILL * 256) STW(p.Y, DM * YS, (i >> 8) * (YS / 16) + (i & 255), ones);
    for (unsigned i = bid * 256 + tid; i < 5 * SEGB / 16; i += PRE_FILL * 256) STW(p.H, 2u * NSEG * SEGB, i, ones);
    if (bid * 256 + tid < 2 * SEGB / 16) STW(p.H, 2u * NSEG * SEGB, NSEG * SEGB / 16 + bid * 256 + tid, ones);
  }
}

extern "C" __device__ __attribute__((used, noinline)) void code_end() {}

extern "C" void kernel_launch(void* const* d_in, const int* in_sizes, int n_in, void* d_out, int out_size, void* d_ws,
                              size_t ws_size, hipStream_t stream) {
  const u16* A_q = (const u16*)d_in[0];
  const u16* A_k = (const u16*)d_in[1];
  const u16* A_v = (const u16*)d_in[2];
  const u16* A_shift = (const u16*)d_in[3];
  const u16* A_ssmk = (const u16*)d_in[4];
  const u16* A_ssmD = (const u16*)d_in[5];
  const u16* B_mem0 = (const u16*)d_in[7];
  const u16* B_Wq = (const u16*)d_in[8];
  const u16* B_Wk = (const u16*)d_in[9];
  const u16* B_Wv = (const u16*)d_in[10];
  const u16* B_Wo = (const u16*)d_in[11];

  char* ws = (char*)(((uintptr_t)d_ws + ((2u << 20) - 1)) & ~(uintptr_t)((2u << 20) - 1));
  size_t off = 0;
  auto alloc = [&](size_t bytes) { char* q = ws + off; off += (bytes + 4095) & ~(size_t)4095; return q; };
  unsigned* win = (unsigned*)alloc(WIN_BYTES);
  unsigned* ct = (unsigned*)alloc(CT_TOTAL * 4);
  u16* Y = (u16*)alloc((size_t)DM * YS);
  u16* H = (u16*)alloc((size_t)2 * NSEG * SEGB);
  u16* OT = (u16*)alloc((size_t)2 * NSEG * SEGB);
  u16* kvT = (u16*)alloc((size_t)DM * S_LEN * 2);

  PreP pp;
  pp.kk = A_k; pp.vv = A_v; pp.shift_k = A_shift; pp.kvT = kvT;
  pp.mem0 = B_mem0; pp.win = (v4u*)win; pp.Y = (v4u*)Y; pp.H = (v4u*)H; pp.ct = ct;
  k_pre<<<8 + PRE_KV + PRE_FILL, 256, 0, stream>>>(pp);
  MP mp;
  mp.Wq = B_Wq; mp.Wk = B_Wk; mp.Wv = B_Wv; mp.Wo = B_Wo; mp.x0 = A_q; mp.kvT = kvT; mp.ssm_k = A_ssmk; mp.ssm_D = A_ssmD; mp.mem0 = B_mem0;
  mp.out = (u16*)d_out; mp.win = win; mp.ct = ct; mp.Y = Y; mp.H = H; mp.OT = OT;
  const int smem_bytes = L_TOTAL;
  (void)hipFuncSetAttribute((const void*)k_main, hipFuncAttributeMaxDynamicSharedMemorySize, smem_bytes);
  k_main<<<256, 256, smem_bytes, stream>>>(mp);
}
```

```cpp
#include <hip/hip_runtime.h>
#include <hip/hip_bf16.h>
#include <stdint.h>

typedef unsigned short u16;
using bf16x8 = __attribute__((ext_vector_type(8))) short;
using s16x4 = __attribute__((ext_vector_type(4))) short;
using f32x4 = __attribute__((ext_vector_type(4))) float;
using f32x2 = __attribute__((ext_vector_type(2))) float;
using f32x16 = __attribute__((ext_vector_type(16))) float;
using bfx2 = __attribute__((ext_vector_type(2))) __bf16;
typedef unsigned v4u __attribute__((ext_vector_type(4)));
typedef unsigned v2u __attribute__((ext_vector_type(2)));

#define S_LEN 2048
#define DM 576
#define NH 32
#define DH 128
#define HD (NH * DH)
#define SEG 32
#define NSEG 64
#define RGB 9216u
#define SEGB (4u * RGB)

__device__ __forceinline__ unsigned pk2(float a, float b) { f32x2 v = {a, b}; bfx2 r = __builtin_convertvector(v, bfx2); return __builtin_bit_cast(unsigned, r); }
__device__ __forceinline__ float lo2f(unsigned u) { return __uint_as_float(u << 16); }
__device__ __forceinline__ float hi2f(unsigned u) { return __uint_as_float(u & 0xFFFF0000u); }
__device__ __forceinline__ unsigned nosent(unsigned u) { return u < 0xFFFFFFFEu ? u : 0xFFFFFFFEu; }
__device__ __forceinline__ bf16x8 asb8(v4u v) { return __builtin_bit_cast(bf16x8, v); }
#define MFMA32(a, b, c) __builtin_amdgcn_mfma_f32_16x16x32_bf16(asb8(a), asb8(b), c, 0, 0, 0)

typedef __amdgpu_buffer_rsrc_t rsrc_t;
__device__ __forceinline__ rsrc_t mkr(const void* p, unsigned bytes) { return __builtin_amdgcn_make_buffer_rsrc((void*)p, 0, bytes, 0x00020000); }
__device__ __forceinline__ void st16(rsrc_t r, unsigned vo, unsigned so, v4u v) {
  asm volatile("" : "+v"(v));
  __builtin_amdgcn_raw_buffer_store_b128(v, r, vo, so, 16);
  asm volatile("s_nop 1" :: "v"(v));
}
__device__ __forceinline__ void st4(rsrc_t r, unsigned vo, unsigned so, unsigned v) { __builtin_amdgcn_raw_buffer_store_b32(v, r, vo, so, 16); }
__device__ __forceinline__ v4u ld16(rsrc_t r, unsigned vo, unsigned so) { return __builtin_amdgcn_raw_buffer_load_b128(r, vo, so, 16); }
__device__ __forceinline__ unsigned ld4(rsrc_t r, unsigned vo, unsigned so) { return __builtin_amdgcn_raw_buffer_load_b32(r, vo, so, 16); }

#define WIN_BYTES (36u << 20)
#define CH 16384u
#define NREP 4
#define NPR 4
#define MEM_CH 0
#define PM_CH 32
#define PT_CH 128
#define VM_CH 144
#define VT_CH 256
#define BAS_CH 64
#define CTL_CH 67
#define CAL_CH 127
#define CT_TICK 0
#define CT_TMO 256
#define CT_FLIP 352
#define CT_YC 384
#define CT_TOTAL 640
#define YS 4352u
#define SENT 0xFFFFFFFFu
#define SPIN_CAP 400000u

struct MP {
  const u16* Wq; const u16* Wk; const u16* Wv; const u16* Wo; const u16* x0; const u16* kvT; const u16* ssm_k; const u16* ssm_D; const u16* mem0;
  u16* out; unsigned* win; unsigned* ct; u16* Y; u16* H; u16* OT;
};

#define BAR() do { asm volatile("s_waitcnt lgkmcnt(0)\n\ts_barrier" ::: "memory"); } while (0)
#define VM0() __builtin_amdgcn_s_waitcnt(0x0F70)
#define PIN() __builtin_amdgcn_sched_barrier(0)
__device__ __forceinline__ unsigned umax3(unsigned a, unsigned b, unsigned c) { return __builtin_elementwise_max(__builtin_elementwise_max(a, b), c); }
__device__ __forceinline__ unsigned mx4(unsigned m, v4u v) { return umax3(umax3(m, v.x, v.y), v.z, v.w); }
__device__ __forceinline__ unsigned phys(unsigned o) { return (o >> 13) * CH + (o & 8191u); }
template <int N> struct IC { static constexpr int v = N; };
template <int B, int E, typename F> __device__ __forceinline__ void sfor(F&& f) { if constexpr (B < E) { f(IC<B>{}); sfor<B + 1, E>(f); } }

__device__ __forceinline__ bool spin_fail(unsigned& spins, rsrc_t F) {
  if (__builtin_expect((++spins & 1023u) != 0u, 1)) return false;
  if (spins > SPIN_CAP) st4(F, CT_TMO * 4u, 0, 1u);
  VM0();
  return (__builtin_amdgcn_readfirstlane(ld4(F, CT_TMO * 4u, 0)) != 0) | (spins > SPIN_CAP);
}

#define RED8(name, ins) __device__ __forceinline__ float name(float a) { float d, t; asm volatile("s_nop 1\n\t" ins "_dpp %0, %2, %2 row_ror:8 row_mask:0xf bank_mask:0xf\n\t" \
    "v_mov_b32 %1, %0\n\ts_nop 1\n\tv_permlane16_swap_b32 %0, %1\n\t" ins " %0, %0, %1\n\tv_mov_b32 %1, %0\n\ts_nop 1\n\tv_permlane32_swap_b32 %0, %1\n\t" ins " %0, %0, %1" \
    : "=&v"(d), "=&v"(t) : "v"(a)); return d; }
RED8(i_red8add, "v_add_f32")

__device__ __forceinline__ void wait_time(const rsrc_t X, unsigned o, int c) {
  unsigned v = 0;
#pragma nounroll
  for (int i = 0; i < 2000; ++i) {
    asm volatile("" ::: "memory");
    v = __builtin_amdgcn_readfirstlane(ld4(X, o, 0));
    if (v) break;
    __builtin_amdgcn_s_sleep(12);
  }
  v += (unsigned)c;
#pragma nounroll
  while ((unsigned)__builtin_amdgcn_s_memrealtime() - v > 0xFFFFF000u) __builtin_amdgcn_s_sleep(1);
}
__device__ __forceinline__ v4u mkd(const void* p, unsigned bytes) { return (v4u){(unsigned)(uintptr_t)p, (unsigned)((uintptr_t)p >> 32) & 0xFFFFu, bytes, 0x00020000u}; }
typedef __attribute__((address_space(3))) char* lds_ptr;
#define DROP 0x40000000u
#define US(x) __builtin_amdgcn_readfirstlane((int)(x))

#define L_QI 36864
#define L_KT 102400
#define L_VI 36864
#define LO_MEM 0
#define LO_A 12288
#define LO_RM 16384
#define L_ROLE 137728
#define L_TOTAL 137792
__device__ __forceinline__ v2u trd(const char* a) { return __builtin_bit_cast(v2u, __builtin_amdgcn_ds_read_tr16_b64_v4i16((__attribute__((address_space(3))) s16x4*)a)); }
#define QK_ASM \
  "s_nop 4\n" \
  "v_xor_b32 v64, 0x80, %[wv]\n" \
  "v_xor_b32 v65, 32, %[wr]\n" \
  "v_add_u32 v66, 0x10000, %[wr]\n" \
  "v_add_u32 v67, 0x10000, v65\n" \
  "s_cmp_eq_u32 %[grp], 0\n" \
  "s_cbranch_scc1 .Lqk_ws1\n" \
  "s_sleep 8\n" \
  ".Lqk_ws1:\n" \
  "s_add_i32 m0, %[wl], 0\n" \
  "s_mov_b32 s76, 0\n" \
  "buffer_load_dwordx4 %[wv], %[WA], s76 offen lds\n" \
  "s_add_i32 m0, %[wl], 1024\n" \
  "s_mov_b32 s76, 32768\n" \
  "buffer_load_dwordx4 %[wv], %[WA], s76 offen lds\n" \
  "s_add_i32 m0, %[wl], 2048\n" \
  "s_mov_b32 s76, 65536\n" \
  "buffer_load_dwordx4 v64, %[WA], s76 offen lds\n" \
  "s_add_i32 m0, %[wl], 3072\n" \
  "s_mov_b32 s76, 98304\n" \
  "buffer_load_dwordx4 v64, %[WA], s76 offen lds\n" \
  "s_cmp_eq_u32 %[grp], 0\n" \
  "s_cbranch_scc1 .Lqk_ws2\n" \
  "s_sleep 8\n" \
  ".Lqk_ws2:\n" \
  "s_add_i32 m0, %[wl], 16384\n" \
  "s_mov_b32 s76, 0\n" \
  "buffer_load_dwordx4 %[wv], %[WB], s76 offen lds\n" \
  "s_add_i32 m0, %[wl], 17408\n" \
  "s_mov_b32 s76, 32768\n" \
  "buffer_load_dwordx4 %[wv], %[WB], s76 offen lds\n" \
  "s_add_i32 m0, %[wl], 18432\n" \
  "s_mov_b32 s76, 65536\n" \
  "buffer_load_dwordx4 v64, %[WB], s76 offen lds\n" \
  "s_add_i32 m0, %[wl], 19456\n" \
  "s_mov_b32 s76, 98304\n" \
  "buffer_load_dwordx4 v64, %[WB], s76 offen lds\n" \
  "s_cmp_eq_u32 %[grp], 0\n" \
  "s_cbranch_scc1 .Lqk_ws3\n" \
  "s_sleep 8\n" \
  ".Lqk_ws3:\n" \
  "s_add_i32 m0, %[wl], 32768\n" \
  "s_mov_b32 s76, 524288\n" \
  "buffer_load_dwordx4 %[wv], %[WA], s76 offen lds\n" \
  "s_add_i32 m0, %[wl], 33792\n" \
  "s_mov_b32 s76, 557056\n" \
  "buffer_load_dwordx4 %[wv], %[WA], s76 offen lds\n" \
  "s_add_i32 m0, %[wl], 34816\n" \
  "s_mov_b32 s76, 589824\n" \
  "buffer_load_dwordx4 v64, %[WA], s76 offen lds\n" \
  "s_add_i32 m0, %[wl], 35840\n" \
  "s_mov_b32 s76, 622592\n" \
  "buffer_load_dwordx4 v64, %[WA], s76 offen lds\n" \
  "s_cmp_eq_u32 %[grp], 0\n" \
  "s_cbranch_scc1 .Lqk_ws4\n" \
  "s_sleep 8\n" \
  ".Lqk_ws4:\n" \
  "s_add_i32 m0, %[wl], 49152\n" \
  "s_mov_b32 s76, 524288\n" \
  "buffer_load_dwordx4 %[wv], %[WB], s76 offen lds\n" \
  "s_add_i32 m0, %[wl], 50176\n" \
  "s_mov_b32 s76, 557056\n" \
  "buffer_load_dwordx4 %[wv], %[WB], s76 offen lds\n" \
  "s_add_i32 m0, %[wl], 51200\n" \
  "s_mov_b32 s76, 589824\n" \
  "buffer_load_dwordx4 v64, %[WB], s76 offen lds\n" \
  "s_add_i32 m0, %[wl], 52224\n" \
  "s_mov_b32 s76, 622592\n" \
  "buffer_load_dwordx4 v64, %[WB], s76 offen lds\n" \
  "s_cmp_eq_u32 %[grp], 0\n" \
  "s_cbranch_scc1 .Lqk_ws5\n" \
  "s_sleep 8\n" \
  ".Lqk_ws5:\n" \
  "s_add_i32 m0, %[wl], 65536\n" \
  "s_mov_b32 s76, 1048576\n" \
  "buffer_load_dwordx4 %[wv], %[WA], s76 offen lds\n" \
  "s_add_i32 m0, %[wl], 66560\n" \
  "s_mov_b32 s76, 1081344\n" \
  "buffer_load_dwordx4 %[wv], %[WA], s76 offen lds\n" \
  "s_add_i32 m0, %[wl], 67584\n" \
  "s_mov_b32 s76, 1114112\n" \
  "buffer_load_dwordx4 v64, %[WA], s76 offen lds\n" \
  "s_add_i32 m0, %[wl], 68608\n" \
  "s_mov_b32 s76, 1146880\n" \
  "buffer_load_dwordx4 v64, %[WA], s76 offen lds\n" \
  "s_cmp_eq_u32 %[grp], 0\n" \
  "s_cbranch_scc1 .Lqk_ws6\n" \
  "s_sleep 8\n" \
  ".Lqk_ws6:\n" \
  "s_add_i32 m0, %[wl], 81920\n" \
  "s_mov_b32 s76, 1048576\n" \
  "buffer_load_dwordx4 %[wv], %[WB], s76 offen lds\n" \
  "s_add_i32 m0, %[wl], 82944\n" \
  "s_mov_b32 s76, 1081344\n" \
  "buffer_load_dwordx4 %[wv], %[WB], s76 offen lds\n" \
  "s_add_i32 m0, %[wl], 83968\n" \
  "s_mov_b32 s76, 1114112\n" \
  "buffer_load_dwordx4 v64, %[WB], s76 offen lds\n" \
  "s_add_i32 m0, %[wl], 84992\n" \
  "s_mov_b32 s76, 1146880\n" \
  "buffer_load_dwordx4 v64, %[WB], s76 offen lds\n" \
  "s_cmp_eq_u32 %[grp], 0\n" \
  "s_cbranch_scc1 .Lqk_ws7\n" \
  "s_sleep 8\n" \
  ".Lqk_ws7:\n" \
  "s_add_i32 m0, %[wl], 98304\n" \
  "s_mov_b32 s76, 1572864\n" \
  "buffer_load_dwordx4 %[wv], %[WA], s76 offen lds\n" \
  "s_add_i32 m0, %[wl], 99328\n" \
  "s_mov_b32 s76, 1605632\n" \
  "buffer_load_dwordx4 %[wv], %[WA], s76 offen lds\n" \
  "s_add_i32 m0, %[wl], 100352\n" \
  "s_mov_b32 s76, 1638400\n" \
  "buffer_load_dwordx4 v64, %[WA], s76 offen lds\n" \
  "s_add_i32 m0, %[wl], 101376\n" \
  "s_mov_b32 s76, 1671168\n" \
  "buffer_load_dwordx4 v64, %[WA], s76 offen lds\n" \
  "s_cmp_eq_u32 %[grp], 0\n" \
  "s_cbranch_scc1 .Lqk_ws8\n" \
  "s_sleep 8\n" \
  ".Lqk_ws8:\n" \
  "s_add_i32 m0, %[wl], 114688\n" \
  "s_mov_b32 s76, 1572864\n" \
  "buffer_load_dwordx4 %[wv], %[WB], s76 offen lds\n" \
  "s_add_i32 m0, %[wl], 115712\n" \
  "s_mov_b32 s76, 1605632\n" \
  "buffer_load_dwordx4 %[wv], %[WB], s76 offen lds\n" \
  "s_add_i32 m0, %[wl], 116736\n" \
  "s_mov_b32 s76, 1638400\n" \
  "buffer_load_dwordx4 v64, %[WB], s76 offen lds\n" \
  "s_add_i32 m0, %[wl], 117760\n" \
  "s_mov_b32 s76, 1671168\n" \
  "buffer_load_dwordx4 v64, %[WB], s76 offen lds\n" \
  "s_waitcnt vmcnt(28)\n" \
  "s_waitcnt lgkmcnt(0)\n" \
  "s_barrier\n" \
  "ds_read_b64_tr_b16 a[0:1], %[wr] offset:0\n" \
  "ds_read_b64_tr_b16 a[2:3], %[wr] offset:1024\n" \
  "ds_read_b64_tr_b16 a[4:5], %[wr] offset:8192\n" \
  "ds_read_b64_tr_b16 a[6:7], %[wr] offset:9216\n" \
  "ds_read_b64_tr_b16 a[8:9], v65 offset:0\n" \
  "ds_read_b64_tr_b16 a[10:11], v65 offset:1024\n" \
  "ds_read_b64_tr_b16 a[12:13], v65 offset:8192\n" \
  "ds_read_b64_tr_b16 a[14:15], v65 offset:9216\n" \
  "s_waitcnt vmcnt(24)\n" \
  "s_waitcnt lgkmcnt(0)\n" \
  "s_barrier\n" \
  "s_cmp_eq_u32 %[grp], 0\n" \
  "s_cbranch_scc1 .Lqk_ws9\n" \
  "s_sleep 8\n" \
  ".Lqk_ws9:\n" \
  "s_add_i32 m0, %[wl], 0\n" \
  "s_mov_b32 s76, 2097152\n" \
  "buffer_load_dwordx4 %[wv], %[WA], s76 offen lds\n" \
  "s_add_i32 m0, %[wl], 1024\n" \
  "s_mov_b32 s76, 2129920\n" \
  "buffer_load_dwordx4 %[wv], %[WA], s76 offen lds\n" \
  "s_add_i32 m0, %[wl], 2048\n" \
  "s_mov_b32 s76, 2162688\n" \
  "buffer_load_dwordx4 v64, %[WA], s76 offen lds\n" \
  "s_add_i32 m0, %[wl], 3072\n" \
  "s_mov_b32 s76, 2195456\n" \
  "buffer_load_dwordx4 v64, %[WA], s76 offen lds\n" \
  "ds_read_b64_tr_b16 a[16:17], %[wr] offset:16384\n" \
  "ds_read_b64_tr_b16 a[18:19], %[wr] offset:17408\n" \
  "ds_read_b64_tr_b16 a[20:21], %[wr] offset:24576\n" \
  "ds_read_b64_tr_b16 a[22:23], %[wr] offset:25600\n" \
  "ds_read_b64_tr_b16 a[24:25], v65 offset:16384\n" \
  "ds_read_b64_tr_b16 a[26:27], v65 offset:17408\n" \
  "ds_read_b64_tr_b16 a[28:29], v65 offset:24576\n" \
  "ds_read_b64_tr_b16 a[30:31], v65 offset:25600\n" \
  "s_waitcnt vmcnt(24)\n" \
  "s_waitcnt lgkmcnt(0)\n" \
  "s_barrier\n" \
  "s_cmp_eq_u32 %[grp], 0\n" \
  "s_cbranch_scc1 .Lqk_ws10\n" \
  "s_sleep 8\n" \
  ".Lqk_ws10:\n" \
  "s_add_i32 m0, %[wl], 16384\n" \
  "s_mov_b32 s76, 2097152\n" \
  "buffer_load_dwordx4 %[wv], %[WB], s76 offen lds\n" \
  "s_add_i32 m0, %[wl], 17408\n" \
  "s_mov_b32 s76, 2129920\n" \
  "buffer_load_dwordx4 %[wv], %[WB], s76 offen lds\n" \
  "s_add_i32 m0, %[wl], 18432\n" \
  "s_mov_b32 s76, 2162688\n" \
  "buffer_load_dwordx4 v64, %[WB], s76 offen lds\n" \
  "s_add_i32 m0, %[wl], 19456\n" \
  "s_mov_b32 s76, 2195456\n" \
  "buffer_load_dwordx4 v64, %[WB], s76 offen lds\n" \
  "ds_read_b64_tr_b16 a[32:33], %[wr] offset:32768\n" \
  "ds_read_b64_tr_b16 a[34:35], %[wr] offset:33792\n" \
  "ds_read_b64_tr_b16 a[36:37], %[wr] offset:40960\n" \
  "ds_read_b64_tr_b16 a[38:39], %[wr] offset:41984\n" \
  "ds_read_b64_tr_b16 a[40:41], v65 offset:32768\n" \
  "ds_read_b64_tr_b16 a[42:43], v65 offset:33792\n" \
  "ds_read_b64_tr_b16 a[44:45], v65 offset:40960\n" \
  "ds_read_b64_tr_b16 a[46:47], v65 offset:41984\n" \
  "s_waitcnt vmcnt(24)\n" \
  "s_waitcnt lgkmcnt(0)\n" \
  "s_barrier\n" \
  "s_cmp_eq_u32 %[grp], 0\n" \
  "s_cbranch_scc1 .Lqk_ws11\n" \
  "s_sleep 8\n" \
  ".Lqk_ws11:\n" \
  "s_add_i32 m0, %[wl], 32768\n" \
  "s_mov_b32 s76, 2621440\n" \
  "buffer_load_dwordx4 %[wv], %[WA], s76 offen lds\n" \
  "s_add_i32 m0, %[wl], 33792\n" \
  "s_mov_b32 s76, 2654208\n" \
  "buffer_load_dwordx4 %[wv], %[WA], s76 offen lds\n" \
  "s_add_i32 m0, %[wl], 34816\n" \
  "s_mov_b32 s76, 2686976\n" \
  "buffer_load_dwordx4 v64, %[WA], s76 offen lds\n" \
  "s_add_i32 m0, %[wl], 35840\n" \
  "s_mov_b32 s76, 2719744\n" \
  "buffer_load_dwordx4 v64, %[WA], s76 offen lds\n" \
  "ds_read_b64_tr_b16 a[48:49], %[wr] offset:49152\n" \
  "ds_read_b64_tr_b16 a[50:51], %[wr] offset:50176\n" \
  "ds_read_b64_tr_b16 a[52:53], %[wr] offset:57344\n" \
  "ds_read_b64_tr_b16 a[54:55], %[wr] offset:58368\n" \
  "ds_read_b64_tr_b16 a[56:57], v65 offset:49152\n" \
  "ds_read_b64_tr_b16 a[58:59], v65 offset:50176\n" \
  "ds_read_b64_tr_b16 a[60:61], v65 offset:57344\n" \
  "ds_read_b64_tr_b16 a[62:63], v65 offset:58368\n" \
  "s_waitcnt vmcnt(24)\n" \
  "s_waitcnt lgkmcnt(0)\n" \
  "s_barrier\n" \
  "s_cmp_eq_u32 %[grp], 0\n" \
  "s_cbranch_scc1 .Lqk_ws12\n" \
  "s_sleep 8\n" \
  ".Lqk_ws12:\n" \
  "s_add_i32 m0, %[wl], 49152\n" \
  "s_mov_b32 s76, 2621440\n" \
  "buffer_load_dwordx4 %[wv], %[WB], s76 offen lds\n" \
  "s_add_i32 m0, %[wl], 50176\n" \
  "s_mov_b32 s76, 2654208\n" \
  "buffer_load_dwordx4 %[wv], %[WB], s76 offen lds\n" \
  "s_add_i32 m0, %[wl], 51200\n" \
  "s_mov_b32 s76, 2686976\n" \
  "buffer_load_dwordx4 v64, %[WB], s76 offen lds\n" \
  "s_add_i32 m0, %[wl], 52224\n" \
  "s_mov_b32 s76, 2719744\n" \
  "buffer_load_dwordx4 v64, %[WB], s76 offen lds\n" \
  "ds_read_b64_tr_b16 a[64:65], v66 offset:0\n" \
  "ds_read_b64_tr_b16 a[66:67], v66 offset:1024\n" \
  "ds_read_b64_tr_b16 a[68:69], v66 offset:8192\n" \
  "ds_read_b64_tr_b16 a[70:71], v66 offset:9216\n" \
  "ds_read_b64_tr_b16 a[72:73], v67 offset:0\n" \
  "ds_read_b64_tr_b16 a[74:75], v67 offset:1024\n" \
  "ds_read_b64_tr_b16 a[76:77], v67 offset:8192\n" \
  "ds_read_b64_tr_b16 a[78:79], v67 offset:9216\n" \
  "s_waitcnt vmcnt(24)\n" \
  "s_waitcnt lgkmcnt(0)\n" \
  "s_barrier\n" \
  "s_cmp_eq_u32 %[grp], 0\n" \
  "s_cbranch_scc1 .Lqk_ws13\n" \
  "s_sleep 8\n" \
  ".Lqk_ws13:\n" \
  "s_add_i32 m0, %[wl], 65536\n" \
  "s_mov_b32 s76, 3145728\n" \
  "buffer_load_dwordx4 %[wv], %[WA], s76 offen lds\n" \
  "s_add_i32 m0, %[wl], 66560\n" \
  "s_mov_b32 s76, 3178496\n" \
  "buffer_load_dwordx4 %[wv], %[WA], s76 offen lds\n" \
  "s_add_i32 m0, %[wl], 67584\n" \
  "s_mov_b32 s76, 3211264\n" \
  "buffer_load_dwordx4 v64, %[WA], s76 offen lds\n" \
  "s_add_i32 m0, %[wl], 68608\n" \
  "s_mov_b32 s76, 3244032\n" \
  "buffer_load_dwordx4 v64, %[WA], s76 offen lds\n" \
  "ds_read_b64_tr_b16 a[80:81], v66 offset:16384\n" \
  "ds_read_b64_tr_b16 a[82:83], v66 offset:17408\n" \
  "ds_read_b64_tr_b16 a[84:85], v66 offset:24576\n" \
  "ds_read_b64_tr_b16 a[86:87], v66 offset:25600\n" \
  "ds_read_b64_tr_b16 a[88:89], v67 offset:16384\n" \
  "ds_read_b64_tr_b16 a[90:91], v67 offset:17408\n" \
  "ds_read_b64_tr_b16 a[92:93], v67 offset:24576\n" \
  "ds_read_b64_tr_b16 a[94:95], v67 offset:25600\n" \
  "s_waitcnt vmcnt(24)\n" \
  "s_waitcnt lgkmcnt(0)\n" \
  "s_barrier\n" \
  "s_cmp_eq_u32 %[grp], 0\n" \
  "s_cbranch_scc1 .Lqk_ws14\n" \
  "s_sleep 8\n" \
  ".Lqk_ws14:\n" \
  "s_add_i32 m0, %[wl], 81920\n" \
  "s_mov_b32 s76, 3145728\n" \
  "buffer_load_dwordx4 %[wv], %[WB], s76 offen lds\n" \
  "s_add_i32 m0, %[wl], 82944\n" \
  "s_mov_b32 s76, 3178496\n" \
  "buffer_load_dwordx4 %[wv], %[WB], s76 offen lds\n" \
  "s_add_i32 m0, %[wl], 83968\n" \
  "s_mov_b32 s76, 3211264\n" \
  "buffer_load_dwordx4 v64, %[WB], s76 offen lds\n" \
  "s_add_i32 m0, %[wl], 84992\n" \
  "s_mov_b32 s76, 3244032\n" \
  "buffer_load_dwordx4 v64, %[WB], s76 offen lds\n" \
  "ds_read_b64_tr_b16 a[96:97], v66 offset:32768\n" \
  "ds_read_b64_tr_b16 a[98:99], v66 offset:33792\n" \
  "ds_read_b64_tr_b16 a[100:101], v66 offset:40960\n" \
  "ds_read_b64_tr_b16 a[102:103], v66 offset:41984\n" \
  "ds_read_b64_tr_b16 a[104:105], v67 offset:32768\n" \
  "ds_read_b64_tr_b16 a[106:107], v67 offset:33792\n" \
  "ds_read_b64_tr_b16 a[108:109], v67 offset:40960\n" \
  "ds_read_b64_tr_b16 a[110:111], v67 offset:41984\n" \
  "s_waitcnt vmcnt(24)\n" \
  "s_waitcnt lgkmcnt(0)\n" \
  "s_barrier\n" \
  "s_cmp_eq_u32 %[grp], 0\n" \
  "s_cbranch_scc1 .Lqk_ws15\n" \
  "s_sleep 8\n" \
  ".Lqk_ws15:\n" \
  "s_add_i32 m0, %[wl], 98304\n" \
  "s_mov_b32 s76, 3670016\n" \
  "buffer_load_dwordx4 %[wv], %[WA], s76 offen lds\n" \
  "s_add_i32 m0, %[wl], 99328\n" \
  "s_mov_b32 s76, 3702784\n" \
  "buffer_load_dwordx4 %[wv], %[WA], s76 offen lds\n" \
  "s_add_i32 m0, %[wl], 100352\n" \
  "s_mov_b32 s76, 3735552\n" \
  "buffer_load_dwordx4 v64, %[WA], s76 offen lds\n" \
  "s_add_i32 m0, %[wl], 101376\n" \
  "s_mov_b32 s76, 3768320\n" \
  "buffer_load_dwordx4 v64, %[WA], s76 offen lds\n" \
  "ds_read_b64_tr_b16 a[112:113], v66 offset:49152\n" \
  "ds_read_b64_tr_b16 a[114:115], v66 offset:50176\n" \
  "ds_read_b64_tr_b16 a[116:117], v66 offset:57344\n" \
  "ds_read_b64_tr_b16 a[118:119], v66 offset:58368\n" \
  "ds_read_b64_tr_b16 a[120:121], v67 offset:49152\n" \
  "ds_read_b64_tr_b16 a[122:123], v67 offset:50176\n" \
  "ds_read_b64_tr_b16 a[124:125], v67 offset:57344\n" \
  "ds_read_b64_tr_b16 a[126:127], v67 offset:58368\n" \
  "s_waitcnt vmcnt(24)\n" \
  "s_waitcnt lgkmcnt(0)\n" \
  "s_barrier\n" \
  "s_cmp_eq_u32 %[grp], 0\n" \
  "s_cbranch_scc1 .Lqk_ws16\n" \
  "s_sleep 8\n" \
  ".Lqk_ws16:\n" \
  "s_add_i32 m0, %[wl], 114688\n" \
  "s_mov_b32 s76, 3670016\n" \
  "buffer_load_dwordx4 %[wv], %[WB], s76 offen lds\n" \
  "s_add_i32 m0, %[wl], 115712\n" \
  "s_mov_b32 s76, 3702784\n" \
  "buffer_load_dwordx4 %[wv], %[WB], s76 offen lds\n" \
  "s_add_i32 m0, %[wl], 116736\n" \
  "s_mov_b32 s76, 3735552\n" \
  "buffer_load_dwordx4 v64, %[WB], s76 offen lds\n" \
  "s_add_i32 m0, %[wl], 117760\n" \
  "s_mov_b32 s76, 3768320\n" \
  "buffer_load_dwordx4 v64, %[WB], s76 offen lds\n" \
  "ds_read_b64_tr_b16 a[128:129], %[wr] offset:0\n" \
  "ds_read_b64_tr_b16 a[130:131], %[wr] offset:1024\n" \
  "ds_read_b64_tr_b16 a[132:133], %[wr] offset:8192\n" \
  "ds_read_b64_tr_b16 a[134:135], %[wr] offset:9216\n" \
  "ds_read_b64_tr_b16 a[136:137], v65 offset:0\n" \
  "ds_read_b64_tr_b16 a[138:139], v65 offset:1024\n" \
  "ds_read_b64_tr_b16 a[140:141], v65 offset:8192\n" \
  "ds_read_b64_tr_b16 a[142:143], v65 offset:9216\n" \
  "s_waitcnt vmcnt(24)\n" \
  "s_waitcnt lgkmcnt(0)\n" \
  "s_barrier\n" \
  "s_cmp_eq_u32 %[grp], 0\n" \
  "s_cbranch_scc1 .Lqk_ws17\n" \
  "s_sleep 8\n" \
  ".Lqk_ws17:\n" \
  "s_add_i32 m0, %[wl], 0\n" \
  "s_mov_b32 s76, 4194304\n" \
  "buffer_load_dwordx4 %[wv], %[WA], s76 offen lds\n" \
  "s_add_i32 m0, %[wl], 1024\n" \
  "s_mov_b32 s76, 4227072\n" \
  "buffer_load_dwordx4 %[wv], %[WA], s76 offen lds\n" \
  "s_add_i32 m0, %[wl], 2048\n" \
  "s_mov_b32 s76, 4259840\n" \
  "buffer_load_dwordx4 v64, %[WA], s76 offen lds\n" \
  "s_add_i32 m0, %[wl], 3072\n" \
  "s_mov_b32 s76, 4292608\n" \
  "buffer_load_dwordx4 v64, %[WA], s76 offen lds\n" \
  "ds_read_b64_tr_b16 a[144:145], %[wr] offset:16384\n" \
  "ds_read_b64_tr_b16 a[146:147], %[wr] offset:17408\n" \
  "ds_read_b64_tr_b16 a[148:149], %[wr] offset:24576\n" \
  "ds_read_b64_tr_b16 a[150:151], %[wr] offset:25600\n" \
  "ds_read_b64_tr_b16 a[152:153], v65 offset:16384\n" \
  "ds_read_b64_tr_b16 a[154:155], v65 offset:17408\n" \
  "ds_read_b64_tr_b16 a[156:157], v65 offset:24576\n" \
  "ds_read_b64_tr_b16 a[158:159], v65 offset:25600\n" \
  "s_waitcnt vmcnt(24)\n" \
  "s_waitcnt lgkmcnt(0)\n" \
  "s_barrier\n" \
  "s_cmp_eq_u32 %[grp], 0\n" \
  "s_cbranch_scc1 .Lqk_ws18\n" \
  "s_sleep 8\n" \
  ".Lqk_ws18:\n" \
  "s_add_i32 m0, %[wl], 16384\n" \
  "s_mov_b32 s76, 4194304\n" \
  "buffer_load_dwordx4 %[wv], %[WB], s76 offen lds\n" \
  "s_add_i32 m0, %[wl], 17408\n" \
  "s_mov_b32 s76, 4227072\n" \
  "buffer_load_dwordx4 %[wv], %[WB], s76 offen lds\n" \
  "s_add_i32 m0, %[wl], 18432\n" \
  "s_mov_b32 s76, 4259840\n" \
  "buffer_load_dwordx4 v64, %[WB], s76 offen lds\n" \
  "s_add_i32 m0, %[wl], 19456\n" \
  "s_mov_b32 s76, 4292608\n" \
  "buffer_load_dwordx4 v64, %[WB], s76 offen lds\n" \
  "ds_read_b64_tr_b16 a[160:161], %[wr] offset:32768\n" \
  "ds_read_b64_tr_b16 a[162:163], %[wr] offset:33792\n" \
  "ds_read_b64_tr_b16 a[164:165], %[wr] offset:40960\n" \
  "ds_read_b64_tr_b16 a[166:167], %[wr] offset:41984\n" \
  "ds_read_b64_tr_b16 a[168:169], v65 offset:32768\n" \
  "ds_read_b64_tr_b16 a[170:171], v65 offset:33792\n" \
  "ds_read_b64_tr_b16 a[172:173], v65 offset:40960\n" \
  "ds_read_b64_tr_b16 a[174:175], v65 offset:41984\n" \
  "s_waitcnt vmcnt(24)\n" \
  "s_waitcnt lgkmcnt(0)\n" \
  "s_barrier\n" \
  "ds_read_b64_tr_b16 a[176:177], %[wr] offset:49152\n" \
  "ds_read_b64_tr_b16 a[178:179], %[wr] offset:50176\n" \
  "ds_read_b64_tr_b16 a[180:181], %[wr] offset:57344\n" \
  "ds_read_b64_tr_b16 a[182:183], %[wr] offset:58368\n" \
  "ds_read_b64_tr_b16 a[184:185], v65 offset:49152\n" \
  "ds_read_b64_tr_b16 a[186:187], v65 offset:50176\n" \
  "ds_read_b64_tr_b16 a[188:189], v65 offset:57344\n" \
  "ds_read_b64_tr_b16 a[190:191], v65 offset:58368\n" \
  "s_waitcnt vmcnt(20)\n" \
  "s_waitcnt lgkmcnt(0)\n" \
  "s_barrier\n" \
  "ds_read_b64_tr_b16 a[192:193], v66 offset:0\n" \
  "ds_read_b64_tr_b16 a[194:195], v66 offset:1024\n" \
  "ds_read_b64_tr_b16 a[196:197], v66 offset:8192\n" \
  "ds_read_b64_tr_b16 a[198:199], v66 offset:9216\n" \
  "ds_read_b64_tr_b16 a[200:201], v67 offset:0\n" \
  "ds_read_b64_tr_b16 a[202:203], v67 offset:1024\n" \
  "ds_read_b64_tr_b16 a[204:205], v67 offset:8192\n" \
  "ds_read_b64_tr_b16 a[206:207], v67 offset:9216\n" \
  "s_waitcnt vmcnt(16)\n" \
  "s_waitcnt lgkmcnt(0)\n" \
  "s_barrier\n" \
  "ds_read_b64_tr_b16 a[208:209], v66 offset:16384\n" \
  "ds_read_b64_tr_b16 a[210:211], v66 offset:17408\n" \
  "ds_read_b64_tr_b16 a[212:213], v66 offset:24576\n" \
  "ds_read_b64_tr_b16 a[214:215], v66 offset:25600\n" \
  "ds_read_b64_tr_b16 a[216:217], v67 offset:16384\n" \
  "ds_read_b64_tr_b16 a[218:219], v67 offset:17408\n" \
  "ds_read_b64_tr_b16 a[220:221], v67 offset:24576\n" \
  "ds_read_b64_tr_b16 a[222:223], v67 offset:25600\n" \
  "s_waitcnt vmcnt(12)\n" \
  "s_waitcnt lgkmcnt(0)\n" \
  "s_barrier\n" \
  "ds_read_b64_tr_b16 a[224:225], v66 offset:32768\n" \
  "ds_read_b64_tr_b16 a[226:227], v66 offset:33792\n" \
  "ds_read_b64_tr_b16 a[228:229], v66 offset:40960\n" \
  "ds_read_b64_tr_b16 a[230:231], v66 offset:41984\n" \
  "ds_read_b64_tr_b16 a[232:233], v67 offset:32768\n" \
  "ds_read_b64_tr_b16 a[234:235], v67 offset:33792\n" \
  "ds_read_b64_tr_b16 a[236:237], v67 offset:40960\n" \
  "ds_read_b64_tr_b16 a[238:239], v67 offset:41984\n" \
  "s_waitcnt vmcnt(8)\n" \
  "s_waitcnt lgkmcnt(0)\n" \
  "s_barrier\n" \
  "ds_read_b64_tr_b16 a[240:241], v66 offset:49152\n" \
  "ds_read_b64_tr_b16 a[242:243], v66 offset:50176\n" \
  "ds_read_b64_tr_b16 a[244:245], v66 offset:57344\n" \
  "ds_read_b64_tr_b16 a[246:247], v66 offset:58368\n" \
  "ds_read_b64_tr_b16 a[248:249], v67 offset:49152\n" \
  "ds_read_b64_tr_b16 a[250:251], v67 offset:50176\n" \
  "ds_read_b64_tr_b16 a[252:253], v67 offset:57344\n" \
  "ds_read_b64_tr_b16 a[254:255], v67 offset:58368\n" \
  "s_waitcnt vmcnt(4)\n" \
  "s_waitcnt lgkmcnt(0)\n" \
  "s_barrier\n" \
  "ds_read_b64_tr_b16 v[32:33], %[wr] offset:0\n" \
  "ds_read_b64_tr_b16 v[34:35], %[wr] offset:1024\n" \
  "ds_read_b64_tr_b16 v[36:37], %[wr] offset:8192\n" \
  "ds_read_b64_tr_b16 v[38:39], %[wr] offset:9216\n" \
  "ds_read_b64_tr_b16 v[40:41], v65 offset:0\n" \
  "ds_read_b64_tr_b16 v[42:43], v65 offset:1024\n" \
  "ds_read_b64_tr_b16 v[44:45], v65 offset:8192\n" \
  "ds_read_b64_tr_b16 v[46:47], v65 offset:9216\n" \
  "s_waitcnt vmcnt(0)\n" \
  "s_waitcnt lgkmcnt(0)\n" \
  "s_barrier\n" \
  "ds_read_b64_tr_b16 v[48:49], %[wr] offset:16384\n" \
  "ds_read_b64_tr_b16 v[50:51], %[wr] offset:17408\n" \
  "ds_read_b64_tr_b16 v[52:53], %[wr] offset:24576\n" \
  "ds_read_b64_tr_b16 v[54:55], %[wr] offset:25600\n" \
  "ds_read_b64_tr_b16 v[56:57], v65 offset:16384\n" \
  "ds_read_b64_tr_b16 v[58:59], v65 offset:17408\n" \
  "ds_read_b64_tr_b16 v[60:61], v65 offset:24576\n" \
  "ds_read_b64_tr_b16 v[62:63], v65 offset:25600\n" \
  "s_waitcnt lgkmcnt(0)\n" \
  "s_barrier\n" \
  "s_mov_b32 s60, -2\n" \
  "s_mov_b32 s70, 0x3db504f3\n" \
  "s_mov_b32 s71, 0x3e0293ee\n" \
  "v_add_u32 v228, 0x1000, %[mo]\n" \
  "v_mov_b32 v230, 0x7fffffff\n" \
  "v_mov_b32 v232, 0xff800000\n" \
  "s_mov_b32 s78, 0\n" \
  "s_mov_b32 s79, 0\n" \
  "s_mov_b32 s80, 0\n" \
  "s_mov_b32 s65, 2.0\n" \
  "s_mov_b32 s81, -1\n" \
  "s_mov_b32 s87, 2.0\n" \
  "s_lshl_b32 s96, s60, 7\n" \
  "s_add_i32 s96, s96, %[bas]\n" \
  "v_cmp_lt_i32_e64 vcc, %[lim], 0\n" \
  "s_nop 1\n" \
  "v_cndmask_b32 v212, 0, v232, vcc\n" \
  "v_cmp_lt_i32_e64 vcc, %[lim], 1\n" \
  "s_nop 1\n" \
  "v_cndmask_b32 v213, 0, v232, vcc\n" \
  "v_cmp_lt_i32_e64 vcc, %[lim], 2\n" \
  "s_nop 1\n" \
  "v_cndmask_b32 v214, 0, v232, vcc\n" \
  "v_cmp_lt_i32_e64 vcc, %[lim], 3\n" \
  "s_nop 1\n" \
  "v_cndmask_b32 v215, 0, v232, vcc\n" \
  "v_cmp_lt_i32_e64 vcc, %[lim], 16\n" \
  "s_nop 1\n" \
  "v_cndmask_b32 v216, 0, v232, vcc\n" \
  "v_cmp_lt_i32_e64 vcc, %[lim], 17\n" \
  "s_nop 1\n" \
  "v_cndmask_b32 v217, 0, v232, vcc\n" \
  "v_cmp_lt_i32_e64 vcc, %[lim], 18\n" \
  "s_nop 1\n" \
  "v_cndmask_b32 v218, 0, v232, vcc\n" \
  "v_cmp_lt_i32_e64 vcc, %[lim], 19\n" \
  "s_nop 1\n" \
  "v_cndmask_b32 v219, 0, v232, vcc\n" \
  "s_mov_b32 s90, 0\n" \
  "s_waitcnt vmcnt(0)\n" \
  ".Lqk_top:\n" \
  "v_mfma_f32_16x16x32_bf16 v[116:119], v[164:167], a[32:35], v[116:119]\n" \
  "ds_read_b128 v[148:151], %[hA] offset:2048\n" \
  "buffer_store_dwordx2 v[232:233], %[po3], %[X], s65 offen sc1\n" \
  "v_mfma_f32_16x16x32_bf16 v[132:135], v[168:171], a[32:35], v[132:135]\n" \
  "ds_read_b128 v[152:155], %[hA] offset:20480\n" \
  "v_mfma_f32_16x16x32_bf16 v[120:123], v[164:167], a[40:43], v[120:123]\n" \
  "s_add_i32 s67, s60, 2\n" \
  "s_min_i32 s67, s67, 63\n" \
  "s_mul_i32 s61, s67, 36864\n" \
  "v_mfma_f32_16x16x32_bf16 v[136:139], v[168:171], a[40:43], v[136:139]\n" \
  "s_lshl_b32 s67, s60, 16\n" \
  "s_and_b32 s67, s67, 0x10000\n" \
  "s_add_i32 s62, %[hl0], s67\n" \
  "v_mfma_f32_16x16x32_bf16 v[124:127], a[48:51], v[164:167], v[124:127]\n" \
  "s_add_i32 m0, s62, 0\n" \
  "v_mfma_f32_16x16x32_bf16 v[140:143], a[48:51], v[168:171], v[140:143]\n" \
  "s_add_i32 s68, s60, 1\n" \
  "s_lshl_b32 s73, s68, 17\n" \
  "s_and_b32 s73, s73, 0x60000\n" \
  "v_mfma_f32_16x16x32_bf16 v[128:131], a[56:59], v[164:167], v[128:131]\n" \
  "s_add_i32 s63, s87, s73\n" \
  "s_add_i32 s64, s63, 0x4000\n" \
  "v_mfma_f32_16x16x32_bf16 v[144:147], a[56:59], v[168:171], v[144:147]\n" \
  "s_and_b32 s73, s73, 0x20000\n" \
  "s_add_i32 s65, %[pm0], s73\n" \
  "s_lshl_b32 s73, s68, 30\n" \
  "v_mfma_f32_16x16x32_bf16 v[116:119], v[172:175], a[36:39], v[116:119]\n" \
  "ds_read_b128 v[156:159], %[hA] offset:2560\n" \
  "buffer_load_dword v250, off, %[X], s96 offset:128 sc1\n" \
  "v_mfma_f32_16x16x32_bf16 v[132:135], v[176:179], a[36:39], v[132:135]\n" \
  "ds_read_b128 v[160:163], %[hA] offset:20992\n" \
  "v_mov_b64 v[100:101], 0\n" \
  "v_mfma_f32_16x16x32_bf16 v[120:123], v[172:175], a[44:47], v[120:123]\n" \
  "buffer_load_dwordx4 %[hvo], %[HS], s61 offen offset:0 sc1 lds\n" \
  "v_mov_b64 v[102:103], 0\n" \
  "v_mfma_f32_16x16x32_bf16 v[136:139], v[176:179], a[44:47], v[136:139]\n" \
  "s_and_b32 s66, s73, 0x80000000\n" \
  "s_cmp_lt_u32 s68, 64\n" \
  "s_cselect_b32 s65, s65, 2.0\n" \
  "v_mfma_f32_16x16x32_bf16 v[124:127], a[52:55], v[172:175], v[124:127]\n" \
  "s_ashr_i32 s67, s66, 31\n" \
  "v_not_b32 v232, s67\n" \
  "v_mov_b32 v233, s90\n" \
  "v_mfma_f32_16x16x32_bf16 v[140:143], a[52:55], v[176:179], v[140:143]\n" \
  "v_mov_b64 v[104:105], 0\n" \
  "v_mov_b64 v[106:107], 0\n" \
  "v_mfma_f32_16x16x32_bf16 v[128:131], a[60:63], v[172:175], v[128:131]\n" \
  "buffer_load_dwordx4 %[hvo], %[HS], s61 offen offset:1024 sc1 lds\n" \
  "v_mov_b64 v[108:109], 0\n" \
  "v_mfma_f32_16x16x32_bf16 v[144:147], a[60:63], v[176:179], v[144:147]\n" \
  "s_waitcnt lgkmcnt(0)\n" \
  "s_sub_i32 s76, s76, s72\n" \
  "v_mfma_f32_16x16x32_bf16 v[116:119], v[148:151], a[64:67], v[116:119]\n" \
  "ds_read_b128 v[164:167], %[hA] offset:3072\n" \
  "v_mov_b64 v[110:111], 0\n" \
  "v_mfma_f32_16x16x32_bf16 v[132:135], v[152:155], a[64:67], v[132:135]\n" \
  "ds_read_b128 v[168:171], %[hA] offset:21504\n" \
  "v_mov_b64 v[112:113], 0\n" \
  "v_mfma_f32_16x16x32_bf16 v[120:123], v[148:151], a[72:75], v[120:123]\n" \
  "buffer_load_dwordx4 %[hvo], %[HS], s61 offen offset:2048 sc1 lds\n" \
  "v_mov_b64 v[114:115], 0\n" \
  "v_mfma_f32_16x16x32_bf16 v[136:139], v[152:155], a[72:75], v[136:139]\n" \
  "s_min_u32 s76, s76, 4095\n" \
  "s_and_b32 s77, s90, 0xfffff000\n" \
  "s_or_b32 s76, s76, s77\n" \
  "v_mfma_f32_16x16x32_bf16 v[124:127], a[80:83], v[148:151], v[124:127]\n" \
  "v_writelane_b32 v233, s76, 1\n" \
  "v_mfma_f32_16x16x32_bf16 v[140:143], a[80:83], v[152:155], v[140:143]\n" \
  "v_mfma_f32_16x16x32_bf16 v[128:131], a[88:91], v[148:151], v[128:131]\n" \
  "buffer_load_dwordx4 %[hvo], %[HS], s61 offen offset:3072 sc1 lds\n" \
  "v_mfma_f32_16x16x32_bf16 v[144:147], a[88:91], v[152:155], v[144:147]\n" \
  "s_add_i32 s61, s61, 0x1000\n" \
  "s_add_i32 m0, s62, 4096\n" \
  "v_mfma_f32_16x16x32_bf16 v[116:119], v[156:159], a[68:71], v[116:119]\n" \
  "ds_read_b128 v[172:175], %[hA] offset:3584\n" \
  "v_mfma_f32_16x16x32_bf16 v[132:135], v[160:163], a[68:71], v[132:135]\n" \
  "ds_read_b128 v[176:179], %[hA] offset:22016\n" \
  "v_mfma_f32_16x16x32_bf16 v[120:123], v[156:159], a[76:79], v[120:123]\n" \
  "buffer_load_dwordx4 %[hvo], %[HS], s61 offen offset:0 sc1 lds\n" \
  "v_mfma_f32_16x16x32_bf16 v[136:139], v[160:163], a[76:79], v[136:139]\n" \
  "v_mfma_f32_16x16x32_bf16 v[124:127], a[84:87], v[156:159], v[124:127]\n" \
  "v_mfma_f32_16x16x32_bf16 v[140:143], a[84:87], v[160:163], v[140:143]\n" \
  "v_mfma_f32_16x16x32_bf16 v[128:131], a[92:95], v[156:159], v[128:131]\n" \
  "buffer_load_dwordx4 %[hvo], %[HS], s61 offen offset:1024 sc1 lds\n" \
  "v_mfma_f32_16x16x32_bf16 v[144:147], a[92:95], v[160:163], v[144:147]\n" \
  "s_waitcnt lgkmcnt(2)\n" \
  "v_mfma_f32_16x16x32_bf16 v[116:119], v[164:167], a[96:99], v[116:119]\n" \
  "ds_read_b128 v[148:151], %[hA] offset:4096\n" \
  "v_mfma_f32_16x16x32_bf16 v[132:135], v[168:171], a[96:99], v[132:135]\n" \
  "ds_read_b128 v[152:155], %[hA] offset:22528\n" \
  "v_mfma_f32_16x16x32_bf16 v[120:123], v[164:167], a[104:107], v[120:123]\n" \
  "buffer_load_dwordx4 %[hvo], %[HS], s61 offen offset:2048 sc1 lds\n" \
  "v_mfma_f32_16x16x32_bf16 v[136:139], v[168:171], a[104:107], v[136:139]\n" \
  "v_mfma_f32_16x16x32_bf16 v[124:127], a[112:115], v[164:167], v[124:127]\n" \
  "v_mfma_f32_16x16x32_bf16 v[140:143], a[112:115], v[168:171], v[140:143]\n" \
  "v_mfma_f32_16x16x32_bf16 v[128:131], a[120:123], v[164:167], v[128:131]\n" \
  "buffer_load_dwordx4 %[hvo], %[HS], s61 offen offset:3072 sc1 lds\n" \
  "v_mfma_f32_16x16x32_bf16 v[144:147], a[120:123], v[168:171], v[144:147]\n" \
  "s_add_i32 s61, s61, 0x1000\n" \
  "s_add_i32 m0, s62, 8192\n" \
  "s_waitcnt lgkmcnt(2)\n" \
  "v_mfma_f32_16x16x32_bf16 v[116:119], v[172:175], a[100:103], v[116:119]\n" \
  "ds_read_b128 v[156:159], %[hA] offset:4608\n" \
  "v_mfma_f32_16x16x32_bf16 v[132:135], v[176:179], a[100:103], v[132:135]\n" \
  "ds_read_b128 v[160:163], %[hA] offset:23040\n" \
  "v_mfma_f32_16x16x32_bf16 v[120:123], v[172:175], a[108:111], v[120:123]\n" \
  "buffer_load_dwordx4 %[hvo], %[HS], s61 offen offset:0 sc1 lds\n" \
  "v_mfma_f32_16x16x32_bf16 v[136:139], v[176:179], a[108:111], v[136:139]\n" \
  "v_mfma_f32_16x16x32_bf16 v[124:127], a[116:119], v[172:175], v[124:127]\n" \
  "v_mfma_f32_16x16x32_bf16 v[140:143], a[116:119], v[176:179], v[140:143]\n" \
  "buffer_store_dword v232, %[pcl], %[X], s65 offen sc1\n" \
  "v_mfma_f32_16x16x32_bf16 v[128:131], a[124:127], v[172:175], v[128:131]\n" \
  "v_mfma_f32_16x16x32_bf16 v[144:147], a[124:127], v[176:179], v[144:147]\n" \
  "buffer_atomic_umax v233, %[rpo], %[X], 0 offen sc1\n" \
  "s_waitcnt lgkmcnt(2)\n" \
  "v_mfma_f32_16x16x32_bf16 v[116:119], v[148:151], a[128:131], v[116:119]\n" \
  "ds_read_b128 v[164:167], %[hA] offset:5120\n" \
  "v_mfma_f32_16x16x32_bf16 v[132:135], v[152:155], a[128:131], v[132:135]\n" \
  "ds_read_b128 v[168:171], %[hA] offset:23552\n" \
  "v_mfma_f32_16x16x32_bf16 v[120:123], v[148:151], a[136:139], v[120:123]\n" \
  "v_mfma_f32_16x16x32_bf16 v[136:139], v[152:155], a[136:139], v[136:139]\n" \
  "v_mfma_f32_16x16x32_bf16 v[124:127], a[144:147], v[148:151], v[124:127]\n" \
  "v_mfma_f32_16x16x32_bf16 v[140:143], a[144:147], v[152:155], v[140:143]\n" \
  "v_mfma_f32_16x16x32_bf16 v[128:131], a[152:155], v[148:151], v[128:131]\n" \
  "v_mfma_f32_16x16x32_bf16 v[144:147], a[152:155], v[152:155], v[144:147]\n" \
  "s_waitcnt lgkmcnt(2)\n" \
  "v_mfma_f32_16x16x32_bf16 v[116:119], v[156:159], a[132:135], v[116:119]\n" \
  "ds_read_b128 v[172:175], %[hA] offset:5632\n" \
  "v_mfma_f32_16x16x32_bf16 v[132:135], v[160:163], a[132:135], v[132:135]\n" \
  "ds_read_b128 v[176:179], %[hA] offset:24064\n" \
  "v_mfma_f32_16x16x32_bf16 v[120:123], v[156:159], a[140:143], v[120:123]\n" \
  "v_mfma_f32_16x16x32_bf16 v[136:139], v[160:163], a[140:143], v[136:139]\n" \
  "v_mfma_f32_16x16x32_bf16 v[124:127], a[148:151], v[156:159], v[124:127]\n" \
  "s_waitcnt vmcnt(11)\n" \
  "v_readfirstlane_b32 s72, v250\n" \
  "v_mfma_f32_16x16x32_bf16 v[140:143], a[148:151], v[160:163], v[140:143]\n" \
  "v_mfma_f32_16x16x32_bf16 v[128:131], a[156:159], v[156:159], v[128:131]\n" \
  "v_mfma_f32_16x16x32_bf16 v[144:147], a[156:159], v[160:163], v[144:147]\n" \
  "s_waitcnt lgkmcnt(2)\n" \
  "v_mfma_f32_16x16x32_bf16 v[116:119], v[164:167], a[160:163], v[116:119]\n" \
  "ds_read_b128 v[148:151], %[hA] offset:6144\n" \
  "v_mfma_f32_16x16x32_bf16 v[132:135], v[168:171], a[160:163], v[132:135]\n" \
  "ds_read_b128 v[152:155], %[hA] offset:24576\n" \
  "v_mfma_f32_16x16x32_bf16 v[120:123], v[164:167], a[168:171], v[120:123]\n" \
  "v_mfma_f32_16x16x32_bf16 v[136:139], v[168:171], a[168:171], v[136:139]\n" \
  "v_mfma_f32_16x16x32_bf16 v[124:127], a[176:179], v[164:167], v[124:127]\n" \
  "v_mfma_f32_16x16x32_bf16 v[140:143], a[176:179], v[168:171], v[140:143]\n" \
  "v_mfma_f32_16x16x32_bf16 v[128:131], a[184:187], v[164:167], v[128:131]\n" \
  "v_mfma_f32_16x16x32_bf16 v[144:147], a[184:187], v[168:171], v[144:147]\n" \
  "s_waitcnt lgkmcnt(2)\n" \
  "v_mfma_f32_16x16x32_bf16 v[116:119], v[172:175], a[164:167], v[116:119]\n" \
  "ds_read_b128 v[156:159], %[hA] offset:6656\n" \
  "v_mfma_f32_16x16x32_bf16 v[132:135], v[176:179], a[164:167], v[132:135]\n" \
  "ds_read_b128 v[160:163], %[hA] offset:25088\n" \
  "v_mfma_f32_16x16x32_bf16 v[120:123], v[172:175], a[172:175], v[120:123]\n" \
  "v_mfma_f32_16x16x32_bf16 v[136:139], v[176:179], a[172:175], v[136:139]\n" \
  "v_mfma_f32_16x16x32_bf16 v[124:127], a[180:183], v[172:175], v[124:127]\n" \
  "v_mfma_f32_16x16x32_bf16 v[140:143], a[180:183], v[176:179], v[140:143]\n" \
  "v_mfma_f32_16x16x32_bf16 v[128:131], a[188:191], v[172:175], v[128:131]\n" \
  "v_mfma_f32_16x16x32_bf16 v[144:147], a[188:191], v[176:179], v[144:147]\n" \
  "s_waitcnt lgkmcnt(2)\n" \
  "v_mfma_f32_16x16x32_bf16 v[116:119], v[148:151], a[192:195], v[116:119]\n" \
  "ds_read_b128 v[164:167], %[hA] offset:7168\n" \
  "v_mfma_f32_16x16x32_bf16 v[132:135], v[152:155], a[192:195], v[132:135]\n" \
  "ds_read_b128 v[168:171], %[hA] offset:25600\n" \
  "v_mfma_f32_16x16x32_bf16 v[120:123], v[148:151], a[200:203], v[120:123]\n" \
  "v_mfma_f32_16x16x32_bf16 v[136:139], v[152:155], a[200:203], v[136:139]\n" \
  "v_mfma_f32_16x16x32_bf16 v[124:127], a[208:211], v[148:151], v[124:127]\n" \
  "v_mfma_f32_16x16x32_bf16 v[140:143], a[208:211], v[152:155], v[140:143]\n" \
  "v_mfma_f32_16x16x32_bf16 v[128:131], a[216:219], v[148:151], v[128:131]\n" \
  "v_mfma_f32_16x16x32_bf16 v[144:147], a[216:219], v[152:155], v[144:147]\n" \
  "s_waitcnt lgkmcnt(2)\n" \
  "v_mfma_f32_16x16x32_bf16 v[116:119], v[156:159], a[196:199], v[116:119]\n" \
  "ds_read_b128 v[172:175], %[hA] offset:7680\n" \
  "v_mfma_f32_16x16x32_bf16 v[132:135], v[160:163], a[196:199], v[132:135]\n" \
  "ds_read_b128 v[176:179], %[hA] offset:26112\n" \
  "v_mfma_f32_16x16x32_bf16 v[120:123], v[156:159], a[204:207], v[120:123]\n" \
  "ds_read_b128 v[148:151], %[hA] offset:8192\n" \
  "v_mfma_f32_16x16x32_bf16 v[136:139], v[160:163], a[204:207], v[136:139]\n" \
  "ds_read_b128 v[152:155], %[hA] offset:26624\n" \
  "v_mfma_f32_16x16x32_bf16 v[124:127], a[212:215], v[156:159], v[124:127]\n" \
  "v_mfma_f32_16x16x32_bf16 v[140:143], a[212:215], v[160:163], v[140:143]\n" \
  "v_mfma_f32_16x16x32_bf16 v[128:131], a[220:223], v[156:159], v[128:131]\n" \
  "v_mfma_f32_16x16x32_bf16 v[144:147], a[220:223], v[160:163], v[144:147]\n" \
  "s_memrealtime s[74:75]\n" \
  "s_waitcnt lgkmcnt(4)\n" \
  "v_mfma_f32_16x16x32_bf16 v[124:127], a[240:243], v[164:167], v[124:127]\n" \
  "ds_read_b128 v[156:159], %[hA] offset:8704\n" \
  "v_mfma_f32_16x16x32_bf16 v[140:143], a[240:243], v[168:171], v[140:143]\n" \
  "ds_read_b128 v[160:163], %[hA] offset:27136\n" \
  "v_mfma_f32_16x16x32_bf16 v[128:131], a[248:251], v[164:167], v[128:131]\n" \
  "v_mfma_f32_16x16x32_bf16 v[144:147], a[248:251], v[168:171], v[144:147]\n" \
  "s_waitcnt lgkmcnt(0)\n" \
  "s_sub_i32 s91, s74, s72\n" \
  "s_cmp_gt_u32 s91, 14\n" \
  "v_mfma_f32_16x16x32_bf16 v[124:127], a[244:247], v[172:175], v[124:127]\n" \
  "s_cbranch_scc1 .Lqk_late\n" \
  ".Lqk_al_ok:\n" \
  "v_mfma_f32_16x16x32_bf16 v[140:143], a[244:247], v[176:179], v[140:143]\n" \
  "buffer_load_dwordx4 v[64:67], %[mo], %[X], s63 offen offset:0 sc1\n" \
  "v_mfma_f32_16x16x32_bf16 v[128:131], a[252:255], v[172:175], v[128:131]\n" \
  "v_mfma_f32_16x16x32_bf16 v[144:147], a[252:255], v[176:179], v[144:147]\n" \
  "v_mfma_f32_16x16x32_bf16 v[124:127], v[48:51], v[148:151], v[124:127]\n" \
  "buffer_load_dwordx4 v[68:71], %[mo], %[X], s63 offen offset:1024 sc1\n" \
  "v_mfma_f32_16x16x32_bf16 v[140:143], v[48:51], v[152:155], v[140:143]\n" \
  "v_mfma_f32_16x16x32_bf16 v[128:131], v[56:59], v[148:151], v[128:131]\n" \
  "v_mfma_f32_16x16x32_bf16 v[144:147], v[56:59], v[152:155], v[144:147]\n" \
  "buffer_load_dwordx4 v[72:75], %[mo], %[X], s63 offen offset:2048 sc1\n" \
  "v_mfma_f32_16x16x32_bf16 v[124:127], v[52:55], v[156:159], v[124:127]\n" \
  "v_mfma_f32_16x16x32_bf16 v[140:143], v[52:55], v[160:163], v[140:143]\n" \
  "v_mfma_f32_16x16x32_bf16 v[128:131], v[60:63], v[156:159], v[128:131]\n" \
  "buffer_load_dwordx4 v[76:79], %[mo], %[X], s63 offen offset:3072 sc1\n" \
  "v_mfma_f32_16x16x32_bf16 v[144:147], v[60:63], v[160:163], v[144:147]\n" \
  "v_mfma_f32_16x16x32_bf16 v[116:119], v[164:167], a[224:227], v[116:119]\n" \
  "v_mfma_f32_16x16x32_bf16 v[132:135], v[168:171], a[224:227], v[132:135]\n" \
  "buffer_load_dwordx4 v[80:83], v228, %[X], s63 offen offset:0 sc1\n" \
  "v_mfma_f32_16x16x32_bf16 v[120:123], v[164:167], a[232:235], v[120:123]\n" \
  "v_cvt_pk_bf16_f32 v220, v124, v125\n" \
  "v_cvt_pk_bf16_f32 v221, v126, v127\n" \
  "v_mfma_f32_16x16x32_bf16 v[136:139], v[168:171], a[232:235], v[136:139]\n" \
  "v_cvt_pk_bf16_f32 v222, v128, v129\n" \
  "v_cvt_pk_bf16_f32 v223, v130, v131\n" \
  "v_mfma_f32_16x16x32_bf16 v[116:119], v[172:175], a[228:231], v[116:119]\n" \
  "buffer_load_dwordx4 v[84:87], v228, %[X], s63 offen offset:1024 sc1\n" \
  "v_cvt_pk_bf16_f32 v224, v140, v141\n" \
  "v_mfma_f32_16x16x32_bf16 v[132:135], v[176:179], a[228:231], v[132:135]\n" \
  "ds_write_b128 %[ktW], v[220:223] offset:0\n" \
  "v_mfma_f32_16x16x32_bf16 v[120:123], v[172:175], a[236:239], v[120:123]\n" \
  "v_cvt_pk_bf16_f32 v225, v142, v143\n" \
  "v_cvt_pk_bf16_f32 v226, v144, v145\n" \
  "v_mfma_f32_16x16x32_bf16 v[136:139], v[176:179], a[236:239], v[136:139]\n" \
  "buffer_load_dwordx4 v[88:91], v228, %[X], s63 offen offset:2048 sc1\n" \
  "v_cvt_pk_bf16_f32 v227, v146, v147\n" \
  "v_mfma_f32_16x16x32_bf16 v[116:119], v[148:151], v[32:35], v[116:119]\n" \
  "ds_write_b128 %[ktW], v[224:227] offset:4096\n" \
  "v_mfma_f32_16x16x32_bf16 v[132:135], v[152:155], v[32:35], v[132:135]\n" \
  "v_add_f32 v231, v124, v140\n" \
  "v_xor_b32 %[hA], 0x10000, %[hA]\n" \
  "v_mfma_f32_16x16x32_bf16 v[120:123], v[148:151], v[40:43], v[120:123]\n" \
  "buffer_load_dwordx4 v[92:95], v228, %[X], s63 offen offset:3072 sc1\n" \
  "v_mfma_f32_16x16x32_bf16 v[136:139], v[152:155], v[40:43], v[136:139]\n" \
  "v_mfma_f32_16x16x32_bf16 v[116:119], v[156:159], v[36:39], v[116:119]\n" \
  "s_waitcnt vmcnt(10)\n" \
  "s_waitcnt lgkmcnt(0)\n" \
  "s_barrier\n" \
  "v_mfma_f32_16x16x32_bf16 v[132:135], v[160:163], v[36:39], v[132:135]\n" \
  "buffer_load_dwordx4 v[96:99], %[mo], %[X], s64 offen offset:0 sc1\n" \
  "ds_read_b128 v[180:183], %[ktR] offset:0\n" \
  "v_mfma_f32_16x16x32_bf16 v[120:123], v[156:159], v[44:47], v[120:123]\n" \
  "ds_read_b128 v[184:187], %[ktR] offset:1024\n" \
  "v_mfma_f32_16x16x32_bf16 v[136:139], v[160:163], v[44:47], v[136:139]\n" \
  "ds_read_b128 v[188:191], %[ktR] offset:2048\n" \
  ".Lqk_m:\n" \
  "s_waitcnt vmcnt(8)\n" \
  "v_smfmac_f32_16x16x64_bf16 v[100:103], v[64:67], a[0:7], %[idx]\n" \
  "s_add_i32 s60, s60, 1\n" \
  "s_add_i32 s96, s96, 128\n" \
  "ds_read_b128 v[192:195], %[ktR] offset:3072\n" \
  "v_smfmac_f32_16x16x64_bf16 v[104:107], v[64:67], a[8:15], %[idx]\n" \
  "ds_read_b128 v[196:199], %[ktR] offset:4096\n" \
  "v_smfmac_f32_16x16x64_bf16 v[108:111], v[64:67], a[16:23], %[idx]\n" \
  "ds_read_b128 v[200:203], %[ktR] offset:5120\n" \
  "v_cvt_pk_bf16_f32 v220, v116, v117\n" \
  "v_smfmac_f32_16x16x64_bf16 v[112:115], v[64:67], a[24:31], %[idx]\n" \
  "ds_read_b128 v[204:207], %[ktR] offset:6144\n" \
  "s_waitcnt vmcnt(7)\n" \
  "v_smfmac_f32_16x16x64_bf16 v[100:103], v[68:71], a[32:39], %[idx]\n" \
  "ds_read_b128 v[208:211], %[ktR] offset:7168\n" \
  "v_cvt_pk_bf16_f32 v221, v118, v119\n" \
  "v_smfmac_f32_16x16x64_bf16 v[104:107], v[68:71], a[40:47], %[idx]\n" \
  "ds_read_b128 v[164:167], %[hA] offset:1024\n" \
  "v_cvt_pk_bf16_f32 v222, v120, v121\n" \
  "v_smfmac_f32_16x16x64_bf16 v[108:111], v[68:71], a[48:55], %[idx]\n" \
  "ds_read_b128 v[168:171], %[hA] offset:19456\n" \
  "v_cvt_pk_bf16_f32 v223, v122, v123\n" \
  "v_smfmac_f32_16x16x64_bf16 v[112:115], v[68:71], a[56:63], %[idx]\n" \
  "ds_read_b128 v[172:175], %[hA] offset:1536\n" \
  "s_waitcnt vmcnt(6)\n" \
  "v_smfmac_f32_16x16x64_bf16 v[100:103], v[72:75], a[64:71], %[idx]\n" \
  "ds_read_b128 v[176:179], %[hA] offset:19968\n" \
  "v_cvt_pk_bf16_f32 v224, v132, v133\n" \
  "v_smfmac_f32_16x16x64_bf16 v[104:107], v[72:75], a[72:79], %[idx]\n" \
  "ds_read_b128 v[148:151], %[hA] offset:0\n" \
  "v_cvt_pk_bf16_f32 v225, v134, v135\n" \
  "v_smfmac_f32_16x16x64_bf16 v[108:111], v[72:75], a[80:87], %[idx]\n" \
  "ds_read_b128 v[152:155], %[hA] offset:18432\n" \
  "v_cvt_pk_bf16_f32 v226, v136, v137\n" \
  "v_smfmac_f32_16x16x64_bf16 v[112:115], v[72:75], a[88:95], %[idx]\n" \
  "ds_read_b128 v[156:159], %[hA] offset:512\n" \
  "s_waitcnt vmcnt(5)\n" \
  "v_smfmac_f32_16x16x64_bf16 v[100:103], v[76:79], a[96:103], %[idx]\n" \
  "ds_read_b128 v[160:163], %[hA] offset:18944\n" \
  "v_cvt_pk_bf16_f32 v227, v138, v139\n" \
  "v_smfmac_f32_16x16x64_bf16 v[104:107], v[76:79], a[104:111], %[idx]\n" \
  "ds_write_b64 %[qtW], v[220:221] offset:0\n" \
  "v_smfmac_f32_16x16x64_bf16 v[108:111], v[76:79], a[112:119], %[idx]\n" \
  "ds_write_b64 %[qtW], v[222:223] offset:2816\n" \
  "v_smfmac_f32_16x16x64_bf16 v[112:115], v[76:79], a[120:127], %[idx]\n" \
  "s_waitcnt vmcnt(4)\n" \
  "v_smfmac_f32_16x16x64_bf16 v[100:103], v[80:83], a[128:135], %[idx]\n" \
  "ds_write_b64 %[qtW], v[224:225] offset:64\n" \
  "v_smfmac_f32_16x16x64_bf16 v[104:107], v[80:83], a[136:143], %[idx]\n" \
  "ds_write_b64 %[qtW], v[226:227] offset:2880\n" \
  "v_smfmac_f32_16x16x64_bf16 v[108:111], v[80:83], a[144:151], %[idx]\n" \
  "s_sub_i32 s90, s74, s89\n" \
  "s_max_i32 s92, s91, -2\n" \
  "s_add_i32 s89, s72, s92\n" \
  "v_smfmac_f32_16x16x64_bf16 v[112:115], v[80:83], a[152:159], %[idx]\n" \
  "s_waitcnt vmcnt(3)\n" \
  "v_smfmac_f32_16x16x64_bf16 v[100:103], v[84:87], a[160:167], %[idx]\n" \
  "s_ashr_i32 s92, s92, 1\n" \
  "s_add_i32 s90, s90, s92\n" \
  "s_min_u32 s90, s90, 4095\n" \
  "v_smfmac_f32_16x16x64_bf16 v[104:107], v[84:87], a[168:175], %[idx]\n" \
  "s_add_i32 s92, s60, 2\n" \
  "s_lshl_b32 s92, s92, 12\n" \
  "s_or_b32 s90, s90, s92\n" \
  "v_smfmac_f32_16x16x64_bf16 v[108:111], v[84:87], a[176:183], %[idx]\n" \
  "v_smfmac_f32_16x16x64_bf16 v[112:115], v[84:87], a[184:191], %[idx]\n" \
  "s_waitcnt vmcnt(2)\n" \
  "v_smfmac_f32_16x16x64_bf16 v[100:103], v[88:91], a[192:199], %[idx]\n" \
  "v_smfmac_f32_16x16x64_bf16 v[104:107], v[88:91], a[200:207], %[idx]\n" \
  "v_smfmac_f32_16x16x64_bf16 v[108:111], v[88:91], a[208:215], %[idx]\n" \
  "v_smfmac_f32_16x16x64_bf16 v[112:115], v[88:91], a[216:223], %[idx]\n" \
  "s_waitcnt vmcnt(1)\n" \
  "v_smfmac_f32_16x16x64_bf16 v[100:103], v[92:95], a[224:231], %[idx]\n" \
  "v_smfmac_f32_16x16x64_bf16 v[104:107], v[92:95], a[232:239], %[idx]\n" \
  "s_waitcnt vmcnt(0)\n" \
  "v_smfmac_f32_16x16x64_bf16 v[100:103], v[96:99], v[32:39], %[idx]\n" \
  "v_smfmac_f32_16x16x64_bf16 v[104:107], v[96:99], v[40:47], %[idx]\n" \
  "s_nop 1\n" \
  "v_smfmac_f32_16x16x64_bf16 v[108:111], v[92:95], a[240:247], %[idx]\n" \
  "s_nop 1\n" \
  "v_smfmac_f32_16x16x64_bf16 v[112:115], v[92:95], a[248:255], %[idx]\n" \
  "s_waitcnt lgkmcnt(4)\n" \
  "s_nop 0\n" \
  "v_add_f32 v232, v100, v102\n" \
  "v_smfmac_f32_16x16x64_bf16 v[108:111], v[96:99], v[48:55], %[idx]\n" \
  "v_add_f32 v233, v101, v103\n" \
  "v_smfmac_f32_16x16x64_bf16 v[112:115], v[96:99], v[56:63], %[idx]\n" \
  "v_add_f32 v234, v104, v106\n" \
  "v_add_f32 v235, v105, v107\n" \
  "v_cvt_pk_bf16_f32 v236, v232, v233\n" \
  "v_cvt_pk_bf16_f32 v237, v234, v235\n" \
  "ds_write2st64_b32 %[qW], v236, v237 offset1:11\n" \
  "s_nop 0\n" \
  "v_pk_add_f32 v[232:233], v[108:109], v[110:111]\n" \
  "s_nop 0\n" \
  "v_pk_add_f32 v[234:235], v[112:113], v[114:115]\n" \
  "v_cvt_pk_bf16_f32 v236, v232, v233\n" \
  "v_cvt_pk_bf16_f32 v237, v234, v235\n" \
  "ds_write2st64_b32 %[kW], v236, v237 offset1:11\n" \
  "v_fma_f32 v232, v232, v233, v231\n" \
  "v_cmp_u_f32 vcc, v232, v232\n" \
  "v_mfma_f32_16x16x32_bf16 v[116:119], v[148:151], a[0:3], 0\n" \
  "v_mfma_f32_16x16x32_bf16 v[132:135], v[152:155], a[0:3], 0\n" \
  "s_cbranch_vccnz .Lqk_sm\n" \
  ".Lqk_sm_back:\n" \
  "s_waitcnt lgkmcnt(0)\n" \
  "s_barrier\n" \
  "ds_read_b64_tr_b16 v[64:65], %[qR] offset:0\n" \
  "ds_read_b64_tr_b16 v[66:67], %[qR] offset:2816\n" \
  "v_mfma_f32_16x16x32_bf16 v[120:123], v[148:151], a[8:11], 0\n" \
  "ds_read_b64_tr_b16 v[68:69], %[qR] offset:5632\n" \
  "ds_read_b64_tr_b16 v[70:71], %[qR] offset:8448\n" \
  "v_mfma_f32_16x16x32_bf16 v[136:139], v[152:155], a[8:11], 0\n" \
  "ds_read_b64_tr_b16 v[72:73], %[qR] offset:11264\n" \
  "ds_read_b64_tr_b16 v[74:75], %[qR] offset:14080\n" \
  "v_mfma_f32_16x16x32_bf16 v[124:127], a[16:19], v[148:151], 0\n" \
  "ds_read_b64_tr_b16 v[76:77], %[qR] offset:16896\n" \
  "ds_read_b64_tr_b16 v[78:79], %[qR] offset:19712\n" \
  "s_waitcnt lgkmcnt(6)\n" \
  "v_mfma_f32_16x16x32_bf16 v[100:103], v[180:183], v[64:67], v[212:215]\n" \
  "ds_read_b64_tr_b16 v[80:81], %[kR] offset:0\n" \
  "ds_read_b64_tr_b16 v[82:83], %[kR] offset:2816\n" \
  "s_waitcnt lgkmcnt(6)\n" \
  "v_mfma_f32_16x16x32_bf16 v[100:103], v[184:187], v[68:71], v[100:103]\n" \
  "ds_read_b64_tr_b16 v[84:85], %[kR] offset:5632\n" \
  "ds_read_b64_tr_b16 v[86:87], %[kR] offset:8448\n" \
  "s_waitcnt lgkmcnt(6)\n" \
  "v_mfma_f32_16x16x32_bf16 v[100:103], v[188:191], v[72:75], v[100:103]\n" \
  "ds_read_b64_tr_b16 v[88:89], %[kR] offset:11264\n" \
  "ds_read_b64_tr_b16 v[90:91], %[kR] offset:14080\n" \
  "s_waitcnt lgkmcnt(6)\n" \
  "v_mfma_f32_16x16x32_bf16 v[100:103], v[192:195], v[76:79], v[100:103]\n" \
  "ds_read_b64_tr_b16 v[92:93], %[kR] offset:16896\n" \
  "ds_read_b64_tr_b16 v[94:95], %[kR] offset:19712\n" \
  "v_mfma_f32_16x16x32_bf16 v[104:107], v[196:199], v[64:67], v[216:219]\n" \
  "v_mfma_f32_16x16x32_bf16 v[104:107], v[200:203], v[68:71], v[104:107]\n" \
  "v_mfma_f32_16x16x32_bf16 v[104:107], v[204:207], v[72:75], v[104:107]\n" \
  "v_mfma_f32_16x16x32_bf16 v[104:107], v[208:211], v[76:79], v[104:107]\n" \
  "s_waitcnt lgkmcnt(6)\n" \
  "v_mfma_f32_16x16x32_bf16 v[108:111], v[80:83], v[64:67], 0\n" \
  "s_waitcnt lgkmcnt(4)\n" \
  "v_mfma_f32_16x16x32_bf16 v[108:111], v[84:87], v[68:71], v[108:111]\n" \
  "v_max3_f32 v248, v100, v101, v102\n" \
  "s_waitcnt lgkmcnt(2)\n" \
  "v_mfma_f32_16x16x32_bf16 v[108:111], v[88:91], v[72:75], v[108:111]\n" \
  "s_waitcnt lgkmcnt(0)\n" \
  "v_mfma_f32_16x16x32_bf16 v[108:111], v[92:95], v[76:79], v[108:111]\n" \
  "v_max3_f32 v249, v103, v104, v105\n" \
  "v_max3_f32 v248, v248, v106, v107\n" \
  "v_mfma_f32_16x16x32_bf16 v[140:143], a[16:19], v[152:155], 0\n" \
  "s_nop 4\n" \
  "v_max3_f32 v249, v249, v108, v109\n" \
  "v_max_f32 v232, v248, v249\n" \
  "v_max_f32 v233, v248, v249\n" \
  "v_mfma_f32_16x16x32_bf16 v[128:131], a[24:27], v[148:151], 0\n" \
  "s_nop 0\n" \
  "v_permlane16_swap_b32 v232, v233\n" \
  "v_max_f32 v248, v232, v233\n" \
  "v_max_f32 v249, v232, v233\n" \
  "v_mfma_f32_16x16x32_bf16 v[144:147], a[24:27], v[152:155], 0\n" \
  "s_nop 0\n" \
  "v_permlane32_swap_b32 v248, v249\n" \
  "v_max_f32 v248, v248, v249\n" \
  "v_mul_f32_e64 v248, -s71, v248\n" \
  "v_pk_fma_f32 v[100:101], v[100:101], s[70:71], v[248:249] op_sel:[0,1,0] op_sel_hi:[1,1,0]\n" \
  "v_pk_fma_f32 v[102:103], v[102:103], s[70:71], v[248:249] op_sel:[0,1,0] op_sel_hi:[1,1,0]\n" \
  "v_pk_fma_f32 v[104:105], v[104:105], s[70:71], v[248:249] op_sel:[0,1,0] op_sel_hi:[1,1,0]\n" \
  "v_pk_fma_f32 v[106:107], v[106:107], s[70:71], v[248:249] op_sel:[0,1,0] op_sel_hi:[1,1,0]\n" \
  "v_pk_fma_f32 v[108:109], v[108:109], s[70:71], v[248:249] op_sel:[0,1,0] op_sel_hi:[1,1,0]\n" \
  "v_exp_f32 v100, v100\n" \
  "v_exp_f32 v101, v101\n" \
  "v_exp_f32 v102, v102\n" \
  "v_exp_f32 v103, v103\n" \
  "v_exp_f32 v104, v104\n" \
  "v_exp_f32 v105, v105\n" \
  "v_exp_f32 v106, v106\n" \
  "v_exp_f32 v107, v107\n" \
  "v_exp_f32 v108, v108\n" \
  "v_exp_f32 v109, v109\n" \
  "v_pk_add_f32 v[248:249], v[100:101], v[102:103]\n" \
  "v_pk_add_f32 v[232:233], v[104:105], v[106:107]\n" \
  "v_pk_add_f32 v[248:249], v[248:249], v[108:109]\n" \
  "v_pk_add_f32 v[248:249], v[248:249], v[232:233]\n" \
  "v_add_f32 v232, v248, v249\n" \
  "v_add_f32 v233, v248, v249\n" \
  "v_mfma_f32_16x16x32_bf16 v[116:119], v[156:159], a[4:7], v[116:119]\n" \
  "s_nop 0\n" \
  "v_permlane16_swap_b32 v232, v233\n" \
  "v_add_f32 v248, v232, v233\n" \
  "v_add_f32 v249, v232, v233\n" \
  "v_mfma_f32_16x16x32_bf16 v[132:135], v[160:163], a[4:7], v[132:135]\n" \
  "s_nop 0\n" \
  "v_permlane32_swap_b32 v248, v249\n" \
  "v_add_f32 v248, v248, v249\n" \
  "v_rcp_f32 v248, v248\n" \
  "s_cmp_lg_u32 s60, s81\n" \
  "v_bfi_b32 v248, v230, v248, s66\n" \
  "v_pk_mul_f32 v[100:101], v[248:249], v[100:101] op_sel_hi:[0,1]\n" \
  "v_pk_mul_f32 v[102:103], v[248:249], v[102:103] op_sel_hi:[0,1]\n" \
  "v_pk_mul_f32 v[104:105], v[248:249], v[104:105] op_sel_hi:[0,1]\n" \
  "v_pk_mul_f32 v[106:107], v[248:249], v[106:107] op_sel_hi:[0,1]\n" \
  "v_pk_mul_f32 v[108:109], v[248:249], v[108:109] op_sel_hi:[0,1]\n" \
  "v_cvt_pk_bf16_f32 v232, v100, v101\n" \
  "v_cvt_pk_bf16_f32 v233, v102, v103\n" \
  "v_cvt_pk_bf16_f32 v234, v104, v105\n" \
  "v_cvt_pk_bf16_f32 v235, v106, v107\n" \
  "v_cvt_pk_bf16_f32 v236, v108, v109\n" \
  "buffer_store_dwordx4 v[232:235], %[po1], %[X], s65 offen sc1\n" \
  "v_mfma_f32_16x16x32_bf16 v[120:123], v[156:159], a[12:15], v[120:123]\n" \
  "v_mfma_f32_16x16x32_bf16 v[136:139], v[160:163], a[12:15], v[136:139]\n" \
  "buffer_store_dword v236, %[po2], %[X], s65 offen sc1\n" \
  "s_memrealtime s[76:77]\n" \
  "v_mfma_f32_16x16x32_bf16 v[124:127], a[20:23], v[156:159], v[124:127]\n" \
  "v_mfma_f32_16x16x32_bf16 v[140:143], a[20:23], v[160:163], v[140:143]\n" \
  "v_mfma_f32_16x16x32_bf16 v[128:131], a[28:31], v[156:159], v[128:131]\n" \
  "v_mfma_f32_16x16x32_bf16 v[144:147], a[28:31], v[160:163], v[144:147]\n" \
  "s_cbranch_scc1 .Lqk_top\n" \
  "s_cmp_eq_u32 s60, -1\n" \
  "s_cbranch_scc1 .Lqk_entry\n" \
  "buffer_store_dwordx2 v[232:233], %[po3], %[X], s65 offen sc1\n" \
  "s_branch .Lqk_end\n" \
  ".Lqk_sm:\n" \
  "s_cmp_lt_i32 s60, 0\n" \
  "s_cbranch_scc1 .Lqk_sm_back\n" \
  "v_cmp_u_f32 vcc, v231, v231\n" \
  "s_cbranch_vccnz .Lqk_sh\n" \
  "s_mov_b32 s69, 0\n" \
  ".Lqk_smw:\n" \
  "buffer_load_dwordx4 v[64:67], %[mo], %[X], s63 offen offset:0 sc1\n" \
  "buffer_load_dwordx4 v[68:71], %[mo], %[X], s63 offen offset:1024 sc1\n" \
  "buffer_load_dwordx4 v[72:75], %[mo], %[X], s63 offen offset:2048 sc1\n" \
  "buffer_load_dwordx4 v[76:79], %[mo], %[X], s63 offen offset:3072 sc1\n" \
  "buffer_load_dwordx4 v[80:83], v228, %[X], s63 offen offset:0 sc1\n" \
  "buffer_load_dwordx4 v[84:87], v228, %[X], s63 offen offset:1024 sc1\n" \
  "buffer_load_dwordx4 v[88:91], v228, %[X], s63 offen offset:2048 sc1\n" \
  "buffer_load_dwordx4 v[92:95], v228, %[X], s63 offen offset:3072 sc1\n" \
  "buffer_load_dwordx4 v[96:99], %[mo], %[X], s64 offen offset:0 sc1\n" \
  "s_waitcnt vmcnt(0)\n" \
  "v_max3_u32 v229, v64, v65, v66\n" \
  "v_max3_u32 v229, v229, v67, v68\n" \
  "v_max3_u32 v229, v229, v69, v70\n" \
  "v_max3_u32 v229, v229, v71, v72\n" \
  "v_max3_u32 v229, v229, v73, v74\n" \
  "v_max3_u32 v229, v229, v75, v76\n" \
  "v_max3_u32 v229, v229, v77, v78\n" \
  "v_max3_u32 v229, v229, v79, v80\n" \
  "v_max3_u32 v229, v229, v81, v82\n" \
  "v_max3_u32 v229, v229, v83, v84\n" \
  "v_max3_u32 v229, v229, v85, v86\n" \
  "v_max3_u32 v229, v229, v87, v88\n" \
  "v_max3_u32 v229, v229, v89, v90\n" \
  "v_max3_u32 v229, v229, v91, v92\n" \
  "v_max3_u32 v229, v229, v93, v94\n" \
  "v_max3_u32 v229, v229, v95, v96\n" \
  "v_max3_u32 v229, v229, v97, v98\n" \
  "v_max_u32 v229, v229, v99\n" \
  "v_cmp_eq_u32 vcc, -1, v229\n" \
  "s_cbranch_vccz .Lqk_smw_ok\n" \
  "s_add_i32 s69, s69, 1\n" \
  "s_and_b32 s67, s69, 1023\n" \
  "s_cbranch_scc1 .Lqk_smw\n" \
  "buffer_load_dword v229, off, %[F], 0 offset:1024 sc1\n" \
  "s_waitcnt vmcnt(0)\n" \
  "v_readfirstlane_b32 s67, v229\n" \
  "s_cmp_lg_u32 s67, 0\n" \
  "s_cbranch_scc1 .Lqk_smw_ok\n" \
  "s_cmp_lt_u32 s69, 400000\n" \
  "s_cbranch_scc1 .Lqk_smw\n" \
  "v_mov_b32 v229, 1\n" \
  "buffer_store_dword v229, off, %[F], 0 offset:1024 sc1\n" \
  ".Lqk_smw_ok:\n" \
  "v_mov_b64 v[100:101], 0\n" \
  "v_mov_b64 v[102:103], 0\n" \
  "v_mov_b64 v[104:105], 0\n" \
  "v_mov_b64 v[106:107], 0\n" \
  "v_mov_b64 v[108:109], 0\n" \
  "v_mov_b64 v[110:111], 0\n" \
  "v_mov_b64 v[112:113], 0\n" \
  "v_mov_b64 v[114:115], 0\n" \
  "s_nop 1\n" \
  "v_smfmac_f32_16x16x64_bf16 v[100:103], v[64:67], a[0:7], %[idx]\n" \
  "v_smfmac_f32_16x16x64_bf16 v[104:107], v[64:67], a[8:15], %[idx]\n" \
  "v_smfmac_f32_16x16x64_bf16 v[108:111], v[64:67], a[16:23], %[idx]\n" \
  "v_smfmac_f32_16x16x64_bf16 v[112:115], v[64:67], a[24:31], %[idx]\n" \
  "v_smfmac_f32_16x16x64_bf16 v[100:103], v[68:71], a[32:39], %[idx]\n" \
  "v_smfmac_f32_16x16x64_bf16 v[104:107], v[68:71], a[40:47], %[idx]\n" \
  "v_smfmac_f32_16x16x64_bf16 v[108:111], v[68:71], a[48:55], %[idx]\n" \
  "v_smfmac_f32_16x16x64_bf16 v[112:115], v[68:71], a[56:63], %[idx]\n" \
  "v_smfmac_f32_16x16x64_bf16 v[100:103], v[72:75], a[64:71], %[idx]\n" \
  "v_smfmac_f32_16x16x64_bf16 v[104:107], v[72:75], a[72:79], %[idx]\n" \
  "v_smfmac_f32_16x16x64_bf16 v[108:111], v[72:75], a[80:87], %[idx]\n" \
  "v_smfmac_f32_16x16x64_bf16 v[112:115], v[72:75], a[88:95], %[idx]\n" \
  "v_smfmac_f32_16x16x64_bf16 v[100:103], v[76:79], a[96:103], %[idx]\n" \
  "v_smfmac_f32_16x16x64_bf16 v[104:107], v[76:79], a[104:111], %[idx]\n" \
  "v_smfmac_f32_16x16x64_bf16 v[108:111], v[76:79], a[112:119], %[idx]\n" \
  "v_smfmac_f32_16x16x64_bf16 v[112:115], v[76:79], a[120:127], %[idx]\n" \
  "v_smfmac_f32_16x16x64_bf16 v[100:103], v[80:83], a[128:135], %[idx]\n" \
  "v_smfmac_f32_16x16x64_bf16 v[104:107], v[80:83], a[136:143], %[idx]\n" \
  "v_smfmac_f32_16x16x64_bf16 v[108:111], v[80:83], a[144:151], %[idx]\n" \
  "v_smfmac_f32_16x16x64_bf16 v[112:115], v[80:83], a[152:159], %[idx]\n" \
  "v_smfmac_f32_16x16x64_bf16 v[100:103], v[84:87], a[160:167], %[idx]\n" \
  "v_smfmac_f32_16x16x64_bf16 v[104:107], v[84:87], a[168:175], %[idx]\n" \
  "v_smfmac_f32_16x16x64_bf16 v[108:111], v[84:87], a[176:183], %[idx]\n" \
  "v_smfmac_f32_16x16x64_bf16 v[112:115], v[84:87], a[184:191], %[idx]\n" \
  "v_smfmac_f32_16x16x64_bf16 v[100:103], v[88:91], a[192:199], %[idx]\n" \
  "v_smfmac_f32_16x16x64_bf16 v[104:107], v[88:91], a[200:207], %[idx]\n" \
  "v_smfmac_f32_16x16x64_bf16 v[108:111], v[88:91], a[208:215], %[idx]\n" \
  "v_smfmac_f32_16x16x64_bf16 v[112:115], v[88:91], a[216:223], %[idx]\n" \
  "v_smfmac_f32_16x16x64_bf16 v[100:103], v[92:95], a[224:231], %[idx]\n" \
  "v_smfmac_f32_16x16x64_bf16 v[104:107], v[92:95], a[232:239], %[idx]\n" \
  "v_smfmac_f32_16x16x64_bf16 v[108:111], v[92:95], a[240:247], %[idx]\n" \
  "v_smfmac_f32_16x16x64_bf16 v[112:115], v[92:95], a[248:255], %[idx]\n" \
  "v_smfmac_f32_16x16x64_bf16 v[100:103], v[96:99], v[32:39], %[idx]\n" \
  "v_smfmac_f32_16x16x64_bf16 v[104:107], v[96:99], v[40:47], %[idx]\n" \
  "v_smfmac_f32_16x16x64_bf16 v[108:111], v[96:99], v[48:55], %[idx]\n" \
  "v_smfmac_f32_16x16x64_bf16 v[112:115], v[96:99], v[56:63], %[idx]\n" \
  "s_nop 7\n" \
  "v_pk_add_f32 v[232:233], v[100:101], v[102:103]\n" \
  "v_pk_add_f32 v[234:235], v[104:105], v[106:107]\n" \
  "v_cvt_pk_bf16_f32 v236, v232, v233\n" \
  "v_cvt_pk_bf16_f32 v237, v234, v235\n" \
  "ds_write2st64_b32 %[qW], v236, v237 offset1:11\n" \
  "v_pk_add_f32 v[232:233], v[108:109], v[110:111]\n" \
  "v_pk_add_f32 v[234:235], v[112:113], v[114:115]\n" \
  "v_cvt_pk_bf16_f32 v236, v232, v233\n" \
  "v_cvt_pk_bf16_f32 v237, v234, v235\n" \
  "ds_write2st64_b32 %[kW], v236, v237 offset1:11\n" \
  "s_branch .Lqk_sm_back\n" \
  ".Lqk_late:\n" \
  "s_cmp_lt_i32 s60, 0\n" \
  "s_cbranch_scc1 .Lqk_al_ok\n" \
  "s_cmp_gt_u32 s91, -3000\n" \
  "s_cbranch_scc0 .Lqk_late2\n" \
  ".Lqk_wt:\n" \
  "s_memrealtime s[76:77]\n" \
  "s_waitcnt lgkmcnt(0)\n" \
  "s_sub_i32 s76, s76, s72\n" \
  "s_cmp_lt_i32 s76, -8\n" \
  "s_cbranch_scc1 .Lqk_wt\n" \
  "s_min_i32 s75, s76, -2\n" \
  "s_lshl_b32 s75, s75, 2\n" \
  "s_getpc_b64 s[76:77]\n" \
  "s_add_i32 s75, s75, 48\n" \
  "s_add_u32 s76, s76, s75\n" \
  "s_addc_u32 s77, s77, 0\n" \
  "s_setpc_b64 s[76:77]\n" \
  "s_nop 4\n" \
  "s_nop 4\n" \
  "s_nop 4\n" \
  "s_nop 4\n" \
  "s_nop 4\n" \
  "s_nop 4\n" \
  "s_branch .Lqk_al_ok\n" \
  ".Lqk_late2:\n" \
  "s_cmp_lg_u32 s72, 0\n" \
  "s_cbranch_scc1 .Lqk_late3\n" \
  "s_mov_b32 s69, 0\n" \
  ".Lqk_tw:\n" \
  "buffer_load_dword v229, off, %[X], s96 offset:128 sc1\n" \
  "s_waitcnt vmcnt(0)\n" \
  "v_readfirstlane_b32 s72, v229\n" \
  "s_cmp_lg_u32 s72, 0\n" \
  "s_cbranch_scc1 .Lqk_tw_ok\n" \
  "s_sleep 1\n" \
  "s_add_i32 s69, s69, 1\n" \
  "s_cmp_lt_u32 s69, 200\n" \
  "s_cbranch_scc1 .Lqk_tw\n" \
  ".Lqk_tw_ok:\n" \
  "s_sub_i32 s91, s74, s72\n" \
  "s_cmp_gt_u32 s91, -3000\n" \
  "s_cbranch_scc1 .Lqk_wt\n" \
  ".Lqk_late3:\n" \
  "s_branch .Lqk_al_ok\n" \
  ".Lqk_sh:\n" \
  "s_mul_i32 s75, s60, 36864\n" \
  "s_add_i32 s76, s75, 0x1000\n" \
  "s_add_i32 s77, s75, 0x2000\n" \
  "s_add_i32 s60, s60, -1\n" \
  "s_add_i32 s96, s96, -128\n" \
  "v_xor_b32 %[hA], 0x10000, %[hA]\n" \
  "s_waitcnt vmcnt(0) lgkmcnt(0)\n" \
  "s_barrier\n" \
  "s_mov_b32 s69, 0\n" \
  ".Lqk_shw:\n" \
  "buffer_load_dwordx4 v[64:67], %[hvo], %[HS], s75 offen offset:0 sc1\n" \
  "buffer_load_dwordx4 v[68:71], %[hvo], %[HS], s75 offen offset:1024 sc1\n" \
  "buffer_load_dwordx4 v[72:75], %[hvo], %[HS], s75 offen offset:2048 sc1\n" \
  "buffer_load_dwordx4 v[76:79], %[hvo], %[HS], s75 offen offset:3072 sc1\n" \
  "buffer_load_dwordx4 v[80:83], %[hvo], %[HS], s76 offen offset:0 sc1\n" \
  "buffer_load_dwordx4 v[84:87], %[hvo], %[HS], s76 offen offset:1024 sc1\n" \
  "buffer_load_dwordx4 v[88:91], %[hvo], %[HS], s76 offen offset:2048 sc1\n" \
  "buffer_load_dwordx4 v[92:95], %[hvo], %[HS], s76 offen offset:3072 sc1\n" \
  "buffer_load_dwordx4 v[96:99], %[hvo], %[HS], s77 offen offset:0 sc1\n" \
  "s_waitcnt vmcnt(0)\n" \
  "v_max3_u32 v229, v64, v65, v66\n" \
  "v_max3_u32 v229, v229, v67, v68\n" \
  "v_max3_u32 v229, v229, v69, v70\n" \
  "v_max3_u32 v229, v229, v71, v72\n" \
  "v_max3_u32 v229, v229, v73, v74\n" \
  "v_max3_u32 v229, v229, v75, v76\n" \
  "v_max3_u32 v229, v229, v77, v78\n" \
  "v_max3_u32 v229, v229, v79, v80\n" \
  "v_max3_u32 v229, v229, v81, v82\n" \
  "v_max3_u32 v229, v229, v83, v84\n" \
  "v_max3_u32 v229, v229, v85, v86\n" \
  "v_max3_u32 v229, v229, v87, v88\n" \
  "v_max3_u32 v229, v229, v89, v90\n" \
  "v_max3_u32 v229, v229, v91, v92\n" \
  "v_max3_u32 v229, v229, v93, v94\n" \
  "v_max3_u32 v229, v229, v95, v96\n" \
  "v_max3_u32 v229, v229, v97, v98\n" \
  "v_max_u32 v229, v229, v99\n" \
  "v_cmp_eq_u32 vcc, -1, v229\n" \
  "s_cbranch_vccz .Lqk_shw_ok\n" \
  "s_sleep 4\n" \
  "s_add_i32 s69, s69, 1\n" \
  "s_and_b32 s67, s69, 1023\n" \
  "s_cbranch_scc1 .Lqk_shw\n" \
  "buffer_load_dword v229, off, %[F], 0 offset:1024 sc1\n" \
  "s_waitcnt vmcnt(0)\n" \
  "v_readfirstlane_b32 s67, v229\n" \
  "s_cmp_lg_u32 s67, 0\n" \
  "s_cbranch_scc1 .Lqk_shw_ok\n" \
  "s_cmp_lt_u32 s69, 400000\n" \
  "s_cbranch_scc1 .Lqk_shw\n" \
  "v_mov_b32 v229, 1\n" \
  "buffer_store_dword v229, off, %[F], 0 offset:1024 sc1\n" \
  ".Lqk_shw_ok:\n" \
  "s_xor_b32 s67, s62, 0x10000\n" \
  "v_add_u32 v229, s67, %[mo]\n" \
  "ds_write_b128 v229, v[64:67] offset:0\n" \
  "ds_write_b128 v229, v[68:71] offset:1024\n" \
  "ds_write_b128 v229, v[72:75] offset:2048\n" \
  "ds_write_b128 v229, v[76:79] offset:3072\n" \
  "ds_write_b128 v229, v[80:83] offset:4096\n" \
  "ds_write_b128 v229, v[84:87] offset:5120\n" \
  "ds_write_b128 v229, v[88:91] offset:6144\n" \
  "ds_write_b128 v229, v[92:95] offset:7168\n" \
  "ds_write_b128 v229, v[96:99] offset:8192\n" \
  "s_waitcnt lgkmcnt(0)\n" \
  "s_barrier\n" \
  "ds_read_b128 v[148:151], %[hA] offset:0\n" \
  "ds_read_b128 v[152:155], %[hA] offset:18432\n" \
  "ds_read_b128 v[156:159], %[hA] offset:512\n" \
  "ds_read_b128 v[160:163], %[hA] offset:18944\n" \
  "ds_read_b128 v[164:167], %[hA] offset:1024\n" \
  "ds_read_b128 v[168:171], %[hA] offset:19456\n" \
  "ds_read_b128 v[172:175], %[hA] offset:1536\n" \
  "ds_read_b128 v[176:179], %[hA] offset:19968\n" \
  "s_waitcnt lgkmcnt(0)\n" \
  "v_mfma_f32_16x16x32_bf16 v[116:119], v[148:151], a[0:3], 0\n" \
  "v_mfma_f32_16x16x32_bf16 v[132:135], v[152:155], a[0:3], 0\n" \
  "v_mfma_f32_16x16x32_bf16 v[120:123], v[148:151], a[8:11], 0\n" \
  "v_mfma_f32_16x16x32_bf16 v[136:139], v[152:155], a[8:11], 0\n" \
  "v_mfma_f32_16x16x32_bf16 v[124:127], a[16:19], v[148:151], 0\n" \
  "v_mfma_f32_16x16x32_bf16 v[140:143], a[16:19], v[152:155], 0\n" \
  "v_mfma_f32_16x16x32_bf16 v[128:131], a[24:27], v[148:151], 0\n" \
  "v_mfma_f32_16x16x32_bf16 v[144:147], a[24:27], v[152:155], 0\n" \
  "v_mfma_f32_16x16x32_bf16 v[116:119], v[156:159], a[4:7], v[116:119]\n" \
  "v_mfma_f32_16x16x32_bf16 v[132:135], v[160:163], a[4:7], v[132:135]\n" \
  "v_mfma_f32_16x16x32_bf16 v[120:123], v[156:159], a[12:15], v[120:123]\n" \
  "v_mfma_f32_16x16x32_bf16 v[136:139], v[160:163], a[12:15], v[136:139]\n" \
  "v_mfma_f32_16x16x32_bf16 v[124:127], a[20:23], v[156:159], v[124:127]\n" \
  "v_mfma_f32_16x16x32_bf16 v[140:143], a[20:23], v[160:163], v[140:143]\n" \
  "v_mfma_f32_16x16x32_bf16 v[128:131], a[28:31], v[156:159], v[128:131]\n" \
  "v_mfma_f32_16x16x32_bf16 v[144:147], a[28:31], v[160:163], v[144:147]\n" \
  "v_mfma_f32_16x16x32_bf16 v[116:119], v[164:167], a[32:35], v[116:119]\n" \
  "ds_read_b128 v[148:151], %[hA] offset:2048\n" \
  "v_mfma_f32_16x16x32_bf16 v[132:135], v[168:171], a[32:35], v[132:135]\n" \
  "ds_read_b128 v[152:155], %[hA] offset:20480\n" \
  "v_mfma_f32_16x16x32_bf16 v[120:123], v[164:167], a[40:43], v[120:123]\n" \
  "v_mfma_f32_16x16x32_bf16 v[136:139], v[168:171], a[40:43], v[136:139]\n" \
  "v_mfma_f32_16x16x32_bf16 v[124:127], a[48:51], v[164:167], v[124:127]\n" \
  "v_mfma_f32_16x16x32_bf16 v[140:143], a[48:51], v[168:171], v[140:143]\n" \
  "v_mfma_f32_16x16x32_bf16 v[128:131], a[56:59], v[164:167], v[128:131]\n" \
  "v_mfma_f32_16x16x32_bf16 v[144:147], a[56:59], v[168:171], v[144:147]\n" \
  "v_mfma_f32_16x16x32_bf16 v[116:119], v[172:175], a[36:39], v[116:119]\n" \
  "ds_read_b128 v[156:159], %[hA] offset:2560\n" \
  "v_mfma_f32_16x16x32_bf16 v[132:135], v[176:179], a[36:39], v[132:135]\n" \
  "ds_read_b128 v[160:163], %[hA] offset:20992\n" \
  "v_mfma_f32_16x16x32_bf16 v[120:123], v[172:175], a[44:47], v[120:123]\n" \
  "v_mfma_f32_16x16x32_bf16 v[136:139], v[176:179], a[44:47], v[136:139]\n" \
  "v_mfma_f32_16x16x32_bf16 v[124:127], a[52:55], v[172:175], v[124:127]\n" \
  "v_mfma_f32_16x16x32_bf16 v[140:143], a[52:55], v[176:179], v[140:143]\n" \
  "v_mfma_f32_16x16x32_bf16 v[128:131], a[60:63], v[172:175], v[128:131]\n" \
  "v_mfma_f32_16x16x32_bf16 v[144:147], a[60:63], v[176:179], v[144:147]\n" \
  "s_waitcnt lgkmcnt(2)\n" \
  "v_mfma_f32_16x16x32_bf16 v[116:119], v[148:151], a[64:67], v[116:119]\n" \
  "ds_read_b128 v[164:167], %[hA] offset:3072\n" \
  "v_mfma_f32_16x16x32_bf16 v[132:135], v[152:155], a[64:67], v[132:135]\n" \
  "ds_read_b128 v[168:171], %[hA] offset:21504\n" \
  "v_mfma_f32_16x16x32_bf16 v[120:123], v[148:151], a[72:75], v[120:123]\n" \
  "v_mfma_f32_16x16x32_bf16 v[136:139], v[152:155], a[72:75], v[136:139]\n" \
  "v_mfma_f32_16x16x32_bf16 v[124:127], a[80:83], v[148:151], v[124:127]\n" \
  "v_mfma_f32_16x16x32_bf16 v[140:143], a[80:83], v[152:155], v[140:143]\n" \
  "v_mfma_f32_16x16x32_bf16 v[128:131], a[88:91], v[148:151], v[128:131]\n" \
  "v_mfma_f32_16x16x32_bf16 v[144:147], a[88:91], v[152:155], v[144:147]\n" \
  "s_waitcnt lgkmcnt(2)\n" \
  "v_mfma_f32_16x16x32_bf16 v[116:119], v[156:159], a[68:71], v[116:119]\n" \
  "ds_read_b128 v[172:175], %[hA] offset:3584\n" \
  "v_mfma_f32_16x16x32_bf16 v[132:135], v[160:163], a[68:71], v[132:135]\n" \
  "ds_read_b128 v[176:179], %[hA] offset:22016\n" \
  "v_mfma_f32_16x16x32_bf16 v[120:123], v[156:159], a[76:79], v[120:123]\n" \
  "v_mfma_f32_16x16x32_bf16 v[136:139], v[160:163], a[76:79], v[136:139]\n" \
  "v_mfma_f32_16x16x32_bf16 v[124:127], a[84:87], v[156:159], v[124:127]\n" \
  "v_mfma_f32_16x16x32_bf16 v[140:143], a[84:87], v[160:163], v[140:143]\n" \
  "v_mfma_f32_16x16x32_bf16 v[128:131], a[92:95], v[156:159], v[128:131]\n" \
  "v_mfma_f32_16x16x32_bf16 v[144:147], a[92:95], v[160:163], v[144:147]\n" \
  "s_waitcnt lgkmcnt(2)\n" \
  "v_mfma_f32_16x16x32_bf16 v[116:119], v[164:167], a[96:99], v[116:119]\n" \
  "ds_read_b128 v[148:151], %[hA] offset:4096\n" \
  "v_mfma_f32_16x16x32_bf16 v[132:135], v[168:171], a[96:99], v[132:135]\n" \
  "ds_read_b128 v[152:155], %[hA] offset:22528\n" \
  "v_mfma_f32_16x16x32_bf16 v[120:123], v[164:167], a[104:107], v[120:123]\n" \
  "v_mfma_f32_16x16x32_bf16 v[136:139], v[168:171], a[104:107], v[136:139]\n" \
  "v_mfma_f32_16x16x32_bf16 v[124:127], a[112:115], v[164:167], v[124:127]\n" \
  "v_mfma_f32_16x16x32_bf16 v[140:143], a[112:115], v[168:171], v[140:143]\n" \
  "v_mfma_f32_16x16x32_bf16 v[128:131], a[120:123], v[164:167], v[128:131]\n" \
  "v_mfma_f32_16x16x32_bf16 v[144:147], a[120:123], v[168:171], v[144:147]\n" \
  "s_waitcnt lgkmcnt(2)\n" \
  "v_mfma_f32_16x16x32_bf16 v[116:119], v[172:175], a[100:103], v[116:119]\n" \
  "ds_read_b128 v[156:159], %[hA] offset:4608\n" \
  "v_mfma_f32_16x16x32_bf16 v[132:135], v[176:179], a[100:103], v[132:135]\n" \
  "ds_read_b128 v[160:163], %[hA] offset:23040\n" \
  "v_mfma_f32_16x16x32_bf16 v[120:123], v[172:175], a[108:111], v[120:123]\n" \
  "v_mfma_f32_16x16x32_bf16 v[136:139], v[176:179], a[108:111], v[136:139]\n" \
  "v_mfma_f32_16x16x32_bf16 v[124:127], a[116:119], v[172:175], v[124:127]\n" \
  "v_mfma_f32_16x16x32_bf16 v[140:143], a[116:119], v[176:179], v[140:143]\n" \
  "v_mfma_f32_16x16x32_bf16 v[128:131], a[124:127], v[172:175], v[128:131]\n" \
  "v_mfma_f32_16x16x32_bf16 v[144:147], a[124:127], v[176:179], v[144:147]\n" \
  "s_waitcnt lgkmcnt(2)\n" \
  "v_mfma_f32_16x16x32_bf16 v[116:119], v[148:151], a[128:131], v[116:119]\n" \
  "ds_read_b128 v[164:167], %[hA] offset:5120\n" \
  "v_mfma_f32_16x16x32_bf16 v[132:135], v[152:155], a[128:131], v[132:135]\n" \
  "ds_read_b128 v[168:171], %[hA] offset:23552\n" \
  "v_mfma_f32_16x16x32_bf16 v[120:123], v[148:151], a[136:139], v[120:123]\n" \
  "v_mfma_f32_16x16x32_bf16 v[136:139], v[152:155], a[136:139], v[136:139]\n" \
  "v_mfma_f32_16x16x32_bf16 v[124:127], a[144:147], v[148:151], v[124:127]\n" \
  "v_mfma_f32_16x16x32_bf16 v[140:143], a[144:147], v[152:155], v[140:143]\n" \
  "v_mfma_f32_16x16x32_bf16 v[128:131], a[152:155], v[148:151], v[128:131]\n" \
  "v_mfma_f32_16x16x32_bf16 v[144:147], a[152:155], v[152:155], v[144:147]\n" \
  "s_waitcnt lgkmcnt(2)\n" \
  "v_mfma_f32_16x16x32_bf16 v[116:119], v[156:159], a[132:135], v[116:119]\n" \
  "ds_read_b128 v[172:175], %[hA] offset:5632\n" \
  "v_mfma_f32_16x16x32_bf16 v[132:135], v[160:163], a[132:135], v[132:135]\n" \
  "ds_read_b128 v[176:179], %[hA] offset:24064\n" \
  "v_mfma_f32_16x16x32_bf16 v[120:123], v[156:159], a[140:143], v[120:123]\n" \
  "v_mfma_f32_16x16x32_bf16 v[136:139], v[160:163], a[140:143], v[136:139]\n" \
  "v_mfma_f32_16x16x32_bf16 v[124:127], a[148:151], v[156:159], v[124:127]\n" \
  "v_mfma_f32_16x16x32_bf16 v[140:143], a[148:151], v[160:163], v[140:143]\n" \
  "v_mfma_f32_16x16x32_bf16 v[128:131], a[156:159], v[156:159], v[128:131]\n" \
  "v_mfma_f32_16x16x32_bf16 v[144:147], a[156:159], v[160:163], v[144:147]\n" \
  "s_waitcnt lgkmcnt(2)\n" \
  "v_mfma_f32_16x16x32_bf16 v[116:119], v[164:167], a[160:163], v[116:119]\n" \
  "ds_read_b128 v[148:151], %[hA] offset:6144\n" \
  "v_mfma_f32_16x16x32_bf16 v[132:135], v[168:171], a[160:163], v[132:135]\n" \
  "ds_read_b128 v[152:155], %[hA] offset:24576\n" \
  "v_mfma_f32_16x16x32_bf16 v[120:123], v[164:167], a[168:171], v[120:123]\n" \
  "v_mfma_f32_16x16x32_bf16 v[136:139], v[168:171], a[168:171], v[136:139]\n" \
  "v_mfma_f32_16x16x32_bf16 v[124:127], a[176:179], v[164:167], v[124:127]\n" \
  "v_mfma_f32_16x16x32_bf16 v[140:143], a[176:179], v[168:171], v[140:143]\n" \
  "v_mfma_f32_16x16x32_bf16 v[128:131], a[184:187], v[164:167], v[128:131]\n" \
  "v_mfma_f32_16x16x32_bf16 v[144:147], a[184:187], v[168:171], v[144:147]\n" \
  "s_waitcnt lgkmcnt(2)\n" \
  "v_mfma_f32_16x16x32_bf16 v[116:119], v[172:175], a[164:167], v[116:119]\n" \
  "ds_read_b128 v[156:159], %[hA] offset:6656\n" \
  "v_mfma_f32_16x16x32_bf16 v[132:135], v[176:179], a[164:167], v[132:135]\n" \
  "ds_read_b128 v[160:163], %[hA] offset:25088\n" \
  "v_mfma_f32_16x16x32_bf16 v[120:123], v[172:175], a[172:175], v[120:123]\n" \
  "v_mfma_f32_16x16x32_bf16 v[136:139], v[176:179], a[172:175], v[136:139]\n" \
  "v_mfma_f32_16x16x32_bf16 v[124:127], a[180:183], v[172:175], v[124:127]\n" \
  "v_mfma_f32_16x16x32_bf16 v[140:143], a[180:183], v[176:179], v[140:143]\n" \
  "v_mfma_f32_16x16x32_bf16 v[128:131], a[188:191], v[172:175], v[128:131]\n" \
  "v_mfma_f32_16x16x32_bf16 v[144:147], a[188:191], v[176:179], v[144:147]\n" \
  "s_waitcnt lgkmcnt(2)\n" \
  "v_mfma_f32_16x16x32_bf16 v[116:119], v[148:151], a[192:195], v[116:119]\n" \
  "ds_read_b128 v[164:167], %[hA] offset:7168\n" \
  "v_mfma_f32_16x16x32_bf16 v[132:135], v[152:155], a[192:195], v[132:135]\n" \
  "ds_read_b128 v[168:171], %[hA] offset:25600\n" \
  "v_mfma_f32_16x16x32_bf16 v[120:123], v[148:151], a[200:203], v[120:123]\n" \
  "v_mfma_f32_16x16x32_bf16 v[136:139], v[152:155], a[200:203], v[136:139]\n" \
  "v_mfma_f32_16x16x32_bf16 v[124:127], a[208:211], v[148:151], v[124:127]\n" \
  "v_mfma_f32_16x16x32_bf16 v[140:143], a[208:211], v[152:155], v[140:143]\n" \
  "v_mfma_f32_16x16x32_bf16 v[128:131], a[216:219], v[148:151], v[128:131]\n" \
  "v_mfma_f32_16x16x32_bf16 v[144:147], a[216:219], v[152:155], v[144:147]\n" \
  "s_waitcnt lgkmcnt(2)\n" \
  "v_mfma_f32_16x16x32_bf16 v[116:119], v[156:159], a[196:199], v[116:119]\n" \
  "ds_read_b128 v[172:175], %[hA] offset:7680\n" \
  "v_mfma_f32_16x16x32_bf16 v[132:135], v[160:163], a[196:199], v[132:135]\n" \
  "ds_read_b128 v[176:179], %[hA] offset:26112\n" \
  "v_mfma_f32_16x16x32_bf16 v[120:123], v[156:159], a[204:207], v[120:123]\n" \
  "ds_read_b128 v[148:151], %[hA] offset:8192\n" \
  "v_mfma_f32_16x16x32_bf16 v[136:139], v[160:163], a[204:207], v[136:139]\n" \
  "ds_read_b128 v[152:155], %[hA] offset:26624\n" \
  "v_mfma_f32_16x16x32_bf16 v[124:127], a[212:215], v[156:159], v[124:127]\n" \
  "v_mfma_f32_16x16x32_bf16 v[140:143], a[212:215], v[160:163], v[140:143]\n" \
  "v_mfma_f32_16x16x32_bf16 v[128:131], a[220:223], v[156:159], v[128:131]\n" \
  "v_mfma_f32_16x16x32_bf16 v[144:147], a[220:223], v[160:163], v[144:147]\n" \
  "s_waitcnt lgkmcnt(4)\n" \
  "v_mfma_f32_16x16x32_bf16 v[124:127], a[240:243], v[164:167], v[124:127]\n" \
  "ds_read_b128 v[156:159], %[hA] offset:8704\n" \
  "v_mfma_f32_16x16x32_bf16 v[140:143], a[240:243], v[168:171], v[140:143]\n" \
  "ds_read_b128 v[160:163], %[hA] offset:27136\n" \
  "v_mfma_f32_16x16x32_bf16 v[128:131], a[248:251], v[164:167], v[128:131]\n" \
  "v_mfma_f32_16x16x32_bf16 v[144:147], a[248:251], v[168:171], v[144:147]\n" \
  "s_waitcnt lgkmcnt(4)\n" \
  "v_mfma_f32_16x16x32_bf16 v[124:127], a[244:247], v[172:175], v[124:127]\n" \
  "v_mfma_f32_16x16x32_bf16 v[140:143], a[244:247], v[176:179], v[140:143]\n" \
  "v_mfma_f32_16x16x32_bf16 v[128:131], a[252:255], v[172:175], v[128:131]\n" \
  "v_mfma_f32_16x16x32_bf16 v[144:147], a[252:255], v[176:179], v[144:147]\n" \
  "s_waitcnt lgkmcnt(2)\n" \
  "v_mfma_f32_16x16x32_bf16 v[124:127], v[48:51], v[148:151], v[124:127]\n" \
  "v_mfma_f32_16x16x32_bf16 v[140:143], v[48:51], v[152:155], v[140:143]\n" \
  "v_mfma_f32_16x16x32_bf16 v[128:131], v[56:59], v[148:151], v[128:131]\n" \
  "v_mfma_f32_16x16x32_bf16 v[144:147], v[56:59], v[152:155], v[144:147]\n" \
  "s_waitcnt lgkmcnt(0)\n" \
  "v_mfma_f32_16x16x32_bf16 v[124:127], v[52:55], v[156:159], v[124:127]\n" \
  "v_mfma_f32_16x16x32_bf16 v[140:143], v[52:55], v[160:163], v[140:143]\n" \
  "v_mfma_f32_16x16x32_bf16 v[128:131], v[60:63], v[156:159], v[128:131]\n" \
  "v_mfma_f32_16x16x32_bf16 v[144:147], v[60:63], v[160:163], v[144:147]\n" \
  "v_mfma_f32_16x16x32_bf16 v[116:119], v[164:167], a[224:227], v[116:119]\n" \
  "v_mfma_f32_16x16x32_bf16 v[132:135], v[168:171], a[224:227], v[132:135]\n" \
  "v_mfma_f32_16x16x32_bf16 v[120:123], v[164:167], a[232:235], v[120:123]\n" \
  "v_mfma_f32_16x16x32_bf16 v[136:139], v[168:171], a[232:235], v[136:139]\n" \
  "v_mfma_f32_16x16x32_bf16 v[116:119], v[172:175], a[228:231], v[116:119]\n" \
  "v_mfma_f32_16x16x32_bf16 v[132:135], v[176:179], a[228:231], v[132:135]\n" \
  "v_mfma_f32_16x16x32_bf16 v[120:123], v[172:175], a[236:239], v[120:123]\n" \
  "v_mfma_f32_16x16x32_bf16 v[136:139], v[176:179], a[236:239], v[136:139]\n" \
  "v_mfma_f32_16x16x32_bf16 v[116:119], v[148:151], v[32:35], v[116:119]\n" \
  "v_mfma_f32_16x16x32_bf16 v[132:135], v[152:155], v[32:35], v[132:135]\n" \
  "v_mfma_f32_16x16x32_bf16 v[120:123], v[148:151], v[40:43], v[120:123]\n" \
  "v_mfma_f32_16x16x32_bf16 v[136:139], v[152:155], v[40:43], v[136:139]\n" \
  "v_mfma_f32_16x16x32_bf16 v[116:119], v[156:159], v[36:39], v[116:119]\n" \
  "v_mfma_f32_16x16x32_bf16 v[132:135], v[160:163], v[36:39], v[132:135]\n" \
  "v_mfma_f32_16x16x32_bf16 v[120:123], v[156:159], v[44:47], v[120:123]\n" \
  "v_mfma_f32_16x16x32_bf16 v[136:139], v[160:163], v[44:47], v[136:139]\n" \
  "buffer_load_dwordx4 v[64:67], %[mo], %[X], s63 offen offset:0 sc1\n" \
  "buffer_load_dwordx4 v[68:71], %[mo], %[X], s63 offen offset:1024 sc1\n" \
  "buffer_load_dwordx4 v[72:75], %[mo], %[X], s63 offen offset:2048 sc1\n" \
  "buffer_load_dwordx4 v[76:79], %[mo], %[X], s63 offen offset:3072 sc1\n" \
  "buffer_load_dwordx4 v[80:83], v228, %[X], s63 offen offset:0 sc1\n" \
  "buffer_load_dwordx4 v[84:87], v228, %[X], s63 offen offset:1024 sc1\n" \
  "buffer_load_dwordx4 v[88:91], v228, %[X], s63 offen offset:2048 sc1\n" \
  "buffer_load_dwordx4 v[92:95], v228, %[X], s63 offen offset:3072 sc1\n" \
  "buffer_load_dwordx4 v[96:99], %[mo], %[X], s64 offen offset:0 sc1\n" \
  "v_mov_b64 v[100:101], 0\n" \
  "v_mov_b64 v[102:103], 0\n" \
  "v_mov_b64 v[104:105], 0\n" \
  "v_mov_b64 v[106:107], 0\n" \
  "v_mov_b64 v[108:109], 0\n" \
  "v_mov_b64 v[110:111], 0\n" \
  "v_mov_b64 v[112:113], 0\n" \
  "v_mov_b64 v[114:115], 0\n" \
  "v_cvt_pk_bf16_f32 v220, v124, v125\n" \
  "v_cvt_pk_bf16_f32 v221, v126, v127\n" \
  "v_cvt_pk_bf16_f32 v222, v128, v129\n" \
  "v_cvt_pk_bf16_f32 v223, v130, v131\n" \
  "v_cvt_pk_bf16_f32 v224, v140, v141\n" \
  "v_cvt_pk_bf16_f32 v225, v142, v143\n" \
  "v_cvt_pk_bf16_f32 v226, v144, v145\n" \
  "v_cvt_pk_bf16_f32 v227, v146, v147\n" \
  "ds_write_b128 %[ktW], v[220:223] offset:0\n" \
  "ds_write_b128 %[ktW], v[224:227] offset:4096\n" \
  "v_xor_b32 %[hA], 0x10000, %[hA]\n" \
  "v_mov_b32 v231, 0\n" \
  "s_waitcnt lgkmcnt(0)\n" \
  "s_barrier\n" \
  "ds_read_b128 v[180:183], %[ktR] offset:0\n" \
  "ds_read_b128 v[184:187], %[ktR] offset:1024\n" \
  "ds_read_b128 v[188:191], %[ktR] offset:2048\n" \
  "ds_read_b128 v[192:195], %[ktR] offset:3072\n" \
  "ds_read_b128 v[196:199], %[ktR] offset:4096\n" \
  "ds_read_b128 v[200:203], %[ktR] offset:5120\n" \
  "ds_read_b128 v[204:207], %[ktR] offset:6144\n" \
  "ds_read_b128 v[208:211], %[ktR] offset:7168\n" \
  "s_waitcnt lgkmcnt(0)\n" \
  "s_branch .Lqk_m\n" \
  ".Lqk_entry:\n" \
  "s_mov_b32 s81, 63\n" \
  "s_mov_b32 s87, %[mr0]\n" \
  "s_cmp_eq_u32 %[grp], 0\n" \
  "s_cbranch_scc1 .Lqk_go\n" \
  "s_mov_b32 s69, 0\n" \
  "s_xor_b32 s75, %[bas], 0x6000\n" \
  ".Lqk_g1:\n" \
  "buffer_load_dword v229, off, %[X], s75 offset:384 sc1\n" \
  "s_waitcnt vmcnt(0)\n" \
  "v_readfirstlane_b32 s74, v229\n" \
  "s_cmp_lg_u32 s74, 0\n" \
  "s_cbranch_scc1 .Lqk_g2\n" \
  "s_sleep 8\n" \
  "s_add_i32 s69, s69, 1\n" \
  "s_cmp_lt_u32 s69, 400\n" \
  "s_cbranch_scc1 .Lqk_g1\n" \
  ".Lqk_g2:\n" \
  "s_add_i32 s74, s74, %[g1t]\n" \
  ".Lqk_g3:\n" \
  "s_memrealtime s[76:77]\n" \
  "s_waitcnt lgkmcnt(0)\n" \
  "s_sub_i32 s76, s76, s74\n" \
  "s_cmp_gt_u32 s76, -3000\n" \
  "s_cbranch_scc1 .Lqk_g3\n" \
  ".Lqk_go:\n" \
  "s_waitcnt vmcnt(0) lgkmcnt(0)\n" \
  "s_barrier\n" \
  "s_mov_b32 s75, 0\n" \
  "s_movk_i32 s76, 0x1000\n" \
  "s_movk_i32 s77, 0x2000\n" \
  "s_mov_b32 s69, 0\n" \
  ".Lqk_enw:\n" \
  "buffer_load_dwordx4 v[64:67], %[hvo], %[HS], s75 offen offset:0 sc1\n" \
  "buffer_load_dwordx4 v[68:71], %[hvo], %[HS], s75 offen offset:1024 sc1\n" \
  "buffer_load_dwordx4 v[72:75], %[hvo], %[HS], s75 offen offset:2048 sc1\n" \
  "buffer_load_dwordx4 v[76:79], %[hvo], %[HS], s75 offen offset:3072 sc1\n" \
  "buffer_load_dwordx4 v[80:83], %[hvo], %[HS], s76 offen offset:0 sc1\n" \
  "buffer_load_dwordx4 v[84:87], %[hvo], %[HS], s76 offen offset:1024 sc1\n" \
  "buffer_load_dwordx4 v[88:91], %[hvo], %[HS], s76 offen offset:2048 sc1\n" \
  "buffer_load_dwordx4 v[92:95], %[hvo], %[HS], s76 offen offset:3072 sc1\n" \
  "buffer_load_dwordx4 v[96:99], %[hvo], %[HS], s77 offen offset:0 sc1\n" \
  "s_waitcnt vmcnt(0)\n" \
  "v_max3_u32 v229, v64, v65, v66\n" \
  "v_max3_u32 v229, v229, v67, v68\n" \
  "v_max3_u32 v229, v229, v69, v70\n" \
  "v_max3_u32 v229, v229, v71, v72\n" \
  "v_max3_u32 v229, v229, v73, v74\n" \
  "v_max3_u32 v229, v229, v75, v76\n" \
  "v_max3_u32 v229, v229, v77, v78\n" \
  "v_max3_u32 v229, v229, v79, v80\n" \
  "v_max3_u32 v229, v229, v81, v82\n" \
  "v_max3_u32 v229, v229, v83, v84\n" \
  "v_max3_u32 v229, v229, v85, v86\n" \
  "v_max3_u32 v229, v229, v87, v88\n" \
  "v_max3_u32 v229, v229, v89, v90\n" \
  "v_max3_u32 v229, v229, v91, v92\n" \
  "v_max3_u32 v229, v229, v93, v94\n" \
  "v_max3_u32 v229, v229, v95, v96\n" \
  "v_max3_u32 v229, v229, v97, v98\n" \
  "v_max_u32 v229, v229, v99\n" \
  "v_cmp_eq_u32 vcc, -1, v229\n" \
  "s_cbranch_vccz .Lqk_enw_ok\n" \
  "s_sleep 4\n" \
  "s_add_i32 s69, s69, 1\n" \
  "s_and_b32 s67, s69, 1023\n" \
  "s_cbranch_scc1 .Lqk_enw\n" \
  "buffer_load_dword v229, off, %[F], 0 offset:1024 sc1\n" \
  "s_waitcnt vmcnt(0)\n" \
  "v_readfirstlane_b32 s67, v229\n" \
  "s_cmp_lg_u32 s67, 0\n" \
  "s_cbranch_scc1 .Lqk_enw_ok\n" \
  "s_cmp_lt_u32 s69, 400000\n" \
  "s_cbranch_scc1 .Lqk_enw\n" \
  "v_mov_b32 v229, 1\n" \
  "buffer_store_dword v229, off, %[F], 0 offset:1024 sc1\n" \
  ".Lqk_enw_ok:\n" \
  "ds_write_b128 %[hvo], v[64:67] offset:0\n" \
  "ds_write_b128 %[hvo], v[68:71] offset:1024\n" \
  "ds_write_b128 %[hvo], v[72:75] offset:2048\n" \
  "ds_write_b128 %[hvo], v[76:79] offset:3072\n" \
  "ds_write_b128 %[hvo], v[80:83] offset:4096\n" \
  "ds_write_b128 %[hvo], v[84:87] offset:5120\n" \
  "ds_write_b128 %[hvo], v[88:91] offset:6144\n" \
  "ds_write_b128 %[hvo], v[92:95] offset:7168\n" \
  "ds_write_b128 %[hvo], v[96:99] offset:8192\n" \
  "s_mov_b32 s90, 0\n" \
  "s_mov_b32 s78, 0\n" \
  "s_waitcnt lgkmcnt(0)\n" \
  "s_barrier\n" \
  "v_mov_b32 v236, 1\n" \
  "s_add_i32 s76, %[bas], -384\n" \
  "buffer_atomic_add v236, %[rdy], %[X], s76 offen sc1\n" \
  "ds_read_b128 v[148:151], %[hA] offset:0\n" \
  "ds_read_b128 v[152:155], %[hA] offset:18432\n" \
  "ds_read_b128 v[156:159], %[hA] offset:512\n" \
  "ds_read_b128 v[160:163], %[hA] offset:18944\n" \
  "ds_read_b128 v[164:167], %[hA] offset:1024\n" \
  "ds_read_b128 v[168:171], %[hA] offset:19456\n" \
  "ds_read_b128 v[172:175], %[hA] offset:1536\n" \
  "ds_read_b128 v[176:179], %[hA] offset:19968\n" \
  "s_waitcnt lgkmcnt(0)\n" \
  "v_mfma_f32_16x16x32_bf16 v[116:119], v[148:151], a[0:3], 0\n" \
  "v_mfma_f32_16x16x32_bf16 v[132:135], v[152:155], a[0:3], 0\n" \
  "v_mfma_f32_16x16x32_bf16 v[120:123], v[148:151], a[8:11], 0\n" \
  "v_mfma_f32_16x16x32_bf16 v[136:139], v[152:155], a[8:11], 0\n" \
  "v_mfma_f32_16x16x32_bf16 v[124:127], a[16:19], v[148:151], 0\n" \
  "v_mfma_f32_16x16x32_bf16 v[140:143], a[16:19], v[152:155], 0\n" \
  "v_mfma_f32_16x16x32_bf16 v[128:131], a[24:27], v[148:151], 0\n" \
  "v_mfma_f32_16x16x32_bf16 v[144:147], a[24:27], v[152:155], 0\n" \
  "v_mfma_f32_16x16x32_bf16 v[116:119], v[156:159], a[4:7], v[116:119]\n" \
  "v_mfma_f32_16x16x32_bf16 v[132:135], v[160:163], a[4:7], v[132:135]\n" \
  "v_mfma_f32_16x16x32_bf16 v[120:123], v[156:159], a[12:15], v[120:123]\n" \
  "v_mfma_f32_16x16x32_bf16 v[136:139], v[160:163], a[12:15], v[136:139]\n" \
  "v_mfma_f32_16x16x32_bf16 v[124:127], a[20:23], v[156:159], v[124:127]\n" \
  "v_mfma_f32_16x16x32_bf16 v[140:143], a[20:23], v[160:163], v[140:143]\n" \
  "v_mfma_f32_16x16x32_bf16 v[128:131], a[28:31], v[156:159], v[128:131]\n" \
  "v_mfma_f32_16x16x32_bf16 v[144:147], a[28:31], v[160:163], v[144:147]\n" \
  "s_waitcnt lgkmcnt(0)\n" \
  "s_branch .Lqk_top\n" \
  ".Lqk_end:\n"
#define QK_CLOB "v32", "v33", "v34", "v35", "v36", "v37", "v38", "v39", "v40", "v41", "v42", "v43", "v44", "v45", "v46", "v47", "v48", "v49", "v50", "v51", "v52", "v53", "v54", "v55", "v56", "v57", "v58", "v59", "v60", "v61", "v62", "v63", "v64", "v65", "v66", "v67", "v68", "v69", "v70", "v71", "v72", "v73", "v74", "v75", "v76", "v77", "v78", "v79", "v80", "v81", "v82", "v83", "v84", "v85", "v86", "v87", "v88", "v89", "v90", "v91", "v92", "v93", "v94", "v95", "v96", "v97", "v98", "v99", "v100", "v101", "v102", "v103", "v104", "v105", "v106", "v107", "v108", "v109", "v110", "v111", "v112", "v113", "v114", "v115", "v116", "v117", "v118", "v119", "v120", "v121", "v122", "v123", "v124", "v125", "v126", "v127", "v128", "v129", "v130", "v131", "v132", "v133", "v134", "v135", "v136", "v137", "v138", "v139", "v140", "v141", "v142", "v143", "v144", "v145", "v146", "v147", "v148", "v149", "v150", "v151", "v152", "v153", "v154", "v155", "v156", "v157", "v158", "v159", "v160", "v161", "v162", "v163", "v164", "v165", "v166", "v167", "v168", "v169", "v170", "v171", "v172", "v173", "v174", "v175", "v176", "v177", "v178", "v179", "v180", "v181", "v182", "v183", "v184", "v185", "v186", "v187", "v188", "v189", "v190", "v191", "v192", "v193", "v194", "v195", "v196", "v197", "v198", "v199", "v200", "v201", "v202", "v203", "v204", "v205", "v206", "v207", "v208", "v209", "v210", "v211", "v212", "v213", "v214", "v215", "v216", "v217", "v218", "v219", "v220", "v221", "v222", "v223", "v224", "v225", "v226", "v227", "v228", "v229", "v230", "v231", "v232", "v233", "v234", "v235", "v236", "v237", "v238", "v239", "v240", "v241", "v242", "v243", "v244", "v245", "v246", "v247", "v248", "v249", "v250", "v251", "v252", "v253", "v254", "v255", "s60", "s61", "s62", "s63", "s64", "s65", "s66", "s67", "s68", "s69", "s70", "s71", "s72", "s73", "s74", "s75", "s76", "s77", "s78", "s79", "s80", "s81", "s82", "s83", "s84", "s85", "s86", "s87", "s88", "s89", "s90", "s91", "s92", "s93", "s94", "s95", "s96", "a0", "a1", "a2", "a3", "a4", "a5", "a6", "a7", "a8", "a9", "a10", "a11", "a12", "a13", "a14", "a15", "a16", "a17", "a18", "a19", "a20", "a21", "a22", "a23", "a24", "a25", "a26", "a27", "a28", "a29", "a30", "a31", "a32", "a33", "a34", "a35", "a36", "a37", "a38", "a39", "a40", "a41", "a42", "a43", "a44", "a45", "a46", "a47", "a48", "a49", "a50", "a51", "a52", "a53", "a54", "a55", "a56", "a57", "a58", "a59", "a60", "a61", "a62", "a63", "a64", "a65", "a66", "a67", "a68", "a69", "a70", "a71", "a72", "a73", "a74", "a75", "a76", "a77", "a78", "a79", "a80", "a81", "a82", "a83", "a84", "a85", "a86", "a87", "a88", "a89", "a90", "a91", "a92", "a93", "a94", "a95", "a96", "a97", "a98", "a99", "a100", "a101", "a102", "a103", "a104", "a105", "a106", "a107", "a108", "a109", "a110", "a111", "a112", "a113", "a114", "a115", "a116", "a117", "a118", "a119", "a120", "a121", "a122", "a123", "a124", "a125", "a126", "a127", "a128", "a129", "a130", "a131", "a132", "a133", "a134", "a135", "a136", "a137", "a138", "a139", "a140", "a141", "a142", "a143", "a144", "a145", "a146", "a147", "a148", "a149", "a150", "a151", "a152", "a153", "a154", "a155", "a156", "a157", "a158", "a159", "a160", "a161", "a162", "a163", "a164", "a165", "a166", "a167", "a168", "a169", "a170", "a171", "a172", "a173", "a174", "a175", "a176", "a177", "a178", "a179", "a180", "a181", "a182", "a183", "a184", "a185", "a186", "a187", "a188", "a189", "a190", "a191", "a192", "a193", "a194", "a195", "a196", "a197", "a198", "a199", "a200", "a201", "a202", "a203", "a204", "a205", "a206", "a207", "a208", "a209", "a210", "a211", "a212", "a213", "a214", "a215", "a216", "a217", "a218", "a219", "a220", "a221", "a222", "a223", "a224", "a225", "a226", "a227", "a228", "a229", "a230", "a231", "a232", "a233", "a234", "a235", "a236", "a237", "a238", "a239", "a240", "a241", "a242", "a243", "a244", "a245", "a246", "a247", "a248", "a249", "a250", "a251", "a252", "a253", "a254", "a255", "vcc", "scc", "m0", "memory"
#define VT_ASM \
  "s_nop 4\n" \
  "s_mov_b32 s60, -1\n" \
  "s_mov_b32 s81, 0\n" \
  "s_mov_b32 s87, 2.0\n" \
  "s_mov_b32 s78, 0\n" \
  "s_mov_b32 s79, 0\n" \
  "s_mov_b32 s80, 0\n" \
  "s_add_i32 s67, %[bas], -384\n" \
  "s_mov_b32 s66, 0\n" \
  ".Lvt_top:\n" \
  "s_memrealtime s[74:75]\n" \
  "s_waitcnt lgkmcnt(0)\n" \
  "s_sub_i32 s91, s74, s66\n" \
  "s_cmp_gt_u32 s91, 14\n" \
  "s_cbranch_scc1 .Lvt_late\n" \
  ".Lvt_al_ok:\n" \
  "v_mfma_f32_16x16x32_bf16 v[64:67], a[0:3], v[80:83], 0\n" \
  "ds_read_b128 v[104:107], %[hA] offset:1536\n" \
  "v_mfma_f32_16x16x32_bf16 v[72:75], a[0:3], v[84:87], 0\n" \
  "ds_read_b128 v[108:111], %[hA] offset:19968\n" \
  "v_mfma_f32_16x16x32_bf16 v[68:71], a[8:11], v[80:83], 0\n" \
  "v_mov_b32 v225, s60\n" \
  "s_add_i32 s67, s67, 128\n" \
  "v_mfma_f32_16x16x32_bf16 v[76:79], a[8:11], v[84:87], 0\n" \
  "v_mfma_f32_16x16x32_bf16 v[64:67], a[4:7], v[88:91], v[64:67]\n" \
  "ds_read_b128 v[80:83], %[hA] offset:2048\n" \
  "v_mfma_f32_16x16x32_bf16 v[72:75], a[4:7], v[92:95], v[72:75]\n" \
  "ds_read_b128 v[84:87], %[hA] offset:20480\n" \
  "v_mfma_f32_16x16x32_bf16 v[68:71], a[12:15], v[88:91], v[68:71]\n" \
  "s_sub_i32 s90, s74, s89\n" \
  "s_max_i32 s92, s91, -2\n" \
  "s_add_i32 s89, s66, s92\n" \
  "v_mfma_f32_16x16x32_bf16 v[76:79], a[12:15], v[92:95], v[76:79]\n" \
  "v_mfma_f32_16x16x32_bf16 v[64:67], a[16:19], v[96:99], v[64:67]\n" \
  "ds_read_b128 v[88:91], %[hA] offset:2560\n" \
  "v_mfma_f32_16x16x32_bf16 v[72:75], a[16:19], v[100:103], v[72:75]\n" \
  "ds_read_b128 v[92:95], %[hA] offset:20992\n" \
  "v_mfma_f32_16x16x32_bf16 v[68:71], a[24:27], v[96:99], v[68:71]\n" \
  "s_ashr_i32 s92, s92, 1\n" \
  "s_add_i32 s90, s90, s92\n" \
  "s_min_u32 s90, s90, 4095\n" \
  "v_mfma_f32_16x16x32_bf16 v[76:79], a[24:27], v[100:103], v[76:79]\n" \
  "s_waitcnt lgkmcnt(4)\n" \
  "v_mfma_f32_16x16x32_bf16 v[64:67], a[20:23], v[104:107], v[64:67]\n" \
  "ds_read_b128 v[96:99], %[hA] offset:3072\n" \
  "v_mfma_f32_16x16x32_bf16 v[72:75], a[20:23], v[108:111], v[72:75]\n" \
  "ds_read_b128 v[100:103], %[hA] offset:21504\n" \
  "v_mfma_f32_16x16x32_bf16 v[68:71], a[28:31], v[104:107], v[68:71]\n" \
  "s_add_i32 s92, s60, 2\n" \
  "s_lshl_b32 s92, s92, 12\n" \
  "s_or_b32 s90, s90, s92\n" \
  "v_mfma_f32_16x16x32_bf16 v[76:79], a[28:31], v[108:111], v[76:79]\n" \
  "v_mov_b32 v227, s90\n" \
  "s_waitcnt lgkmcnt(4)\n" \
  "v_mfma_f32_16x16x32_bf16 v[64:67], a[32:35], v[80:83], v[64:67]\n" \
  "ds_read_b128 v[104:107], %[hA] offset:3584\n" \
  "v_mfma_f32_16x16x32_bf16 v[72:75], a[32:35], v[84:87], v[72:75]\n" \
  "ds_read_b128 v[108:111], %[hA] offset:22016\n" \
  "v_mfma_f32_16x16x32_bf16 v[68:71], a[40:43], v[80:83], v[68:71]\n" \
  "v_mfma_f32_16x16x32_bf16 v[76:79], a[40:43], v[84:87], v[76:79]\n" \
  "s_waitcnt lgkmcnt(4)\n" \
  "v_mfma_f32_16x16x32_bf16 v[64:67], a[36:39], v[88:91], v[64:67]\n" \
  "ds_read_b128 v[80:83], %[hA] offset:4096\n" \
  "v_mfma_f32_16x16x32_bf16 v[72:75], a[36:39], v[92:95], v[72:75]\n" \
  "ds_read_b128 v[84:87], %[hA] offset:22528\n" \
  "v_mfma_f32_16x16x32_bf16 v[68:71], a[44:47], v[88:91], v[68:71]\n" \
  "v_mfma_f32_16x16x32_bf16 v[76:79], a[44:47], v[92:95], v[76:79]\n" \
  "s_waitcnt lgkmcnt(4)\n" \
  "v_mfma_f32_16x16x32_bf16 v[64:67], a[48:51], v[96:99], v[64:67]\n" \
  "ds_read_b128 v[88:91], %[hA] offset:4608\n" \
  "v_mfma_f32_16x16x32_bf16 v[72:75], a[48:51], v[100:103], v[72:75]\n" \
  "ds_read_b128 v[92:95], %[hA] offset:23040\n" \
  "v_mfma_f32_16x16x32_bf16 v[68:71], a[56:59], v[96:99], v[68:71]\n" \
  "s_waitcnt vmcnt(0)\n" \
  "s_barrier\n" \
  "buffer_store_dword v225, %[flo], %[X], s87 offen sc1\n" \
  "v_mfma_f32_16x16x32_bf16 v[76:79], a[56:59], v[100:103], v[76:79]\n" \
  "s_waitcnt lgkmcnt(4)\n" \
  "v_mfma_f32_16x16x32_bf16 v[64:67], a[52:55], v[104:107], v[64:67]\n" \
  "ds_read_b128 v[96:99], %[hA] offset:5120\n" \
  "v_mfma_f32_16x16x32_bf16 v[72:75], a[52:55], v[108:111], v[72:75]\n" \
  "ds_read_b128 v[100:103], %[hA] offset:23552\n" \
  "v_mfma_f32_16x16x32_bf16 v[68:71], a[60:63], v[104:107], v[68:71]\n" \
  "buffer_load_dwordx2 v[228:229], off, %[X], s67 sc1\n" \
  "v_mfma_f32_16x16x32_bf16 v[76:79], a[60:63], v[108:111], v[76:79]\n" \
  "s_waitcnt lgkmcnt(4)\n" \
  "v_mfma_f32_16x16x32_bf16 v[64:67], a[64:67], v[80:83], v[64:67]\n" \
  "ds_read_b128 v[104:107], %[hA] offset:5632\n" \
  "v_mfma_f32_16x16x32_bf16 v[72:75], a[64:67], v[84:87], v[72:75]\n" \
  "ds_read_b128 v[108:111], %[hA] offset:24064\n" \
  "v_mfma_f32_16x16x32_bf16 v[68:71], a[72:75], v[80:83], v[68:71]\n" \
  "buffer_atomic_umax v227, %[rpo], %[X], 0 offen sc1\n" \
  "v_mfma_f32_16x16x32_bf16 v[76:79], a[72:75], v[84:87], v[76:79]\n" \
  "s_waitcnt lgkmcnt(4)\n" \
  "v_mfma_f32_16x16x32_bf16 v[64:67], a[68:71], v[88:91], v[64:67]\n" \
  "ds_read_b128 v[80:83], %[hA] offset:6144\n" \
  "v_mfma_f32_16x16x32_bf16 v[72:75], a[68:71], v[92:95], v[72:75]\n" \
  "ds_read_b128 v[84:87], %[hA] offset:24576\n" \
  "v_mfma_f32_16x16x32_bf16 v[68:71], a[76:79], v[88:91], v[68:71]\n" \
  "s_add_i32 s64, s60, 1\n" \
  "s_min_i32 s61, s64, 63\n" \
  "s_mul_i32 s61, s61, 36864\n" \
  "v_mfma_f32_16x16x32_bf16 v[76:79], a[76:79], v[92:95], v[76:79]\n" \
  "s_waitcnt lgkmcnt(4)\n" \
  "v_mfma_f32_16x16x32_bf16 v[64:67], a[80:83], v[96:99], v[64:67]\n" \
  "ds_read_b128 v[88:91], %[hA] offset:6656\n" \
  "v_mfma_f32_16x16x32_bf16 v[72:75], a[80:83], v[100:103], v[72:75]\n" \
  "ds_read_b128 v[92:95], %[hA] offset:25088\n" \
  "v_mfma_f32_16x16x32_bf16 v[68:71], a[88:91], v[96:99], v[68:71]\n" \
  "s_lshl_b32 s62, s64, 16\n" \
  "s_and_b32 s62, s62, 0x10000\n" \
  "s_add_i32 s62, %[hl0], s62\n" \
  "v_mfma_f32_16x16x32_bf16 v[76:79], a[88:91], v[100:103], v[76:79]\n" \
  "s_waitcnt lgkmcnt(4)\n" \
  "v_mfma_f32_16x16x32_bf16 v[64:67], a[84:87], v[104:107], v[64:67]\n" \
  "ds_read_b128 v[96:99], %[hA] offset:7168\n" \
  "v_mfma_f32_16x16x32_bf16 v[72:75], a[84:87], v[108:111], v[72:75]\n" \
  "ds_read_b128 v[100:103], %[hA] offset:25600\n" \
  "v_mfma_f32_16x16x32_bf16 v[68:71], a[92:95], v[104:107], v[68:71]\n" \
  "s_and_b32 s63, s60, 7\n" \
  "s_lshl_b32 s63, s63, 22\n" \
  "s_add_i32 m0, s62, 0\n" \
  "v_mfma_f32_16x16x32_bf16 v[76:79], a[92:95], v[108:111], v[76:79]\n" \
  "s_waitcnt lgkmcnt(4)\n" \
  "v_mfma_f32_16x16x32_bf16 v[64:67], a[96:99], v[80:83], v[64:67]\n" \
  "ds_read_b128 v[104:107], %[hA] offset:7680\n" \
  "v_mfma_f32_16x16x32_bf16 v[72:75], a[96:99], v[84:87], v[72:75]\n" \
  "ds_read_b128 v[108:111], %[hA] offset:26112\n" \
  "v_mfma_f32_16x16x32_bf16 v[68:71], a[104:107], v[80:83], v[68:71]\n" \
  "s_cmp_lt_i32 s60, 0\n" \
  "s_cselect_b32 s63, 2.0, s63\n" \
  "s_cselect_b32 s61, 2.0, s61\n" \
  "v_mfma_f32_16x16x32_bf16 v[76:79], a[104:107], v[84:87], v[76:79]\n" \
  "s_waitcnt lgkmcnt(4)\n" \
  "v_mfma_f32_16x16x32_bf16 v[64:67], a[100:103], v[88:91], v[64:67]\n" \
  "ds_read_b128 v[80:83], %[hA] offset:8192\n" \
  "v_mfma_f32_16x16x32_bf16 v[72:75], a[100:103], v[92:95], v[72:75]\n" \
  "ds_read_b128 v[84:87], %[hA] offset:26624\n" \
  "v_mfma_f32_16x16x32_bf16 v[68:71], a[108:111], v[88:91], v[68:71]\n" \
  "v_mfma_f32_16x16x32_bf16 v[76:79], a[108:111], v[92:95], v[76:79]\n" \
  "s_waitcnt lgkmcnt(4)\n" \
  "v_mfma_f32_16x16x32_bf16 v[64:67], a[112:115], v[96:99], v[64:67]\n" \
  "ds_read_b128 v[88:91], %[hA] offset:8704\n" \
  "v_mfma_f32_16x16x32_bf16 v[72:75], a[112:115], v[100:103], v[72:75]\n" \
  "ds_read_b128 v[92:95], %[hA] offset:27136\n" \
  "v_mfma_f32_16x16x32_bf16 v[68:71], a[120:123], v[96:99], v[68:71]\n" \
  "v_mfma_f32_16x16x32_bf16 v[76:79], a[120:123], v[100:103], v[76:79]\n" \
  "s_waitcnt lgkmcnt(4)\n" \
  "v_mfma_f32_16x16x32_bf16 v[64:67], a[116:119], v[104:107], v[64:67]\n" \
  "v_mfma_f32_16x16x32_bf16 v[72:75], a[116:119], v[108:111], v[72:75]\n" \
  "v_mfma_f32_16x16x32_bf16 v[68:71], a[124:127], v[104:107], v[68:71]\n" \
  "v_mfma_f32_16x16x32_bf16 v[76:79], a[124:127], v[108:111], v[76:79]\n" \
  "s_waitcnt lgkmcnt(2)\n" \
  "v_mfma_f32_16x16x32_bf16 v[64:67], a[128:131], v[80:83], v[64:67]\n" \
  "buffer_load_dwordx4 %[hvo], %[HS], s61 offen offset:0 sc1 lds\n" \
  "v_mfma_f32_16x16x32_bf16 v[72:75], a[128:131], v[84:87], v[72:75]\n" \
  "v_mfma_f32_16x16x32_bf16 v[68:71], a[136:139], v[80:83], v[68:71]\n" \
  "v_mfma_f32_16x16x32_bf16 v[76:79], a[136:139], v[84:87], v[76:79]\n" \
  "s_waitcnt lgkmcnt(0)\n" \
  "v_mfma_f32_16x16x32_bf16 v[64:67], a[132:135], v[88:91], v[64:67]\n" \
  "buffer_load_dwordx4 %[hvo], %[HS], s61 offen offset:1024 sc1 lds\n" \
  "v_mfma_f32_16x16x32_bf16 v[72:75], a[132:135], v[92:95], v[72:75]\n" \
  "v_mfma_f32_16x16x32_bf16 v[68:71], a[140:143], v[88:91], v[68:71]\n" \
  "s_nop 2\n" \
  "v_mfma_f32_16x16x32_bf16 v[76:79], a[140:143], v[92:95], v[76:79]\n" \
  "buffer_load_dwordx4 %[hvo], %[HS], s61 offen offset:2048 sc1 lds\n" \
  "v_cvt_pk_bf16_f32 v112, v64, v65\n" \
  "v_cvt_pk_bf16_f32 v113, v66, v67\n" \
  "s_nop 0\n" \
  "v_cvt_pk_bf16_f32 v114, v68, v69\n" \
  "v_cvt_pk_bf16_f32 v115, v70, v71\n" \
  "ds_write_b128 %[vW], v[112:115]\n" \
  "v_cvt_pk_bf16_f32 v116, v72, v73\n" \
  "v_cvt_pk_bf16_f32 v117, v74, v75\n" \
  "v_cvt_pk_bf16_f32 v118, v76, v77\n" \
  "v_cvt_pk_bf16_f32 v119, v78, v79\n" \
  "ds_write_b128 %[vW], v[116:119] offset:4096\n" \
  "buffer_load_dwordx4 %[hvo], %[HS], s61 offen offset:3072 sc1 lds\n" \
  "v_cmp_u_f32 vcc, v64, v72\n" \
  "s_cbranch_vccnz .Lvt_sh\n" \
  ".Lvt_sh_back:\n" \
  "s_waitcnt lgkmcnt(0)\n" \
  "s_barrier\n" \
  "s_add_i32 s61, s61, 0x1000\n" \
  "s_add_i32 m0, s62, 4096\n" \
  "s_nop 0\n" \
  "buffer_load_dwordx4 %[hvo], %[HS], s61 offen offset:0 sc1 lds\n" \
  "ds_read_b128 v[120:123], %[vR] offset:0\n" \
  "ds_read_b128 v[136:139], %[vR] offset:4096\n" \
  "ds_read_b128 v[124:127], %[vR] offset:1024\n" \
  "ds_read_b128 v[140:143], %[vR] offset:5120\n" \
  "ds_read_b128 v[128:131], %[vR] offset:2048\n" \
  "ds_read_b128 v[144:147], %[vR] offset:6144\n" \
  "ds_read_b128 v[132:135], %[vR] offset:3072\n" \
  "ds_read_b128 v[148:151], %[vR] offset:7168\n" \
  "buffer_load_dwordx4 %[hvo], %[HS], s61 offen offset:1024 sc1 lds\n" \
  "s_waitcnt lgkmcnt(6)\n" \
  "v_mfma_f32_16x16x32_bf16 v[152:155], v[120:123], a[144:147], 0\n" \
  "buffer_load_dwordx4 %[hvo], %[HS], s61 offen offset:2048 sc1 lds\n" \
  "v_mfma_f32_16x16x32_bf16 v[156:159], v[136:139], a[144:147], 0\n" \
  "s_waitcnt lgkmcnt(4)\n" \
  "v_mfma_f32_16x16x32_bf16 v[152:155], v[124:127], a[148:151], v[152:155]\n" \
  "v_mfma_f32_16x16x32_bf16 v[156:159], v[140:143], a[148:151], v[156:159]\n" \
  "s_waitcnt lgkmcnt(2)\n" \
  "v_mfma_f32_16x16x32_bf16 v[152:155], v[128:131], a[152:155], v[152:155]\n" \
  "buffer_load_dwordx4 %[hvo], %[HS], s61 offen offset:3072 sc1 lds\n" \
  "v_mfma_f32_16x16x32_bf16 v[156:159], v[144:147], a[152:155], v[156:159]\n" \
  "s_waitcnt lgkmcnt(0)\n" \
  "v_mfma_f32_16x16x32_bf16 v[152:155], v[132:135], a[156:159], v[152:155]\n" \
  "v_mfma_f32_16x16x32_bf16 v[156:159], v[148:151], a[156:159], v[156:159]\n" \
  "v_mfma_f32_16x16x32_bf16 v[160:163], v[120:123], a[160:163], 0\n" \
  "s_add_i32 s61, s61, 0x1000\n" \
  "s_add_i32 m0, s62, 8192\n" \
  "s_nop 0\n" \
  "buffer_load_dwordx4 %[hvo], %[HS], s61 offen offset:0 sc1 lds\n" \
  "v_mfma_f32_16x16x32_bf16 v[164:167], v[136:139], a[160:163], 0\n" \
  "v_mfma_f32_16x16x32_bf16 v[160:163], v[124:127], a[164:167], v[160:163]\n" \
  "v_cvt_pk_bf16_f32 v176, v152, v153\n" \
  "v_cvt_pk_bf16_f32 v177, v154, v155\n" \
  "v_mfma_f32_16x16x32_bf16 v[164:167], v[140:143], a[164:167], v[164:167]\n" \
  "v_cvt_pk_bf16_f32 v178, v156, v157\n" \
  "v_cvt_pk_bf16_f32 v179, v158, v159\n" \
  "v_mfma_f32_16x16x32_bf16 v[160:163], v[128:131], a[168:171], v[160:163]\n" \
  "buffer_store_dwordx4 v[176:179], %[sto], %[X], s63 offen sc1\n" \
  "s_add_i32 s63, s63, 0x10000\n" \
  "v_mfma_f32_16x16x32_bf16 v[164:167], v[144:147], a[168:171], v[164:167]\n" \
  "v_mfma_f32_16x16x32_bf16 v[160:163], v[132:135], a[172:175], v[160:163]\n" \
  "v_mfma_f32_16x16x32_bf16 v[164:167], v[148:151], a[172:175], v[164:167]\n" \
  "v_mfma_f32_16x16x32_bf16 v[168:171], v[120:123], a[176:179], 0\n" \
  "s_nop 0\n" \
  "v_mfma_f32_16x16x32_bf16 v[172:175], v[136:139], a[176:179], 0\n" \
  "s_nop 2\n" \
  "v_mfma_f32_16x16x32_bf16 v[168:171], v[124:127], a[180:183], v[168:171]\n" \
  "v_cvt_pk_bf16_f32 v180, v160, v161\n" \
  "v_cvt_pk_bf16_f32 v181, v162, v163\n" \
  "v_mfma_f32_16x16x32_bf16 v[172:175], v[140:143], a[180:183], v[172:175]\n" \
  "v_cvt_pk_bf16_f32 v182, v164, v165\n" \
  "v_cvt_pk_bf16_f32 v183, v166, v167\n" \
  "v_mfma_f32_16x16x32_bf16 v[168:171], v[128:131], a[184:187], v[168:171]\n" \
  "buffer_store_dwordx4 v[180:183], %[sto], %[X], s63 offen sc1\n" \
  "s_add_i32 s63, s63, 0x10000\n" \
  "v_mfma_f32_16x16x32_bf16 v[172:175], v[144:147], a[184:187], v[172:175]\n" \
  "v_mfma_f32_16x16x32_bf16 v[168:171], v[132:135], a[188:191], v[168:171]\n" \
  "v_mfma_f32_16x16x32_bf16 v[172:175], v[148:151], a[188:191], v[172:175]\n" \
  "v_mfma_f32_16x16x32_bf16 v[152:155], v[120:123], a[192:195], 0\n" \
  "s_nop 0\n" \
  "v_mfma_f32_16x16x32_bf16 v[156:159], v[136:139], a[192:195], 0\n" \
  "s_nop 2\n" \
  "v_mfma_f32_16x16x32_bf16 v[152:155], v[124:127], a[196:199], v[152:155]\n" \
  "v_cvt_pk_bf16_f32 v184, v168, v169\n" \
  "v_cvt_pk_bf16_f32 v185, v170, v171\n" \
  "v_mfma_f32_16x16x32_bf16 v[156:159], v[140:143], a[196:199], v[156:159]\n" \
  "v_cvt_pk_bf16_f32 v186, v172, v173\n" \
  "v_cvt_pk_bf16_f32 v187, v174, v175\n" \
  "v_mfma_f32_16x16x32_bf16 v[152:155], v[128:131], a[200:203], v[152:155]\n" \
  "buffer_store_dwordx4 v[184:187], %[sto], %[X], s63 offen sc1\n" \
  "s_add_i32 s63, s63, 0x10000\n" \
  "v_mfma_f32_16x16x32_bf16 v[156:159], v[144:147], a[200:203], v[156:159]\n" \
  "v_mfma_f32_16x16x32_bf16 v[152:155], v[132:135], a[204:207], v[152:155]\n" \
  "v_mfma_f32_16x16x32_bf16 v[156:159], v[148:151], a[204:207], v[156:159]\n" \
  "v_mfma_f32_16x16x32_bf16 v[160:163], v[120:123], a[208:211], 0\n" \
  "s_nop 0\n" \
  "v_mfma_f32_16x16x32_bf16 v[164:167], v[136:139], a[208:211], 0\n" \
  "s_nop 2\n" \
  "v_mfma_f32_16x16x32_bf16 v[160:163], v[124:127], a[212:215], v[160:163]\n" \
  "v_cvt_pk_bf16_f32 v176, v152, v153\n" \
  "v_cvt_pk_bf16_f32 v177, v154, v155\n" \
  "v_mfma_f32_16x16x32_bf16 v[164:167], v[140:143], a[212:215], v[164:167]\n" \
  "v_cvt_pk_bf16_f32 v178, v156, v157\n" \
  "v_cvt_pk_bf16_f32 v179, v158, v159\n" \
  "v_mfma_f32_16x16x32_bf16 v[160:163], v[128:131], a[216:219], v[160:163]\n" \
  "buffer_store_dwordx4 v[176:179], %[sto], %[X], s63 offen sc1\n" \
  "s_add_i32 s63, s63, 0x10000\n" \
  "v_mfma_f32_16x16x32_bf16 v[164:167], v[144:147], a[216:219], v[164:167]\n" \
  "v_mfma_f32_16x16x32_bf16 v[160:163], v[132:135], a[220:223], v[160:163]\n" \
  "v_mfma_f32_16x16x32_bf16 v[164:167], v[148:151], a[220:223], v[164:167]\n" \
  "v_mfma_f32_16x16x32_bf16 v[168:171], v[120:123], a[224:227], 0\n" \
  "s_nop 0\n" \
  "v_mfma_f32_16x16x32_bf16 v[172:175], v[136:139], a[224:227], 0\n" \
  "s_nop 2\n" \
  "v_mfma_f32_16x16x32_bf16 v[168:171], v[124:127], a[228:231], v[168:171]\n" \
  "v_cvt_pk_bf16_f32 v180, v160, v161\n" \
  "v_cvt_pk_bf16_f32 v181, v162, v163\n" \
  "v_mfma_f32_16x16x32_bf16 v[172:175], v[140:143], a[228:231], v[172:175]\n" \
  "v_cvt_pk_bf16_f32 v182, v164, v165\n" \
  "v_cvt_pk_bf16_f32 v183, v166, v167\n" \
  "v_mfma_f32_16x16x32_bf16 v[168:171], v[128:131], a[232:235], v[168:171]\n" \
  "buffer_store_dwordx4 v[180:183], %[sto], %[X], s63 offen sc1\n" \
  "s_add_i32 s63, s63, %[px]\n" \
  "v_mfma_f32_16x16x32_bf16 v[172:175], v[144:147], a[232:235], v[172:175]\n" \
  "v_mfma_f32_16x16x32_bf16 v[168:171], v[132:135], a[236:239], v[168:171]\n" \
  "v_mfma_f32_16x16x32_bf16 v[172:175], v[148:151], a[236:239], v[172:175]\n" \
  "v_mfma_f32_16x16x32_bf16 v[152:155], v[120:123], a[240:243], 0\n" \
  "s_nop 0\n" \
  "v_mfma_f32_16x16x32_bf16 v[156:159], v[136:139], a[240:243], 0\n" \
  "s_nop 2\n" \
  "v_mfma_f32_16x16x32_bf16 v[152:155], v[124:127], a[244:247], v[152:155]\n" \
  "v_cvt_pk_bf16_f32 v184, v168, v169\n" \
  "v_cvt_pk_bf16_f32 v185, v170, v171\n" \
  "v_mfma_f32_16x16x32_bf16 v[156:159], v[140:143], a[244:247], v[156:159]\n" \
  "v_cvt_pk_bf16_f32 v186, v172, v173\n" \
  "v_cvt_pk_bf16_f32 v187, v174, v175\n" \
  "v_mfma_f32_16x16x32_bf16 v[152:155], v[128:131], a[248:251], v[152:155]\n" \
  "buffer_store_dwordx4 v[184:187], %[sto], %[X], s63 offen sc1\n" \
  "s_add_i32 s63, s63, 0x10000\n" \
  "v_mfma_f32_16x16x32_bf16 v[156:159], v[144:147], a[248:251], v[156:159]\n" \
  "v_mfma_f32_16x16x32_bf16 v[152:155], v[132:135], a[252:255], v[152:155]\n" \
  "v_mfma_f32_16x16x32_bf16 v[156:159], v[148:151], a[252:255], v[156:159]\n" \
  "v_mfma_f32_16x16x32_bf16 v[160:163], v[120:123], v[32:35], 0\n" \
  "s_nop 0\n" \
  "v_mfma_f32_16x16x32_bf16 v[164:167], v[136:139], v[32:35], 0\n" \
  "s_nop 2\n" \
  "v_mfma_f32_16x16x32_bf16 v[160:163], v[124:127], v[36:39], v[160:163]\n" \
  "v_cvt_pk_bf16_f32 v176, v152, v153\n" \
  "v_cvt_pk_bf16_f32 v177, v154, v155\n" \
  "v_mfma_f32_16x16x32_bf16 v[164:167], v[140:143], v[36:39], v[164:167]\n" \
  "v_cvt_pk_bf16_f32 v178, v156, v157\n" \
  "v_cvt_pk_bf16_f32 v179, v158, v159\n" \
  "v_mfma_f32_16x16x32_bf16 v[160:163], v[128:131], v[40:43], v[160:163]\n" \
  "buffer_store_dwordx4 v[176:179], %[sto], %[X], s63 offen sc1\n" \
  "s_add_i32 s63, s63, 0x10000\n" \
  "v_mfma_f32_16x16x32_bf16 v[164:167], v[144:147], v[40:43], v[164:167]\n" \
  "v_mfma_f32_16x16x32_bf16 v[160:163], v[132:135], v[44:47], v[160:163]\n" \
  "v_mfma_f32_16x16x32_bf16 v[164:167], v[148:151], v[44:47], v[164:167]\n" \
  "v_mfma_f32_16x16x32_bf16 v[168:171], v[120:123], v[48:51], 0\n" \
  "s_nop 0\n" \
  "v_mfma_f32_16x16x32_bf16 v[172:175], v[136:139], v[48:51], 0\n" \
  "s_nop 2\n" \
  "v_mfma_f32_16x16x32_bf16 v[168:171], v[124:127], v[52:55], v[168:171]\n" \
  "v_cvt_pk_bf16_f32 v180, v160, v161\n" \
  "v_cvt_pk_bf16_f32 v181, v162, v163\n" \
  "v_mfma_f32_16x16x32_bf16 v[172:175], v[140:143], v[52:55], v[172:175]\n" \
  "v_cvt_pk_bf16_f32 v182, v164, v165\n" \
  "v_cvt_pk_bf16_f32 v183, v166, v167\n" \
  "v_mfma_f32_16x16x32_bf16 v[168:171], v[128:131], v[56:59], v[168:171]\n" \
  "buffer_store_dwordx4 v[180:183], %[sto], %[X], s63 offen sc1\n" \
  "s_add_i32 s63, s63, 0x10000\n" \
  "v_mfma_f32_16x16x32_bf16 v[172:175], v[144:147], v[56:59], v[172:175]\n" \
  "v_mfma_f32_16x16x32_bf16 v[168:171], v[132:135], v[60:63], v[168:171]\n" \
  "s_nop 2\n" \
  "v_mfma_f32_16x16x32_bf16 v[172:175], v[148:151], v[60:63], v[172:175]\n" \
  "s_nop 3\n" \
  "v_cvt_pk_bf16_f32 v184, v168, v169\n" \
  "v_cvt_pk_bf16_f32 v185, v170, v171\n" \
  "s_nop 1\n" \
  "v_cvt_pk_bf16_f32 v186, v172, v173\n" \
  "v_cvt_pk_bf16_f32 v187, v174, v175\n" \
  "buffer_store_dwordx4 v[184:187], %[sto], %[X], s63 offen sc1\n" \
  "s_add_i32 s63, s63, 0x10000\n" \
  "v_xor_b32 %[hA], 0x10000, %[hA]\n" \
  "s_waitcnt vmcnt(9)\n" \
  "s_barrier\n" \
  "ds_read_b128 v[80:83], %[hA] offset:0\n" \
  "ds_read_b128 v[84:87], %[hA] offset:18432\n" \
  "ds_read_b128 v[88:91], %[hA] offset:512\n" \
  "ds_read_b128 v[92:95], %[hA] offset:18944\n" \
  "ds_read_b128 v[96:99], %[hA] offset:1024\n" \
  "ds_read_b128 v[100:103], %[hA] offset:19456\n" \
  "v_readfirstlane_b32 s64, v228\n" \
  "s_cmp_eq_u32 s64, 0\n" \
  "s_cbranch_scc1 .Lvt_w\n" \
  ".Lvt_w_ok:\n" \
  "v_readfirstlane_b32 s66, v229\n" \
  "s_add_i32 s66, s66, %[vtc]\n" \
  "s_add_i32 s60, s60, 1\n" \
  "s_cmp_lg_u32 s60, s81\n" \
  "s_cbranch_scc1 .Lvt_top\n" \
  "s_cmp_eq_u32 s60, 0\n" \
  "s_cbranch_scc1 .Lvt_entry\n" \
  "v_mov_b32 v225, 64\n" \
  "s_waitcnt vmcnt(0)\n" \
  "s_barrier\n" \
  "buffer_store_dword v225, %[flo], %[X], s87 offen sc1\n" \
  "s_branch .Lvt_end\n" \
  ".Lvt_entry:\n" \
  "s_mov_b32 s81, 64\n" \
  "s_mov_b32 s87, 0\n" \
  "s_waitcnt vmcnt(0) lgkmcnt(0)\n" \
  "s_barrier\n" \
  "v_xor_b32 v188, 0x80, %[wv]\n" \
  "v_xor_b32 v189, 32, %[wr]\n" \
  "v_add_u32 v190, 0x10000, %[wr]\n" \
  "v_add_u32 v191, 0x10000, v189\n" \
  "s_cmp_eq_u32 %[grp], 0\n" \
  "s_cbranch_scc1 .Lvt_ws1\n" \
  "s_sleep 8\n" \
  ".Lvt_ws1:\n" \
  "s_add_i32 m0, %[wl], 0\n" \
  "s_mov_b32 s76, 0\n" \
  "buffer_load_dwordx4 %[wv], %[WA], s76 offen lds\n" \
  "s_add_i32 m0, %[wl], 1024\n" \
  "s_mov_b32 s76, 32768\n" \
  "buffer_load_dwordx4 %[wv], %[WA], s76 offen lds\n" \
  "s_add_i32 m0, %[wl], 2048\n" \
  "s_mov_b32 s76, 65536\n" \
  "buffer_load_dwordx4 v188, %[WA], s76 offen lds\n" \
  "s_add_i32 m0, %[wl], 3072\n" \
  "s_mov_b32 s76, 98304\n" \
  "buffer_load_dwordx4 v188, %[WA], s76 offen lds\n" \
  "s_cmp_eq_u32 %[grp], 0\n" \
  "s_cbranch_scc1 .Lvt_ws2\n" \
  "s_sleep 8\n" \
  ".Lvt_ws2:\n" \
  "s_add_i32 m0, %[wl], 16384\n" \
  "s_mov_b32 s76, 524288\n" \
  "buffer_load_dwordx4 %[wv], %[WA], s76 offen lds\n" \
  "s_add_i32 m0, %[wl], 17408\n" \
  "s_mov_b32 s76, 557056\n" \
  "buffer_load_dwordx4 %[wv], %[WA], s76 offen lds\n" \
  "s_add_i32 m0, %[wl], 18432\n" \
  "s_mov_b32 s76, 589824\n" \
  "buffer_load_dwordx4 v188, %[WA], s76 offen lds\n" \
  "s_add_i32 m0, %[wl], 19456\n" \
  "s_mov_b32 s76, 622592\n" \
  "buffer_load_dwordx4 v188, %[WA], s76 offen lds\n" \
  "s_cmp_eq_u32 %[grp], 0\n" \
  "s_cbranch_scc1 .Lvt_ws3\n" \
  "s_sleep 8\n" \
  ".Lvt_ws3:\n" \
  "s_add_i32 m0, %[wl], 32768\n" \
  "s_mov_b32 s76, 1048576\n" \
  "buffer_load_dwordx4 %[wv], %[WA], s76 offen lds\n" \
  "s_add_i32 m0, %[wl], 33792\n" \
  "s_mov_b32 s76, 1081344\n" \
  "buffer_load_dwordx4 %[wv], %[WA], s76 offen lds\n" \
  "s_add_i32 m0, %[wl], 34816\n" \
  "s_mov_b32 s76, 1114112\n" \
  "buffer_load_dwordx4 v188, %[WA], s76 offen lds\n" \
  "s_add_i32 m0, %[wl], 35840\n" \
  "s_mov_b32 s76, 1146880\n" \
  "buffer_load_dwordx4 v188, %[WA], s76 offen lds\n" \
  "s_cmp_eq_u32 %[grp], 0\n" \
  "s_cbranch_scc1 .Lvt_ws4\n" \
  "s_sleep 8\n" \
  ".Lvt_ws4:\n" \
  "s_add_i32 m0, %[wl], 49152\n" \
  "s_mov_b32 s76, 1572864\n" \
  "buffer_load_dwordx4 %[wv], %[WA], s76 offen lds\n" \
  "s_add_i32 m0, %[wl], 50176\n" \
  "s_mov_b32 s76, 1605632\n" \
  "buffer_load_dwordx4 %[wv], %[WA], s76 offen lds\n" \
  "s_add_i32 m0, %[wl], 51200\n" \
  "s_mov_b32 s76, 1638400\n" \
  "buffer_load_dwordx4 v188, %[WA], s76 offen lds\n" \
  "s_add_i32 m0, %[wl], 52224\n" \
  "s_mov_b32 s76, 1671168\n" \
  "buffer_load_dwordx4 v188, %[WA], s76 offen lds\n" \
  "s_cmp_eq_u32 %[grp], 0\n" \
  "s_cbranch_scc1 .Lvt_ws5\n" \
  "s_sleep 8\n" \
  ".Lvt_ws5:\n" \
  "s_add_i32 m0, %[wl], 65536\n" \
  "s_mov_b32 s76, 2097152\n" \
  "buffer_load_dwordx4 %[wv], %[WA], s76 offen lds\n" \
  "s_add_i32 m0, %[wl], 66560\n" \
  "s_mov_b32 s76, 2129920\n" \
  "buffer_load_dwordx4 %[wv], %[WA], s76 offen lds\n" \
  "s_add_i32 m0, %[wl], 67584\n" \
  "s_mov_b32 s76, 2162688\n" \
  "buffer_load_dwordx4 v188, %[WA], s76 offen lds\n" \
  "s_add_i32 m0, %[wl], 68608\n" \
  "s_mov_b32 s76, 2195456\n" \
  "buffer_load_dwordx4 v188, %[WA], s76 offen lds\n" \
  "s_cmp_eq_u32 %[grp], 0\n" \
  "s_cbranch_scc1 .Lvt_ws6\n" \
  "s_sleep 8\n" \
  ".Lvt_ws6:\n" \
  "s_add_i32 m0, %[wl], 81920\n" \
  "s_mov_b32 s76, 2621440\n" \
  "buffer_load_dwordx4 %[wv], %[WA], s76 offen lds\n" \
  "s_add_i32 m0, %[wl], 82944\n" \
  "s_mov_b32 s76, 2654208\n" \
  "buffer_load_dwordx4 %[wv], %[WA], s76 offen lds\n" \
  "s_add_i32 m0, %[wl], 83968\n" \
  "s_mov_b32 s76, 2686976\n" \
  "buffer_load_dwordx4 v188, %[WA], s76 offen lds\n" \
  "s_add_i32 m0, %[wl], 84992\n" \
  "s_mov_b32 s76, 2719744\n" \
  "buffer_load_dwordx4 v188, %[WA], s76 offen lds\n" \
  "s_cmp_eq_u32 %[grp], 0\n" \
  "s_cbranch_scc1 .Lvt_ws7\n" \
  "s_sleep 8\n" \
  ".Lvt_ws7:\n" \
  "s_add_i32 m0, %[wl], 98304\n" \
  "s_mov_b32 s76, 3145728\n" \
  "buffer_load_dwordx4 %[wv], %[WA], s76 offen lds\n" \
  "s_add_i32 m0, %[wl], 99328\n" \
  "s_mov_b32 s76, 3178496\n" \
  "buffer_load_dwordx4 %[wv], %[WA], s76 offen lds\n" \
  "s_add_i32 m0, %[wl], 100352\n" \
  "s_mov_b32 s76, 3211264\n" \
  "buffer_load_dwordx4 v188, %[WA], s76 offen lds\n" \
  "s_add_i32 m0, %[wl], 101376\n" \
  "s_mov_b32 s76, 3244032\n" \
  "buffer_load_dwordx4 v188, %[WA], s76 offen lds\n" \
  "s_cmp_eq_u32 %[grp], 0\n" \
  "s_cbranch_scc1 .Lvt_ws8\n" \
  "s_sleep 8\n" \
  ".Lvt_ws8:\n" \
  "s_add_i32 m0, %[wl], 114688\n" \
  "s_mov_b32 s76, 3670016\n" \
  "buffer_load_dwordx4 %[wv], %[WA], s76 offen lds\n" \
  "s_add_i32 m0, %[wl], 115712\n" \
  "s_mov_b32 s76, 3702784\n" \
  "buffer_load_dwordx4 %[wv], %[WA], s76 offen lds\n" \
  "s_add_i32 m0, %[wl], 116736\n" \
  "s_mov_b32 s76, 3735552\n" \
  "buffer_load_dwordx4 v188, %[WA], s76 offen lds\n" \
  "s_add_i32 m0, %[wl], 117760\n" \
  "s_mov_b32 s76, 3768320\n" \
  "buffer_load_dwordx4 v188, %[WA], s76 offen lds\n" \
  "s_waitcnt vmcnt(28)\n" \
  "s_waitcnt lgkmcnt(0)\n" \
  "s_barrier\n" \
  "ds_read_b64_tr_b16 a[0:1], %[wr] offset:0\n" \
  "ds_read_b64_tr_b16 a[2:3], %[wr] offset:1024\n" \
  "ds_read_b64_tr_b16 a[4:5], %[wr] offset:8192\n" \
  "ds_read_b64_tr_b16 a[6:7], %[wr] offset:9216\n" \
  "ds_read_b64_tr_b16 a[8:9], v189 offset:0\n" \
  "ds_read_b64_tr_b16 a[10:11], v189 offset:1024\n" \
  "ds_read_b64_tr_b16 a[12:13], v189 offset:8192\n" \
  "ds_read_b64_tr_b16 a[14:15], v189 offset:9216\n" \
  "s_waitcnt vmcnt(24)\n" \
  "s_waitcnt lgkmcnt(0)\n" \
  "s_barrier\n" \
  "s_cmp_eq_u32 %[grp], 0\n" \
  "s_cbranch_scc1 .Lvt_ws9\n" \
  "s_sleep 8\n" \
  ".Lvt_ws9:\n" \
  "s_add_i32 m0, %[wl], 0\n" \
  "s_mov_b32 s76, 4194304\n" \
  "buffer_load_dwordx4 %[wv], %[WA], s76 offen lds\n" \
  "s_add_i32 m0, %[wl], 1024\n" \
  "s_mov_b32 s76, 4227072\n" \
  "buffer_load_dwordx4 %[wv], %[WA], s76 offen lds\n" \
  "s_add_i32 m0, %[wl], 2048\n" \
  "s_mov_b32 s76, 4259840\n" \
  "buffer_load_dwordx4 v188, %[WA], s76 offen lds\n" \
  "s_add_i32 m0, %[wl], 3072\n" \
  "s_mov_b32 s76, 4292608\n" \
  "buffer_load_dwordx4 v188, %[WA], s76 offen lds\n" \
  "ds_read_b64_tr_b16 a[16:17], %[wr] offset:16384\n" \
  "ds_read_b64_tr_b16 a[18:19], %[wr] offset:17408\n" \
  "ds_read_b64_tr_b16 a[20:21], %[wr] offset:24576\n" \
  "ds_read_b64_tr_b16 a[22:23], %[wr] offset:25600\n" \
  "ds_read_b64_tr_b16 a[24:25], v189 offset:16384\n" \
  "ds_read_b64_tr_b16 a[26:27], v189 offset:17408\n" \
  "ds_read_b64_tr_b16 a[28:29], v189 offset:24576\n" \
  "ds_read_b64_tr_b16 a[30:31], v189 offset:25600\n" \
  "s_waitcnt vmcnt(24)\n" \
  "s_waitcnt lgkmcnt(0)\n" \
  "s_barrier\n" \
  "ds_read_b64_tr_b16 a[32:33], %[wr] offset:32768\n" \
  "ds_read_b64_tr_b16 a[34:35], %[wr] offset:33792\n" \
  "ds_read_b64_tr_b16 a[36:37], %[wr] offset:40960\n" \
  "ds_read_b64_tr_b16 a[38:39], %[wr] offset:41984\n" \
  "ds_read_b64_tr_b16 a[40:41], v189 offset:32768\n" \
  "ds_read_b64_tr_b16 a[42:43], v189 offset:33792\n" \
  "ds_read_b64_tr_b16 a[44:45], v189 offset:40960\n" \
  "ds_read_b64_tr_b16 a[46:47], v189 offset:41984\n" \
  "s_waitcnt vmcnt(20)\n" \
  "s_waitcnt lgkmcnt(0)\n" \
  "s_barrier\n" \
  "ds_read_b64_tr_b16 a[48:49], %[wr] offset:49152\n" \
  "ds_read_b64_tr_b16 a[50:51], %[wr] offset:50176\n" \
  "ds_read_b64_tr_b16 a[52:53], %[wr] offset:57344\n" \
  "ds_read_b64_tr_b16 a[54:55], %[wr] offset:58368\n" \
  "ds_read_b64_tr_b16 a[56:57], v189 offset:49152\n" \
  "ds_read_b64_tr_b16 a[58:59], v189 offset:50176\n" \
  "ds_read_b64_tr_b16 a[60:61], v189 offset:57344\n" \
  "ds_read_b64_tr_b16 a[62:63], v189 offset:58368\n" \
  "s_waitcnt vmcnt(16)\n" \
  "s_waitcnt lgkmcnt(0)\n" \
  "s_barrier\n" \
  "s_cmp_eq_u32 %[grp], 0\n" \
  "s_cbranch_scc1 .Lvt_ws10\n" \
  "s_sleep 16\n" \
  ".Lvt_ws10:\n" \
  "s_add_i32 m0, %[hl0], 16384\n" \
  "s_mov_b32 s76, 0\n" \
  "buffer_load_dwordx4 %[hvo], %[WB], s76 offen offset:0 lds\n" \
  "buffer_load_dwordx4 %[hvo], %[WB], s76 offen offset:1024 lds\n" \
  "buffer_load_dwordx4 %[hvo], %[WB], s76 offen offset:2048 lds\n" \
  "buffer_load_dwordx4 %[hvo], %[WB], s76 offen offset:3072 lds\n" \
  "s_add_i32 m0, %[hl0], 20480\n" \
  "s_mov_b32 s76, 4096\n" \
  "buffer_load_dwordx4 %[hvo], %[WB], s76 offen offset:0 lds\n" \
  "buffer_load_dwordx4 %[hvo], %[WB], s76 offen offset:1024 lds\n" \
  "buffer_load_dwordx4 %[hvo], %[WB], s76 offen offset:2048 lds\n" \
  "buffer_load_dwordx4 %[hvo], %[WB], s76 offen offset:3072 lds\n" \
  "s_add_i32 m0, %[hl0], 24576\n" \
  "s_mov_b32 s76, 8192\n" \
  "buffer_load_dwordx4 %[hvo], %[WB], s76 offen offset:0 lds\n" \
  "ds_read_b64_tr_b16 a[64:65], v190 offset:0\n" \
  "ds_read_b64_tr_b16 a[66:67], v190 offset:1024\n" \
  "ds_read_b64_tr_b16 a[68:69], v190 offset:8192\n" \
  "ds_read_b64_tr_b16 a[70:71], v190 offset:9216\n" \
  "ds_read_b64_tr_b16 a[72:73], v191 offset:0\n" \
  "ds_read_b64_tr_b16 a[74:75], v191 offset:1024\n" \
  "ds_read_b64_tr_b16 a[76:77], v191 offset:8192\n" \
  "ds_read_b64_tr_b16 a[78:79], v191 offset:9216\n" \
  "s_waitcnt vmcnt(21)\n" \
  "s_waitcnt lgkmcnt(0)\n" \
  "s_barrier\n" \
  "ds_read_b64_tr_b16 a[80:81], v190 offset:16384\n" \
  "ds_read_b64_tr_b16 a[82:83], v190 offset:17408\n" \
  "ds_read_b64_tr_b16 a[84:85], v190 offset:24576\n" \
  "ds_read_b64_tr_b16 a[86:87], v190 offset:25600\n" \
  "ds_read_b64_tr_b16 a[88:89], v191 offset:16384\n" \
  "ds_read_b64_tr_b16 a[90:91], v191 offset:17408\n" \
  "ds_read_b64_tr_b16 a[92:93], v191 offset:24576\n" \
  "ds_read_b64_tr_b16 a[94:95], v191 offset:25600\n" \
  "s_waitcnt vmcnt(17)\n" \
  "s_waitcnt lgkmcnt(0)\n" \
  "s_barrier\n" \
  "s_cmp_eq_u32 %[grp], 0\n" \
  "s_cbranch_scc1 .Lvt_ws11\n" \
  "s_sleep 16\n" \
  ".Lvt_ws11:\n" \
  "s_add_i32 m0, %[hl0], 53248\n" \
  "s_mov_b32 s76, 36864\n" \
  "buffer_load_dwordx4 %[hvo], %[WB], s76 offen offset:0 lds\n" \
  "buffer_load_dwordx4 %[hvo], %[WB], s76 offen offset:1024 lds\n" \
  "buffer_load_dwordx4 %[hvo], %[WB], s76 offen offset:2048 lds\n" \
  "buffer_load_dwordx4 %[hvo], %[WB], s76 offen offset:3072 lds\n" \
  "s_add_i32 m0, %[hl0], 57344\n" \
  "s_mov_b32 s76, 40960\n" \
  "buffer_load_dwordx4 %[hvo], %[WB], s76 offen offset:0 lds\n" \
  "buffer_load_dwordx4 %[hvo], %[WB], s76 offen offset:1024 lds\n" \
  "buffer_load_dwordx4 %[hvo], %[WB], s76 offen offset:2048 lds\n" \
  "buffer_load_dwordx4 %[hvo], %[WB], s76 offen offset:3072 lds\n" \
  "s_add_i32 m0, %[hl0], 61440\n" \
  "s_mov_b32 s76, 45056\n" \
  "buffer_load_dwordx4 %[hvo], %[WB], s76 offen offset:0 lds\n" \
  "ds_read_b64_tr_b16 a[96:97], v190 offset:32768\n" \
  "ds_read_b64_tr_b16 a[98:99], v190 offset:33792\n" \
  "ds_read_b64_tr_b16 a[100:101], v190 offset:40960\n" \
  "ds_read_b64_tr_b16 a[102:103], v190 offset:41984\n" \
  "ds_read_b64_tr_b16 a[104:105], v191 offset:32768\n" \
  "ds_read_b64_tr_b16 a[106:107], v191 offset:33792\n" \
  "ds_read_b64_tr_b16 a[108:109], v191 offset:40960\n" \
  "ds_read_b64_tr_b16 a[110:111], v191 offset:41984\n" \
  "s_waitcnt vmcnt(22)\n" \
  "s_waitcnt lgkmcnt(0)\n" \
  "s_barrier\n" \
  "ds_read_b64_tr_b16 a[112:113], v190 offset:49152\n" \
  "ds_read_b64_tr_b16 a[114:115], v190 offset:50176\n" \
  "ds_read_b64_tr_b16 a[116:117], v190 offset:57344\n" \
  "ds_read_b64_tr_b16 a[118:119], v190 offset:58368\n" \
  "ds_read_b64_tr_b16 a[120:121], v191 offset:49152\n" \
  "ds_read_b64_tr_b16 a[122:123], v191 offset:50176\n" \
  "ds_read_b64_tr_b16 a[124:125], v191 offset:57344\n" \
  "ds_read_b64_tr_b16 a[126:127], v191 offset:58368\n" \
  "s_waitcnt vmcnt(18)\n" \
  "s_waitcnt lgkmcnt(0)\n" \
  "s_barrier\n" \
  "s_cmp_eq_u32 %[grp], 0\n" \
  "s_cbranch_scc1 .Lvt_ws12\n" \
  "s_sleep 16\n" \
  ".Lvt_ws12:\n" \
  "s_add_i32 m0, %[hl0], 90112\n" \
  "s_mov_b32 s76, 73728\n" \
  "buffer_load_dwordx4 %[hvo], %[WB], s76 offen offset:0 lds\n" \
  "buffer_load_dwordx4 %[hvo], %[WB], s76 offen offset:1024 lds\n" \
  "buffer_load_dwordx4 %[hvo], %[WB], s76 offen offset:2048 lds\n" \
  "buffer_load_dwordx4 %[hvo], %[WB], s76 offen offset:3072 lds\n" \
  "s_add_i32 m0, %[hl0], 94208\n" \
  "s_mov_b32 s76, 77824\n" \
  "buffer_load_dwordx4 %[hvo], %[WB], s76 offen offset:0 lds\n" \
  "buffer_load_dwordx4 %[hvo], %[WB], s76 offen offset:1024 lds\n" \
  "buffer_load_dwordx4 %[hvo], %[WB], s76 offen offset:2048 lds\n" \
  "buffer_load_dwordx4 %[hvo], %[WB], s76 offen offset:3072 lds\n" \
  "s_add_i32 m0, %[hl0], 98304\n" \
  "s_mov_b32 s76, 81920\n" \
  "buffer_load_dwordx4 %[hvo], %[WB], s76 offen offset:0 lds\n" \
  "ds_read_b64_tr_b16 a[128:129], %[wr] offset:0\n" \
  "ds_read_b64_tr_b16 a[130:131], %[wr] offset:1024\n" \
  "ds_read_b64_tr_b16 a[132:133], %[wr] offset:8192\n" \
  "ds_read_b64_tr_b16 a[134:135], %[wr] offset:9216\n" \
  "ds_read_b64_tr_b16 a[136:137], v189 offset:0\n" \
  "ds_read_b64_tr_b16 a[138:139], v189 offset:1024\n" \
  "ds_read_b64_tr_b16 a[140:141], v189 offset:8192\n" \
  "ds_read_b64_tr_b16 a[142:143], v189 offset:9216\n" \
  "s_waitcnt vmcnt(18)\n" \
  "s_waitcnt lgkmcnt(0)\n" \
  "s_barrier\n" \
  "v_add_u32 v192, 16384, %[wr2]\n" \
  "ds_read_b64_tr_b16 a[144:145], v192 offset:0\n" \
  "ds_read_b64_tr_b16 a[146:147], v192 offset:18432\n" \
  "ds_read_b64_tr_b16 a[160:161], v192 offset:32\n" \
  "ds_read_b64_tr_b16 a[162:163], v192 offset:18464\n" \
  "ds_read_b64_tr_b16 a[176:177], v192 offset:64\n" \
  "ds_read_b64_tr_b16 a[178:179], v192 offset:18496\n" \
  "ds_read_b64_tr_b16 a[192:193], v192 offset:96\n" \
  "ds_read_b64_tr_b16 a[194:195], v192 offset:18528\n" \
  "ds_read_b64_tr_b16 a[208:209], v192 offset:128\n" \
  "ds_read_b64_tr_b16 a[210:211], v192 offset:18560\n" \
  "ds_read_b64_tr_b16 a[224:225], v192 offset:160\n" \
  "ds_read_b64_tr_b16 a[226:227], v192 offset:18592\n" \
  "ds_read_b64_tr_b16 a[240:241], v192 offset:192\n" \
  "ds_read_b64_tr_b16 a[242:243], v192 offset:18624\n" \
  "ds_read_b64_tr_b16 v[32:33], v192 offset:224\n" \
  "ds_read_b64_tr_b16 v[34:35], v192 offset:18656\n" \
  "ds_read_b64_tr_b16 v[48:49], v192 offset:256\n" \
  "ds_read_b64_tr_b16 v[50:51], v192 offset:18688\n" \
  "s_waitcnt vmcnt(9)\n" \
  "s_waitcnt lgkmcnt(0)\n" \
  "s_barrier\n" \
  "s_cmp_eq_u32 %[grp], 0\n" \
  "s_cbranch_scc1 .Lvt_ws13\n" \
  "s_sleep 16\n" \
  ".Lvt_ws13:\n" \
  "s_add_i32 m0, %[hl0], 0\n" \
  "s_mov_b32 s76, 110592\n" \
  "buffer_load_dwordx4 %[hvo], %[WB], s76 offen offset:0 lds\n" \
  "buffer_load_dwordx4 %[hvo], %[WB], s76 offen offset:1024 lds\n" \
  "buffer_load_dwordx4 %[hvo], %[WB], s76 offen offset:2048 lds\n" \
  "buffer_load_dwordx4 %[hvo], %[WB], s76 offen offset:3072 lds\n" \
  "s_add_i32 m0, %[hl0], 4096\n" \
  "s_mov_b32 s76, 114688\n" \
  "buffer_load_dwordx4 %[hvo], %[WB], s76 offen offset:0 lds\n" \
  "buffer_load_dwordx4 %[hvo], %[WB], s76 offen offset:1024 lds\n" \
  "buffer_load_dwordx4 %[hvo], %[WB], s76 offen offset:2048 lds\n" \
  "buffer_load_dwordx4 %[hvo], %[WB], s76 offen offset:3072 lds\n" \
  "s_add_i32 m0, %[hl0], 8192\n" \
  "s_mov_b32 s76, 118784\n" \
  "buffer_load_dwordx4 %[hvo], %[WB], s76 offen offset:0 lds\n" \
  "v_add_u32 v192, 53248, %[wr2]\n" \
  "ds_read_b64_tr_b16 a[148:149], v192 offset:0\n" \
  "ds_read_b64_tr_b16 a[150:151], v192 offset:18432\n" \
  "ds_read_b64_tr_b16 a[164:165], v192 offset:32\n" \
  "ds_read_b64_tr_b16 a[166:167], v192 offset:18464\n" \
  "ds_read_b64_tr_b16 a[180:181], v192 offset:64\n" \
  "ds_read_b64_tr_b16 a[182:183], v192 offset:18496\n" \
  "ds_read_b64_tr_b16 a[196:197], v192 offset:96\n" \
  "ds_read_b64_tr_b16 a[198:199], v192 offset:18528\n" \
  "ds_read_b64_tr_b16 a[212:213], v192 offset:128\n" \
  "ds_read_b64_tr_b16 a[214:215], v192 offset:18560\n" \
  "ds_read_b64_tr_b16 a[228:229], v192 offset:160\n" \
  "ds_read_b64_tr_b16 a[230:231], v192 offset:18592\n" \
  "ds_read_b64_tr_b16 a[244:245], v192 offset:192\n" \
  "ds_read_b64_tr_b16 a[246:247], v192 offset:18624\n" \
  "ds_read_b64_tr_b16 v[36:37], v192 offset:224\n" \
  "ds_read_b64_tr_b16 v[38:39], v192 offset:18656\n" \
  "ds_read_b64_tr_b16 v[52:53], v192 offset:256\n" \
  "ds_read_b64_tr_b16 v[54:55], v192 offset:18688\n" \
  "s_waitcnt vmcnt(9)\n" \
  "s_waitcnt lgkmcnt(0)\n" \
  "s_barrier\n" \
  "v_add_u32 v192, 90112, %[wr2]\n" \
  "ds_read_b64_tr_b16 a[152:153], v192 offset:0\n" \
  "ds_read_b64_tr_b16 a[154:155], v192 offset:18432\n" \
  "ds_read_b64_tr_b16 a[168:169], v192 offset:32\n" \
  "ds_read_b64_tr_b16 a[170:171], v192 offset:18464\n" \
  "ds_read_b64_tr_b16 a[184:185], v192 offset:64\n" \
  "ds_read_b64_tr_b16 a[186:187], v192 offset:18496\n" \
  "ds_read_b64_tr_b16 a[200:201], v192 offset:96\n" \
  "ds_read_b64_tr_b16 a[202:203], v192 offset:18528\n" \
  "ds_read_b64_tr_b16 a[216:217], v192 offset:128\n" \
  "ds_read_b64_tr_b16 a[218:219], v192 offset:18560\n" \
  "ds_read_b64_tr_b16 a[232:233], v192 offset:160\n" \
  "ds_read_b64_tr_b16 a[234:235], v192 offset:18592\n" \
  "ds_read_b64_tr_b16 a[248:249], v192 offset:192\n" \
  "ds_read_b64_tr_b16 a[250:251], v192 offset:18624\n" \
  "ds_read_b64_tr_b16 v[40:41], v192 offset:224\n" \
  "ds_read_b64_tr_b16 v[42:43], v192 offset:18656\n" \
  "ds_read_b64_tr_b16 v[56:57], v192 offset:256\n" \
  "ds_read_b64_tr_b16 v[58:59], v192 offset:18688\n" \
  "s_waitcnt vmcnt(0)\n" \
  "s_waitcnt lgkmcnt(0)\n" \
  "s_barrier\n" \
  "v_add_u32 v192, 0, %[wr2]\n" \
  "ds_read_b64_tr_b16 a[156:157], v192 offset:0\n" \
  "ds_read_b64_tr_b16 a[158:159], v192 offset:18432\n" \
  "ds_read_b64_tr_b16 a[172:173], v192 offset:32\n" \
  "ds_read_b64_tr_b16 a[174:175], v192 offset:18464\n" \
  "ds_read_b64_tr_b16 a[188:189], v192 offset:64\n" \
  "ds_read_b64_tr_b16 a[190:191], v192 offset:18496\n" \
  "ds_read_b64_tr_b16 a[204:205], v192 offset:96\n" \
  "ds_read_b64_tr_b16 a[206:207], v192 offset:18528\n" \
  "ds_read_b64_tr_b16 a[220:221], v192 offset:128\n" \
  "ds_read_b64_tr_b16 a[222:223], v192 offset:18560\n" \
  "ds_read_b64_tr_b16 a[236:237], v192 offset:160\n" \
  "ds_read_b64_tr_b16 a[238:239], v192 offset:18592\n" \
  "ds_read_b64_tr_b16 a[252:253], v192 offset:192\n" \
  "ds_read_b64_tr_b16 a[254:255], v192 offset:18624\n" \
  "ds_read_b64_tr_b16 v[44:45], v192 offset:224\n" \
  "ds_read_b64_tr_b16 v[46:47], v192 offset:18656\n" \
  "ds_read_b64_tr_b16 v[60:61], v192 offset:256\n" \
  "ds_read_b64_tr_b16 v[62:63], v192 offset:18688\n" \
  "s_waitcnt lgkmcnt(0)\n" \
  "s_barrier\n" \
  "s_cmp_eq_u32 %[grp], 0\n" \
  "s_cbranch_scc1 .Lvt_go\n" \
  "s_mov_b32 s65, 0\n" \
  "s_xor_b32 s75, %[bas], 0x6000\n" \
  ".Lvt_g1:\n" \
  "buffer_load_dword v224, off, %[X], s75 offset:384 sc1\n" \
  "s_waitcnt vmcnt(0)\n" \
  "v_readfirstlane_b32 s74, v224\n" \
  "s_cmp_lg_u32 s74, 0\n" \
  "s_cbranch_scc1 .Lvt_g2\n" \
  "s_sleep 8\n" \
  "s_add_i32 s65, s65, 1\n" \
  "s_cmp_lt_u32 s65, 400\n" \
  "s_cbranch_scc1 .Lvt_g1\n" \
  ".Lvt_g2:\n" \
  "s_add_i32 s74, s74, %[g1t]\n" \
  ".Lvt_g3:\n" \
  "s_memrealtime s[76:77]\n" \
  "s_waitcnt lgkmcnt(0)\n" \
  "s_sub_i32 s76, s76, s74\n" \
  "s_cmp_gt_u32 s76, -3000\n" \
  "s_cbranch_scc1 .Lvt_g3\n" \
  ".Lvt_go:\n" \
  "s_waitcnt vmcnt(0) lgkmcnt(0)\n" \
  "s_barrier\n" \
  "s_mov_b32 s75, 0\n" \
  "s_movk_i32 s76, 0x1000\n" \
  "s_movk_i32 s77, 0x2000\n" \
  "s_mov_b32 s65, 0\n" \
  ".Lvt_enw:\n" \
  "buffer_load_dwordx4 v[188:191], %[hvo], %[HS], s75 offen offset:0 sc1\n" \
  "buffer_load_dwordx4 v[192:195], %[hvo], %[HS], s75 offen offset:1024 sc1\n" \
  "buffer_load_dwordx4 v[196:199], %[hvo], %[HS], s75 offen offset:2048 sc1\n" \
  "buffer_load_dwordx4 v[200:203], %[hvo], %[HS], s75 offen offset:3072 sc1\n" \
  "buffer_load_dwordx4 v[204:207], %[hvo], %[HS], s76 offen offset:0 sc1\n" \
  "buffer_load_dwordx4 v[208:211], %[hvo], %[HS], s76 offen offset:1024 sc1\n" \
  "buffer_load_dwordx4 v[212:215], %[hvo], %[HS], s76 offen offset:2048 sc1\n" \
  "buffer_load_dwordx4 v[216:219], %[hvo], %[HS], s76 offen offset:3072 sc1\n" \
  "buffer_load_dwordx4 v[220:223], %[hvo], %[HS], s77 offen offset:0 sc1\n" \
  "s_waitcnt vmcnt(0)\n" \
  "v_max3_u32 v224, v188, v189, v190\n" \
  "v_max3_u32 v224, v224, v191, v192\n" \
  "v_max3_u32 v224, v224, v193, v194\n" \
  "v_max3_u32 v224, v224, v195, v196\n" \
  "v_max3_u32 v224, v224, v197, v198\n" \
  "v_max3_u32 v224, v224, v199, v200\n" \
  "v_max3_u32 v224, v224, v201, v202\n" \
  "v_max3_u32 v224, v224, v203, v204\n" \
  "v_max3_u32 v224, v224, v205, v206\n" \
  "v_max3_u32 v224, v224, v207, v208\n" \
  "v_max3_u32 v224, v224, v209, v210\n" \
  "v_max3_u32 v224, v224, v211, v212\n" \
  "v_max3_u32 v224, v224, v213, v214\n" \
  "v_max3_u32 v224, v224, v215, v216\n" \
  "v_max3_u32 v224, v224, v217, v218\n" \
  "v_max3_u32 v224, v224, v219, v220\n" \
  "v_max3_u32 v224, v224, v221, v222\n" \
  "v_max_u32 v224, v224, v223\n" \
  "v_cmp_eq_u32 vcc, -1, v224\n" \
  "s_cbranch_vccz .Lvt_enw_ok\n" \
  "s_sleep 4\n" \
  "s_add_i32 s65, s65, 1\n" \
  "s_and_b32 s64, s65, 1023\n" \
  "s_cbranch_scc1 .Lvt_enw\n" \
  "buffer_load_dword v224, off, %[F], 0 offset:1024 sc1\n" \
  "s_waitcnt vmcnt(0)\n" \
  "v_readfirstlane_b32 s64, v224\n" \
  "s_cmp_lg_u32 s64, 0\n" \
  "s_cbranch_scc1 .Lvt_enw_ok\n" \
  "s_cmp_lt_u32 s65, 400000\n" \
  "s_cbranch_scc1 .Lvt_enw\n" \
  "v_mov_b32 v224, 1\n" \
  "buffer_store_dword v224, off, %[F], 0 offset:1024 sc1\n" \
  ".Lvt_enw_ok:\n" \
  "ds_write_b128 %[hvo], v[188:191] offset:0\n" \
  "ds_write_b128 %[hvo], v[192:195] offset:1024\n" \
  "ds_write_b128 %[hvo], v[196:199] offset:2048\n" \
  "ds_write_b128 %[hvo], v[200:203] offset:3072\n" \
  "ds_write_b128 %[hvo], v[204:207] offset:4096\n" \
  "ds_write_b128 %[hvo], v[208:211] offset:5120\n" \
  "ds_write_b128 %[hvo], v[212:215] offset:6144\n" \
  "ds_write_b128 %[hvo], v[216:219] offset:7168\n" \
  "ds_write_b128 %[hvo], v[220:223] offset:8192\n" \
  "s_waitcnt lgkmcnt(0)\n" \
  "s_barrier\n" \
  "v_mov_b32 v226, 1\n" \
  "s_add_i32 s76, %[bas], -384\n" \
  "buffer_atomic_add v226, %[rdy], %[X], s76 offen sc1\n" \
  "ds_read_b128 v[80:83], %[hA] offset:0\n" \
  "ds_read_b128 v[84:87], %[hA] offset:18432\n" \
  "ds_read_b128 v[88:91], %[hA] offset:512\n" \
  "ds_read_b128 v[92:95], %[hA] offset:18944\n" \
  "ds_read_b128 v[96:99], %[hA] offset:1024\n" \
  "ds_read_b128 v[100:103], %[hA] offset:19456\n" \
  "s_waitcnt lgkmcnt(0)\n" \
  "s_branch .Lvt_al_ok\n" \
  ".Lvt_w:\n" \
  "s_cmp_lt_i32 s60, 1\n" \
  "s_cbranch_scc1 .Lvt_w_ok\n" \
  "s_mov_b32 s65, 0\n" \
  ".Lvt_ww:\n" \
  "buffer_load_dwordx2 v[228:229], off, %[X], s67 sc1\n" \
  "s_waitcnt vmcnt(0)\n" \
  "v_readfirstlane_b32 s64, v228\n" \
  "s_cmp_lg_u32 s64, 0\n" \
  "s_cbranch_scc1 .Lvt_w_ok\n" \
  "s_sleep 2\n" \
  "s_add_i32 s65, s65, 1\n" \
  "s_and_b32 s64, s65, 1023\n" \
  "s_cbranch_scc1 .Lvt_ww\n" \
  "buffer_load_dword v224, off, %[F], 0 offset:1024 sc1\n" \
  "s_waitcnt vmcnt(0)\n" \
  "v_readfirstlane_b32 s64, v224\n" \
  "s_cmp_lg_u32 s64, 0\n" \
  "s_cbranch_scc1 .Lvt_ww_ok\n" \
  "s_cmp_lt_u32 s65, 400000\n" \
  "s_cbranch_scc1 .Lvt_ww\n" \
  "v_mov_b32 v224, 1\n" \
  "buffer_store_dword v224, off, %[F], 0 offset:1024 sc1\n" \
  ".Lvt_ww_ok:\n" \
  "s_branch .Lvt_w_ok\n" \
  ".Lvt_sh:\n" \
  "s_cmp_lt_i32 s60, 0\n" \
  "s_cbranch_scc1 .Lvt_sh_back\n" \
  "s_mul_i32 s75, s60, 36864\n" \
  "s_add_i32 s76, s75, 0x1000\n" \
  "s_add_i32 s77, s75, 0x2000\n" \
  "s_waitcnt lgkmcnt(0)\n" \
  "s_barrier\n" \
  "s_mov_b32 s65, 0\n" \
  ".Lvt_shw:\n" \
  "buffer_load_dwordx4 v[188:191], %[hvo], %[HS], s75 offen offset:0 sc1\n" \
  "buffer_load_dwordx4 v[192:195], %[hvo], %[HS], s75 offen offset:1024 sc1\n" \
  "buffer_load_dwordx4 v[196:199], %[hvo], %[HS], s75 offen offset:2048 sc1\n" \
  "buffer_load_dwordx4 v[200:203], %[hvo], %[HS], s75 offen offset:3072 sc1\n" \
  "buffer_load_dwordx4 v[204:207], %[hvo], %[HS], s76 offen offset:0 sc1\n" \
  "buffer_load_dwordx4 v[208:211], %[hvo], %[HS], s76 offen offset:1024 sc1\n" \
  "buffer_load_dwordx4 v[212:215], %[hvo], %[HS], s76 offen offset:2048 sc1\n" \
  "buffer_load_dwordx4 v[216:219], %[hvo], %[HS], s76 offen offset:3072 sc1\n" \
  "buffer_load_dwordx4 v[220:223], %[hvo], %[HS], s77 offen offset:0 sc1\n" \
  "s_waitcnt vmcnt(0)\n" \
  "v_max3_u32 v224, v188, v189, v190\n" \
  "v_max3_u32 v224, v224, v191, v192\n" \
  "v_max3_u32 v224, v224, v193, v194\n" \
  "v_max3_u32 v224, v224, v195, v196\n" \
  "v_max3_u32 v224, v224, v197, v198\n" \
  "v_max3_u32 v224, v224, v199, v200\n" \
  "v_max3_u32 v224, v224, v201, v202\n" \
  "v_max3_u32 v224, v224, v203, v204\n" \
  "v_max3_u32 v224, v224, v205, v206\n" \
  "v_max3_u32 v224, v224, v207, v208\n" \
  "v_max3_u32 v224, v224, v209, v210\n" \
  "v_max3_u32 v224, v224, v211, v212\n" \
  "v_max3_u32 v224, v224, v213, v214\n" \
  "v_max3_u32 v224, v224, v215, v216\n" \
  "v_max3_u32 v224, v224, v217, v218\n" \
  "v_max3_u32 v224, v224, v219, v220\n" \
  "v_max3_u32 v224, v224, v221, v222\n" \
  "v_max_u32 v224, v224, v223\n" \
  "v_cmp_eq_u32 vcc, -1, v224\n" \
  "s_cbranch_vccz .Lvt_shw_ok\n" \
  "s_sleep 4\n" \
  "s_add_i32 s65, s65, 1\n" \
  "s_and_b32 s64, s65, 1023\n" \
  "s_cbranch_scc1 .Lvt_shw\n" \
  "buffer_load_dword v224, off, %[F], 0 offset:1024 sc1\n" \
  "s_waitcnt vmcnt(0)\n" \
  "v_readfirstlane_b32 s64, v224\n" \
  "s_cmp_lg_u32 s64, 0\n" \
  "s_cbranch_scc1 .Lvt_shw_ok\n" \
  "s_cmp_lt_u32 s65, 400000\n" \
  "s_cbranch_scc1 .Lvt_shw\n" \
  "v_mov_b32 v224, 1\n" \
  "buffer_store_dword v224, off, %[F], 0 offset:1024 sc1\n" \
  ".Lvt_shw_ok:\n" \
  "s_xor_b32 s64, s62, 0x10000\n" \
  "v_add_u32 v224, s64, %[la]\n" \
  "ds_write_b128 v224, v[188:191] offset:0\n" \
  "ds_write_b128 v224, v[192:195] offset:1024\n" \
  "ds_write_b128 v224, v[196:199] offset:2048\n" \
  "ds_write_b128 v224, v[200:203] offset:3072\n" \
  "ds_write_b128 v224, v[204:207] offset:4096\n" \
  "ds_write_b128 v224, v[208:211] offset:5120\n" \
  "ds_write_b128 v224, v[212:215] offset:6144\n" \
  "ds_write_b128 v224, v[216:219] offset:7168\n" \
  "ds_write_b128 v224, v[220:223] offset:8192\n" \
  "s_waitcnt lgkmcnt(0)\n" \
  "s_barrier\n" \
  "ds_read_b128 v[80:83], %[hA] offset:0\n" \
  "ds_read_b128 v[84:87], %[hA] offset:18432\n" \
  "ds_read_b128 v[88:91], %[hA] offset:512\n" \
  "ds_read_b128 v[92:95], %[hA] offset:18944\n" \
  "ds_read_b128 v[96:99], %[hA] offset:1024\n" \
  "ds_read_b128 v[100:103], %[hA] offset:19456\n" \
  "s_waitcnt lgkmcnt(4)\n" \
  "v_mfma_f32_16x16x32_bf16 v[64:67], a[0:3], v[80:83], 0\n" \
  "ds_read_b128 v[104:107], %[hA] offset:1536\n" \
  "v_mfma_f32_16x16x32_bf16 v[72:75], a[0:3], v[84:87], 0\n" \
  "ds_read_b128 v[108:111], %[hA] offset:19968\n" \
  "v_mfma_f32_16x16x32_bf16 v[68:71], a[8:11], v[80:83], 0\n" \
  "v_mfma_f32_16x16x32_bf16 v[76:79], a[8:11], v[84:87], 0\n" \
  "s_waitcnt lgkmcnt(4)\n" \
  "v_mfma_f32_16x16x32_bf16 v[64:67], a[4:7], v[88:91], v[64:67]\n" \
  "ds_read_b128 v[80:83], %[hA] offset:2048\n" \
  "v_mfma_f32_16x16x32_bf16 v[72:75], a[4:7], v[92:95], v[72:75]\n" \
  "ds_read_b128 v[84:87], %[hA] offset:20480\n" \
  "v_mfma_f32_16x16x32_bf16 v[68:71], a[12:15], v[88:91], v[68:71]\n" \
  "v_mfma_f32_16x16x32_bf16 v[76:79], a[12:15], v[92:95], v[76:79]\n" \
  "s_waitcnt lgkmcnt(4)\n" \
  "v_mfma_f32_16x16x32_bf16 v[64:67], a[16:19], v[96:99], v[64:67]\n" \
  "ds_read_b128 v[88:91], %[hA] offset:2560\n" \
  "v_mfma_f32_16x16x32_bf16 v[72:75], a[16:19], v[100:103], v[72:75]\n" \
  "ds_read_b128 v[92:95], %[hA] offset:20992\n" \
  "v_mfma_f32_16x16x32_bf16 v[68:71], a[24:27], v[96:99], v[68:71]\n" \
  "v_mfma_f32_16x16x32_bf16 v[76:79], a[24:27], v[100:103], v[76:79]\n" \
  "s_waitcnt lgkmcnt(4)\n" \
  "v_mfma_f32_16x16x32_bf16 v[64:67], a[20:23], v[104:107], v[64:67]\n" \
  "ds_read_b128 v[96:99], %[hA] offset:3072\n" \
  "v_mfma_f32_16x16x32_bf16 v[72:75], a[20:23], v[108:111], v[72:75]\n" \
  "ds_read_b128 v[100:103], %[hA] offset:21504\n" \
  "v_mfma_f32_16x16x32_bf16 v[68:71], a[28:31], v[104:107], v[68:71]\n" \
  "v_mfma_f32_16x16x32_bf16 v[76:79], a[28:31], v[108:111], v[76:79]\n" \
  "s_waitcnt lgkmcnt(4)\n" \
  "v_mfma_f32_16x16x32_bf16 v[64:67], a[32:35], v[80:83], v[64:67]\n" \
  "ds_read_b128 v[104:107], %[hA] offset:3584\n" \
  "v_mfma_f32_16x16x32_bf16 v[72:75], a[32:35], v[84:87], v[72:75]\n" \
  "ds_read_b128 v[108:111], %[hA] offset:22016\n" \
  "v_mfma_f32_16x16x32_bf16 v[68:71], a[40:43], v[80:83], v[68:71]\n" \
  "v_mfma_f32_16x16x32_bf16 v[76:79], a[40:43], v[84:87], v[76:79]\n" \
  "s_waitcnt lgkmcnt(4)\n" \
  "v_mfma_f32_16x16x32_bf16 v[64:67], a[36:39], v[88:91], v[64:67]\n" \
  "ds_read_b128 v[80:83], %[hA] offset:4096\n" \
  "v_mfma_f32_16x16x32_bf16 v[72:75], a[36:39], v[92:95], v[72:75]\n" \
  "ds_read_b128 v[84:87], %[hA] offset:22528\n" \
  "v_mfma_f32_16x16x32_bf16 v[68:71], a[44:47], v[88:91], v[68:71]\n" \
  "v_mfma_f32_16x16x32_bf16 v[76:79], a[44:47], v[92:95], v[76:79]\n" \
  "s_waitcnt lgkmcnt(4)\n" \
  "v_mfma_f32_16x16x32_bf16 v[64:67], a[48:51], v[96:99], v[64:67]\n" \
  "ds_read_b128 v[88:91], %[hA] offset:4608\n" \
  "v_mfma_f32_16x16x32_bf16 v[72:75], a[48:51], v[100:103], v[72:75]\n" \
  "ds_read_b128 v[92:95], %[hA] offset:23040\n" \
  "v_mfma_f32_16x16x32_bf16 v[68:71], a[56:59], v[96:99], v[68:71]\n" \
  "v_mfma_f32_16x16x32_bf16 v[76:79], a[56:59], v[100:103], v[76:79]\n" \
  "s_waitcnt lgkmcnt(4)\n" \
  "v_mfma_f32_16x16x32_bf16 v[64:67], a[52:55], v[104:107], v[64:67]\n" \
  "ds_read_b128 v[96:99], %[hA] offset:5120\n" \
  "v_mfma_f32_16x16x32_bf16 v[72:75], a[52:55], v[108:111], v[72:75]\n" \
  "ds_read_b128 v[100:103], %[hA] offset:23552\n" \
  "v_mfma_f32_16x16x32_bf16 v[68:71], a[60:63], v[104:107], v[68:71]\n" \
  "v_mfma_f32_16x16x32_bf16 v[76:79], a[60:63], v[108:111], v[76:79]\n" \
  "s_waitcnt lgkmcnt(4)\n" \
  "v_mfma_f32_16x16x32_bf16 v[64:67], a[64:67], v[80:83], v[64:67]\n" \
  "ds_read_b128 v[104:107], %[hA] offset:5632\n" \
  "v_mfma_f32_16x16x32_bf16 v[72:75], a[64:67], v[84:87], v[72:75]\n" \
  "ds_read_b128 v[108:111], %[hA] offset:24064\n" \
  "v_mfma_f32_16x16x32_bf16 v[68:71], a[72:75], v[80:83], v[68:71]\n" \
  "v_mfma_f32_16x16x32_bf16 v[76:79], a[72:75], v[84:87], v[76:79]\n" \
  "s_waitcnt lgkmcnt(4)\n" \
  "v_mfma_f32_16x16x32_bf16 v[64:67], a[68:71], v[88:91], v[64:67]\n" \
  "ds_read_b128 v[80:83], %[hA] offset:6144\n" \
  "v_mfma_f32_16x16x32_bf16 v[72:75], a[68:71], v[92:95], v[72:75]\n" \
  "ds_read_b128 v[84:87], %[hA] offset:24576\n" \
  "v_mfma_f32_16x16x32_bf16 v[68:71], a[76:79], v[88:91], v[68:71]\n" \
  "v_mfma_f32_16x16x32_bf16 v[76:79], a[76:79], v[92:95], v[76:79]\n" \
  "s_waitcnt lgkmcnt(4)\n" \
  "v_mfma_f32_16x16x32_bf16 v[64:67], a[80:83], v[96:99], v[64:67]\n" \
  "ds_read_b128 v[88:91], %[hA] offset:6656\n" \
  "v_mfma_f32_16x16x32_bf16 v[72:75], a[80:83], v[100:103], v[72:75]\n" \
  "ds_read_b128 v[92:95], %[hA] offset:25088\n" \
  "v_mfma_f32_16x16x32_bf16 v[68:71], a[88:91], v[96:99], v[68:71]\n" \
  "v_mfma_f32_16x16x32_bf16 v[76:79], a[88:91], v[100:103], v[76:79]\n" \
  "s_waitcnt lgkmcnt(4)\n" \
  "v_mfma_f32_16x16x32_bf16 v[64:67], a[84:87], v[104:107], v[64:67]\n" \
  "ds_read_b128 v[96:99], %[hA] offset:7168\n" \
  "v_mfma_f32_16x16x32_bf16 v[72:75], a[84:87], v[108:111], v[72:75]\n" \
  "ds_read_b128 v[100:103], %[hA] offset:25600\n" \
  "v_mfma_f32_16x16x32_bf16 v[68:71], a[92:95], v[104:107], v[68:71]\n" \
  "v_mfma_f32_16x16x32_bf16 v[76:79], a[92:95], v[108:111], v[76:79]\n" \
  "s_waitcnt lgkmcnt(4)\n" \
  "v_mfma_f32_16x16x32_bf16 v[64:67], a[96:99], v[80:83], v[64:67]\n" \
  "ds_read_b128 v[104:107], %[hA] offset:7680\n" \
  "v_mfma_f32_16x16x32_bf16 v[72:75], a[96:99], v[84:87], v[72:75]\n" \
  "ds_read_b128 v[108:111], %[hA] offset:26112\n" \
  "v_mfma_f32_16x16x32_bf16 v[68:71], a[104:107], v[80:83], v[68:71]\n" \
  "v_mfma_f32_16x16x32_bf16 v[76:79], a[104:107], v[84:87], v[76:79]\n" \
  "s_waitcnt lgkmcnt(4)\n" \
  "v_mfma_f32_16x16x32_bf16 v[64:67], a[100:103], v[88:91], v[64:67]\n" \
  "ds_read_b128 v[80:83], %[hA] offset:8192\n" \
  "v_mfma_f32_16x16x32_bf16 v[72:75], a[100:103], v[92:95], v[72:75]\n" \
  "ds_read_b128 v[84:87], %[hA] offset:26624\n" \
  "v_mfma_f32_16x16x32_bf16 v[68:71], a[108:111], v[88:91], v[68:71]\n" \
  "v_mfma_f32_16x16x32_bf16 v[76:79], a[108:111], v[92:95], v[76:79]\n" \
  "s_waitcnt lgkmcnt(4)\n" \
  "v_mfma_f32_16x16x32_bf16 v[64:67], a[112:115], v[96:99], v[64:67]\n" \
  "ds_read_b128 v[88:91], %[hA] offset:8704\n" \
  "v_mfma_f32_16x16x32_bf16 v[72:75], a[112:115], v[100:103], v[72:75]\n" \
  "ds_read_b128 v[92:95], %[hA] offset:27136\n" \
  "v_mfma_f32_16x16x32_bf16 v[68:71], a[120:123], v[96:99], v[68:71]\n" \
  "v_mfma_f32_16x16x32_bf16 v[76:79], a[120:123], v[100:103], v[76:79]\n" \
  "s_waitcnt lgkmcnt(4)\n" \
  "v_mfma_f32_16x16x32_bf16 v[64:67], a[116:119], v[104:107], v[64:67]\n" \
  "v_mfma_f32_16x16x32_bf16 v[72:75], a[116:119], v[108:111], v[72:75]\n" \
  "v_mfma_f32_16x16x32_bf16 v[68:71], a[124:127], v[104:107], v[68:71]\n" \
  "v_mfma_f32_16x16x32_bf16 v[76:79], a[124:127], v[108:111], v[76:79]\n" \
  "s_waitcnt lgkmcnt(2)\n" \
  "v_mfma_f32_16x16x32_bf16 v[64:67], a[128:131], v[80:83], v[64:67]\n" \
  "v_mfma_f32_16x16x32_bf16 v[72:75], a[128:131], v[84:87], v[72:75]\n" \
  "v_mfma_f32_16x16x32_bf16 v[68:71], a[136:139], v[80:83], v[68:71]\n" \
  "v_mfma_f32_16x16x32_bf16 v[76:79], a[136:139], v[84:87], v[76:79]\n" \
  "s_waitcnt lgkmcnt(0)\n" \
  "v_mfma_f32_16x16x32_bf16 v[64:67], a[132:135], v[88:91], v[64:67]\n" \
  "v_mfma_f32_16x16x32_bf16 v[72:75], a[132:135], v[92:95], v[72:75]\n" \
  "v_mfma_f32_16x16x32_bf16 v[68:71], a[140:143], v[88:91], v[68:71]\n" \
  "v_mfma_f32_16x16x32_bf16 v[76:79], a[140:143], v[92:95], v[76:79]\n" \
  "s_nop 7\n" \
  "v_cvt_pk_bf16_f32 v112, v64, v65\n" \
  "v_cvt_pk_bf16_f32 v113, v66, v67\n" \
  "v_cvt_pk_bf16_f32 v114, v68, v69\n" \
  "v_cvt_pk_bf16_f32 v115, v70, v71\n" \
  "ds_write_b128 %[vW], v[112:115]\n" \
  "v_cvt_pk_bf16_f32 v116, v72, v73\n" \
  "v_cvt_pk_bf16_f32 v117, v74, v75\n" \
  "v_cvt_pk_bf16_f32 v118, v76, v77\n" \
  "v_cvt_pk_bf16_f32 v119, v78, v79\n" \
  "ds_write_b128 %[vW], v[116:119] offset:4096\n" \
  "s_branch .Lvt_sh_back\n" \
  ".Lvt_late:\n" \
  "s_cmp_lt_i32 s60, 0\n" \
  "s_cbranch_scc1 .Lvt_al_ok\n" \
  "s_cmp_gt_u32 s91, -3000\n" \
  "s_cbranch_scc0 .Lvt_late2\n" \
  ".Lvt_wt:\n" \
  "s_memrealtime s[76:77]\n" \
  "s_waitcnt lgkmcnt(0)\n" \
  "s_sub_i32 s76, s76, s66\n" \
  "s_cmp_lt_i32 s76, -8\n" \
  "s_cbranch_scc1 .Lvt_wt\n" \
  "s_min_i32 s75, s76, -2\n" \
  "s_lshl_b32 s75, s75, 2\n" \
  "s_getpc_b64 s[76:77]\n" \
  "s_add_i32 s75, s75, 48\n" \
  "s_add_u32 s76, s76, s75\n" \
  "s_addc_u32 s77, s77, 0\n" \
  "s_setpc_b64 s[76:77]\n" \
  "s_nop 4\n" \
  "s_nop 4\n" \
  "s_nop 4\n" \
  "s_nop 4\n" \
  "s_nop 4\n" \
  "s_nop 4\n" \
  "s_branch .Lvt_al_ok\n" \
  ".Lvt_late2:\n" \
  "s_cmp_lg_u32 s66, 0\n" \
  "s_cbranch_scc1 .Lvt_late3\n" \
  "s_mov_b32 s65, 0\n" \
  ".Lvt_tw:\n" \
  "buffer_load_dword v224, off, %[X], s67 sc1\n" \
  "s_waitcnt vmcnt(0)\n" \
  "v_readfirstlane_b32 s66, v224\n" \
  "s_cmp_lg_u32 s66, 0\n" \
  "s_cbranch_scc1 .Lvt_tw_ok\n" \
  "s_sleep 1\n" \
  "s_add_i32 s65, s65, 1\n" \
  "s_cmp_lt_u32 s65, 200\n" \
  "s_cbranch_scc1 .Lvt_tw\n" \
  ".Lvt_tw_ok:\n" \
  "s_sub_i32 s91, s74, s66\n" \
  "s_cmp_gt_u32 s91, -3000\n" \
  "s_cbranch_scc1 .Lvt_wt\n" \
  ".Lvt_late3:\n" \
  "s_branch .Lvt_al_ok\n" \
  ".Lvt_end:\n"
#define VT_CLOB "v32", "v33", "v34", "v35", "v36", "v37", "v38", "v39", "v40", "v41", "v42", "v43", "v44", "v45", "v46", "v47", "v48", "v49", "v50", "v51", "v52", "v53", "v54", "v55", "v56", "v57", "v58", "v59", "v60", "v61", "v62", "v63", "v64", "v65", "v66", "v67", "v68", "v69", "v70", "v71", "v72", "v73", "v74", "v75", "v76", "v77", "v78", "v79", "v80", "v81", "v82", "v83", "v84", "v85", "v86", "v87", "v88", "v89", "v90", "v91", "v92", "v93", "v94", "v95", "v96", "v97", "v98", "v99", "v100", "v101", "v102", "v103", "v104", "v105", "v106", "v107", "v108", "v109", "v110", "v111", "v112", "v113", "v114", "v115", "v116", "v117", "v118", "v119", "v120", "v121", "v122", "v123", "v124", "v125", "v126", "v127", "v128", "v129", "v130", "v131", "v132", "v133", "v134", "v135", "v136", "v137", "v138", "v139", "v140", "v141", "v142", "v143", "v144", "v145", "v146", "v147", "v148", "v149", "v150", "v151", "v152", "v153", "v154", "v155", "v156", "v157", "v158", "v159", "v160", "v161", "v162", "v163", "v164", "v165", "v166", "v167", "v168", "v169", "v170", "v171", "v172", "v173", "v174", "v175", "v176", "v177", "v178", "v179", "v180", "v181", "v182", "v183", "v184", "v185", "v186", "v187", "v188", "v189", "v190", "v191", "v192", "v193", "v194", "v195", "v196", "v197", "v198", "v199", "v200", "v201", "v202", "v203", "v204", "v205", "v206", "v207", "v208", "v209", "v210", "v211", "v212", "v213", "v214", "v215", "v216", "v217", "v218", "v219", "v220", "v221", "v222", "v223", "v224", "v225", "v226", "v227", "v228", "v229", "v230", "v231", "v232", "v233", "v234", "v235", "v236", "v237", "v238", "v239", "v240", "v241", "v242", "v243", "v244", "v245", "v246", "v247", "v248", "v249", "v250", "v251", "v252", "v253", "v254", "v255", "s60", "s61", "s62", "s63", "s64", "s65", "s66", "s67", "s68", "s69", "s70", "s71", "s72", "s73", "s74", "s75", "s76", "s77", "s78", "s79", "s80", "s81", "s82", "s83", "s84", "s85", "s86", "s87", "s88", "s89", "s90", "s91", "s92", "s93", "a0", "a1", "a2", "a3", "a4", "a5", "a6", "a7", "a8", "a9", "a10", "a11", "a12", "a13", "a14", "a15", "a16", "a17", "a18", "a19", "a20", "a21", "a22", "a23", "a24", "a25", "a26", "a27", "a28", "a29", "a30", "a31", "a32", "a33", "a34", "a35", "a36", "a37", "a38", "a39", "a40", "a41", "a42", "a43", "a44", "a45", "a46", "a47", "a48", "a49", "a50", "a51", "a52", "a53", "a54", "a55", "a56", "a57", "a58", "a59", "a60", "a61", "a62", "a63", "a64", "a65", "a66", "a67", "a68", "a69", "a70", "a71", "a72", "a73", "a74", "a75", "a76", "a77", "a78", "a79", "a80", "a81", "a82", "a83", "a84", "a85", "a86", "a87", "a88", "a89", "a90", "a91", "a92", "a93", "a94", "a95", "a96", "a97", "a98", "a99", "a100", "a101", "a102", "a103", "a104", "a105", "a106", "a107", "a108", "a109", "a110", "a111", "a112", "a113", "a114", "a115", "a116", "a117", "a118", "a119", "a120", "a121", "a122", "a123", "a124", "a125", "a126", "a127", "a128", "a129", "a130", "a131", "a132", "a133", "a134", "a135", "a136", "a137", "a138", "a139", "a140", "a141", "a142", "a143", "a144", "a145", "a146", "a147", "a148", "a149", "a150", "a151", "a152", "a153", "a154", "a155", "a156", "a157", "a158", "a159", "a160", "a161", "a162", "a163", "a164", "a165", "a166", "a167", "a168", "a169", "a170", "a171", "a172", "a173", "a174", "a175", "a176", "a177", "a178", "a179", "a180", "a181", "a182", "a183", "a184", "a185", "a186", "a187", "a188", "a189", "a190", "a191", "a192", "a193", "a194", "a195", "a196", "a197", "a198", "a199", "a200", "a201", "a202", "a203", "a204", "a205", "a206", "a207", "a208", "a209", "a210", "a211", "a212", "a213", "a214", "a215", "a216", "a217", "a218", "a219", "a220", "a221", "a222", "a223", "a224", "a225", "a226", "a227", "a228", "a229", "a230", "a231", "a232", "a233", "a234", "a235", "a236", "a237", "a238", "a239", "a240", "a241", "a242", "a243", "a244", "a245", "a246", "a247", "a248", "a249", "a250", "a251", "a252", "a253", "a254", "a255", "vcc", "scc", "m0", "memory"
#define O_ASM \
  "s_nop 4\n" \
  "s_mov_b32 s56, -2\n" \
  "v_add_u32 v240, 0x1000, %[la]\n" \
  "s_mov_b32 s78, 0\n" \
  "s_mov_b32 s79, 0\n" \
  "s_mov_b32 s80, 0\n" \
  "s_mov_b32 s87, 2.0\n" \
  "s_mov_b32 s88, 0x7fff\n" \
  "s_mov_b32 s72, 0\n" \
  "v_mov_b64 v[244:245], -1\n" \
  "v_mov_b64 v[246:247], -1\n" \
  "s_mov_b32 s68, 2.0\n" \
  "s_mov_b32 s70, 2.0\n" \
  "v_mov_b32 v242, 0\n" \
  "s_mov_b32 s67, 589824\n" \
  "s_add_i32 s94, %[bas], -256\n" \
  ".Lo_top:\n" \
  "s_memrealtime s[74:75]\n" \
  "s_mov_b32 s73, s86\n" \
  "s_add_i32 s56, s56, 1\n" \
  "s_add_i32 s94, s94, 128\n" \
  "s_lshl_b32 s62, s56, 17\n" \
  "s_and_b32 s63, s62, 0x60000\n" \
  "s_add_i32 s57, s87, s63\n" \
  "s_mov_b32 m0, %[ml0]\n" \
  "s_xor_b32 s76, s94, 0x6000\n" \
  "buffer_load_dwordx3 v[248:250], %[cpo], %[X], 0 offen sc1\n" \
  "buffer_load_dword v253, %[cpa], %[X], s76 offen sc1\n" \
  "v_readfirstlane_b32 s65, v238\n" \
  "v_readfirstlane_b32 s86, v239\n" \
  "s_cmp_lg_u32 s65, 0\n" \
  "s_cselect_b32 s76, %[oc1], 0\n" \
  "s_add_i32 s65, s65, s76\n" \
  "s_waitcnt lgkmcnt(0)\n" \
  "s_sub_i32 s73, s74, s73\n" \
  "s_sub_i32 s91, s74, s65\n" \
  "s_cmp_gt_u32 s91, 14\n" \
  "s_cbranch_scc1 .Loa_late\n" \
  ".Loa_al_ok:\n" \
  "buffer_load_dwordx4 %[c0], %[X], s57 offen sc1 lds\n" \
  "s_add_i32 s57, s57, 0x1000\n" \
  "s_add_i32 m0, %[ml0], 0x1000\n" \
  "s_nop 1\n" \
  "buffer_load_dwordx4 %[c0], %[X], s57 offen sc1 lds\n" \
  "s_add_i32 s57, s57, 0x3000\n" \
  "s_add_i32 m0, %[ml0], 0x2000\n" \
  "s_nop 1\n" \
  "buffer_load_dwordx4 %[c2], %[X], s57 offen sc1 lds\n" \
  "buffer_load_dword v242, %[flo], %[X], 0 offen sc1\n" \
  "s_add_i32 s76, s56, 1\n" \
  "s_lshl_b32 s77, s76, 12\n" \
  "s_min_u32 s72, s72, 4095\n" \
  "s_min_u32 s73, s73, 4095\n" \
  "s_or_b32 s72, s72, s77\n" \
  "s_or_b32 s73, s73, s77\n" \
  "v_writelane_b32 v252, s72, 1\n" \
  "v_writelane_b32 v252, s73, 2\n" \
  "s_bfe_i32 s63, s56, 0x10001\n" \
  "s_and_b32 s60, s63, 0x80008000\n" \
  "s_add_i32 s63, s62, 0x40000\n" \
  "s_and_b32 s63, s63, 0x60000\n" \
  "s_add_i32 s58, %[mw0], s63\n" \
  "s_cmp_gt_i32 s56, 1\n" \
  "s_cselect_b32 s58, s58, 2.0\n" \
  "s_mov_b32 s71, s70\n" \
  "s_mov_b32 s70, s68\n" \
  "s_add_i32 s67, s67, 589824\n" \
  "s_cmp_eq_u32 s67, 1769472\n" \
  "s_cselect_b32 s67, 0, s67\n" \
  "s_add_i32 s68, %[vm0], s67\n" \
  "s_add_i32 s63, s62, 0x20000\n" \
  "s_and_b32 s63, s63, 0x60000\n" \
  "s_add_i32 s61, %[mw0], s63\n" \
  "s_cmp_lt_u32 s56, 63\n" \
  "s_cselect_b32 s61, s61, 2.0\n" \
  "s_and_b32 s62, s62, 0x20000\n" \
  "s_add_i32 s59, %[pm0], s62\n" \
  "s_and_b32 s63, s56, 7\n" \
  "s_lshl_b32 s69, s63, 22\n" \
  "s_add_i32 s69, s69, %[vt0]\n" \
  "s_cmp_lt_i32 s56, 0\n" \
  "s_cselect_b32 s66, 2.0, s67\n" \
  "s_cselect_b32 s68, 2.0, s68\n" \
  "s_cselect_b32 s59, 2.0, s59\n" \
  "s_cselect_b32 s69, 2.0, s69\n" \
  "s_add_i32 s63, s56, 1\n" \
  "v_mov_b64 v[194:195], 0\n" \
  "v_mov_b64 v[196:197], 0\n" \
  "v_mov_b64 v[198:199], 0\n" \
  "v_mov_b64 v[200:201], 0\n" \
  "v_mov_b64 v[158:159], 0\n" \
  "v_mov_b64 v[160:161], 0\n" \
  "v_mov_b64 v[162:163], 0\n" \
  "v_mov_b64 v[164:165], 0\n" \
  "v_mov_b64 v[166:167], 0\n" \
  "v_mov_b64 v[168:169], 0\n" \
  "v_mov_b64 v[170:171], 0\n" \
  "v_mov_b64 v[172:173], 0\n" \
  "v_mov_b64 v[174:175], 0\n" \
  "v_mov_b64 v[176:177], 0\n" \
  "v_mov_b64 v[178:179], 0\n" \
  "v_mov_b64 v[180:181], 0\n" \
  "v_mov_b64 v[182:183], 0\n" \
  "v_mov_b64 v[184:185], 0\n" \
  "v_mov_b64 v[186:187], 0\n" \
  "v_mov_b64 v[188:189], 0\n" \
  "v_mov_b64 v[190:191], 0\n" \
  "v_mov_b64 v[192:193], 0\n" \
  "buffer_store_dwordx4 v[244:247], %[la], %[X], s71 offen sc1\n" \
  "buffer_store_dwordx4 v[244:247], %[la], %[X], s71 offen offset:1024 sc1\n" \
  "s_waitcnt vmcnt(6)\n" \
  "s_cmp_eq_u32 %[first], 0\n" \
  "s_cbranch_scc1 .Lo_cc\n" \
  ".Lo_cc_back:\n" \
  "s_waitcnt vmcnt(2)\n" \
  "v_cmp_gt_i32 vcc, s63, v242\n" \
  "s_barrier\n" \
  "ds_read_b128 v[102:105], %[mA] offset:0\n" \
  "ds_read_b128 v[106:109], %[mA] offset:1024\n" \
  "ds_read_b128 v[110:113], %[mA] offset:2048\n" \
  "ds_read_b128 v[114:117], %[mA] offset:3072\n" \
  "ds_read_b128 v[118:121], %[mA] offset:4096\n" \
  "buffer_store_dword v244, %[lm], %[X], s58 offen sc1\n" \
  "buffer_atomic_umax v252, %[rpo], %[X], 0 offen sc1\n" \
  "s_cbranch_vccnz .Lo_sf\n" \
  ".Lo_sf_back:\n" \
  "s_waitcnt lgkmcnt(4)\n" \
  "v_smfmac_f32_16x16x64_bf16 v[194:197], v[102:105], a[0:7], %[idx]\n" \
  "ds_read_b128 v[122:125], %[mA] offset:5120\n" \
  "v_smfmac_f32_16x16x64_bf16 v[198:201], v[102:105], a[8:15], %[idx]\n" \
  "s_waitcnt lgkmcnt(4)\n" \
  "v_smfmac_f32_16x16x64_bf16 v[194:197], v[106:109], a[16:23], %[idx]\n" \
  "ds_read_b128 v[126:129], %[mA] offset:6144\n" \
  "v_smfmac_f32_16x16x64_bf16 v[198:201], v[106:109], a[24:31], %[idx]\n" \
  "s_waitcnt lgkmcnt(4)\n" \
  "v_smfmac_f32_16x16x64_bf16 v[194:197], v[110:113], a[32:39], %[idx]\n" \
  "ds_read_b128 v[130:133], %[mA] offset:7168\n" \
  "v_smfmac_f32_16x16x64_bf16 v[198:201], v[110:113], a[40:47], %[idx]\n" \
  "s_waitcnt lgkmcnt(4)\n" \
  "v_smfmac_f32_16x16x64_bf16 v[194:197], v[114:117], a[48:55], %[idx]\n" \
  "ds_read_b128 v[134:137], %[mA8] offset:0\n" \
  "v_smfmac_f32_16x16x64_bf16 v[198:201], v[114:117], a[56:63], %[idx]\n" \
  "buffer_load_dwordx4 v[62:65], %[la], %[X], s69 offen offset:0 sc1\n" \
  "s_waitcnt lgkmcnt(4)\n" \
  "v_smfmac_f32_16x16x64_bf16 v[194:197], v[118:121], a[64:71], %[idx]\n" \
  "v_smfmac_f32_16x16x64_bf16 v[198:201], v[118:121], a[72:79], %[idx]\n" \
  "s_waitcnt lgkmcnt(3)\n" \
  "v_smfmac_f32_16x16x64_bf16 v[194:197], v[122:125], a[80:87], %[idx]\n" \
  "v_smfmac_f32_16x16x64_bf16 v[198:201], v[122:125], a[88:95], %[idx]\n" \
  "buffer_load_dwordx4 v[66:69], %[la], %[X], s69 offen offset:1024 sc1\n" \
  "s_waitcnt lgkmcnt(2)\n" \
  "v_smfmac_f32_16x16x64_bf16 v[194:197], v[126:129], a[96:103], %[idx]\n" \
  "v_smfmac_f32_16x16x64_bf16 v[198:201], v[126:129], a[104:111], %[idx]\n" \
  "s_waitcnt lgkmcnt(1)\n" \
  "v_smfmac_f32_16x16x64_bf16 v[194:197], v[130:133], a[112:119], %[idx]\n" \
  "v_smfmac_f32_16x16x64_bf16 v[198:201], v[130:133], a[120:127], %[idx]\n" \
  "buffer_load_dwordx4 v[70:73], %[la], %[X], s69 offen offset:2048 sc1\n" \
  "s_waitcnt lgkmcnt(0)\n" \
  "v_smfmac_f32_16x16x64_bf16 v[194:197], v[134:137], a[128:135], %[idx]\n" \
  "s_nop 2\n" \
  "v_smfmac_f32_16x16x64_bf16 v[198:201], v[134:137], a[136:143], %[idx]\n" \
  "s_nop 3\n" \
  "v_pk_add_f32 v[194:195], v[194:195], v[196:197]\n" \
  "s_nop 2\n" \
  "v_pk_add_f32 v[198:199], v[198:199], v[200:201]\n" \
  "v_cvt_pk_bf16_f32 v196, v194, v195\n" \
  "v_cvt_pk_bf16_f32 v197, v198, v199\n" \
  "ds_write2_b32 %[aW], v196, v197 offset1:16\n" \
  "v_cmp_u_f32 vcc, v194, v195\n" \
  "s_cbranch_vccnz .Lo_sm\n" \
  ".Lo_sm_back:\n" \
  "s_cmp_eq_u32 %[first], 0\n" \
  "s_cbranch_scc1 .Lo_cd\n" \
  ".Lo_cd_back:\n" \
  "s_waitcnt lgkmcnt(0)\n" \
  "s_barrier\n" \
  "ds_read_b64_tr_b16 v[210:211], %[aR] offset:0\n" \
  "ds_read_b64_tr_b16 v[212:213], %[aR] offset:128\n" \
  "ds_read_b64_tr_b16 v[214:215], %[aR] offset:1024\n" \
  "ds_read_b64_tr_b16 v[216:217], %[aR] offset:1152\n" \
  "buffer_load_dwordx4 v[74:77], %[la], %[X], s69 offen offset:3072 sc1\n" \
  "s_waitcnt lgkmcnt(2)\n" \
  "v_smfmac_f32_16x16x64_bf16 v[158:161], v[210:213], a[144:151], %[idx]\n" \
  "buffer_load_dwordx4 v[78:81], v240, %[X], s69 offen offset:0 sc1\n" \
  "s_waitcnt lgkmcnt(0)\n" \
  "v_smfmac_f32_16x16x64_bf16 v[158:161], v[214:217], a[152:159], %[idx]\n" \
  "s_nop 1\n" \
  "v_smfmac_f32_16x16x64_bf16 v[162:165], v[210:213], a[160:167], %[idx]\n" \
  "s_nop 1\n" \
  "v_smfmac_f32_16x16x64_bf16 v[162:165], v[214:217], a[168:175], %[idx]\n" \
  "s_nop 1\n" \
  "v_smfmac_f32_16x16x64_bf16 v[166:169], v[210:213], a[176:183], %[idx]\n" \
  "buffer_load_dwordx4 v[82:85], v240, %[X], s69 offen offset:1024 sc1\n" \
  "v_add_f32 v158, v158, v160\n" \
  "v_smfmac_f32_16x16x64_bf16 v[166:169], v[214:217], a[184:191], %[idx]\n" \
  "v_add_f32 v159, v159, v161\n" \
  "v_smfmac_f32_16x16x64_bf16 v[170:173], v[210:213], a[192:199], %[idx]\n" \
  "v_cvt_pk_bf16_f32 v218, v158, v159\n" \
  "v_smfmac_f32_16x16x64_bf16 v[170:173], v[214:217], a[200:207], %[idx]\n" \
  "v_add_f32 v162, v162, v164\n" \
  "v_smfmac_f32_16x16x64_bf16 v[174:177], v[210:213], a[208:215], %[idx]\n" \
  "buffer_load_dwordx4 v[86:89], v240, %[X], s69 offen offset:2048 sc1\n" \
  "v_add_f32 v163, v163, v165\n" \
  "v_smfmac_f32_16x16x64_bf16 v[174:177], v[214:217], a[216:223], %[idx]\n" \
  "v_cvt_pk_bf16_f32 v219, v162, v163\n" \
  "v_smfmac_f32_16x16x64_bf16 v[178:181], v[210:213], a[224:231], %[idx]\n" \
  "v_add_f32 v166, v166, v168\n" \
  "v_smfmac_f32_16x16x64_bf16 v[178:181], v[214:217], a[232:239], %[idx]\n" \
  "v_add_f32 v167, v167, v169\n" \
  "v_smfmac_f32_16x16x64_bf16 v[182:185], v[210:213], a[240:247], %[idx]\n" \
  "buffer_load_dwordx4 v[90:93], v240, %[X], s69 offen offset:3072 sc1\n" \
  "v_cvt_pk_bf16_f32 v220, v166, v167\n" \
  "v_smfmac_f32_16x16x64_bf16 v[182:185], v[214:217], a[248:255], %[idx]\n" \
  "v_add_f32 v170, v170, v172\n" \
  "v_smfmac_f32_16x16x64_bf16 v[186:189], v[210:213], v[30:37], %[idx]\n" \
  "v_add_f32 v171, v171, v173\n" \
  "v_smfmac_f32_16x16x64_bf16 v[186:189], v[214:217], v[38:45], %[idx]\n" \
  "v_cvt_pk_bf16_f32 v221, v170, v171\n" \
  "v_smfmac_f32_16x16x64_bf16 v[190:193], v[210:213], v[46:53], %[idx]\n" \
  "v_add_f32 v174, v174, v176\n" \
  "v_smfmac_f32_16x16x64_bf16 v[190:193], v[214:217], v[54:61], %[idx]\n" \
  "v_add_f32 v175, v175, v177\n" \
  "v_permlane32_swap_b32 v218, v220\n" \
  "v_permlane32_swap_b32 v219, v221\n" \
  "v_cvt_pk_bf16_f32 v222, v174, v175\n" \
  "v_pk_add_f32 v[178:179], v[178:179], v[180:181]\n" \
  "v_cvt_pk_bf16_f32 v223, v178, v179\n" \
  "v_permlane16_swap_b32 v218, v219\n" \
  "v_permlane16_swap_b32 v220, v221\n" \
  "v_min_u32 v218, -2, v218\n" \
  "buffer_store_dwordx4 v[218:221], %[vmo], %[X], s66 offen sc1\n" \
  "s_add_i32 s66, s66, 0x10000\n" \
  "v_pk_add_f32 v[182:183], v[182:183], v[184:185]\n" \
  "v_cvt_pk_bf16_f32 v224, v182, v183\n" \
  "v_pk_add_f32 v[186:187], v[186:187], v[188:189]\n" \
  "v_cvt_pk_bf16_f32 v225, v186, v187\n" \
  "v_pk_add_f32 v[190:191], v[190:191], v[192:193]\n" \
  "v_cvt_pk_bf16_f32 v226, v190, v191\n" \
  "v_permlane32_swap_b32 v222, v224\n" \
  "v_permlane32_swap_b32 v223, v225\n" \
  "v_min_u32 v226, -2, v226\n" \
  "buffer_store_dword v226, %[vmo1], %[X], s66 offen sc1\n" \
  "v_permlane16_swap_b32 v222, v223\n" \
  "v_permlane16_swap_b32 v224, v225\n" \
  "v_min_u32 v222, -2, v222\n" \
  "buffer_store_dwordx4 v[222:225], %[vmo], %[X], s66 offen sc1\n" \
  "s_cmp_eq_u32 s56, 63\n" \
  "s_cbranch_scc1 .Lo_end\n" \
  "buffer_load_dword v243, off, %[X], %[ufo] sc1\n" \
  "s_memrealtime s[74:75]\n" \
  "s_sub_i32 s76, s88, s86\n" \
  "s_cmp_lt_u32 s76, 1000\n" \
  "s_cselect_b32 s86, s88, s86\n" \
  "s_waitcnt lgkmcnt(0)\n" \
  "s_sub_i32 s72, s74, s65\n" \
  "s_add_i32 s72, s72, %[vl]\n" \
  "s_waitcnt lgkmcnt(0)\n" \
  "s_sub_i32 s91, s74, s86\n" \
  "s_cmp_gt_u32 s91, 14\n" \
  "s_cbranch_scc1 .Lob_late\n" \
  ".Lob_al_ok:\n" \
  "buffer_load_dwordx4 v[94:97], %[la], %[X], s68 offen offset:0 sc1\n" \
  "s_nop 7\n" \
  "buffer_load_dwordx4 v[98:101], %[la], %[X], s68 offen offset:1024 sc1\n" \
  "s_nop 7\n" \
  "buffer_load_dwordx4 v[138:141], %[la], %[X], s59 offen offset:0 sc1\n" \
  "s_nop 7\n" \
  "buffer_load_dwordx4 v[142:145], %[la], %[X], s59 offen offset:1024 sc1\n" \
  "s_nop 7\n" \
  "buffer_load_dwordx4 v[146:149], %[la], %[X], s59 offen offset:2048 sc1\n" \
  "s_nop 7\n" \
  "buffer_load_dwordx4 v[150:153], %[la], %[X], s59 offen offset:3072 sc1\n" \
  "s_nop 7\n" \
  "buffer_load_dwordx4 v[154:157], v240, %[X], s59 offen offset:0 sc1\n" \
  "s_nop 7\n" \
  "s_nop 15\n" \
  "buffer_load_dwordx2 v[238:239], off, %[X], s94 offset:128 sc1\n" \
  "s_waitcnt vmcnt(8)\n" \
  "v_subrev_u32 v241, s56, v243\n" \
  "s_waitcnt vmcnt(5)\n" \
  "v_mfma_f32_16x16x32_bf16 v[202:205], v[62:65], v[138:141], 0\n" \
  "v_mfma_f32_16x16x32_bf16 v[206:209], v[66:69], v[138:141], 0\n" \
  "v_bitop3_b32 v241, v241, v138, s60 bitop3:0xf6\n" \
  "s_waitcnt vmcnt(4)\n" \
  "v_mfma_f32_16x16x32_bf16 v[202:205], v[70:73], v[142:145], v[202:205]\n" \
  "v_mfma_f32_16x16x32_bf16 v[206:209], v[74:77], v[142:145], v[206:209]\n" \
  "v_bitop3_b32 v241, v241, v142, s60 bitop3:0xf6\n" \
  "s_waitcnt vmcnt(3)\n" \
  "v_mfma_f32_16x16x32_bf16 v[202:205], v[78:81], v[146:149], v[202:205]\n" \
  "v_mfma_f32_16x16x32_bf16 v[206:209], v[82:85], v[146:149], v[206:209]\n" \
  "v_bitop3_b32 v241, v241, v146, s60 bitop3:0xf6\n" \
  "s_waitcnt vmcnt(2)\n" \
  "v_mfma_f32_16x16x32_bf16 v[202:205], v[86:89], v[150:153], v[202:205]\n" \
  "v_mfma_f32_16x16x32_bf16 v[206:209], v[90:93], v[150:153], v[206:209]\n" \
  "v_bitop3_b32 v241, v241, v150, s60 bitop3:0xf6\n" \
  "s_waitcnt vmcnt(1)\n" \
  "v_mfma_f32_16x16x32_bf16 v[202:205], v[94:97], v[154:157], v[202:205]\n" \
  "v_mfma_f32_16x16x32_bf16 v[206:209], v[98:101], v[154:157], v[206:209]\n" \
  "v_bitop3_b32 v236, v154, v155, s60 bitop3:0x7e\n" \
  "v_bitop3_b32 v237, v156, v157, s60 bitop3:0x7e\n" \
  "v_or3_b32 v241, v241, v236, v237\n" \
  "v_ashrrev_i32 v236, 31, v241\n" \
  "s_nop 3\n" \
  "v_add_f32_dpp v202, v206, v202 row_ror:8 row_mask:0xf bank_mask:0xf\n" \
  "v_add_f32_dpp v203, v207, v203 row_ror:8 row_mask:0xf bank_mask:0xf\n" \
  "v_add_f32_dpp v204, v208, v204 row_ror:8 row_mask:0xf bank_mask:0xf\n" \
  "v_add_f32_dpp v205, v209, v205 row_ror:8 row_mask:0xf bank_mask:0xf\n" \
  "ds_write_b128 %[rmW], v[202:205]\n" \
  "v_fma_f32 v236, v202, v203, v236\n" \
  "v_add_f32 v237, v204, v205\n" \
  "v_cmp_u_f32 vcc, v236, v237\n" \
  "s_cbranch_vccnz .Lo_sq\n" \
  ".Lo_sq_back:\n" \
  "s_waitcnt lgkmcnt(0)\n" \
  "s_barrier\n" \
  "ds_read2st64_b64 v[228:231], %[rmA] offset0:0 offset1:2\n" \
  "ds_read2st64_b64 v[232:235], %[rmA] offset0:4 offset1:6\n" \
  "s_waitcnt lgkmcnt(1)\n" \
  "v_pk_add_f32 v[228:229], v[228:229], v[230:231]\n" \
  "s_waitcnt lgkmcnt(0)\n" \
  "v_pk_add_f32 v[232:233], v[232:233], v[234:235]\n" \
  "v_pk_add_f32 v[228:229], v[228:229], v[232:233]\n" \
  "v_cvt_pk_bf16_f32 v228, v228, v229\n" \
  "v_xor_b32 v228, s60, v228\n" \
  "v_min_u32 v228, -2, v228\n" \
  "buffer_store_dword v228, %[lm], %[X], s61 offen sc1\n" \
  "s_waitcnt vmcnt(1)\n" \
  "s_cmp_lg_u32 s56, -1\n" \
  "s_cbranch_scc1 .Lo_top\n" \
  "s_mov_b32 s87, %[mr0]\n" \
  "s_waitcnt vmcnt(0) lgkmcnt(0)\n" \
  "s_barrier\n" \
  "v_xor_b32 v102, 0x80, %[wv]\n" \
  "v_xor_b32 v103, 32, %[wr]\n" \
  "v_add_u32 v104, 0x10000, %[wr]\n" \
  "v_add_u32 v105, 0x10000, v103\n" \
  "s_cmp_eq_u32 %[grp], 0\n" \
  "s_cbranch_scc1 .Lo_ws1\n" \
  "s_sleep 8\n" \
  ".Lo_ws1:\n" \
  "s_add_i32 m0, %[wl], 0\n" \
  "s_mov_b32 s76, 0\n" \
  "buffer_load_dwordx4 %[wv], %[WA], s76 offen lds\n" \
  "s_add_i32 m0, %[wl], 1024\n" \
  "s_mov_b32 s76, 32768\n" \
  "buffer_load_dwordx4 %[wv], %[WA], s76 offen lds\n" \
  "s_add_i32 m0, %[wl], 2048\n" \
  "s_mov_b32 s76, 65536\n" \
  "buffer_load_dwordx4 v102, %[WA], s76 offen lds\n" \
  "s_add_i32 m0, %[wl], 3072\n" \
  "s_mov_b32 s76, 98304\n" \
  "buffer_load_dwordx4 v102, %[WA], s76 offen lds\n" \
  "s_cmp_eq_u32 %[grp], 0\n" \
  "s_cbranch_scc1 .Lo_ws2\n" \
  "s_sleep 8\n" \
  ".Lo_ws2:\n" \
  "s_add_i32 m0, %[wl], 16384\n" \
  "s_mov_b32 s76, 524288\n" \
  "buffer_load_dwordx4 %[wv], %[WA], s76 offen lds\n" \
  "s_add_i32 m0, %[wl], 17408\n" \
  "s_mov_b32 s76, 557056\n" \
  "buffer_load_dwordx4 %[wv], %[WA], s76 offen lds\n" \
  "s_add_i32 m0, %[wl], 18432\n" \
  "s_mov_b32 s76, 589824\n" \
  "buffer_load_dwordx4 v102, %[WA], s76 offen lds\n" \
  "s_add_i32 m0, %[wl], 19456\n" \
  "s_mov_b32 s76, 622592\n" \
  "buffer_load_dwordx4 v102, %[WA], s76 offen lds\n" \
  "s_cmp_eq_u32 %[grp], 0\n" \
  "s_cbranch_scc1 .Lo_ws3\n" \
  "s_sleep 8\n" \
  ".Lo_ws3:\n" \
  "s_add_i32 m0, %[wl], 32768\n" \
  "s_mov_b32 s76, 1048576\n" \
  "buffer_load_dwordx4 %[wv], %[WA], s76 offen lds\n" \
  "s_add_i32 m0, %[wl], 33792\n" \
  "s_mov_b32 s76, 1081344\n" \
  "buffer_load_dwordx4 %[wv], %[WA], s76 offen lds\n" \
  "s_add_i32 m0, %[wl], 34816\n" \
  "s_mov_b32 s76, 1114112\n" \
  "buffer_load_dwordx4 v102, %[WA], s76 offen lds\n" \
  "s_add_i32 m0, %[wl], 35840\n" \
  "s_mov_b32 s76, 1146880\n" \
  "buffer_load_dwordx4 v102, %[WA], s76 offen lds\n" \
  "s_cmp_eq_u32 %[grp], 0\n" \
  "s_cbranch_scc1 .Lo_ws4\n" \
  "s_sleep 8\n" \
  ".Lo_ws4:\n" \
  "s_add_i32 m0, %[wl], 49152\n" \
  "s_mov_b32 s76, 1572864\n" \
  "buffer_load_dwordx4 %[wv], %[WA], s76 offen lds\n" \
  "s_add_i32 m0, %[wl], 50176\n" \
  "s_mov_b32 s76, 1605632\n" \
  "buffer_load_dwordx4 %[wv], %[WA], s76 offen lds\n" \
  "s_add_i32 m0, %[wl], 51200\n" \
  "s_mov_b32 s76, 1638400\n" \
  "buffer_load_dwordx4 v102, %[WA], s76 offen lds\n" \
  "s_add_i32 m0, %[wl], 52224\n" \
  "s_mov_b32 s76, 1671168\n" \
  "buffer_load_dwordx4 v102, %[WA], s76 offen lds\n" \
  "s_cmp_eq_u32 %[grp], 0\n" \
  "s_cbranch_scc1 .Lo_ws5\n" \
  "s_sleep 8\n" \
  ".Lo_ws5:\n" \
  "s_add_i32 m0, %[wl], 65536\n" \
  "s_mov_b32 s76, 2097152\n" \
  "buffer_load_dwordx4 %[wv], %[WA], s76 offen lds\n" \
  "s_add_i32 m0, %[wl], 66560\n" \
  "s_mov_b32 s76, 2129920\n" \
  "buffer_load_dwordx4 %[wv], %[WA], s76 offen lds\n" \
  "s_add_i32 m0, %[wl], 67584\n" \
  "s_mov_b32 s76, 2162688\n" \
  "buffer_load_dwordx4 v102, %[WA], s76 offen lds\n" \
  "s_add_i32 m0, %[wl], 68608\n" \
  "s_mov_b32 s76, 2195456\n" \
  "buffer_load_dwordx4 v102, %[WA], s76 offen lds\n" \
  "s_cmp_eq_u32 %[grp], 0\n" \
  "s_cbranch_scc1 .Lo_ws6\n" \
  "s_sleep 8\n" \
  ".Lo_ws6:\n" \
  "s_add_i32 m0, %[wl], 81920\n" \
  "s_mov_b32 s76, 2621440\n" \
  "buffer_load_dwordx4 %[wv], %[WA], s76 offen lds\n" \
  "s_add_i32 m0, %[wl], 82944\n" \
  "s_mov_b32 s76, 2654208\n" \
  "buffer_load_dwordx4 %[wv], %[WA], s76 offen lds\n" \
  "s_add_i32 m0, %[wl], 83968\n" \
  "s_mov_b32 s76, 2686976\n" \
  "buffer_load_dwordx4 v102, %[WA], s76 offen lds\n" \
  "s_add_i32 m0, %[wl], 84992\n" \
  "s_mov_b32 s76, 2719744\n" \
  "buffer_load_dwordx4 v102, %[WA], s76 offen lds\n" \
  "s_cmp_eq_u32 %[grp], 0\n" \
  "s_cbranch_scc1 .Lo_ws7\n" \
  "s_sleep 8\n" \
  ".Lo_ws7:\n" \
  "s_add_i32 m0, %[wl], 98304\n" \
  "s_mov_b32 s76, 3145728\n" \
  "buffer_load_dwordx4 %[wv], %[WA], s76 offen lds\n" \
  "s_add_i32 m0, %[wl], 99328\n" \
  "s_mov_b32 s76, 3178496\n" \
  "buffer_load_dwordx4 %[wv], %[WA], s76 offen lds\n" \
  "s_add_i32 m0, %[wl], 100352\n" \
  "s_mov_b32 s76, 3211264\n" \
  "buffer_load_dwordx4 v102, %[WA], s76 offen lds\n" \
  "s_add_i32 m0, %[wl], 101376\n" \
  "s_mov_b32 s76, 3244032\n" \
  "buffer_load_dwordx4 v102, %[WA], s76 offen lds\n" \
  "s_cmp_eq_u32 %[grp], 0\n" \
  "s_cbranch_scc1 .Lo_ws8\n" \
  "s_sleep 8\n" \
  ".Lo_ws8:\n" \
  "s_add_i32 m0, %[wl], 114688\n" \
  "s_mov_b32 s76, 3670016\n" \
  "buffer_load_dwordx4 %[wv], %[WA], s76 offen lds\n" \
  "s_add_i32 m0, %[wl], 115712\n" \
  "s_mov_b32 s76, 3702784\n" \
  "buffer_load_dwordx4 %[wv], %[WA], s76 offen lds\n" \
  "s_add_i32 m0, %[wl], 116736\n" \
  "s_mov_b32 s76, 3735552\n" \
  "buffer_load_dwordx4 v102, %[WA], s76 offen lds\n" \
  "s_add_i32 m0, %[wl], 117760\n" \
  "s_mov_b32 s76, 3768320\n" \
  "buffer_load_dwordx4 v102, %[WA], s76 offen lds\n" \
  "s_waitcnt vmcnt(28)\n" \
  "s_waitcnt lgkmcnt(0)\n" \
  "s_barrier\n" \
  "ds_read_b64_tr_b16 a[0:1], %[wr] offset:0\n" \
  "ds_read_b64_tr_b16 a[2:3], %[wr] offset:1024\n" \
  "ds_read_b64_tr_b16 a[4:5], %[wr] offset:8192\n" \
  "ds_read_b64_tr_b16 a[6:7], %[wr] offset:9216\n" \
  "ds_read_b64_tr_b16 a[8:9], v103 offset:0\n" \
  "ds_read_b64_tr_b16 a[10:11], v103 offset:1024\n" \
  "ds_read_b64_tr_b16 a[12:13], v103 offset:8192\n" \
  "ds_read_b64_tr_b16 a[14:15], v103 offset:9216\n" \
  "s_waitcnt vmcnt(24)\n" \
  "s_waitcnt lgkmcnt(0)\n" \
  "s_barrier\n" \
  "s_cmp_eq_u32 %[grp], 0\n" \
  "s_cbranch_scc1 .Lo_ws9\n" \
  "s_sleep 8\n" \
  ".Lo_ws9:\n" \
  "s_add_i32 m0, %[wl], 0\n" \
  "s_mov_b32 s76, 4194304\n" \
  "buffer_load_dwordx4 %[wv], %[WA], s76 offen lds\n" \
  "s_add_i32 m0, %[wl], 1024\n" \
  "s_mov_b32 s76, 4227072\n" \
  "buffer_load_dwordx4 %[wv], %[WA], s76 offen lds\n" \
  "s_add_i32 m0, %[wl], 2048\n" \
  "s_mov_b32 s76, 4259840\n" \
  "buffer_load_dwordx4 v102, %[WA], s76 offen lds\n" \
  "s_add_i32 m0, %[wl], 3072\n" \
  "s_mov_b32 s76, 4292608\n" \
  "buffer_load_dwordx4 v102, %[WA], s76 offen lds\n" \
  "ds_read_b64_tr_b16 a[16:17], %[wr] offset:16384\n" \
  "ds_read_b64_tr_b16 a[18:19], %[wr] offset:17408\n" \
  "ds_read_b64_tr_b16 a[20:21], %[wr] offset:24576\n" \
  "ds_read_b64_tr_b16 a[22:23], %[wr] offset:25600\n" \
  "ds_read_b64_tr_b16 a[24:25], v103 offset:16384\n" \
  "ds_read_b64_tr_b16 a[26:27], v103 offset:17408\n" \
  "ds_read_b64_tr_b16 a[28:29], v103 offset:24576\n" \
  "ds_read_b64_tr_b16 a[30:31], v103 offset:25600\n" \
  "s_waitcnt vmcnt(24)\n" \
  "s_waitcnt lgkmcnt(0)\n" \
  "s_barrier\n" \
  "ds_read_b64_tr_b16 a[32:33], %[wr] offset:32768\n" \
  "ds_read_b64_tr_b16 a[34:35], %[wr] offset:33792\n" \
  "ds_read_b64_tr_b16 a[36:37], %[wr] offset:40960\n" \
  "ds_read_b64_tr_b16 a[38:39], %[wr] offset:41984\n" \
  "ds_read_b64_tr_b16 a[40:41], v103 offset:32768\n" \
  "ds_read_b64_tr_b16 a[42:43], v103 offset:33792\n" \
  "ds_read_b64_tr_b16 a[44:45], v103 offset:40960\n" \
  "ds_read_b64_tr_b16 a[46:47], v103 offset:41984\n" \
  "s_waitcnt vmcnt(20)\n" \
  "s_waitcnt lgkmcnt(0)\n" \
  "s_barrier\n" \
  "ds_read_b64_tr_b16 a[48:49], %[wr] offset:49152\n" \
  "ds_read_b64_tr_b16 a[50:51], %[wr] offset:50176\n" \
  "ds_read_b64_tr_b16 a[52:53], %[wr] offset:57344\n" \
  "ds_read_b64_tr_b16 a[54:55], %[wr] offset:58368\n" \
  "ds_read_b64_tr_b16 a[56:57], v103 offset:49152\n" \
  "ds_read_b64_tr_b16 a[58:59], v103 offset:50176\n" \
  "ds_read_b64_tr_b16 a[60:61], v103 offset:57344\n" \
  "ds_read_b64_tr_b16 a[62:63], v103 offset:58368\n" \
  "s_waitcnt vmcnt(16)\n" \
  "s_waitcnt lgkmcnt(0)\n" \
  "s_barrier\n" \
  "s_cmp_eq_u32 %[grp], 0\n" \
  "s_cbranch_scc1 .Lo_ws10\n" \
  "s_sleep 16\n" \
  ".Lo_ws10:\n" \
  "s_add_i32 m0, %[hl0], 16384\n" \
  "s_mov_b32 s76, 0\n" \
  "buffer_load_dwordx4 %[hvo], %[WB], s76 offen offset:0 lds\n" \
  "buffer_load_dwordx4 %[hvo], %[WB], s76 offen offset:1024 lds\n" \
  "buffer_load_dwordx4 %[hvo], %[WB], s76 offen offset:2048 lds\n" \
  "buffer_load_dwordx4 %[hvo], %[WB], s76 offen offset:3072 lds\n" \
  "s_add_i32 m0, %[hl0], 20480\n" \
  "s_mov_b32 s76, 4096\n" \
  "buffer_load_dwordx4 %[hvo], %[WB], s76 offen offset:0 lds\n" \
  "buffer_load_dwordx4 %[hvo], %[WB], s76 offen offset:1024 lds\n" \
  "buffer_load_dwordx4 %[hvo], %[WB], s76 offen offset:2048 lds\n" \
  "buffer_load_dwordx4 %[hvo], %[WB], s76 offen offset:3072 lds\n" \
  "s_add_i32 m0, %[hl0], 24576\n" \
  "s_mov_b32 s76, 8192\n" \
  "buffer_load_dwordx4 %[hvo], %[WB], s76 offen offset:0 lds\n" \
  "ds_read_b64_tr_b16 a[64:65], v104 offset:0\n" \
  "ds_read_b64_tr_b16 a[66:67], v104 offset:1024\n" \
  "ds_read_b64_tr_b16 a[68:69], v104 offset:8192\n" \
  "ds_read_b64_tr_b16 a[70:71], v104 offset:9216\n" \
  "ds_read_b64_tr_b16 a[72:73], v105 offset:0\n" \
  "ds_read_b64_tr_b16 a[74:75], v105 offset:1024\n" \
  "ds_read_b64_tr_b16 a[76:77], v105 offset:8192\n" \
  "ds_read_b64_tr_b16 a[78:79], v105 offset:9216\n" \
  "s_waitcnt vmcnt(21)\n" \
  "s_waitcnt lgkmcnt(0)\n" \
  "s_barrier\n" \
  "ds_read_b64_tr_b16 a[80:81], v104 offset:16384\n" \
  "ds_read_b64_tr_b16 a[82:83], v104 offset:17408\n" \
  "ds_read_b64_tr_b16 a[84:85], v104 offset:24576\n" \
  "ds_read_b64_tr_b16 a[86:87], v104 offset:25600\n" \
  "ds_read_b64_tr_b16 a[88:89], v105 offset:16384\n" \
  "ds_read_b64_tr_b16 a[90:91], v105 offset:17408\n" \
  "ds_read_b64_tr_b16 a[92:93], v105 offset:24576\n" \
  "ds_read_b64_tr_b16 a[94:95], v105 offset:25600\n" \
  "s_waitcnt vmcnt(17)\n" \
  "s_waitcnt lgkmcnt(0)\n" \
  "s_barrier\n" \
  "s_cmp_eq_u32 %[grp], 0\n" \
  "s_cbranch_scc1 .Lo_ws11\n" \
  "s_sleep 16\n" \
  ".Lo_ws11:\n" \
  "s_add_i32 m0, %[hl0], 53248\n" \
  "s_mov_b32 s76, 36864\n" \
  "buffer_load_dwordx4 %[hvo], %[WB], s76 offen offset:0 lds\n" \
  "buffer_load_dwordx4 %[hvo], %[WB], s76 offen offset:1024 lds\n" \
  "buffer_load_dwordx4 %[hvo], %[WB], s76 offen offset:2048 lds\n" \
  "buffer_load_dwordx4 %[hvo], %[WB], s76 offen offset:3072 lds\n" \
  "s_add_i32 m0, %[hl0], 57344\n" \
  "s_mov_b32 s76, 40960\n" \
  "buffer_load_dwordx4 %[hvo], %[WB], s76 offen offset:0 lds\n" \
  "buffer_load_dwordx4 %[hvo], %[WB], s76 offen offset:1024 lds\n" \
  "buffer_load_dwordx4 %[hvo], %[WB], s76 offen offset:2048 lds\n" \
  "buffer_load_dwordx4 %[hvo], %[WB], s76 offen offset:3072 lds\n" \
  "s_add_i32 m0, %[hl0], 61440\n" \
  "s_mov_b32 s76, 45056\n" \
  "buffer_load_dwordx4 %[hvo], %[WB], s76 offen offset:0 lds\n" \
  "ds_read_b64_tr_b16 a[96:97], v104 offset:32768\n" \
  "ds_read_b64_tr_b16 a[98:99], v104 offset:33792\n" \
  "ds_read_b64_tr_b16 a[100:101], v104 offset:40960\n" \
  "ds_read_b64_tr_b16 a[102:103], v104 offset:41984\n" \
  "ds_read_b64_tr_b16 a[104:105], v105 offset:32768\n" \
  "ds_read_b64_tr_b16 a[106:107], v105 offset:33792\n" \
  "ds_read_b64_tr_b16 a[108:109], v105 offset:40960\n" \
  "ds_read_b64_tr_b16 a[110:111], v105 offset:41984\n" \
  "s_waitcnt vmcnt(22)\n" \
  "s_waitcnt lgkmcnt(0)\n" \
  "s_barrier\n" \
  "ds_read_b64_tr_b16 a[112:113], v104 offset:49152\n" \
  "ds_read_b64_tr_b16 a[114:115], v104 offset:50176\n" \
  "ds_read_b64_tr_b16 a[116:117], v104 offset:57344\n" \
  "ds_read_b64_tr_b16 a[118:119], v104 offset:58368\n" \
  "ds_read_b64_tr_b16 a[120:121], v105 offset:49152\n" \
  "ds_read_b64_tr_b16 a[122:123], v105 offset:50176\n" \
  "ds_read_b64_tr_b16 a[124:125], v105 offset:57344\n" \
  "ds_read_b64_tr_b16 a[126:127], v105 offset:58368\n" \
  "s_waitcnt vmcnt(18)\n" \
  "s_waitcnt lgkmcnt(0)\n" \
  "s_barrier\n" \
  "s_cmp_eq_u32 %[grp], 0\n" \
  "s_cbranch_scc1 .Lo_ws12\n" \
  "s_sleep 16\n" \
  ".Lo_ws12:\n" \
  "s_add_i32 m0, %[hl0], 90112\n" \
  "s_mov_b32 s76, 73728\n" \
  "buffer_load_dwordx4 %[hvo], %[WB], s76 offen offset:0 lds\n" \
  "buffer_load_dwordx4 %[hvo], %[WB], s76 offen offset:1024 lds\n" \
  "buffer_load_dwordx4 %[hvo], %[WB], s76 offen offset:2048 lds\n" \
  "buffer_load_dwordx4 %[hvo], %[WB], s76 offen offset:3072 lds\n" \
  "s_add_i32 m0, %[hl0], 94208\n" \
  "s_mov_b32 s76, 77824\n" \
  "buffer_load_dwordx4 %[hvo], %[WB], s76 offen offset:0 lds\n" \
  "buffer_load_dwordx4 %[hvo], %[WB], s76 offen offset:1024 lds\n" \
  "buffer_load_dwordx4 %[hvo], %[WB], s76 offen offset:2048 lds\n" \
  "buffer_load_dwordx4 %[hvo], %[WB], s76 offen offset:3072 lds\n" \
  "s_add_i32 m0, %[hl0], 98304\n" \
  "s_mov_b32 s76, 81920\n" \
  "buffer_load_dwordx4 %[hvo], %[WB], s76 offen offset:0 lds\n" \
  "ds_read_b64_tr_b16 a[128:129], %[wr] offset:0\n" \
  "ds_read_b64_tr_b16 a[130:131], %[wr] offset:1024\n" \
  "ds_read_b64_tr_b16 a[132:133], %[wr] offset:8192\n" \
  "ds_read_b64_tr_b16 a[134:135], %[wr] offset:9216\n" \
  "ds_read_b64_tr_b16 a[136:137], v103 offset:0\n" \
  "ds_read_b64_tr_b16 a[138:139], v103 offset:1024\n" \
  "ds_read_b64_tr_b16 a[140:141], v103 offset:8192\n" \
  "ds_read_b64_tr_b16 a[142:143], v103 offset:9216\n" \
  "s_waitcnt vmcnt(18)\n" \
  "s_waitcnt lgkmcnt(0)\n" \
  "s_barrier\n" \
  "v_add_u32 v106, 16384, %[wr2]\n" \
  "ds_read_b64_tr_b16 a[144:145], v106 offset:0\n" \
  "ds_read_b64_tr_b16 a[146:147], v106 offset:18432\n" \
  "ds_read_b64_tr_b16 a[160:161], v106 offset:32\n" \
  "ds_read_b64_tr_b16 a[162:163], v106 offset:18464\n" \
  "ds_read_b64_tr_b16 a[176:177], v106 offset:64\n" \
  "ds_read_b64_tr_b16 a[178:179], v106 offset:18496\n" \
  "ds_read_b64_tr_b16 a[192:193], v106 offset:96\n" \
  "ds_read_b64_tr_b16 a[194:195], v106 offset:18528\n" \
  "ds_read_b64_tr_b16 a[208:209], v106 offset:128\n" \
  "ds_read_b64_tr_b16 a[210:211], v106 offset:18560\n" \
  "ds_read_b64_tr_b16 a[224:225], v106 offset:160\n" \
  "ds_read_b64_tr_b16 a[226:227], v106 offset:18592\n" \
  "ds_read_b64_tr_b16 a[240:241], v106 offset:192\n" \
  "ds_read_b64_tr_b16 a[242:243], v106 offset:18624\n" \
  "ds_read_b64_tr_b16 v[30:31], v106 offset:224\n" \
  "ds_read_b64_tr_b16 v[32:33], v106 offset:18656\n" \
  "ds_read_b64_tr_b16 v[46:47], v106 offset:256\n" \
  "ds_read_b64_tr_b16 v[48:49], v106 offset:18688\n" \
  "s_waitcnt vmcnt(9)\n" \
  "s_waitcnt lgkmcnt(0)\n" \
  "s_barrier\n" \
  "s_cmp_eq_u32 %[grp], 0\n" \
  "s_cbranch_scc1 .Lo_ws13\n" \
  "s_sleep 16\n" \
  ".Lo_ws13:\n" \
  "s_add_i32 m0, %[hl0], 0\n" \
  "s_mov_b32 s76, 110592\n" \
  "buffer_load_dwordx4 %[hvo], %[WB], s76 offen offset:0 lds\n" \
  "buffer_load_dwordx4 %[hvo], %[WB], s76 offen offset:1024 lds\n" \
  "buffer_load_dwordx4 %[hvo], %[WB], s76 offen offset:2048 lds\n" \
  "buffer_load_dwordx4 %[hvo], %[WB], s76 offen offset:3072 lds\n" \
  "s_add_i32 m0, %[hl0], 4096\n" \
  "s_mov_b32 s76, 114688\n" \
  "buffer_load_dwordx4 %[hvo], %[WB], s76 offen offset:0 lds\n" \
  "buffer_load_dwordx4 %[hvo], %[WB], s76 offen offset:1024 lds\n" \
  "buffer_load_dwordx4 %[hvo], %[WB], s76 offen offset:2048 lds\n" \
  "buffer_load_dwordx4 %[hvo], %[WB], s76 offen offset:3072 lds\n" \
  "s_add_i32 m0, %[hl0], 8192\n" \
  "s_mov_b32 s76, 118784\n" \
  "buffer_load_dwordx4 %[hvo], %[WB], s76 offen offset:0 lds\n" \
  "v_add_u32 v106, 53248, %[wr2]\n" \
  "ds_read_b64_tr_b16 a[148:149], v106 offset:0\n" \
  "ds_read_b64_tr_b16 a[150:151], v106 offset:18432\n" \
  "ds_read_b64_tr_b16 a[164:165], v106 offset:32\n" \
  "ds_read_b64_tr_b16 a[166:167], v106 offset:18464\n" \
  "ds_read_b64_tr_b16 a[180:181], v106 offset:64\n" \
  "ds_read_b64_tr_b16 a[182:183], v106 offset:18496\n" \
  "ds_read_b64_tr_b16 a[196:197], v106 offset:96\n" \
  "ds_read_b64_tr_b16 a[198:199], v106 offset:18528\n" \
  "ds_read_b64_tr_b16 a[212:213], v106 offset:128\n" \
  "ds_read_b64_tr_b16 a[214:215], v106 offset:18560\n" \
  "ds_read_b64_tr_b16 a[228:229], v106 offset:160\n" \
  "ds_read_b64_tr_b16 a[230:231], v106 offset:18592\n" \
  "ds_read_b64_tr_b16 a[244:245], v106 offset:192\n" \
  "ds_read_b64_tr_b16 a[246:247], v106 offset:18624\n" \
  "ds_read_b64_tr_b16 v[34:35], v106 offset:224\n" \
  "ds_read_b64_tr_b16 v[36:37], v106 offset:18656\n" \
  "ds_read_b64_tr_b16 v[50:51], v106 offset:256\n" \
  "ds_read_b64_tr_b16 v[52:53], v106 offset:18688\n" \
  "s_waitcnt vmcnt(9)\n" \
  "s_waitcnt lgkmcnt(0)\n" \
  "s_barrier\n" \
  "v_add_u32 v106, 90112, %[wr2]\n" \
  "ds_read_b64_tr_b16 a[152:153], v106 offset:0\n" \
  "ds_read_b64_tr_b16 a[154:155], v106 offset:18432\n" \
  "ds_read_b64_tr_b16 a[168:169], v106 offset:32\n" \
  "ds_read_b64_tr_b16 a[170:171], v106 offset:18464\n" \
  "ds_read_b64_tr_b16 a[184:185], v106 offset:64\n" \
  "ds_read_b64_tr_b16 a[186:187], v106 offset:18496\n" \
  "ds_read_b64_tr_b16 a[200:201], v106 offset:96\n" \
  "ds_read_b64_tr_b16 a[202:203], v106 offset:18528\n" \
  "ds_read_b64_tr_b16 a[216:217], v106 offset:128\n" \
  "ds_read_b64_tr_b16 a[218:219], v106 offset:18560\n" \
  "ds_read_b64_tr_b16 a[232:233], v106 offset:160\n" \
  "ds_read_b64_tr_b16 a[234:235], v106 offset:18592\n" \
  "ds_read_b64_tr_b16 a[248:249], v106 offset:192\n" \
  "ds_read_b64_tr_b16 a[250:251], v106 offset:18624\n" \
  "ds_read_b64_tr_b16 v[38:39], v106 offset:224\n" \
  "ds_read_b64_tr_b16 v[40:41], v106 offset:18656\n" \
  "ds_read_b64_tr_b16 v[54:55], v106 offset:256\n" \
  "ds_read_b64_tr_b16 v[56:57], v106 offset:18688\n" \
  "s_waitcnt vmcnt(0)\n" \
  "s_waitcnt lgkmcnt(0)\n" \
  "s_barrier\n" \
  "v_add_u32 v106, 0, %[wr2]\n" \
  "ds_read_b64_tr_b16 a[156:157], v106 offset:0\n" \
  "ds_read_b64_tr_b16 a[158:159], v106 offset:18432\n" \
  "ds_read_b64_tr_b16 a[172:173], v106 offset:32\n" \
  "ds_read_b64_tr_b16 a[174:175], v106 offset:18464\n" \
  "ds_read_b64_tr_b16 a[188:189], v106 offset:64\n" \
  "ds_read_b64_tr_b16 a[190:191], v106 offset:18496\n" \
  "ds_read_b64_tr_b16 a[204:205], v106 offset:96\n" \
  "ds_read_b64_tr_b16 a[206:207], v106 offset:18528\n" \
  "ds_read_b64_tr_b16 a[220:221], v106 offset:128\n" \
  "ds_read_b64_tr_b16 a[222:223], v106 offset:18560\n" \
  "ds_read_b64_tr_b16 a[236:237], v106 offset:160\n" \
  "ds_read_b64_tr_b16 a[238:239], v106 offset:18592\n" \
  "ds_read_b64_tr_b16 a[252:253], v106 offset:192\n" \
  "ds_read_b64_tr_b16 a[254:255], v106 offset:18624\n" \
  "ds_read_b64_tr_b16 v[42:43], v106 offset:224\n" \
  "ds_read_b64_tr_b16 v[44:45], v106 offset:18656\n" \
  "ds_read_b64_tr_b16 v[58:59], v106 offset:256\n" \
  "ds_read_b64_tr_b16 v[60:61], v106 offset:18688\n" \
  "s_waitcnt lgkmcnt(0)\n" \
  "s_barrier\n" \
  "v_mov_b32 v236, 1\n" \
  "s_add_i32 s76, %[bas], -384\n" \
  "buffer_atomic_add v236, %[rdy], %[X], s76 offen sc1\n" \
  "s_cmp_eq_u32 %[grp], 0\n" \
  "s_cbranch_scc1 .Loa_go\n" \
  "s_mov_b32 s64, 0\n" \
  "s_xor_b32 s75, %[bas], 0x6000\n" \
  ".Loa_g1:\n" \
  "buffer_load_dword v236, off, %[X], s75 offset:384 sc1\n" \
  "s_waitcnt vmcnt(0)\n" \
  "v_readfirstlane_b32 s74, v236\n" \
  "s_cmp_lg_u32 s74, 0\n" \
  "s_cbranch_scc1 .Loa_g2\n" \
  "s_sleep 8\n" \
  "s_add_i32 s64, s64, 1\n" \
  "s_cmp_lt_u32 s64, 400\n" \
  "s_cbranch_scc1 .Loa_g1\n" \
  ".Loa_g2:\n" \
  "s_add_i32 s74, s74, %[g1t]\n" \
  ".Loa_g3:\n" \
  "s_memrealtime s[76:77]\n" \
  "s_waitcnt lgkmcnt(0)\n" \
  "s_sub_i32 s76, s76, s74\n" \
  "s_cmp_gt_u32 s76, -3000\n" \
  "s_cbranch_scc1 .Loa_g3\n" \
  ".Loa_go:\n" \
  "s_cmp_lg_u32 %[first], 0\n" \
  "s_cbranch_scc1 .Loa_r2\n" \
  "s_mov_b32 s64, 0\n" \
  "s_add_i32 s76, %[bas], -384\n" \
  ".Loa_r0:\n" \
  "buffer_load_dword v236, off, %[X], s76 sc1\n" \
  "s_waitcnt vmcnt(0)\n" \
  "v_readfirstlane_b32 s74, v236\n" \
  "s_cmp_ge_u32 s74, 100\n" \
  "s_cbranch_scc1 .Loa_r1\n" \
  "s_sleep 1\n" \
  "s_add_i32 s64, s64, 1\n" \
  "s_cmp_lt_u32 s64, 2000\n" \
  "s_cbranch_scc1 .Loa_r0\n" \
  ".Loa_r1:\n" \
  "s_memrealtime s[74:75]\n" \
  "s_waitcnt lgkmcnt(0)\n" \
  "s_add_i32 s74, s74, %[x0]\n" \
  "s_or_b32 s74, s74, 1\n" \
  "s_add_i32 s75, s74, 125\n" \
  "v_mov_b32 v236, s74\n" \
  "v_mov_b32 v237, s75\n" \
  "buffer_store_dwordx2 v[236:237], %[cpw], %[X], %[bas] offen offset:0 sc1\n" \
  "s_add_i32 s74, s75, 95\n" \
  "s_or_b32 s74, s74, 1\n" \
  "s_mul_i32 s75, %[tau], 156\n" \
  "s_lshr_b32 s75, s75, 8\n" \
  "s_add_i32 s75, s75, s74\n" \
  "v_mov_b32 v236, s74\n" \
  "v_mov_b32 v237, s75\n" \
  "buffer_store_dwordx2 v[236:237], %[cpw], %[X], %[bas] offen offset:128 sc1\n" \
  ".Loa_r2:\n" \
  "s_sub_i32 s95, %[tau], %[mrg]\n" \
  "s_mul_i32 s96, %[tau], 156\n" \
  "s_lshr_b32 s96, s96, 8\n" \
  "s_sub_i32 s97, %[tau], s96\n" \
  "s_sub_i32 s96, s96, %[d1]\n" \
  "s_sub_i32 s97, s97, %[d2]\n" \
  "s_mov_b32 s98, 4\n" \
  "s_mov_b32 s72, 0\n" \
  "s_waitcnt vmcnt(0)\n" \
  "v_mov_b64 v[238:239], 0\n" \
  "s_branch .Lo_top\n" \
  ".Lo_sq:\n" \
  "s_cmp_lt_i32 s56, 0\n" \
  "s_cbranch_scc1 .Lo_sq_back\n" \
  "s_mov_b32 s64, 0\n" \
  ".Lo_sqw:\n" \
  "buffer_load_dword v243, off, %[X], %[ufo] sc1\n" \
  "buffer_load_dwordx4 v[94:97], %[la], %[X], s68 offen offset:0 sc1\n" \
  "buffer_load_dwordx4 v[98:101], %[la], %[X], s68 offen offset:1024 sc1\n" \
  "buffer_load_dwordx4 v[138:141], %[la], %[X], s59 offen offset:0 sc1\n" \
  "buffer_load_dwordx4 v[142:145], %[la], %[X], s59 offen offset:1024 sc1\n" \
  "buffer_load_dwordx4 v[146:149], %[la], %[X], s59 offen offset:2048 sc1\n" \
  "buffer_load_dwordx4 v[150:153], %[la], %[X], s59 offen offset:3072 sc1\n" \
  "buffer_load_dwordx4 v[154:157], v240, %[X], s59 offen offset:0 sc1\n" \
  "s_waitcnt vmcnt(4)\n" \
  "v_subrev_u32 v241, s56, v243\n" \
  "s_waitcnt vmcnt(4)\n" \
  "v_mfma_f32_16x16x32_bf16 v[202:205], v[62:65], v[138:141], 0\n" \
  "v_mfma_f32_16x16x32_bf16 v[206:209], v[66:69], v[138:141], 0\n" \
  "v_bitop3_b32 v241, v241, v138, s60 bitop3:0xf6\n" \
  "s_waitcnt vmcnt(3)\n" \
  "v_mfma_f32_16x16x32_bf16 v[202:205], v[70:73], v[142:145], v[202:205]\n" \
  "v_mfma_f32_16x16x32_bf16 v[206:209], v[74:77], v[142:145], v[206:209]\n" \
  "v_bitop3_b32 v241, v241, v142, s60 bitop3:0xf6\n" \
  "s_waitcnt vmcnt(2)\n" \
  "v_mfma_f32_16x16x32_bf16 v[202:205], v[78:81], v[146:149], v[202:205]\n" \
  "v_mfma_f32_16x16x32_bf16 v[206:209], v[82:85], v[146:149], v[206:209]\n" \
  "v_bitop3_b32 v241, v241, v146, s60 bitop3:0xf6\n" \
  "s_waitcnt vmcnt(1)\n" \
  "v_mfma_f32_16x16x32_bf16 v[202:205], v[86:89], v[150:153], v[202:205]\n" \
  "v_mfma_f32_16x16x32_bf16 v[206:209], v[90:93], v[150:153], v[206:209]\n" \
  "v_bitop3_b32 v241, v241, v150, s60 bitop3:0xf6\n" \
  "s_waitcnt vmcnt(0)\n" \
  "v_mfma_f32_16x16x32_bf16 v[202:205], v[94:97], v[154:157], v[202:205]\n" \
  "v_mfma_f32_16x16x32_bf16 v[206:209], v[98:101], v[154:157], v[206:209]\n" \
  "v_bitop3_b32 v236, v154, v155, s60 bitop3:0x7e\n" \
  "v_bitop3_b32 v237, v156, v157, s60 bitop3:0x7e\n" \
  "v_or3_b32 v241, v241, v236, v237\n" \
  "v_max3_u32 v236, v95, v96, v97\n" \
  "v_max3_u32 v236, v236, v99, v100\n" \
  "v_max_u32 v236, v236, v101\n" \
  "v_and_b32 v236, %[w4], v236\n" \
  "v_ashrrev_i32 v241, 31, v241\n" \
  "v_max3_u32 v236, v236, v94, v98\n" \
  "v_max_u32 v236, v236, v241\n" \
  "v_cmp_eq_u32 vcc, -1, v236\n" \
  "s_cbranch_vccz .Lo_sqw_ok\n" \
  "s_add_i32 s64, s64, 1\n" \
  "s_and_b32 s77, s64, 1023\n" \
  "s_cbranch_scc1 .Lo_sqw\n" \
  "buffer_load_dword v237, off, %[F], 0 offset:1024 sc1\n" \
  "s_waitcnt vmcnt(0)\n" \
  "v_readfirstlane_b32 s77, v237\n" \
  "s_cmp_lg_u32 s77, 0\n" \
  "s_cbranch_scc1 .Lo_sqw_ok\n" \
  "s_cmp_lt_u32 s64, 400000\n" \
  "s_cbranch_scc1 .Lo_sqw\n" \
  "v_mov_b32 v237, 1\n" \
  "buffer_store_dword v237, off, %[F], 0 offset:1024 sc1\n" \
  ".Lo_sqw_ok:\n" \
  "v_add_f32_dpp v202, v206, v202 row_ror:8 row_mask:0xf bank_mask:0xf\n" \
  "v_add_f32_dpp v203, v207, v203 row_ror:8 row_mask:0xf bank_mask:0xf\n" \
  "v_add_f32_dpp v204, v208, v204 row_ror:8 row_mask:0xf bank_mask:0xf\n" \
  "v_add_f32_dpp v205, v209, v205 row_ror:8 row_mask:0xf bank_mask:0xf\n" \
  "ds_write_b128 %[rmW], v[202:205]\n" \
  "s_memrealtime s[88:89]\n" \
  "s_waitcnt lgkmcnt(0)\n" \
  "s_add_i32 s88, s88, %[od1]\n" \
  "s_add_i32 s88, s88, 40\n" \
  "s_branch .Lo_sq_back\n" \
  ".Lo_sm:\n" \
  "s_cmp_lt_i32 s56, 0\n" \
  "s_cbranch_scc1 .Lo_sm_back\n" \
  "s_add_i32 s76, s57, -0x4000\n" \
  "v_mov_b64 v[194:195], 0\n" \
  "v_mov_b64 v[196:197], 0\n" \
  "v_mov_b64 v[198:199], 0\n" \
  "v_mov_b64 v[200:201], 0\n" \
  "s_mov_b32 s64, 0\n" \
  ".Lo_smw:\n" \
  "buffer_load_dwordx4 v[102:105], %[la], %[X], s76 offen offset:0 sc1\n" \
  "buffer_load_dwordx4 v[106:109], %[la], %[X], s76 offen offset:1024 sc1\n" \
  "buffer_load_dwordx4 v[110:113], %[la], %[X], s76 offen offset:2048 sc1\n" \
  "buffer_load_dwordx4 v[114:117], %[la], %[X], s76 offen offset:3072 sc1\n" \
  "buffer_load_dwordx4 v[118:121], v240, %[X], s76 offen offset:0 sc1\n" \
  "buffer_load_dwordx4 v[122:125], v240, %[X], s76 offen offset:1024 sc1\n" \
  "buffer_load_dwordx4 v[126:129], v240, %[X], s76 offen offset:2048 sc1\n" \
  "buffer_load_dwordx4 v[130:133], v240, %[X], s76 offen offset:3072 sc1\n" \
  "buffer_load_dwordx4 v[134:137], %[la], %[X], s57 offen offset:0 sc1\n" \
  "s_waitcnt vmcnt(0)\n" \
  "v_max3_u32 v237, v102, v103, v104\n" \
  "v_max3_u32 v237, v237, v105, v106\n" \
  "v_max3_u32 v237, v237, v107, v108\n" \
  "v_max3_u32 v237, v237, v109, v110\n" \
  "v_max3_u32 v237, v237, v111, v112\n" \
  "v_max3_u32 v237, v237, v113, v114\n" \
  "v_max3_u32 v237, v237, v115, v116\n" \
  "v_max3_u32 v237, v237, v117, v118\n" \
  "v_max3_u32 v237, v237, v119, v120\n" \
  "v_max3_u32 v237, v237, v121, v122\n" \
  "v_max3_u32 v237, v237, v123, v124\n" \
  "v_max3_u32 v237, v237, v125, v126\n" \
  "v_max3_u32 v237, v237, v127, v128\n" \
  "v_max3_u32 v237, v237, v129, v130\n" \
  "v_max3_u32 v237, v237, v131, v132\n" \
  "v_max3_u32 v237, v237, v133, v134\n" \
  "v_max3_u32 v237, v237, v135, v136\n" \
  "v_max_u32 v237, v237, v137\n" \
  "v_cmp_eq_u32 vcc, -1, v237\n" \
  "s_cbranch_vccz .Lo_smw_ok\n" \
  "s_add_i32 s64, s64, 1\n" \
  "s_and_b32 s77, s64, 1023\n" \
  "s_cbranch_scc1 .Lo_smw\n" \
  "buffer_load_dword v237, off, %[F], 0 offset:1024 sc1\n" \
  "s_waitcnt vmcnt(0)\n" \
  "v_readfirstlane_b32 s77, v237\n" \
  "s_cmp_lg_u32 s77, 0\n" \
  "s_cbranch_scc1 .Lo_smw_ok\n" \
  "s_cmp_lt_u32 s64, 400000\n" \
  "s_cbranch_scc1 .Lo_smw\n" \
  "v_mov_b32 v237, 1\n" \
  "buffer_store_dword v237, off, %[F], 0 offset:1024 sc1\n" \
  ".Lo_smw_ok:\n" \
  "s_memrealtime s[88:89]\n" \
  "v_smfmac_f32_16x16x64_bf16 v[194:197], v[102:105], a[0:7], %[idx]\n" \
  "v_smfmac_f32_16x16x64_bf16 v[198:201], v[102:105], a[8:15], %[idx]\n" \
  "v_smfmac_f32_16x16x64_bf16 v[194:197], v[106:109], a[16:23], %[idx]\n" \
  "v_smfmac_f32_16x16x64_bf16 v[198:201], v[106:109], a[24:31], %[idx]\n" \
  "v_smfmac_f32_16x16x64_bf16 v[194:197], v[110:113], a[32:39], %[idx]\n" \
  "v_smfmac_f32_16x16x64_bf16 v[198:201], v[110:113], a[40:47], %[idx]\n" \
  "v_smfmac_f32_16x16x64_bf16 v[194:197], v[114:117], a[48:55], %[idx]\n" \
  "v_smfmac_f32_16x16x64_bf16 v[198:201], v[114:117], a[56:63], %[idx]\n" \
  "v_smfmac_f32_16x16x64_bf16 v[194:197], v[118:121], a[64:71], %[idx]\n" \
  "v_smfmac_f32_16x16x64_bf16 v[198:201], v[118:121], a[72:79], %[idx]\n" \
  "v_smfmac_f32_16x16x64_bf16 v[194:197], v[122:125], a[80:87], %[idx]\n" \
  "v_smfmac_f32_16x16x64_bf16 v[198:201], v[122:125], a[88:95], %[idx]\n" \
  "v_smfmac_f32_16x16x64_bf16 v[194:197], v[126:129], a[96:103], %[idx]\n" \
  "v_smfmac_f32_16x16x64_bf16 v[198:201], v[126:129], a[104:111], %[idx]\n" \
  "v_smfmac_f32_16x16x64_bf16 v[194:197], v[130:133], a[112:119], %[idx]\n" \
  "v_smfmac_f32_16x16x64_bf16 v[198:201], v[130:133], a[120:127], %[idx]\n" \
  "v_smfmac_f32_16x16x64_bf16 v[194:197], v[134:137], a[128:135], %[idx]\n" \
  "v_smfmac_f32_16x16x64_bf16 v[198:201], v[134:137], a[136:143], %[idx]\n" \
  "s_waitcnt lgkmcnt(0)\n" \
  "s_add_i32 s88, s88, %[od1]\n" \
  "s_nop 5\n" \
  "v_pk_add_f32 v[194:195], v[194:195], v[196:197]\n" \
  "v_pk_add_f32 v[198:199], v[198:199], v[200:201]\n" \
  "v_cvt_pk_bf16_f32 v196, v194, v195\n" \
  "v_cvt_pk_bf16_f32 v197, v198, v199\n" \
  "ds_write2_b32 %[aW], v196, v197 offset1:16\n" \
  "s_branch .Lo_sm_back\n" \
  ".Lo_sf:\n" \
  "s_mov_b32 s64, 0\n" \
  ".Lo_sfw:\n" \
  "buffer_load_dword v242, %[flo], %[X], 0 offen sc1\n" \
  "s_waitcnt vmcnt(0)\n" \
  "v_cmp_gt_i32 vcc, s63, v242\n" \
  "s_cbranch_vccz .Lo_sfw_ok\n" \
  "s_sleep 1\n" \
  "s_add_i32 s64, s64, 1\n" \
  "s_and_b32 s77, s64, 1023\n" \
  "s_cbranch_scc1 .Lo_sfw\n" \
  "buffer_load_dword v237, off, %[F], 0 offset:1024 sc1\n" \
  "s_waitcnt vmcnt(0)\n" \
  "v_readfirstlane_b32 s77, v237\n" \
  "s_cmp_lg_u32 s77, 0\n" \
  "s_cbranch_scc1 .Lo_sfw_ok\n" \
  "s_cmp_lt_u32 s64, 400000\n" \
  "s_cbranch_scc1 .Lo_sfw\n" \
  "v_mov_b32 v237, 1\n" \
  "buffer_store_dword v237, off, %[F], 0 offset:1024 sc1\n" \
  ".Lo_sfw_ok:\n" \
  "s_branch .Lo_sf_back\n" \
  ".Lo_cc:\n" \
  "v_and_b32 v248, 0xfff, v248\n" \
  "v_and_b32 v249, 0xfff, v249\n" \
  "v_and_b32 v250, 0xfff, v250\n" \
  "v_max_u32_dpp v248, v248, v248 quad_perm:[1,0,3,2] row_mask:0xf bank_mask:0xf\n" \
  "v_max_u32_dpp v249, v249, v249 quad_perm:[1,0,3,2] row_mask:0xf bank_mask:0xf\n" \
  "v_max_u32_dpp v250, v250, v250 quad_perm:[1,0,3,2] row_mask:0xf bank_mask:0xf\n" \
  "v_max_u32_dpp v248, v248, v248 quad_perm:[2,3,0,1] row_mask:0xf bank_mask:0xf\n" \
  "v_max_u32_dpp v249, v249, v249 quad_perm:[2,3,0,1] row_mask:0xf bank_mask:0xf\n" \
  "v_max_u32_dpp v250, v250, v250 quad_perm:[2,3,0,1] row_mask:0xf bank_mask:0xf\n" \
  "v_max_u32_dpp v248, v248, v248 row_half_mirror row_mask:0xf bank_mask:0xf\n" \
  "v_max_u32_dpp v249, v249, v249 row_half_mirror row_mask:0xf bank_mask:0xf\n" \
  "v_max_u32_dpp v250, v250, v250 row_half_mirror row_mask:0xf bank_mask:0xf\n" \
  "s_cmp_lt_i32 s56, s98\n" \
  "s_cbranch_scc1 .Lo_cd1\n" \
  "v_readfirstlane_b32 s76, v248\n" \
  "s_sub_i32 s76, s76, s95\n" \
  "s_add_i32 s77, s76, 1\n" \
  "s_ashr_i32 s77, s77, 2\n" \
  "s_max_i32 s76, s76, s77\n" \
  "s_max_i32 s76, s76, %[dn]\n" \
  "s_min_i32 s76, s76, %[up]\n" \
  "s_add_i32 s95, s95, s76\n" \
  "v_readfirstlane_b32 s76, v249\n" \
  "s_sub_i32 s76, s76, s96\n" \
  "s_add_i32 s77, s76, 1\n" \
  "s_ashr_i32 s77, s77, 2\n" \
  "s_max_i32 s76, s76, s77\n" \
  "s_max_i32 s76, s76, %[dn]\n" \
  "s_min_i32 s76, s76, %[up]\n" \
  "s_add_i32 s96, s96, s76\n" \
  "v_readfirstlane_b32 s76, v250\n" \
  "s_sub_i32 s76, s76, s97\n" \
  "s_add_i32 s77, s76, 1\n" \
  "s_ashr_i32 s77, s77, 2\n" \
  "s_max_i32 s76, s76, s77\n" \
  "s_max_i32 s76, s76, %[dn]\n" \
  "s_min_i32 s76, s76, %[up]\n" \
  "s_add_i32 s97, s97, s76\n" \
  ".Lo_cd1:\n" \
  "s_add_i32 s93, s96, %[d1]\n" \
  "s_add_i32 s90, s97, %[d2]\n" \
  "s_add_i32 s90, s90, s93\n" \
  "s_add_i32 s92, s95, %[mrg]\n" \
  "s_max_i32 s92, s92, s90\n" \
  "s_sub_i32 s90, s92, s90\n" \
  "s_lshr_b32 s90, s90, 1\n" \
  "s_add_i32 s93, s93, s90\n" \
  "s_sub_i32 s90, s65, %[oc1]\n" \
  "s_add_i32 s90, s90, s92\n" \
  "v_readfirstlane_b32 s76, v253\n" \
  "s_cmp_lt_i32 s56, 60\n" \
  "s_cselect_b32 s77, %[gg], 0\n" \
  "s_cselect_b32 s76, s76, 0\n" \
  "s_add_i32 s99, s76, %[lag]\n" \
  "s_sub_i32 s99, s99, s90\n" \
  "s_max_i32 s99, s99, 0\n" \
  "s_cmp_lg_u32 s76, 0\n" \
  "s_cselect_b32 s99, s99, s77\n" \
  "s_add_i32 s90, s90, s99\n" \
  "s_branch .Lo_cc_back\n" \
  ".Lo_cd:\n" \
  "s_cmp_lt_i32 s56, 1\n" \
  "s_cbranch_scc1 .Lo_cd_back\n" \
  "s_memrealtime s[74:75]\n" \
  "s_waitcnt lgkmcnt(0)\n" \
  "s_sub_i32 s76, s74, s65\n" \
  "s_cmp_lt_u32 s76, 88\n" \
  "s_cbranch_scc1 .Lo_cd2\n" \
  "s_add_i32 s76, s74, s92\n" \
  "s_add_i32 s76, s76, %[sh1]\n" \
  "s_sub_i32 s77, s76, s90\n" \
  "s_cmp_gt_u32 s77, -3000\n" \
  "s_cselect_b32 s90, s90, s76\n" \
  "s_add_i32 s98, s56, 4\n" \
  ".Lo_cd2:\n" \
  "s_max_u32 s90, s90, 1\n" \
  "s_add_i32 s93, s93, s90\n" \
  "v_mov_b32 v248, s90\n" \
  "v_mov_b32 v249, s93\n" \
  "buffer_store_dwordx2 v[248:249], %[cpw], %[X], s94 offen offset:128 sc1\n" \
  "s_branch .Lo_cd_back\n" \
  ".Loa_late:\n" \
  "s_cmp_lt_i32 s56, 0\n" \
  "s_cbranch_scc1 .Loa_al_ok\n" \
  "s_cmp_gt_u32 s91, -3000\n" \
  "s_cbranch_scc0 .Loa_late2\n" \
  ".Loa_wt:\n" \
  "s_memrealtime s[76:77]\n" \
  "s_waitcnt lgkmcnt(0)\n" \
  "s_sub_i32 s76, s76, s65\n" \
  "s_cmp_lt_i32 s76, -8\n" \
  "s_cbranch_scc1 .Loa_wt\n" \
  "s_min_i32 s75, s76, -2\n" \
  "s_lshl_b32 s75, s75, 2\n" \
  "s_getpc_b64 s[76:77]\n" \
  "s_add_i32 s75, s75, 48\n" \
  "s_add_u32 s76, s76, s75\n" \
  "s_addc_u32 s77, s77, 0\n" \
  "s_setpc_b64 s[76:77]\n" \
  "s_nop 4\n" \
  "s_nop 4\n" \
  "s_nop 4\n" \
  "s_nop 4\n" \
  "s_nop 4\n" \
  "s_nop 4\n" \
  "s_branch .Loa_al_ok\n" \
  ".Loa_late2:\n" \
  "s_cmp_lg_u32 s65, 0\n" \
  "s_cbranch_scc1 .Loa_late3\n" \
  "s_mov_b32 s64, 0\n" \
  ".Loa_tw:\n" \
  "buffer_load_dword v236, off, %[X], s94 sc1\n" \
  "s_waitcnt vmcnt(0)\n" \
  "v_readfirstlane_b32 s65, v236\n" \
  "s_cmp_lg_u32 s65, 0\n" \
  "s_cbranch_scc1 .Loa_tw_ok\n" \
  "s_sleep 1\n" \
  "s_add_i32 s64, s64, 1\n" \
  "s_cmp_lt_u32 s64, 200\n" \
  "s_cbranch_scc1 .Loa_tw\n" \
  ".Loa_tw_ok:\n" \
  "s_sub_i32 s91, s74, s65\n" \
  "s_cmp_gt_u32 s91, -3000\n" \
  "s_cbranch_scc1 .Loa_wt\n" \
  ".Loa_late3:\n" \
  "s_branch .Loa_al_ok\n" \
  ".Lob_late:\n" \
  "s_cmp_lt_i32 s56, 0\n" \
  "s_cbranch_scc1 .Lob_al_ok\n" \
  "s_cmp_gt_u32 s91, -3000\n" \
  "s_cbranch_scc0 .Lob_late2\n" \
  ".Lob_wt:\n" \
  "s_memrealtime s[76:77]\n" \
  "s_waitcnt lgkmcnt(0)\n" \
  "s_sub_i32 s76, s76, s86\n" \
  "s_cmp_lt_i32 s76, -8\n" \
  "s_cbranch_scc1 .Lob_wt\n" \
  "s_min_i32 s75, s76, -2\n" \
  "s_lshl_b32 s75, s75, 2\n" \
  "s_getpc_b64 s[76:77]\n" \
  "s_add_i32 s75, s75, 48\n" \
  "s_add_u32 s76, s76, s75\n" \
  "s_addc_u32 s77, s77, 0\n" \
  "s_setpc_b64 s[76:77]\n" \
  "s_nop 4\n" \
  "s_nop 4\n" \
  "s_nop 4\n" \
  "s_nop 4\n" \
  "s_nop 4\n" \
  "s_nop 4\n" \
  "s_branch .Lob_al_ok\n" \
  ".Lob_late2:\n" \
  "s_cmp_lg_u32 s86, 0\n" \
  "s_cbranch_scc1 .Lob_late3\n" \
  "s_mov_b32 s64, 0\n" \
  ".Lob_tw:\n" \
  "buffer_load_dword v236, off, %[X], s94 offset:4 sc1\n" \
  "s_waitcnt vmcnt(0)\n" \
  "v_readfirstlane_b32 s86, v236\n" \
  "s_cmp_lg_u32 s86, 0\n" \
  "s_cbranch_scc1 .Lob_tw_ok\n" \
  "s_sleep 1\n" \
  "s_add_i32 s64, s64, 1\n" \
  "s_cmp_lt_u32 s64, 200\n" \
  "s_cbranch_scc1 .Lob_tw\n" \
  ".Lob_tw_ok:\n" \
  "s_sub_i32 s91, s74, s86\n" \
  "s_cmp_gt_u32 s91, -3000\n" \
  "s_cbranch_scc1 .Lob_wt\n" \
  ".Lob_late3:\n" \
  "s_branch .Lob_al_ok\n" \
  ".Lo_end:\n"
#define O_CLOB "v30", "v31", "v32", "v33", "v34", "v35", "v36", "v37", "v38", "v39", "v40", "v41", "v42", "v43", "v44", "v45", "v46", "v47", "v48", "v49", "v50", "v51", "v52", "v53", "v54", "v55", "v56", "v57", "v58", "v59", "v60", "v61", "v62", "v63", "v64", "v65", "v66", "v67", "v68", "v69", "v70", "v71", "v72", "v73", "v74", "v75", "v76", "v77", "v78", "v79", "v80", "v81", "v82", "v83", "v84", "v85", "v86", "v87", "v88", "v89", "v90", "v91", "v92", "v93", "v94", "v95", "v96", "v97", "v98", "v99", "v100", "v101", "v102", "v103", "v104", "v105", "v106", "v107", "v108", "v109", "v110", "v111", "v112", "v113", "v114", "v115", "v116", "v117", "v118", "v119", "v120", "v121", "v122", "v123", "v124", "v125", "v126", "v127", "v128", "v129", "v130", "v131", "v132", "v133", "v134", "v135", "v136", "v137", "v138", "v139", "v140", "v141", "v142", "v143", "v144", "v145", "v146", "v147", "v148", "v149", "v150", "v151", "v152", "v153", "v154", "v155", "v156", "v157", "v158", "v159", "v160", "v161", "v162", "v163", "v164", "v165", "v166", "v167", "v168", "v169", "v170", "v171", "v172", "v173", "v174", "v175", "v176", "v177", "v178", "v179", "v180", "v181", "v182", "v183", "v184", "v185", "v186", "v187", "v188", "v189", "v190", "v191", "v192", "v193", "v194", "v195", "v196", "v197", "v198", "v199", "v200", "v201", "v202", "v203", "v204", "v205", "v206", "v207", "v208", "v209", "v210", "v211", "v212", "v213", "v214", "v215", "v216", "v217", "v218", "v219", "v220", "v221", "v222", "v223", "v224", "v225", "v226", "v227", "v228", "v229", "v230", "v231", "v232", "v233", "v234", "v235", "v236", "v237", "v238", "v239", "v240", "v241", "v242", "v243", "v244", "v245", "v246", "v247", "v248", "v249", "v250", "v251", "v252", "v253", "v254", "v255", "s56", "s57", "s58", "s59", "s60", "s61", "s62", "s63", "s64", "s65", "s66", "s67", "s68", "s69", "s70", "s71", "s72", "s73", "s74", "s75", "s76", "s77", "s78", "s79", "s80", "s81", "s82", "s83", "s84", "s85", "s86", "s87", "s88", "s89", "s90", "s91", "s92", "s93", "s94", "s95", "s96", "s97", "s98", "s99", "a0", "a1", "a2", "a3", "a4", "a5", "a6", "a7", "a8", "a9", "a10", "a11", "a12", "a13", "a14", "a15", "a16", "a17", "a18", "a19", "a20", "a21", "a22", "a23", "a24", "a25", "a26", "a27", "a28", "a29", "a30", "a31", "a32", "a33", "a34", "a35", "a36", "a37", "a38", "a39", "a40", "a41", "a42", "a43", "a44", "a45", "a46", "a47", "a48", "a49", "a50", "a51", "a52", "a53", "a54", "a55", "a56", "a57", "a58", "a59", "a60", "a61", "a62", "a63", "a64", "a65", "a66", "a67", "a68", "a69", "a70", "a71", "a72", "a73", "a74", "a75", "a76", "a77", "a78", "a79", "a80", "a81", "a82", "a83", "a84", "a85", "a86", "a87", "a88", "a89", "a90", "a91", "a92", "a93", "a94", "a95", "a96", "a97", "a98", "a99", "a100", "a101", "a102", "a103", "a104", "a105", "a106", "a107", "a108", "a109", "a110", "a111", "a112", "a113", "a114", "a115", "a116", "a117", "a118", "a119", "a120", "a121", "a122", "a123", "a124", "a125", "a126", "a127", "a128", "a129", "a130", "a131", "a132", "a133", "a134", "a135", "a136", "a137", "a138", "a139", "a140", "a141", "a142", "a143", "a144", "a145", "a146", "a147", "a148", "a149", "a150", "a151", "a152", "a153", "a154", "a155", "a156", "a157", "a158", "a159", "a160", "a161", "a162", "a163", "a164", "a165", "a166", "a167", "a168", "a169", "a170", "a171", "a172", "a173", "a174", "a175", "a176", "a177", "a178", "a179", "a180", "a181", "a182", "a183", "a184", "a185", "a186", "a187", "a188", "a189", "a190", "a191", "a192", "a193", "a194", "a195", "a196", "a197", "a198", "a199", "a200", "a201", "a202", "a203", "a204", "a205", "a206", "a207", "a208", "a209", "a210", "a211", "a212", "a213", "a214", "a215", "a216", "a217", "a218", "a219", "a220", "a221", "a222", "a223", "a224", "a225", "a226", "a227", "a228", "a229", "a230", "a231", "a232", "a233", "a234", "a235", "a236", "a237", "a238", "a239", "a240", "a241", "a242", "a243", "a244", "a245", "a246", "a247", "a248", "a249", "a250", "a251", "a252", "a253", "a254", "a255", "vcc", "scc", "m0", "memory"
#define U_ASM \
  "s_nop 4\n" \
  "s_mov_b32 s56, -2\n" \
  "v_add_u32 v183, 0x1000, %[la]\n" \
  "s_mov_b32 s78, 0\n" \
  "s_mov_b32 s79, 0\n" \
  "s_mov_b32 s80, 0\n" \
  "s_mov_b32 s81, -1\n" \
  "s_mov_b32 s87, 2.0\n" \
  "s_mov_b32 s88, 2.0\n" \
  "s_mov_b32 s66, 589824\n" \
  "s_add_i32 s94, %[bas], -256\n" \
  "s_waitcnt vmcnt(0)\n" \
  ".Lu_top:\n" \
  "s_add_i32 s56, s56, 1\n" \
  "s_add_i32 s94, s94, 128\n" \
  "v_readfirstlane_b32 s64, v182\n" \
  "s_bfe_i32 s62, s56, 0x10001\n" \
  "s_and_b32 s60, s62, 0x80008000\n" \
  "s_mul_i32 s59, s56, 36864\n" \
  "s_add_i32 s59, s59, %[ow]\n" \
  "s_lshl_b32 s61, s56, 17\n" \
  "s_and_b32 s61, s61, 0x20000\n" \
  "s_add_i32 s57, %[pt0], s61\n" \
  "s_add_i32 s66, s66, 589824\n" \
  "s_cmp_eq_u32 s66, 1769472\n" \
  "s_cselect_b32 s66, 0, s66\n" \
  "s_add_i32 s70, %[vm0], s66\n" \
  "s_cmp_lt_i32 s56, 0\n" \
  "s_cselect_b32 s59, 2.0, s59\n" \
  "s_cselect_b32 s57, 2.0, s57\n" \
  "s_cselect_b32 s70, 2.0, s70\n" \
  "s_add_i32 s58, s57, 0x4000\n" \
  "s_add_i32 s71, s70, 0x4000\n" \
  "s_cmp_lg_u32 s64, 0\n" \
  "s_cselect_b32 s76, %[uc2], 0\n" \
  "s_add_i32 s64, s64, s76\n" \
  "s_memrealtime s[74:75]\n" \
  "s_waitcnt lgkmcnt(0)\n" \
  "s_sub_i32 s91, s74, s64\n" \
  "s_cmp_gt_u32 s91, 14\n" \
  "s_cbranch_scc1 .Lub_late\n" \
  ".Lub_al_ok:\n" \
  "buffer_load_dwordx4 v[128:131], %[la], %[X], s70 offen offset:0 sc1\n" \
  "s_add_i32 s62, s56, 1\n" \
  "s_and_b32 s65, s62, 7\n" \
  "s_lshl_b32 s65, s65, 22\n" \
  "s_add_i32 s65, s65, s88\n" \
  "s_cmp_lt_i32 s56, 63\n" \
  "s_cselect_b32 s65, s65, 2.0\n" \
  "s_min_i32 s61, s62, 63\n" \
  "s_add_i32 s61, s61, 1\n" \
  "v_mov_b32 v185, s62\n" \
  "buffer_load_dwordx4 v[132:135], %[la], %[X], s70 offen offset:1024 sc1\n" \
  "s_nop 7\n" \
  "buffer_load_dwordx4 v[136:139], %[la], %[X], s71 offen offset:0 sc1\n" \
  "s_nop 7\n" \
  "buffer_load_dwordx4 v[140:143], %[la], %[X], s71 offen offset:1024 sc1\n" \
  "s_nop 7\n" \
  "buffer_load_dwordx4 v[40:43], %[la], %[X], s57 offen offset:2048 sc1\n" \
  "s_nop 7\n" \
  "buffer_load_dwordx4 v[44:47], %[la], %[X], s57 offen offset:3072 sc1\n" \
  "s_nop 7\n" \
  "buffer_load_dwordx4 v[48:51], v183, %[X], s57 offen offset:0 sc1\n" \
  "s_nop 7\n" \
  "buffer_load_dwordx4 v[52:55], v183, %[X], s57 offen offset:1024 sc1\n" \
  "s_waitcnt vmcnt(8)\n" \
  "v_accvgpr_read_b32 v96, a0\n" \
  "v_accvgpr_read_b32 v97, a1\n" \
  "v_accvgpr_read_b32 v98, a8\n" \
  "v_accvgpr_read_b32 v99, a9\n" \
  "v_accvgpr_read_b32 v100, a4\n" \
  "v_accvgpr_read_b32 v101, a5\n" \
  "v_accvgpr_read_b32 v102, a12\n" \
  "v_accvgpr_read_b32 v103, a13\n" \
  "buffer_load_dwordx4 v[56:59], v183, %[X], s57 offen offset:2048 sc1\n" \
  "v_accvgpr_read_b32 v104, a16\n" \
  "v_accvgpr_read_b32 v105, a17\n" \
  "v_accvgpr_read_b32 v106, a24\n" \
  "v_accvgpr_read_b32 v107, a25\n" \
  "v_accvgpr_read_b32 v108, a20\n" \
  "v_accvgpr_read_b32 v109, a21\n" \
  "v_accvgpr_read_b32 v110, a28\n" \
  "v_accvgpr_read_b32 v111, a29\n" \
  "buffer_load_dwordx4 v[60:63], v183, %[X], s57 offen offset:3072 sc1\n" \
  "v_accvgpr_read_b32 v112, a32\n" \
  "v_accvgpr_read_b32 v113, a33\n" \
  "v_accvgpr_read_b32 v114, a40\n" \
  "v_accvgpr_read_b32 v115, a41\n" \
  "v_accvgpr_read_b32 v116, a36\n" \
  "v_accvgpr_read_b32 v117, a37\n" \
  "v_accvgpr_read_b32 v118, a44\n" \
  "v_accvgpr_read_b32 v119, a45\n" \
  "buffer_load_dwordx4 v[64:67], %[la], %[X], s58 offen offset:0 sc1\n" \
  "v_accvgpr_read_b32 v120, a48\n" \
  "v_accvgpr_read_b32 v121, a49\n" \
  "v_accvgpr_read_b32 v122, a56\n" \
  "v_accvgpr_read_b32 v123, a57\n" \
  "v_accvgpr_read_b32 v124, a52\n" \
  "v_accvgpr_read_b32 v125, a53\n" \
  "v_accvgpr_read_b32 v126, a60\n" \
  "v_accvgpr_read_b32 v127, a61\n" \
  "buffer_load_dwordx4 v[68:71], %[la], %[X], s58 offen offset:1024 sc1\n" \
  "s_nop 7\n" \
  "buffer_load_dwordx4 v[72:75], %[la], %[X], s58 offen offset:2048 sc1\n" \
  "s_nop 7\n" \
  "buffer_load_dwordx4 v[76:79], %[la], %[X], s58 offen offset:3072 sc1\n" \
  "s_nop 7\n" \
  "buffer_load_dword v184, %[flo], %[X], 0 offen sc1\n" \
  "s_nop 7\n" \
  "s_waitcnt vmcnt(10)\n" \
  "v_mfma_f32_16x16x32_bf16 v[148:151], v[128:131], v[40:43], 0\n" \
  "v_bitop3_b32 v176, v40, v41, s60 bitop3:0x7e\n" \
  "v_bitop3_b32 v177, v42, v43, s60 bitop3:0x7e\n" \
  "v_mfma_f32_16x16x32_bf16 v[156:159], v[136:139], v[40:43], 0\n" \
  "buffer_load_dwordx4 v[32:35], %[la], %[X], s57 offen offset:0 sc1\n" \
  "s_waitcnt vmcnt(10)\n" \
  "v_mfma_f32_16x16x32_bf16 v[148:151], v[132:135], v[44:47], v[148:151]\n" \
  "v_bitop3_b32 v178, v44, v45, s60 bitop3:0x7e\n" \
  "v_or3_b32 v176, v176, v177, v178\n" \
  "v_mfma_f32_16x16x32_bf16 v[156:159], v[140:143], v[44:47], v[156:159]\n" \
  "v_bitop3_b32 v177, v46, v47, s60 bitop3:0x7e\n" \
  "buffer_load_dwordx4 v[36:39], %[la], %[X], s57 offen offset:1024 sc1\n" \
  "s_waitcnt vmcnt(10)\n" \
  "v_mfma_f32_16x16x32_bf16 v[148:151], a[0:3], v[48:51], v[148:151]\n" \
  "v_mfma_f32_16x16x32_bf16 v[156:159], a[32:35], v[48:51], v[156:159]\n" \
  "buffer_load_dwordx4 v[80:83], v183, %[X], s58 offen offset:0 sc1\n" \
  "v_max3_u32 v180, v129, v130, v131\n" \
  "v_max3_u32 v180, v180, v133, v134\n" \
  "v_max_u32 v180, v180, v135\n" \
  "v_and_b32 v180, %[w40], v180\n" \
  "v_max3_u32 v180, v180, v128, v132\n" \
  "v_max3_u32 v181, v137, v138, v139\n" \
  "v_max3_u32 v181, v181, v141, v142\n" \
  "v_max_u32 v181, v181, v143\n" \
  "v_and_b32 v181, %[w41], v181\n" \
  "v_max3_u32 v181, v181, v136, v140\n" \
  "s_waitcnt vmcnt(10)\n" \
  "v_mfma_f32_16x16x32_bf16 v[148:151], a[4:7], v[52:55], v[148:151]\n" \
  "v_bitop3_b32 v178, v48, v52, s60 bitop3:0x7e\n" \
  "v_or3_b32 v176, v176, v177, v178\n" \
  "v_mfma_f32_16x16x32_bf16 v[156:159], a[36:39], v[52:55], v[156:159]\n" \
  "buffer_load_dwordx4 v[84:87], v183, %[X], s58 offen offset:1024 sc1\n" \
  "s_waitcnt vmcnt(10)\n" \
  "v_mfma_f32_16x16x32_bf16 v[148:151], a[8:11], v[56:59], v[148:151]\n" \
  "v_mfma_f32_16x16x32_bf16 v[156:159], a[40:43], v[56:59], v[156:159]\n" \
  "buffer_load_dwordx4 v[88:91], v183, %[X], s58 offen offset:2048 sc1\n" \
  "s_waitcnt vmcnt(10)\n" \
  "v_mfma_f32_16x16x32_bf16 v[148:151], a[12:15], v[60:63], v[148:151]\n" \
  "v_bitop3_b32 v177, v56, v60, s60 bitop3:0x7e\n" \
  "v_mfma_f32_16x16x32_bf16 v[156:159], a[44:47], v[60:63], v[156:159]\n" \
  "buffer_load_dwordx4 v[92:95], v183, %[X], s58 offen offset:3072 sc1\n" \
  "s_waitcnt vmcnt(10)\n" \
  "v_mfma_f32_16x16x32_bf16 v[148:151], a[16:19], v[64:67], v[148:151]\n" \
  "v_mfma_f32_16x16x32_bf16 v[156:159], a[48:51], v[64:67], v[156:159]\n" \
  "buffer_load_dword v182, off, %[X], s94 offset:132 sc1\n" \
  "s_waitcnt vmcnt(10)\n" \
  "v_mfma_f32_16x16x32_bf16 v[148:151], a[20:23], v[68:71], v[148:151]\n" \
  "v_bitop3_b32 v178, v64, v68, s60 bitop3:0x7e\n" \
  "v_or3_b32 v176, v176, v177, v178\n" \
  "v_mfma_f32_16x16x32_bf16 v[156:159], a[52:55], v[68:71], v[156:159]\n" \
  "s_waitcnt vmcnt(9)\n" \
  "v_mfma_f32_16x16x32_bf16 v[148:151], a[24:27], v[72:75], v[148:151]\n" \
  "v_bitop3_b32 v176, v176, v72, s60 bitop3:0xf6\n" \
  "v_mfma_f32_16x16x32_bf16 v[156:159], a[56:59], v[72:75], v[156:159]\n" \
  "s_waitcnt vmcnt(8)\n" \
  "v_mfma_f32_16x16x32_bf16 v[148:151], a[28:31], v[76:79], v[148:151]\n" \
  "v_bitop3_b32 v176, v176, v76, s60 bitop3:0xf6\n" \
  "v_mfma_f32_16x16x32_bf16 v[156:159], a[60:63], v[76:79], v[156:159]\n" \
  "s_waitcnt vmcnt(7)\n" \
  "v_subrev_u32 v184, s61, v184\n" \
  "v_or_b32 v176, v176, v184\n" \
  "v_ashrrev_i32 v176, 31, v176\n" \
  "v_max3_u32 v180, v180, v181, v176\n" \
  "v_cmp_eq_u32 vcc, -1, v180\n" \
  "s_cbranch_vccnz .Lu_sp\n" \
  ".Lu_sp_back:\n" \
  "s_waitcnt vmcnt(6)\n" \
  "v_mfma_f32_16x16x32_bf16 v[144:147], v[128:131], v[32:35], 0\n" \
  "v_bitop3_b32 v176, v32, v33, s60 bitop3:0x7e\n" \
  "v_bitop3_b32 v177, v34, v35, s60 bitop3:0x7e\n" \
  "v_mfma_f32_16x16x32_bf16 v[152:155], v[136:139], v[32:35], 0\n" \
  "ds_write_b128 %[rtW], v[148:151] offset:1024\n" \
  "s_waitcnt vmcnt(5)\n" \
  "v_mfma_f32_16x16x32_bf16 v[144:147], v[132:135], v[36:39], v[144:147]\n" \
  "v_bitop3_b32 v178, v36, v37, s60 bitop3:0x7e\n" \
  "v_or3_b32 v176, v176, v177, v178\n" \
  "v_mfma_f32_16x16x32_bf16 v[152:155], v[140:143], v[36:39], v[152:155]\n" \
  "v_bitop3_b32 v177, v38, v39, s60 bitop3:0x7e\n" \
  "ds_write_b128 %[rtW], v[156:159] offset:9216\n" \
  "s_waitcnt vmcnt(4)\n" \
  "v_mfma_f32_16x16x32_bf16 v[144:147], v[96:99], v[80:83], v[144:147]\n" \
  "v_bitop3_b32 v178, v80, v82, s60 bitop3:0x7e\n" \
  "v_or3_b32 v176, v176, v177, v178\n" \
  "v_mfma_f32_16x16x32_bf16 v[152:155], v[112:115], v[80:83], v[152:155]\n" \
  "s_waitcnt vmcnt(3)\n" \
  "v_mfma_f32_16x16x32_bf16 v[144:147], v[100:103], v[84:87], v[144:147]\n" \
  "v_bitop3_b32 v177, v84, v86, s60 bitop3:0x7e\n" \
  "v_mfma_f32_16x16x32_bf16 v[152:155], v[116:119], v[84:87], v[152:155]\n" \
  "buffer_load_dwordx4 a[0:3], %[la], %[X], s65 offen offset:0 sc1\n" \
  "s_waitcnt vmcnt(3)\n" \
  "v_mfma_f32_16x16x32_bf16 v[144:147], v[104:107], v[88:91], v[144:147]\n" \
  "v_bitop3_b32 v178, v88, v90, s60 bitop3:0x7e\n" \
  "v_or3_b32 v176, v176, v177, v178\n" \
  "v_mfma_f32_16x16x32_bf16 v[152:155], v[120:123], v[88:91], v[152:155]\n" \
  "buffer_load_dwordx4 a[4:7], %[la], %[X], s65 offen offset:1024 sc1\n" \
  "s_waitcnt vmcnt(3)\n" \
  "v_mfma_f32_16x16x32_bf16 v[144:147], v[108:111], v[92:95], v[144:147]\n" \
  "v_bitop3_b32 v176, v176, v92, s60 bitop3:0xf6\n" \
  "v_bitop3_b32 v176, v176, v94, s60 bitop3:0xf6\n" \
  "v_mfma_f32_16x16x32_bf16 v[152:155], v[124:127], v[92:95], v[152:155]\n" \
  "buffer_load_dwordx4 a[8:11], %[la], %[X], s65 offen offset:2048 sc1\n" \
  "v_cmp_gt_i32 vcc, 0, v176\n" \
  "s_cbranch_vccnz .Lu_sr\n" \
  ".Lu_sr_back:\n" \
  "s_nop 1\n" \
  "ds_write_b128 %[rtW], v[144:147] offset:0\n" \
  "buffer_load_dwordx4 a[12:15], %[la], %[X], s65 offen offset:3072 sc1\n" \
  "s_nop 0\n" \
  "ds_write_b128 %[rtW], v[152:155] offset:8192\n" \
  "buffer_load_dwordx4 a[16:19], v183, %[X], s65 offen offset:0 sc1\n" \
  "s_waitcnt lgkmcnt(0)\n" \
  "s_barrier\n" \
  "ds_read2st64_b64 v[160:163], %[rtA] offset0:0 offset1:4\n" \
  "ds_read2st64_b64 v[164:167], %[rtA] offset0:8 offset1:12\n" \
  "ds_read2st64_b64 v[168:171], %[rtA] offset0:16 offset1:20\n" \
  "ds_read2st64_b64 v[172:175], %[rtA] offset0:24 offset1:28\n" \
  "buffer_store_dword v185, %[ufo], %[X], s87 offen sc1\n" \
  "buffer_load_dwordx4 a[20:23], v183, %[X], s65 offen offset:1024 sc1\n" \
  "s_waitcnt lgkmcnt(2)\n" \
  "v_pk_add_f32 v[160:161], v[160:161], v[162:163]\n" \
  "v_pk_add_f32 v[164:165], v[164:165], v[166:167]\n" \
  "v_pk_add_f32 v[160:161], v[160:161], v[164:165]\n" \
  "v_cvt_pk_bf16_f32 v160, v160, v161\n" \
  "v_xor_b32 v160, s60, v160\n" \
  "v_min_u32 v160, -2, v160\n" \
  "buffer_store_dword v160, %[lo], %[OS], s59 offen offset:0 sc1\n" \
  "buffer_load_dwordx4 a[24:27], v183, %[X], s65 offen offset:2048 sc1\n" \
  "s_waitcnt lgkmcnt(0)\n" \
  "v_pk_add_f32 v[168:169], v[168:169], v[170:171]\n" \
  "v_pk_add_f32 v[172:173], v[172:173], v[174:175]\n" \
  "v_pk_add_f32 v[168:169], v[168:169], v[172:173]\n" \
  "v_cvt_pk_bf16_f32 v168, v168, v169\n" \
  "v_xor_b32 v168, s60, v168\n" \
  "v_min_u32 v168, -2, v168\n" \
  "buffer_store_dword v168, %[lo], %[OS], s59 offen offset:256 sc1\n" \
  "s_barrier\n" \
  "s_waitcnt vmcnt(10)\n" \
  "buffer_load_dwordx4 a[28:31], v183, %[X], s65 offen offset:3072 sc1\n" \
  "s_add_i32 s65, s65, 0x10000\n" \
  "s_nop 7\n" \
  "buffer_load_dwordx4 a[32:35], %[la], %[X], s65 offen offset:0 sc1\n" \
  "s_nop 7\n" \
  "buffer_load_dwordx4 a[36:39], %[la], %[X], s65 offen offset:1024 sc1\n" \
  "s_nop 7\n" \
  "buffer_load_dwordx4 a[40:43], %[la], %[X], s65 offen offset:2048 sc1\n" \
  "s_nop 7\n" \
  "buffer_load_dwordx4 a[44:47], %[la], %[X], s65 offen offset:3072 sc1\n" \
  "s_nop 7\n" \
  "buffer_load_dwordx4 a[48:51], v183, %[X], s65 offen offset:0 sc1\n" \
  "s_nop 7\n" \
  "buffer_load_dwordx4 a[52:55], v183, %[X], s65 offen offset:1024 sc1\n" \
  "s_nop 7\n" \
  "buffer_load_dwordx4 a[56:59], v183, %[X], s65 offen offset:2048 sc1\n" \
  "s_nop 7\n" \
  "buffer_load_dwordx4 a[60:63], v183, %[X], s65 offen offset:3072 sc1\n" \
  "s_add_i32 s65, s65, 0x10000\n" \
  "s_nop 7\n" \
  "s_cmp_lg_u32 s56, s81\n" \
  "s_cbranch_scc1 .Lu_top\n" \
  "s_cmp_eq_u32 s56, -1\n" \
  "s_cbranch_scc0 .Lu_end\n" \
  "s_mov_b32 s81, 63\n" \
  "s_mov_b32 s87, 0\n" \
  "s_mov_b32 s88, %[vt0]\n" \
  "s_cmp_eq_u32 %[grp], 0\n" \
  "s_cbranch_scc1 .Lub_go\n" \
  "s_mov_b32 s63, 0\n" \
  "s_xor_b32 s75, %[bas], 0x6000\n" \
  ".Lub_g1:\n" \
  "buffer_load_dword v180, off, %[X], s75 offset:384 sc1\n" \
  "s_waitcnt vmcnt(0)\n" \
  "v_readfirstlane_b32 s74, v180\n" \
  "s_cmp_lg_u32 s74, 0\n" \
  "s_cbranch_scc1 .Lub_g2\n" \
  "s_sleep 8\n" \
  "s_add_i32 s63, s63, 1\n" \
  "s_cmp_lt_u32 s63, 400\n" \
  "s_cbranch_scc1 .Lub_g1\n" \
  ".Lub_g2:\n" \
  "s_add_i32 s74, s74, %[g1t]\n" \
  ".Lub_g3:\n" \
  "s_memrealtime s[76:77]\n" \
  "s_waitcnt lgkmcnt(0)\n" \
  "s_sub_i32 s76, s76, s74\n" \
  "s_cmp_gt_u32 s76, -3000\n" \
  "s_cbranch_scc1 .Lub_g3\n" \
  ".Lub_go:\n" \
  "s_add_i32 s62, s56, 1\n" \
  "s_and_b32 s65, s62, 7\n" \
  "s_lshl_b32 s65, s65, 22\n" \
  "s_add_i32 s65, s65, s88\n" \
  "s_cmp_lt_i32 s56, 63\n" \
  "s_cselect_b32 s65, s65, 2.0\n" \
  "s_min_i32 s61, s62, 63\n" \
  "s_add_i32 s61, s61, 1\n" \
  "s_mov_b32 s63, 0\n" \
  ".Lu_enw:\n" \
  "buffer_load_dword v184, %[flo], %[X], 0 offen sc1\n" \
  "s_waitcnt vmcnt(0)\n" \
  "v_cmp_gt_i32 vcc, s61, v184\n" \
  "s_cbranch_vccz .Lu_enw_ok\n" \
  "s_sleep 4\n" \
  "s_add_i32 s63, s63, 1\n" \
  "s_and_b32 s77, s63, 1023\n" \
  "s_cbranch_scc1 .Lu_enw\n" \
  "buffer_load_dword v181, off, %[F], 0 offset:1024 sc1\n" \
  "s_waitcnt vmcnt(0)\n" \
  "v_readfirstlane_b32 s77, v181\n" \
  "s_cmp_lg_u32 s77, 0\n" \
  "s_cbranch_scc1 .Lu_enw_ok\n" \
  "s_cmp_lt_u32 s63, 400000\n" \
  "s_cbranch_scc1 .Lu_enw\n" \
  "v_mov_b32 v181, 1\n" \
  "buffer_store_dword v181, off, %[F], 0 offset:1024 sc1\n" \
  ".Lu_enw_ok:\n" \
  "buffer_load_dwordx4 a[0:3], %[la], %[X], s65 offen offset:0 sc1\n" \
  "buffer_load_dwordx4 a[4:7], %[la], %[X], s65 offen offset:1024 sc1\n" \
  "buffer_load_dwordx4 a[8:11], %[la], %[X], s65 offen offset:2048 sc1\n" \
  "buffer_load_dwordx4 a[12:15], %[la], %[X], s65 offen offset:3072 sc1\n" \
  "buffer_load_dwordx4 a[16:19], v183, %[X], s65 offen offset:0 sc1\n" \
  "buffer_load_dwordx4 a[20:23], v183, %[X], s65 offen offset:1024 sc1\n" \
  "buffer_load_dwordx4 a[24:27], v183, %[X], s65 offen offset:2048 sc1\n" \
  "buffer_load_dwordx4 a[28:31], v183, %[X], s65 offen offset:3072 sc1\n" \
  "s_add_i32 s65, s65, 0x10000\n" \
  "buffer_load_dwordx4 a[32:35], %[la], %[X], s65 offen offset:0 sc1\n" \
  "buffer_load_dwordx4 a[36:39], %[la], %[X], s65 offen offset:1024 sc1\n" \
  "buffer_load_dwordx4 a[40:43], %[la], %[X], s65 offen offset:2048 sc1\n" \
  "buffer_load_dwordx4 a[44:47], %[la], %[X], s65 offen offset:3072 sc1\n" \
  "buffer_load_dwordx4 a[48:51], v183, %[X], s65 offen offset:0 sc1\n" \
  "buffer_load_dwordx4 a[52:55], v183, %[X], s65 offen offset:1024 sc1\n" \
  "buffer_load_dwordx4 a[56:59], v183, %[X], s65 offen offset:2048 sc1\n" \
  "buffer_load_dwordx4 a[60:63], v183, %[X], s65 offen offset:3072 sc1\n" \
  "s_add_i32 s65, s65, 0x10000\n" \
  "v_mov_b32 v182, 0\n" \
  "s_branch .Lu_top\n" \
  ".Lu_sp:\n" \
  "s_cmp_lt_i32 s56, 0\n" \
  "s_cbranch_scc1 .Lu_sp_back\n" \
  "s_mov_b32 s63, 0\n" \
  ".Lu_spw:\n" \
  "buffer_load_dwordx4 v[128:131], %[la], %[X], s70 offen offset:0 sc1\n" \
  "buffer_load_dwordx4 v[132:135], %[la], %[X], s70 offen offset:1024 sc1\n" \
  "buffer_load_dwordx4 v[136:139], %[la], %[X], s71 offen offset:0 sc1\n" \
  "buffer_load_dwordx4 v[140:143], %[la], %[X], s71 offen offset:1024 sc1\n" \
  "buffer_load_dwordx4 v[40:43], %[la], %[X], s57 offen offset:2048 sc1\n" \
  "buffer_load_dwordx4 v[44:47], %[la], %[X], s57 offen offset:3072 sc1\n" \
  "buffer_load_dwordx4 v[48:51], v183, %[X], s57 offen offset:0 sc1\n" \
  "buffer_load_dwordx4 v[52:55], v183, %[X], s57 offen offset:1024 sc1\n" \
  "buffer_load_dwordx4 v[56:59], v183, %[X], s57 offen offset:2048 sc1\n" \
  "buffer_load_dwordx4 v[60:63], v183, %[X], s57 offen offset:3072 sc1\n" \
  "buffer_load_dwordx4 v[64:67], %[la], %[X], s58 offen offset:0 sc1\n" \
  "buffer_load_dwordx4 v[68:71], %[la], %[X], s58 offen offset:1024 sc1\n" \
  "buffer_load_dwordx4 v[72:75], %[la], %[X], s58 offen offset:2048 sc1\n" \
  "buffer_load_dwordx4 v[76:79], %[la], %[X], s58 offen offset:3072 sc1\n" \
  "buffer_load_dword v184, %[flo], %[X], 0 offen sc1\n" \
  "s_waitcnt vmcnt(0)\n" \
  "v_max3_u32 v180, v129, v130, v131\n" \
  "v_max3_u32 v180, v180, v133, v134\n" \
  "v_max_u32 v180, v180, v135\n" \
  "v_and_b32 v180, %[w40], v180\n" \
  "v_max3_u32 v180, v180, v128, v132\n" \
  "v_max3_u32 v181, v137, v138, v139\n" \
  "v_max3_u32 v181, v181, v141, v142\n" \
  "v_max_u32 v181, v181, v143\n" \
  "v_and_b32 v181, %[w41], v181\n" \
  "v_max3_u32 v181, v181, v136, v140\n" \
  "v_bitop3_b32 v176, v40, v41, s60 bitop3:0x7e\n" \
  "v_bitop3_b32 v177, v42, v43, s60 bitop3:0x7e\n" \
  "v_bitop3_b32 v178, v44, v45, s60 bitop3:0x7e\n" \
  "v_or3_b32 v176, v176, v177, v178\n" \
  "v_bitop3_b32 v177, v46, v47, s60 bitop3:0x7e\n" \
  "v_bitop3_b32 v178, v48, v52, s60 bitop3:0x7e\n" \
  "v_or3_b32 v176, v176, v177, v178\n" \
  "v_bitop3_b32 v177, v56, v60, s60 bitop3:0x7e\n" \
  "v_bitop3_b32 v178, v64, v68, s60 bitop3:0x7e\n" \
  "v_or3_b32 v176, v176, v177, v178\n" \
  "v_bitop3_b32 v176, v176, v72, s60 bitop3:0xf6\n" \
  "v_bitop3_b32 v176, v176, v76, s60 bitop3:0xf6\n" \
  "v_subrev_u32 v184, s61, v184\n" \
  "v_or_b32 v176, v176, v184\n" \
  "v_ashrrev_i32 v176, 31, v176\n" \
  "v_max3_u32 v180, v180, v181, v176\n" \
  "v_cmp_eq_u32 vcc, -1, v180\n" \
  "s_cbranch_vccz .Lu_spw_ok\n" \
  "s_sleep 2\n" \
  "s_add_i32 s63, s63, 1\n" \
  "s_and_b32 s77, s63, 1023\n" \
  "s_cbranch_scc1 .Lu_spw\n" \
  "buffer_load_dword v181, off, %[F], 0 offset:1024 sc1\n" \
  "s_waitcnt vmcnt(0)\n" \
  "v_readfirstlane_b32 s77, v181\n" \
  "s_cmp_lg_u32 s77, 0\n" \
  "s_cbranch_scc1 .Lu_spw_ok\n" \
  "s_cmp_lt_u32 s63, 400000\n" \
  "s_cbranch_scc1 .Lu_spw\n" \
  "v_mov_b32 v181, 1\n" \
  "buffer_store_dword v181, off, %[F], 0 offset:1024 sc1\n" \
  ".Lu_spw_ok:\n" \
  "buffer_load_dwordx4 v[32:35], %[la], %[X], s57 offen offset:0 sc1\n" \
  "buffer_load_dwordx4 v[36:39], %[la], %[X], s57 offen offset:1024 sc1\n" \
  "buffer_load_dwordx4 v[80:83], v183, %[X], s58 offen offset:0 sc1\n" \
  "buffer_load_dwordx4 v[84:87], v183, %[X], s58 offen offset:1024 sc1\n" \
  "buffer_load_dwordx4 v[88:91], v183, %[X], s58 offen offset:2048 sc1\n" \
  "buffer_load_dwordx4 v[92:95], v183, %[X], s58 offen offset:3072 sc1\n" \
  "v_mfma_f32_16x16x32_bf16 v[148:151], v[128:131], v[40:43], 0\n" \
  "v_mfma_f32_16x16x32_bf16 v[156:159], v[136:139], v[40:43], 0\n" \
  "v_mfma_f32_16x16x32_bf16 v[148:151], v[132:135], v[44:47], v[148:151]\n" \
  "v_mfma_f32_16x16x32_bf16 v[156:159], v[140:143], v[44:47], v[156:159]\n" \
  "v_mfma_f32_16x16x32_bf16 v[148:151], a[0:3], v[48:51], v[148:151]\n" \
  "v_mfma_f32_16x16x32_bf16 v[156:159], a[32:35], v[48:51], v[156:159]\n" \
  "v_mfma_f32_16x16x32_bf16 v[148:151], a[4:7], v[52:55], v[148:151]\n" \
  "v_mfma_f32_16x16x32_bf16 v[156:159], a[36:39], v[52:55], v[156:159]\n" \
  "v_mfma_f32_16x16x32_bf16 v[148:151], a[8:11], v[56:59], v[148:151]\n" \
  "v_mfma_f32_16x16x32_bf16 v[156:159], a[40:43], v[56:59], v[156:159]\n" \
  "v_mfma_f32_16x16x32_bf16 v[148:151], a[12:15], v[60:63], v[148:151]\n" \
  "v_mfma_f32_16x16x32_bf16 v[156:159], a[44:47], v[60:63], v[156:159]\n" \
  "v_mfma_f32_16x16x32_bf16 v[148:151], a[16:19], v[64:67], v[148:151]\n" \
  "v_mfma_f32_16x16x32_bf16 v[156:159], a[48:51], v[64:67], v[156:159]\n" \
  "v_mfma_f32_16x16x32_bf16 v[148:151], a[20:23], v[68:71], v[148:151]\n" \
  "v_mfma_f32_16x16x32_bf16 v[156:159], a[52:55], v[68:71], v[156:159]\n" \
  "v_mfma_f32_16x16x32_bf16 v[148:151], a[24:27], v[72:75], v[148:151]\n" \
  "v_mfma_f32_16x16x32_bf16 v[156:159], a[56:59], v[72:75], v[156:159]\n" \
  "v_mfma_f32_16x16x32_bf16 v[148:151], a[28:31], v[76:79], v[148:151]\n" \
  "v_mfma_f32_16x16x32_bf16 v[156:159], a[60:63], v[76:79], v[156:159]\n" \
  "s_waitcnt vmcnt(0)\n" \
  "s_branch .Lu_sp_back\n" \
  ".Lu_sr:\n" \
  "s_cmp_lt_i32 s56, 0\n" \
  "s_cbranch_scc1 .Lu_sr_back\n" \
  "s_mov_b32 s63, 0\n" \
  ".Lu_srw:\n" \
  "buffer_load_dwordx4 v[32:35], %[la], %[X], s57 offen offset:0 sc1\n" \
  "buffer_load_dwordx4 v[36:39], %[la], %[X], s57 offen offset:1024 sc1\n" \
  "buffer_load_dwordx4 v[80:83], v183, %[X], s58 offen offset:0 sc1\n" \
  "buffer_load_dwordx4 v[84:87], v183, %[X], s58 offen offset:1024 sc1\n" \
  "buffer_load_dwordx4 v[88:91], v183, %[X], s58 offen offset:2048 sc1\n" \
  "buffer_load_dwordx4 v[92:95], v183, %[X], s58 offen offset:3072 sc1\n" \
  "s_waitcnt vmcnt(0)\n" \
  "v_bitop3_b32 v176, v32, v33, s60 bitop3:0x7e\n" \
  "v_bitop3_b32 v177, v34, v35, s60 bitop3:0x7e\n" \
  "v_bitop3_b32 v178, v36, v37, s60 bitop3:0x7e\n" \
  "v_or3_b32 v176, v176, v177, v178\n" \
  "v_bitop3_b32 v177, v38, v39, s60 bitop3:0x7e\n" \
  "v_bitop3_b32 v178, v80, v82, s60 bitop3:0x7e\n" \
  "v_or3_b32 v176, v176, v177, v178\n" \
  "v_bitop3_b32 v177, v84, v86, s60 bitop3:0x7e\n" \
  "v_bitop3_b32 v178, v88, v90, s60 bitop3:0x7e\n" \
  "v_or3_b32 v176, v176, v177, v178\n" \
  "v_bitop3_b32 v176, v176, v92, s60 bitop3:0xf6\n" \
  "v_bitop3_b32 v176, v176, v94, s60 bitop3:0xf6\n" \
  "v_cmp_gt_i32 vcc, 0, v176\n" \
  "s_cbranch_vccz .Lu_srw_ok\n" \
  "s_sleep 2\n" \
  "s_add_i32 s63, s63, 1\n" \
  "s_and_b32 s77, s63, 1023\n" \
  "s_cbranch_scc1 .Lu_srw\n" \
  "buffer_load_dword v181, off, %[F], 0 offset:1024 sc1\n" \
  "s_waitcnt vmcnt(0)\n" \
  "v_readfirstlane_b32 s77, v181\n" \
  "s_cmp_lg_u32 s77, 0\n" \
  "s_cbranch_scc1 .Lu_srw_ok\n" \
  "s_cmp_lt_u32 s63, 400000\n" \
  "s_cbranch_scc1 .Lu_srw\n" \
  "v_mov_b32 v181, 1\n" \
  "buffer_store_dword v181, off, %[F], 0 offset:1024 sc1\n" \
  ".Lu_srw_ok:\n" \
  "v_mfma_f32_16x16x32_bf16 v[144:147], v[128:131], v[32:35], 0\n" \
  "v_mfma_f32_16x16x32_bf16 v[152:155], v[136:139], v[32:35], 0\n" \
  "v_mfma_f32_16x16x32_bf16 v[144:147], v[132:135], v[36:39], v[144:147]\n" \
  "v_mfma_f32_16x16x32_bf16 v[152:155], v[140:143], v[36:39], v[152:155]\n" \
  "v_mfma_f32_16x16x32_bf16 v[144:147], v[96:99], v[80:83], v[144:147]\n" \
  "v_mfma_f32_16x16x32_bf16 v[152:155], v[112:115], v[80:83], v[152:155]\n" \
  "v_mfma_f32_16x16x32_bf16 v[144:147], v[100:103], v[84:87], v[144:147]\n" \
  "v_mfma_f32_16x16x32_bf16 v[152:155], v[116:119], v[84:87], v[152:155]\n" \
  "v_mfma_f32_16x16x32_bf16 v[144:147], v[104:107], v[88:91], v[144:147]\n" \
  "v_mfma_f32_16x16x32_bf16 v[152:155], v[120:123], v[88:91], v[152:155]\n" \
  "v_mfma_f32_16x16x32_bf16 v[144:147], v[108:111], v[92:95], v[144:147]\n" \
  "v_mfma_f32_16x16x32_bf16 v[152:155], v[124:127], v[92:95], v[152:155]\n" \
  "s_nop 7\n" \
  "s_branch .Lu_sr_back\n" \
  ".Lub_late:\n" \
  "s_cmp_lt_i32 s56, 0\n" \
  "s_cbranch_scc1 .Lub_al_ok\n" \
  "s_cmp_gt_u32 s91, -3000\n" \
  "s_cbranch_scc0 .Lub_late2\n" \
  ".Lub_wt:\n" \
  "s_memrealtime s[76:77]\n" \
  "s_waitcnt lgkmcnt(0)\n" \
  "s_sub_i32 s76, s76, s64\n" \
  "s_cmp_lt_i32 s76, -8\n" \
  "s_cbranch_scc1 .Lub_wt\n" \
  "s_min_i32 s75, s76, -2\n" \
  "s_lshl_b32 s75, s75, 2\n" \
  "s_getpc_b64 s[76:77]\n" \
  "s_add_i32 s75, s75, 48\n" \
  "s_add_u32 s76, s76, s75\n" \
  "s_addc_u32 s77, s77, 0\n" \
  "s_setpc_b64 s[76:77]\n" \
  "s_nop 4\n" \
  "s_nop 4\n" \
  "s_nop 4\n" \
  "s_nop 4\n" \
  "s_nop 4\n" \
  "s_nop 4\n" \
  "s_branch .Lub_al_ok\n" \
  ".Lub_late2:\n" \
  "s_cmp_lg_u32 s64, 0\n" \
  "s_cbranch_scc1 .Lub_late3\n" \
  "s_mov_b32 s63, 0\n" \
  ".Lub_tw:\n" \
  "buffer_load_dword v180, off, %[X], s94 offset:4 sc1\n" \
  "s_waitcnt vmcnt(0)\n" \
  "v_readfirstlane_b32 s64, v180\n" \
  "s_cmp_lg_u32 s64, 0\n" \
  "s_cbranch_scc1 .Lub_tw_ok\n" \
  "s_sleep 1\n" \
  "s_add_i32 s63, s63, 1\n" \
  "s_cmp_lt_u32 s63, 200\n" \
  "s_cbranch_scc1 .Lub_tw\n" \
  ".Lub_tw_ok:\n" \
  "s_sub_i32 s91, s74, s64\n" \
  "s_cmp_gt_u32 s91, -3000\n" \
  "s_cbranch_scc1 .Lub_wt\n" \
  ".Lub_late3:\n" \
  "s_branch .Lub_al_ok\n" \
  ".Lu_end:\n" \
  "s_waitcnt vmcnt(0)\n"
#define U_CLOB "v32", "v33", "v34", "v35", "v36", "v37", "v38", "v39", "v40", "v41", "v42", "v43", "v44", "v45", "v46", "v47", "v48", "v49", "v50", "v51", "v52", "v53", "v54", "v55", "v56", "v57", "v58", "v59", "v60", "v61", "v62", "v63", "v64", "v65", "v66", "v67", "v68", "v69", "v70", "v71", "v72", "v73", "v74", "v75", "v76", "v77", "v78", "v79", "v80", "v81", "v82", "v83", "v84", "v85", "v86", "v87", "v88", "v89", "v90", "v91", "v92", "v93", "v94", "v95", "v96", "v97", "v98", "v99", "v100", "v101", "v102", "v103", "v104", "v105", "v106", "v107", "v108", "v109", "v110", "v111", "v112", "v113", "v114", "v115", "v116", "v117", "v118", "v119", "v120", "v121", "v122", "v123", "v124", "v125", "v126", "v127", "v128", "v129", "v130", "v131", "v132", "v133", "v134", "v135", "v136", "v137", "v138", "v139", "v140", "v141", "v142", "v143", "v144", "v145", "v146", "v147", "v148", "v149", "v150", "v151", "v152", "v153", "v154", "v155", "v156", "v157", "v158", "v159", "v160", "v161", "v162", "v163", "v164", "v165", "v166", "v167", "v168", "v169", "v170", "v171", "v172", "v173", "v174", "v175", "v176", "v177", "v178", "v179", "v180", "v181", "v182", "v183", "v184", "v185", "v186", "v187", "v188", "v189", "v190", "v191", "v192", "v193", "v194", "v195", "v196", "v197", "v198", "v199", "v200", "v201", "v202", "v203", "v204", "v205", "v206", "v207", "v208", "v209", "v210", "v211", "v212", "v213", "v214", "v215", "v216", "v217", "v218", "v219", "v220", "v221", "v222", "v223", "v224", "v225", "v226", "v227", "v228", "v229", "v230", "v231", "v232", "v233", "v234", "v235", "v236", "v237", "v238", "v239", "v240", "v241", "v242", "v243", "v244", "v245", "v246", "v247", "v248", "v249", "v250", "v251", "v252", "v253", "v254", "v255", "s56", "s57", "s58", "s59", "s60", "s61", "s62", "s63", "s64", "s65", "s66", "s67", "s68", "s69", "s70", "s71", "s72", "s73", "s74", "s75", "s76", "s77", "s78", "s79", "s80", "s81", "s82", "s83", "s84", "s85", "s86", "s87", "s88", "s89", "s90", "s91", "s92", "s93", "s94", "a0", "a1", "a2", "a3", "a4", "a5", "a6", "a7", "a8", "a9", "a10", "a11", "a12", "a13", "a14", "a15", "a16", "a17", "a18", "a19", "a20", "a21", "a22", "a23", "a24", "a25", "a26", "a27", "a28", "a29", "a30", "a31", "a32", "a33", "a34", "a35", "a36", "a37", "a38", "a39", "a40", "a41", "a42", "a43", "a44", "a45", "a46", "a47", "a48", "a49", "a50", "a51", "a52", "a53", "a54", "a55", "a56", "a57", "a58", "a59", "a60", "a61", "a62", "a63", "a64", "a65", "a66", "a67", "a68", "a69", "a70", "a71", "a72", "a73", "a74", "a75", "a76", "a77", "a78", "a79", "a80", "a81", "a82", "a83", "a84", "a85", "a86", "a87", "a88", "a89", "a90", "a91", "a92", "a93", "a94", "a95", "a96", "a97", "a98", "a99", "a100", "a101", "a102", "a103", "a104", "a105", "a106", "a107", "a108", "a109", "a110", "a111", "a112", "a113", "a114", "a115", "a116", "a117", "a118", "a119", "a120", "a121", "a122", "a123", "a124", "a125", "a126", "a127", "a128", "a129", "a130", "a131", "a132", "a133", "a134", "a135", "a136", "a137", "a138", "a139", "a140", "a141", "a142", "a143", "a144", "a145", "a146", "a147", "a148", "a149", "a150", "a151", "a152", "a153", "a154", "a155", "a156", "a157", "a158", "a159", "a160", "a161", "a162", "a163", "a164", "a165", "a166", "a167", "a168", "a169", "a170", "a171", "a172", "a173", "a174", "a175", "a176", "a177", "a178", "a179", "a180", "a181", "a182", "a183", "a184", "a185", "a186", "a187", "a188", "a189", "a190", "a191", "a192", "a193", "a194", "a195", "a196", "a197", "a198", "a199", "a200", "a201", "a202", "a203", "a204", "a205", "a206", "a207", "a208", "a209", "a210", "a211", "a212", "a213", "a214", "a215", "a216", "a217", "a218", "a219", "a220", "a221", "a222", "a223", "a224", "a225", "a226", "a227", "a228", "a229", "a230", "a231", "a232", "a233", "a234", "a235", "a236", "a237", "a238", "a239", "a240", "a241", "a242", "a243", "a244", "a245", "a246", "a247", "a248", "a249", "a250", "a251", "a252", "a253", "a254", "a255", "vcc", "scc", "m0", "memory"
#define TAU 236
#define SH1 40
#define MRG 2
#define XST 150
#define G1T 100
#define VTC -8
#define UC2 5
#define OC1 3
#define D1 10
#define D2 8
#define VL 7
#define OD1 110
#define LAG 355
#define WST(A, B, nb) [WA] "s"(mkd(A, (DM * HD - h * DH) * 2u)), [WB] "s"(mkd(B, nb)), [wl] "s"(US(w * 4096)), \
  [wv] "v"((unsigned)((16 * w + g) * 8192 + (fr ^ (2 * g)) * 16)), [wr] "v"((unsigned)((8 * g + (fr >> 2)) * 256 + ((4 * w + ((fr & 3) >> 1)) ^ (2 * (fr >> 2) + 8 * (g & 1))) * 16 + (fr & 1) * 8))
#define WST2 WST(p.Wv + h * DH, p.Wo + (size_t)h * (DH * DM), DH * DM * 2u), [wr2] "v"((unsigned)((4 * g + (fr >> 2)) * 1152 + 288 * w + 8 * (fr & 3)))
#define SCHED [g1t] "s"((15)), [bas] "s"(US(3u * gb + BAS_CH * CH + 384u)), [rdy] "v"(tid == 0 ? 0u : DROP)
__device__ __forceinline__ unsigned qrow(int r) { return (unsigned)(L_QI + (r >> 4) * 2816 + (r & 15) * 160 + ((r >> 2) & 3) * 8); }
__device__ __forceinline__ void qk_wg(const MP& p, int role, int grp, int h, int rep, unsigned gb) {
  const int tid = threadIdx.x, lane = tid & 63, w = __builtin_amdgcn_readfirstlane(tid >> 6);
  const int fr = lane & 15, g = lane >> 4;
  const int jj = (h >> 1) & 3, hh = h & 1;
  const unsigned pb = w < NPR ? (unsigned)(16 * (w >> 1) + 4 * (w & 1) + (h >> 3)) * CH + (unsigned)(((h >> 3) & 1) * 1024) : DROP;
  const unsigned ptb = (unsigned)(PT_CH - PM_CH) * CH + (unsigned)(h >> 3) * 2u * CH + 8192u - 2u * gb;
  const int qc = 8 * (w & 1) + (fr & 7), T = w >> 1;
  const unsigned po1 = fr < 8 ? pb + (unsigned)(jj * 1024 + (g * 16 + hh * 8 + fr) * 16) : T ? ptb + phys((unsigned)((4 + (h & 7)) * 1024 + (g * 16 + qc) * 16)) : DROP;
  const unsigned po2 = fr < 8 ? pb + (unsigned)(4096 + (jj * 16 + hh * 8 + fr) * 16 + g * 4) : ptb + (unsigned)((T * 2 + hh) * 1024 + (jj * 16 + qc) * 16 + g * 4);
  const unsigned po3 = fr < 8 || T ? DROP : ptb + phys((unsigned)((12 + (jj & 2) + hh) * 1024 + (g * 16 + qc) * 16 + (jj & 1) * 8));
  unsigned hA = (unsigned)(65536 + (fr >> 3) * RGB + g * 128 + (lane & 7) * 16);
  asm volatile(QK_ASM
    : [hA] "+v"(hA)
    : [X] "s"(mkd(p.win, WIN_BYTES)), [HS] "s"(mkd(p.H + (size_t)grp * (NSEG * SEGB / 2), NSEG * SEGB)), [F] "s"(mkd(p.ct, CT_TOTAL * 4u)),
      [mr0] "s"(US(gb + (unsigned)(MEM_CH + 2 * rep) * CH)), [pm0] "s"(US(gb + PM_CH * CH)), [hl0] "s"(US((unsigned)w * RGB)), WST(p.Wq + h * DH, p.Wk + h * DH, (DM * HD - h * DH) * 2u), [grp] "s"(US(grp)),
      SCHED, [rpo] "v"(tid < 2 ? gb + CTL_CH * CH + (unsigned)(5120 + 256 * rep + 4 * tid) : DROP),
      [mo] "v"((unsigned)lane * 16u), [idx] "v"((lane & 2) ? 0xEEEEu : 0x4444u),
      [qW] "v"(qrow(32 * w + fr) + 4 * g), [kW] "v"(qrow(32 * w + fr) + 128 + 8 * g),
      [qR] "v"(qrow(4 * g + (fr >> 2)) + ((fr & 2) ? 16 + 32 * w : 0) + 8 * (fr & 1)), [kR] "v"(qrow(4 * g + (fr >> 2)) + 128 + 8 * (fr & 3)),
      [qtW] "v"(qrow(32 * w + fr) + 16 + 32 * (g >> 1) + 8 * (g & 1)), [ktW] "v"(L_KT + (unsigned)(w * 1024 + lane * 16)), [ktR] "v"(L_KT + (unsigned)lane * 16u),
      [hvo] "v"((unsigned)w * RGB + (unsigned)lane * 16u), [po1] "v"(po1), [po2] "v"(po2), [po3] "v"(po3), [pcl] "v"(fr == 0 ? po1 : lane == 1 ? po2 : DROP),
      [lim] "v"(fr >= 8 ? 16 * T + qc - 4 * g : 1000)
    : QK_CLOB);
}

__device__ __forceinline__ void vt_wg(const MP& p, int role, int grp, int h, int rep, unsigned gb) {
  const int tid = threadIdx.x, lane = tid & 63, w = __builtin_amdgcn_readfirstlane(tid >> 6);
  const int fr = lane & 15, g = lane >> 4;
  unsigned hA = (unsigned)(65536 + (fr >> 3) * RGB + g * 128 + (lane & 7) * 16);
  asm volatile(VT_ASM
    : [hA] "+v"(hA)
    : [X] "s"(mkd(p.win, WIN_BYTES)), [HS] "s"(mkd(p.H + (size_t)grp * (NSEG * SEGB / 2), NSEG * SEGB)),
      [F] "s"(mkd(p.ct, CT_TOTAL * 4u)), [hl0] "s"(US((unsigned)w * RGB)), WST2, [grp] "s"(US(grp)),
      SCHED, [vtc] "s"((-14)), [rpo] "v"(tid == 0 ? gb + CTL_CH * CH + (unsigned)(6144 + 256 * rep) : DROP),
      [la] "v"((unsigned)lane * 16u), [vW] "v"(L_VI + (unsigned)(w * 1024 + lane * 16)), [vR] "v"(L_VI + (unsigned)lane * 16u), [hvo] "v"((unsigned)w * RGB + (unsigned)lane * 16u),
      [px] "s"(US(4u * CH + (w == 3 ? 8192u - 2u * gb : 0u))), [sto] "v"(gb + (unsigned)(VT_CH + 36 * w + (h >> 3)) * CH + (unsigned)((h & 7) * 1024 + lane * 16)), [flo] "v"(tid == 0 ? gb + CTL_CH * CH + (unsigned)(7168 + (h >> 3) * 128 + (h & 7) * 4) : DROP)
    : VT_CLOB);
}

__device__ __forceinline__ int o_head(int j) { return 16 * (j / 18) + ((j % 18) & 15); }
__device__ __forceinline__ void o_wg(const MP& p, int role, int grp, int j, int rep, unsigned gb) {
  const int tid = threadIdx.x, lane = tid & 63, w = __builtin_amdgcn_readfirstlane(tid >> 6);
  const int fr = lane & 15, g = lane >> 4;
  const int wsl = (fr * 4 + (g ^ ((fr >> 1) & 3))) * 16;
  const int mr = 2 * g + ((lane >> 2) & 1);
  const int h = o_head(j);
  const unsigned vb = j % 18 < 16 ? (gb ^ 8192u) + (unsigned)(VM_CH + 9 * w) * CH + (unsigned)((h >> 3) * 2048 + (h & 1) * 1024 + ((h >> 1) & 3) * 256 + fr * 16) : DROP;
  unsigned la = (unsigned)lane * 16u;
  asm volatile(O_ASM
    : [la] "+v"(la)
    : [X] "s"(mkd(p.win, WIN_BYTES)),
      [F] "s"(mkd(p.ct, CT_TOTAL * 4u)), [mw0] "s"(US(gb + (unsigned)(MEM_CH + 2 * w) * CH)), [mr0] "s"(US(gb + (unsigned)(MEM_CH + 2 * rep) * CH)), [ufo] "s"(US(gb + CTL_CH * CH + (unsigned)((j >> 1) * 128))),
      [pm0] "s"(US(gb + (unsigned)(PM_CH + (NPR == 4 ? 16 * (rep >> 1) + 4 * (rep & 1) : NPR == 2 ? 4 * (rep >> 1) : 0) + w) * CH + (unsigned)(w & 1) * 1024u)), [vm0] "s"(US((gb ^ 8192u) + (unsigned)(VM_CH + j) * CH + (unsigned)w * 2048u)), [vt0] "s"(US((gb ^ (j < 32 ? 0u : 8192u)) + (unsigned)(VT_CH + 4 * j + w) * CH)),
      [ml0] "s"(US(LO_MEM + (unsigned)w * 1024u)), [hl0] "s"(US((unsigned)w * RGB)), [hvo] "v"((unsigned)w * RGB + (unsigned)lane * 16u), WST2, [grp] "s"(US(grp)),
      SCHED, [tau] "s"((212)), [x0] "s"((215)), [first] "s"(US((j ^ 16) + w)), [cpw] "v"(tid < 16 ? (unsigned)tid * 8u : DROP),
      [mrg] "s"((1)), [lag] "s"((320)), [dn] "s"(-(4)), [up] "s"((8)), [sh1] "s"((0)), [gg] "s"(US(grp * 130)), [d1] "s"((10)), [d2] "s"((6)), [vl] "s"((7)),
      [rpo] "v"(tid == 2 || (tid == 1 && j % 18 < 16) ? gb + CTL_CH * CH + (unsigned)(6144 + 256 * rep + 4 * tid) : DROP),
      [cpo] "v"(j == 16 && !w ? gb + CTL_CH * CH + (unsigned)(5120 + 256 * (lane & 7)) : DROP), [cpa] "v"(j == 16 && !w && grp ? 4u * 128u : DROP),
      [oc1] "s"((3)), [od1] "s"((110)), [w4] "s"(US(j % 9 == 8 ? -1 : 0)),
      [idx] "v"((lane & 2) ? 0xEEEEu : 0x4444u), [mA] "v"(LO_MEM + (unsigned)lane * 16u), [mA8] "v"(LO_MEM + (unsigned)(8192 + g * 1024 + fr * 16)),
      [c0] "v"((unsigned)(w * 1024 + lane * 16)), [c2] "v"(lane < 16 ? (unsigned)(w * 256 + lane * 16) : DROP),
      [aW] "v"(LO_A + (unsigned)((w * 16 + (fr >> 2) * 4 + (fr & 1)) * 32 + g * 8 + (fr & 2) * 2)), [aR] "v"(LO_A + (unsigned)((g * 8 + (fr >> 2)) * 32 + (fr & 3) * 8)),
      [vmo] "v"(vb + (unsigned)g * CH), [vmo1] "v"(vb + 4u * CH + (unsigned)g * 4u), [flo] "v"(gb + CTL_CH * CH + (unsigned)(7168 + w * 128 + (lane & 7) * 4)),
      [rmW] "v"(LO_RM + (unsigned)(w * 1024 + wsl)), [rmA] "v"(LO_RM + (unsigned)((mr * 4 + ((lane & 3) ^ ((mr >> 1) & 3))) * 16 + (lane & 8))),
      [lm] "v"(phys((unsigned)(j * 256 + lane * 4)))
    : O_CLOB);
}

__device__ __forceinline__ void u_wg(const MP& p, int role, int grp, int u, unsigned gb) {
  const int tid = threadIdx.x, lane = tid & 63, w = __builtin_amdgcn_readfirstlane(tid >> 6);
  const int fr = lane & 15, g = lane >> 4, tr = (lane >> 2) & 7;
  unsigned la = (unsigned)lane * 16u;
  asm volatile(U_ASM
    : [la] "+v"(la)
    : [X] "s"(mkd(p.win, WIN_BYTES)), [OS] "s"(mkd(p.OT + (size_t)grp * (NSEG * SEGB / 2), NSEG * SEGB)), [F] "s"(mkd(p.ct, CT_TOTAL * 4u)),
      [pt0] "s"(US((gb ^ 8192u) + (unsigned)(PT_CH + 2 * w) * CH)), [vm0] "s"(US((gb ^ 8192u) + (unsigned)(VM_CH + 2 * u) * CH + (unsigned)w * 2048u)),
      [vt0] "s"(US((gb ^ (u < 16 ? 0u : 8192u)) + (unsigned)(VT_CH + 8 * u + w) * CH)),
      [ow] "s"(US((unsigned)w * RGB)), [grp] "s"(US(grp)), SCHED, [w40] "s"(US(2 * u % 9 == 8 ? -1 : 0)), [w41] "s"(US((2 * u + 1) % 9 == 8 ? -1 : 0)),
      [uc2] "s"((5)),
      [flo] "v"(gb + CTL_CH * CH + (unsigned)(7168 + w * 128 + (lane & 7) * 4)), [ufo] "v"(tid == 0 ? gb + CTL_CH * CH + (unsigned)(u * 128) : DROP),
      [rtW] "v"((unsigned)(w * 2048 + (fr * 4 + (g ^ ((fr >> 1) & 3))) * 16)),
      [rtA] "v"((unsigned)((w >> 1) * 1024 + ((8 * (w & 1) + tr) * 4 + ((2 * (lane >> 5) + ((lane >> 1) & 1)) ^ ((tr >> 1) & 3))) * 16 + (lane & 1) * 8)),
      [lo] "v"((unsigned)(u * 512 + lane * 4))
    : U_CLOB);
}

#define YSLP 2
struct Row9 { unsigned u[9]; };
__device__ __forceinline__ float rnd(float a) { return __uint_as_float(pk2(0.f, a)); }
__device__ __forceinline__ void ln18(const f32x2 (&x)[9], f32x2 (&o)[9], float* st, int lane, int w) {
  f32x2 s2 = x[0];
#pragma unroll
  for (int i = 1; i < 9; ++i) s2 += x[i];
  const float mw = i_red8add(s2[0] + s2[1]) * (1.0f / 144.0f);
  f32x2 q2 = {0.f, 0.f};
#pragma unroll
  for (int i = 0; i < 9; ++i) { const f32x2 d = x[i] - mw; q2 += d * d; }
  const float qw = i_red8add(q2[0] + q2[1]);
  if (lane < 8) *(f32x2*)(st + (lane * 4 + w) * 2) = (f32x2){mw, qw};
  BAR();
  const f32x4 a = *(const f32x4*)(st + (lane & 7) * 8), b = *(const f32x4*)(st + (lane & 7) * 8 + 4);
  const float mean = ((a[0] + a[2]) + (b[0] + b[2])) * 0.25f;
  const float d0 = a[0] - mean, d1 = a[2] - mean, d2 = b[0] - mean, d3 = b[2] - mean;
  const float m2 = ((a[1] + a[3]) + (b[1] + b[3])) + 144.0f * ((d0 * d0 + d1 * d1) + (d2 * d2 + d3 * d3));
  const float var = rnd(m2 * (1.0f / DM));
  const float m = rnd(mean), rden = __builtin_amdgcn_rcpf(rnd(__builtin_amdgcn_sqrtf(rnd(var + 1e-5f))));
#pragma unroll
  for (int i = 0; i < 9; ++i) { const f32x2 d = x[i] - m; o[i] = (f32x2){rnd(d[0]), rnd(d[1])} * rden; }
}
__device__ __forceinline__ void mix_rows(Row9& xa, const Row9& ya, const rsrc_t H, unsigned so, unsigned o0, unsigned oe, float* st, int lane, int w) {
  f32x2 x[9], y[9], ln[9];
#pragma unroll
  for (int i = 0; i < 9; ++i) { x[i] = (f32x2){lo2f(xa.u[i]), hi2f(xa.u[i])}; y[i] = (f32x2){lo2f(ya.u[i]), hi2f(ya.u[i])}; }
  ln18(x, ln, st, lane, w);
#pragma unroll
  for (int i = 0; i < 9; ++i) {
    const f32x2 m = (f32x2){rnd(ln[i][0]), rnd(ln[i][1])} * y[i];
    const f32x2 u = x[i] + (f32x2){rnd(m[0]), rnd(m[1])};
    x[i] = (f32x2){rnd(u[0]), rnd(u[1])};
  }
  ln18(x, ln, st + 64, lane, w);
  unsigned h[9];
#pragma unroll
  for (int i = 0; i < 9; ++i) h[i] = nosent(pk2(ln[i][0], ln[i][1]));
  st16(H, o0, so, (v4u){h[0], h[1], h[2], h[3]});
  st16(H, o0 + 4096u, so, (v4u){h[4], h[5], h[6], h[7]});
  st4(H, oe, so, h[8]);
#pragma unroll
  for (int i = 0; i < 9; ++i) xa.u[i] = pk2(x[i][0], x[i][1]);
}
template <int SLP>
__device__ __forceinline__ void wait9(const rsrc_t O, const rsrc_t F, unsigned so, unsigned o0, unsigned o1, unsigned oe, v4u (&v)[2], unsigned& e) {
  unsigned spins = 0;
#pragma nounroll
  while (true) {
    asm volatile("" ::: "memory");
    v[0] = ld16(O, o0, so); v[1] = ld16(O, o1, so); e = ld4(O, oe, so);
    if (__all(mx4(mx4(e, v[0]), v[1]) != SENT)) break;
    if (spin_fail(spins, F)) break;
    __builtin_amdgcn_s_sleep(SLP);
  }
}
__device__ __forceinline__ void add_rows(Row9& xa, const v4u (&v)[2], unsigned e) {
#pragma unroll
  for (int i = 0; i < 8; ++i) xa.u[i] = pk2(lo2f(xa.u[i]) + lo2f(v[i >> 2][i & 3]), hi2f(xa.u[i]) + hi2f(v[i >> 2][i & 3]));
  xa.u[8] = pk2(lo2f(xa.u[8]) + lo2f(e), hi2f(xa.u[8]) + hi2f(e));
}
template <int SLP>
__device__ __forceinline__ void arm_slots(const MP& p, int s, int rg) {
  const rsrc_t O = mkr(p.OT, 2u * NSEG * SEGB), H = mkr(p.H, 2u * NSEG * SEGB);
  const unsigned so = __builtin_amdgcn_readfirstlane((unsigned)s * SEGB + (unsigned)rg * RGB), o0 = threadIdx.x * 16u;
  const v4u sent = {SENT, SENT, SENT, SENT};
#pragma unroll
  for (int i = 0; i < 3; ++i) {
    const unsigned o = i < 2 || threadIdx.x < 64 ? o0 + 4096u * i : DROP;
    st16(H, o, s + 5 < NSEG ? so + 5u * SEGB : DROP, sent);
    if (SLP && i < 2) __builtin_amdgcn_s_sleep(SLP);
    st16(O, o, so, sent);
    if (SLP && i < 2) __builtin_amdgcn_s_sleep(SLP);
    st16(O, o, NSEG * SEGB + so, sent);
    if (SLP && i < 2) __builtin_amdgcn_s_sleep(SLP);
    st16(H, o, s + 2 < NSEG ? (NSEG + 2) * SEGB + so : DROP, sent);
    if (SLP && i < 2) __builtin_amdgcn_s_sleep(SLP);
  }
}
#define ASLP 6
#define LR_Y 1024
#define LR_SV 10240
#define RSLP 4
__device__ __forceinline__ void row_wg(const MP& p, char* smem, int role, int rw, unsigned flip) {
  const int tid = threadIdx.x, lane = tid & 63, w = __builtin_amdgcn_readfirstlane(tid >> 6);
  const rsrc_t F = mkr(p.ct, CT_TOTAL * 4u), O = mkr(p.OT, 2u * NSEG * SEGB), H = mkr(p.H, 2u * NSEG * SEGB), Y = mkr(p.Y, DM * YS), X = mkr(p.win, WIN_BYTES);
  float* st = (float*)smem;
  const int r8 = tid & 7, pj = (tid >> 3) & 7;
  const unsigned o0 = (unsigned)tid * 16u;
  const unsigned oe = (unsigned)(8192 + pj * 128 + r8 * 16 + w * 4);
  const int rg = rw & 3, s0 = rw >> 2;
  const char* yR = smem + LR_Y + (8 * ((tid >> 4) * 2 + ((lane >> 1) & 1)) + ((lane & 15) >> 2)) * 16 + (lane & 1) * 8;
  const char* yE = smem + LR_Y + (512 + 8 * pj + 2 * w) * 16 + r8 * 2;
  Row9 xp;
#pragma unroll
  for (int i = 0; i < 9; ++i) xp.u[i] = 0u;
#pragma nounroll
  for (int job = rw < 12 ? -1 : 0; job < 30; ++job) {
    const bool isA = job <= 0 || (job & 1);
    const int k = job <= 0 ? 0 : (isA ? (job + 1) >> 1 : (job >> 1) - 1);
    const int s = s0 + 5 * k;
    if (s >= (isA ? NSEG : NSEG + 5)) continue;
    if (job == 1 && rw < 12) {
      unsigned pf = 0;
#pragma unroll
      for (int i = 0; i < 6; ++i) pf += *(const unsigned*)(p.x0 + (size_t)((rw * 6 + i) * 256 + tid) * 64);
      asm volatile("" :: "v"(pf));
    }

    const unsigned to = BAS_CH * CH + 384u + 128u * (unsigned)s + 4u;
    const int tc = (132);
    const unsigned so = __builtin_amdgcn_readfirstlane((unsigned)s * SEGB + (unsigned)rg * RGB);
    char* sv = smem + LR_SV + (k % 3) * 2 * RGB;
    const size_t go = (size_t)(s * SEG + rg * 8 + r8) * DM;
    Row9 xa, ya;
    v4u v[2];
    unsigned e;
    if (isA) {
      const v4u x0 = *(const v4u*)(p.x0 + go + (tid >> 3) * 8), x1 = *(const v4u*)(p.x0 + go + 256 + (tid >> 3) * 8);
      xa.u[8] = *(const unsigned*)(p.x0 + go + 512 + 8 * pj + 2 * w);
#pragma unroll
      for (int i = 0; i < 4; ++i) { xa.u[i] = x0[i]; xa.u[4 + i] = x1[i]; }
      if (job > 0) arm_slots<ASLP>(p, s, rg);
      if (job == 0) {
        unsigned spins = 0;
#pragma nounroll
        while (!__all(ld4(F, (unsigned)(CT_YC + (lane & 7) * 32) * 4u, 0) >= 72u)) { if (spin_fail(spins, F)) break; __builtin_amdgcn_s_sleep(4); asm volatile("" ::: "memory"); }
      }
      if (job >= 0) wait9<YSLP>(Y, F, (unsigned)(s * SEG + rg * 8) * 2u, (unsigned)tid * YS, (unsigned)(tid + 256) * YS, (unsigned)(512 + (tid >> 2)) * YS + (unsigned)(tid & 3) * 4u, v, e);
      else { v[0] = x0; v[1] = x1; e = xa.u[8]; }
      VM0();
      *(v4u*)(smem + LR_Y + o0) = v[0]; *(v4u*)(smem + LR_Y + o0 + 4096) = v[1]; *(unsigned*)(smem + LR_Y + 8192 + tid * 4) = e;
      BAR();
#pragma unroll
      for (int i = 0; i < 2; ++i) {
        const v2u x = trd(yR + 4096 * i), y = trd(yR + 4096 * i + 64);
        ya.u[4 * i] = x.x; ya.u[4 * i + 1] = x.y; ya.u[4 * i + 2] = y.x; ya.u[4 * i + 3] = y.y;
      }
      ya.u[8] = (unsigned)*(const u16*)yE | ((unsigned)*(const u16*)(yE + 16) << 16);
    } else if (s < NSEG) {
#pragma unroll
      for (int i = 0; i < 2; ++i) {
        const v4u a = *(const v4u*)(sv + o0 + 4096 * i), b = *(const v4u*)(sv + RGB + o0 + 4096 * i);
#pragma unroll
        for (int q = 0; q < 4; ++q) { xa.u[4 * i + q] = a[q]; ya.u[4 * i + q] = b[q]; }
      }
      xa.u[8] = *(const unsigned*)(sv + 8192 + tid * 4); ya.u[8] = *(const unsigned*)(sv + RGB + 8192 + tid * 4);
      wait_time(X, flip * 24576u + to, tc);
      wait9<RSLP>(O, F, so, o0, o0 + 4096u, oe, v, e);
      add_rows(xa, v, e);
    }
    if (s < NSEG) mix_rows(xa, ya, H, job < 0 ? 0x40000000u : (isA ? so : NSEG * SEGB + so), o0, oe, st, lane, w);
    if (isA) {
#pragma unroll
      for (int i = 0; i < 2; ++i) {
        *(v4u*)(sv + o0 + 4096 * i) = (v4u){xa.u[4 * i], xa.u[4 * i + 1], xa.u[4 * i + 2], xa.u[4 * i + 3]};
        *(v4u*)(sv + RGB + o0 + 4096 * i) = (v4u){ya.u[4 * i], ya.u[4 * i + 1], ya.u[4 * i + 2], ya.u[4 * i + 3]};
      }
      *(unsigned*)(sv + 8192 + tid * 4) = xa.u[8]; *(unsigned*)(sv + RGB + 8192 + tid * 4) = ya.u[8];
    } else {
      if (k > 0) {
        wait_time(X, (flip ^ 1u) * 24576u + to - 640u, tc);
        wait9<RSLP>(O, F, (NSEG - 5) * SEGB + so, o0, o0 + 4096u, oe, v, e);
        add_rows(xp, v, e);
        u16* po = p.out + go - 5 * SEG * DM;
        *(v4u*)(po + (tid >> 3) * 8) = (v4u){xp.u[0], xp.u[1], xp.u[2], xp.u[3]};
        *(v4u*)(po + 256 + (tid >> 3) * 8) = (v4u){xp.u[4], xp.u[5], xp.u[6], xp.u[7]};
        *(unsigned*)(po + 512 + 8 * pj + 2 * w) = xp.u[8];
      }
      xp = xa;
    }
  }
}

__device__ __forceinline__ unsigned rot16(unsigned u) { return __builtin_amdgcn_alignbit(u, u, 16); }
__device__ __forceinline__ void crev_put(u16* dst, int stride, const v4u& c0, const v4u& c1) {
  const unsigned e[8] = {rot16(c0.w), rot16(c0.z), rot16(c0.y), rot16(c0.x), rot16(c1.w), rot16(c1.z), rot16(c1.y), rot16(c1.x)};
  unsigned u[7];
#pragma unroll
  for (int k = 0; k < 7; ++k) u[k] = __builtin_amdgcn_alignbit(e[k + 1], e[k], 16);
#pragma unroll
  for (int a = 0; a < 4; ++a) {
    *(v4u*)(dst + (2 * a) * stride) = (v4u){e[a], e[a + 1], e[a + 2], e[a + 3]};
    *(v4u*)(dst + (2 * a + 1) * stride) = (v4u){u[a], u[a + 1], u[a + 2], u[a + 3]};
  }
}
__device__ __forceinline__ unsigned y2pair(float y0, float y1, unsigned kv, float Dv) {
  const unsigned yy = pk2(y0, y1), kd = pk2(lo2f(kv) * Dv, hi2f(kv) * Dv);
  return nosent(pk2(lo2f(yy) + lo2f(kd), hi2f(yy) + hi2f(kd)));
}
#define CL_STRIDE 1056
#define LC_WAVE 27136
__device__ __forceinline__ void conv_pass(const u16* aB, const u16* bB, f32x16& acc) {
  v4u A[8], B[8];
#pragma unroll
  for (int q = 0; q < 8; ++q) { A[q] = *(const v4u*)(aB - 32 * (q >> 1) + 16 * (q & 1)); B[q] = *(const v4u*)(bB - 40 * (q >> 1) + 16 * (q & 1)); }
  asm volatile("s_nop 1" : "+v"(acc));
#pragma nounroll
  for (int m4 = 1; m4 <= 8; ++m4) {
    const u16* an = aB - 128 * (m4 & 7);
    const u16* bn = bB - 160 * (m4 & 7);
    PIN();
#pragma unroll
    for (int q = 0; q < 8; ++q) {
      asm volatile("v_mfma_f32_32x32x16_bf16 %0, %1, %2, %0" : "+v"(acc) : "v"(A[q]), "v"(B[q]));
      A[q] = *(const v4u*)(an - 32 * (q >> 1) + 16 * (q & 1)); B[q] = *(const v4u*)(bn - 40 * (q >> 1) + 16 * (q & 1));
      PIN();
    }
  }
  asm volatile("s_nop 7\n\ts_nop 7" : "+v"(acc) :: "memory");
}
template <typename DF>
__device__ __forceinline__ void conv(char* sm, const v4u (&cw)[2][3], const v4u (&kw)[2][2], float Dv, const rsrc_t Y, int d, int lane, DF&& done) {
  u16* crev = (u16*)sm;
  u16* kvs = (u16*)(sm + 16 * CL_STRIDE);
  const v4u z = {0u, 0u, 0u, 0u};
  crev_put(crev + (127 - 2 * lane) * 8, CL_STRIDE, cw[0][1], lane ? cw[0][0] : z);
  crev_put(crev + (126 - 2 * lane) * 8, CL_STRIDE, cw[0][2], cw[0][1]);
  if (lane < 32) *(v4u*)(crev + (lane >> 2) * CL_STRIDE + (128 + (lane & 3)) * 8) = z;
  u16* kp = kvs + (lane >> 1) * 40 + (lane & 1) * 16;
  *(v4u*)kp = z; *(v4u*)(kp + 8) = z;
  *(v4u*)(kp + 1280) = kw[0][0]; *(v4u*)(kp + 1288) = kw[0][1];
  asm volatile("" ::: "memory");
  const int i = lane & 31, h = lane >> 5;
  const int rho = (1023 - i) & 7;
  const u16* aB = crev + rho * CL_STRIDE + (1023 - (i - 8 * h) - rho);
  const u16* bB = kvs + (32 + i) * 40 + 8 * h;
#pragma nounroll
  for (int u = 0; u < 2; ++u) {
    f32x16 acc;
#pragma unroll
    for (int r = 0; r < 16; ++r) acc[r] = 0.f;
    if (u) {
      *(v4u*)(kp + 2560) = kw[1][0]; *(v4u*)(kp + 2568) = kw[1][1];
      asm volatile("" ::: "memory");
      conv_pass(aB, bB + 1280, acc);
      crev_put(crev + (127 - 2 * lane) * 8, CL_STRIDE, cw[1][1], cw[1][0]);
      crev_put(crev + (126 - 2 * lane) * 8, CL_STRIDE, cw[1][2], cw[1][1]);
      if (lane >= 62) {
        crev_put(crev + (255 - 2 * lane) * 8, CL_STRIDE, cw[0][1], cw[0][0]);
        crev_put(crev + (254 - 2 * lane) * 8, CL_STRIDE, cw[0][2], cw[0][1]);
      }
      asm volatile("" ::: "memory");
    }
    conv_pass(aB, bB, acc);
#pragma unroll
    for (int r = 0; r < 4; ++r) {
      const v2u kv4 = *(const v2u*)&kvs[(32 + 32 * u + i) * 40 + 8 * r + 4 * h];
      *(v2u*)&kvs[(96 + i) * 40 + 8 * r + 4 * h] = (v2u){y2pair(acc[4 * r], acc[4 * r + 1], kv4.x, Dv), y2pair(acc[4 * r + 2], acc[4 * r + 3], kv4.y, Dv)};
    }
    asm volatile("" ::: "memory");
#pragma unroll
    for (int k = 0; k < 2; ++k) st16(Y, (unsigned)(k * 64 + lane) * 16u, (unsigned)d * YS + 2048u * u, *(const v4u*)&kvs[(96 + 16 * k + (lane >> 2)) * 40 + 8 * (lane & 3)]);
    asm volatile("" ::: "memory");
    if (!u) done();
  }
}

#define NQE 8
#define NVE 8
#define NOE 10
__global__ __launch_bounds__(256, 1) void k_main(MP p) {
  extern __shared__ __attribute__((aligned(16))) char smem[];
  const int tid = threadIdx.x;
  unsigned cpf = 0;
  {
    unsigned long long pc; unsigned len;
    asm volatile("s_getpc_b64 %0\n.Lcpf:\n\ts_mov_b32 %1, _Z5k_pre4PreP-.Lcpf" : "=s"(pc), "=s"(len));
    cpf = __builtin_amdgcn_raw_buffer_load_b32(mkr((const void*)pc, (int)len > 0 ? len & ~3u : 0u), (unsigned)(tid + 256 * ((blockIdx.x >> 3) & 3)) * 128u, 0, 0);
  }
  unsigned xcc; asm volatile("s_getreg_b32 %0, hwreg(HW_REG_XCC_ID)" : "=s"(xcc));
  xcc &= 7u;
  int* rl = (int*)(smem + L_ROLE);
  if (tid == 0) {
    unsigned b0 = ~0u, b1 = ~0u;
#pragma unroll
    for (int k = 0; k < 4; ++k) { const v4u c = *(const v4u*)&p.ct[CT_FLIP + 4 * k]; b0 = min(b0, min(c.x, c.z)); b1 = min(b1, min(c.y, c.w)); }
    int r = 0;
#pragma nounroll
    for (unsigned k = 0; k < 8; ++k) {
      const unsigned x = xcc ^ k;
      const unsigned tk = __hip_atomic_fetch_add(&p.ct[CT_TICK + x * 32], 1u, __ATOMIC_RELAXED, __HIP_MEMORY_SCOPE_AGENT);
      if (tk < 32u) { r = (int)(x * 32u + tk); break; }
    }
    rl[0] = r; rl[1] = b1 < b0;
  }
  asm volatile("" :: "v"(cpf));
  BAR();
  int role = __builtin_amdgcn_readfirstlane(rl[0]);
  {
    const int o = (role >> 5) & 1, a = (role >> 6) & 1, tk = role & 31;
    const int c1 = o ? 16 - NQE : NQE, c2 = c1 + (o ? 16 - NVE : NVE), c3 = c2 + (o ? 18 - NOE : NOE), c4 = c3 + (o ? 9 - NOE / 2 : NOE / 2);
    const int r = tk < c1 ? 16 * a + o * NQE + tk : tk < c2 ? 32 + 16 * a + o * NVE + tk - c1 : tk < c3 ? 64 + 18 * a + o * NOE + tk - c2
                : tk < c4 ? 100 + 9 * a + o * (NOE / 2) + tk - c3 : 118 + 5 * a + o * (32 - NQE - NVE - NOE - NOE / 2) + tk - c4;
    role = (role & 128) + r;
  }
  const unsigned flip = (unsigned)__builtin_amdgcn_readfirstlane(rl[1]);
  const int i = role & 127, grp = role >> 7;
  const unsigned gb = (((unsigned)grp) ^ flip) * 8192u;
  const int rep = (int)(xcc & 3u);
  const int rw = (grp ^ 1) * 10 + 2 * ((i - 118) % 5) + (i - 118) / 5;
  if (grp || i >= 100) {
    const int lane = tid & 63, w = __builtin_amdgcn_readfirstlane(tid >> 6);
    const int d1 = (i < 118 ? (grp ? i : i + 18) : rw < 12 ? 200 : 124 + rw) * 4 + w;
    if (i >= 118) arm_slots<0>(p, rw >> 2, rw & 3);
    if (d1 < DM) {
      const unsigned dv = (unsigned)p.ssm_D[d1] << 16;
      v4u cw[2][3], kw[2][2];
#pragma unroll
      for (int u = 0; u < 2; ++u) {
        const size_t o = (size_t)d1 * S_LEN + 1024 * u + lane * 16;
        cw[u][0] = *(const v4u*)&p.ssm_k[u || lane ? o - 8 : o]; cw[u][1] = *(const v4u*)&p.ssm_k[o]; cw[u][2] = *(const v4u*)&p.ssm_k[o + 8];
        kw[u][0] = *(const v4u*)&p.kvT[o]; kw[u][1] = *(const v4u*)&p.kvT[o + 8];
      }
      conv(smem + w * LC_WAVE, cw, kw, __uint_as_float(dv), mkr(p.Y, DM * YS), d1, lane, [&]() {
        if (lane == 0) __hip_atomic_fetch_add(&p.ct[CT_YC + (d1 & 7) * 32], 1u, __ATOMIC_RELAXED, __HIP_MEMORY_SCOPE_AGENT);
      });
    }
    BAR();
  }
  if (i < 32) qk_wg(p, role, grp, i, rep, gb);
  else if (i < 64) vt_wg(p, role, grp, i - 32, rep, gb);
  else if (i < 100) o_wg(p, role, grp, i - 64, rep, gb);
  else if (i < 118) u_wg(p, role, grp, i - 100, gb);
  else row_wg(p, smem, role, rw, flip);
}

#define PRE_KV 144
#define PRE_FILL 200
#define NCAL 4
#define FAUX 16
#define STW(base, bytes, idx16, v) __builtin_amdgcn_raw_buffer_store_b128(v, mkr(base, bytes), (unsigned)(idx16) * 16u, 0, FAUX)
struct PreP {
  const u16* kk; const u16* vv; const u16* shift_k; u16* kvT;
  const u16* mem0; v4u* win; v4u* Y; v4u* H; unsigned* ct;
};
__global__ __launch_bounds__(256) void k_pre(PreP p) {
  __shared__ __attribute__((aligned(16))) u16 sh[64 * 136];
  const int tid = threadIdx.x;
  int bid = blockIdx.x;
  if (bid < 32) {
    unsigned long long pc; int len, back;
    asm volatile("s_getpc_b64 %0\n.Lcpf0:\n\ts_mov_b32 %1, code_end-.Lcpf0\n\ts_mov_b32 %2, .Lcpf0-_Z6k_main2MP" : "=s"(pc), "=s"(len), "=s"(back));
    const bool ok = len > 0 && back > 0;
    const unsigned v = __builtin_amdgcn_raw_buffer_load_b32(mkr((const void*)pc, ok ? (unsigned)len & ~3u : 0u), (unsigned)tid * 128u, 0, 0);
    const unsigned u = __builtin_amdgcn_raw_buffer_load_b32(mkr((const void*)(pc - (unsigned)back), ok ? (unsigned)back & ~3u : 0u), (unsigned)(tid + 256 * (bid >> 3)) * 128u, 0, 0);
    asm volatile("" :: "v"(v), "v"(u));
  }
  if (bid < 8) {
    if (bid == 0) for (int i = tid; i < CT_TOTAL; i += 256) if (i < CT_FLIP || i >= CT_YC) p.ct[i] = 0u;
    const rsrc_t X = mkr(p.win, WIN_BYTES);
    unsigned xcc; asm volatile("s_getreg_b32 %0, hwreg(HW_REG_XCC_ID)" : "=s"(xcc));
    int* best = (int*)sh;
    int b0 = 1 << 30, b1 = 1 << 30;
    unsigned acc = 0;
    const unsigned ob = (unsigned)(CAL_CH - bid) * CH + (unsigned)(tid >> 6) * 2048u;
    st16(X, ob + (unsigned)((tid >> 5 & 1) * 8192 + ((tid & 31) >> 3) * 256 + (tid & 7) * 16), 0, (v4u){0u, 0u, 0u, 0u});
    asm volatile("s_waitcnt vmcnt(0)" ::: "memory");
#pragma nounroll
    for (int i = 0; i < NCAL; ++i) {
      const int par = (i ^ (tid >> 6)) & 1;
      const unsigned o = ob + (unsigned)par * 8192u + (unsigned)(i >> 1) * 256u;
      long long t0 = __builtin_amdgcn_s_memtime();
      unsigned v = ld4(X, o, 0);
      asm volatile("s_waitcnt vmcnt(0)" : "+v"(v) :: "memory");
      long long t1 = __builtin_amdgcn_s_memtime();
      acc += v;
      const int d = (int)(t1 - t0);
      if (par ^ (int)((xcc >> 2) & 1u)) b1 = d < b1 ? d : b1; else b0 = d < b0 ? d : b0;
    }
    if ((tid & 63) == 0) { best[(tid >> 6) * 2] = b0; best[(tid >> 6) * 2 + 1] = b1; }
    __syncthreads();
    if (tid < 2) p.ct[CT_FLIP + 2 * bid + tid] = (unsigned)min(min(best[tid], best[2 + tid]), min(best[4 + tid], best[6 + tid])) + acc;
    return;
  }
  bid -= 8;
  if (bid < PRE_KV) {
    const int d0 = (bid % 9) * 64, s0 = (bid / 9) * 128;
    const int dq = tid & 7, sr = tid >> 3;
    const int d = d0 + dq * 8, s = s0 + 4 * sr;
    v4u kr[7], vr[4], sw[4];
#pragma unroll
    for (int a = 0; a < 7; ++a) kr[a] = (s - 3 + a >= 0) ? *(const v4u*)&p.kk[(long)(s - 3 + a) * DM + d] : (v4u){0, 0, 0, 0};
#pragma unroll
    for (int a = 0; a < 4; ++a) { vr[a] = *(const v4u*)&p.vv[(long)(s + a) * DM + d]; sw[a] = *(const v4u*)&p.shift_k[d * 4 + 8 * a]; }
#pragma unroll
    for (int jp = 0; jp < 4; ++jp) {
      const float w0[4] = {lo2f(sw[jp].x), hi2f(sw[jp].x), lo2f(sw[jp].y), hi2f(sw[jp].y)}, w1[4] = {lo2f(sw[jp].z), hi2f(sw[jp].z), lo2f(sw[jp].w), hi2f(sw[jp].w)};
      unsigned o[4];
#pragma unroll
      for (int r = 0; r < 4; ++r) {
        unsigned acc = pk2(lo2f(kr[r][jp]) * w0[3], hi2f(kr[r][jp]) * w1[3]);
#pragma unroll
        for (int a = 1; a < 4; ++a) {
          const unsigned m = pk2(lo2f(kr[r + a][jp]) * w0[3 - a], hi2f(kr[r + a][jp]) * w1[3 - a]);
          acc = pk2(lo2f(acc) + lo2f(m), hi2f(acc) + hi2f(m));
        }
        o[r] = pk2(lo2f(acc) * lo2f(vr[r][jp]), hi2f(acc) * hi2f(vr[r][jp]));
      }
      *(v2u*)&sh[(dq * 8 + 2 * jp) * 136 + 4 * sr] = (v2u){(o[0] & 0xFFFFu) | (o[1] << 16), (o[2] & 0xFFFFu) | (o[3] << 16)};
      *(v2u*)&sh[(dq * 8 + 2 * jp + 1) * 136 + 4 * sr] = (v2u){(o[0] >> 16) | (o[1] & 0xFFFF0000u), (o[2] >> 16) | (o[3] & 0xFFFF0000u)};
    }
    __syncthreads();
#pragma unroll
    for (int i = 0; i < 4; ++i) {
      const int c = tid + 256 * i, dd = c >> 4, q = c & 15;
      STW(p.kvT, DM * S_LEN * 2u, (d0 + dd) * (S_LEN / 8) + s0 / 8 + q, *(const v4u*)&sh[dd * 136 + 8 * q]);
    }
    return;
  }
  bid -= PRE_KV;
  {
    const v4u ones = {0xFFFFFFFFu, 0xFFFFFFFFu, 0xFFFFFFFFu, 0xFFFFFFFFu};
    for (unsigned i = bid * 256 + tid; i < (VM_CH + 108u) * CH / 16u; i += PRE_FILL * 256) {
      if (i >= (CTL_CH + 1u) * CH / 16u && i < PT_CH * CH / 16u) continue;
      v4u v = i / (CH / 4u) == BAS_CH / 4 ? (v4u){0u, 0u, 0u, 0u} : ones;
      const unsigned c = ((i >> 10) & 1u) * 512u + (i & 511u);
      if (i < 2u * NREP * CH / 16u && c < 576u) {
        const unsigned* s = (const unsigned*)&p.mem0[(2 * ((c >> 2) & 3) + (c & 1)) * DM + (c >> 4) * 16 + (c & 2)];
        v = (v4u){nosent(s[0]), nosent(s[2]), nosent(s[4]), nosent(s[6])};
      }
      STW(p.win, WIN_BYTES, i, v);
    }
    for (unsigned i = bid * 256 + tid; i < DM * 256; i += PRE_FILL * 256) STW(p.Y, DM * YS, (i >> 8) * (YS / 16) + (i & 255), ones);
    for (unsigned i = bid * 256 + tid; i < 5 * SEGB / 16; i += PRE_FILL * 256) STW(p.H, 2u * NSEG * SEGB, i, ones);
    if (bid * 256 + tid < 2 * SEGB / 16) STW(p.H, 2u * NSEG * SEGB, NSEG * SEGB / 16 + bid * 256 + tid, ones);
  }
}

extern "C" __device__ __attribute__((used, noinline)) void code_end() {}

extern "C" void kernel_launch(void* const* d_in, const int* in_sizes, int n_in, void* d_out, int out_size, void* d_ws,
                              size_t ws_size, hipStream_t stream) {
  const u16* A_q = (const u16*)d_in[0];
  const u16* A_k = (const u16*)d_in[1];
  const u16* A_v = (const u16*)d_in[2];
  const u16* A_shift = (const u16*)d_in[3];
  const u16* A_ssmk = (const u16*)d_in[4];
  const u16* A_ssmD = (const u16*)d_in[5];
  const u16* B_mem0 = (const u16*)d_in[7];
  const u16* B_Wq = (const u16*)d_in[8];
  const u16* B_Wk = (const u16*)d_in[9];
  const u16* B_Wv = (const u16*)d_in[10];
  const u16* B_Wo = (const u16*)d_in[11];

  char* ws = (char*)(((uintptr_t)d_ws + ((2u << 20) - 1)) & ~(uintptr_t)((2u << 20) - 1));
  size_t off = 0;
  auto alloc = [&](size_t bytes) { char* q = ws + off; off += (bytes + 4095) & ~(size_t)4095; return q; };
  unsigned* win = (unsigned*)alloc(WIN_BYTES);
  unsigned* ct = (unsigned*)alloc(CT_TOTAL * 4);
  u16* Y = (u16*)alloc((size_t)DM * YS);
  u16* H = (u16*)alloc((size_t)2 * NSEG * SEGB);
  u16* OT = (u16*)alloc((size_t)2 * NSEG * SEGB);
  u16* kvT = (u16*)alloc((size_t)DM * S_LEN * 2);

  PreP pp;
  pp.kk = A_k; pp.vv = A_v; pp.shift_k = A_shift; pp.kvT = kvT;
  pp.mem0 = B_mem0; pp.win = (v4u*)win; pp.Y = (v4u*)Y; pp.H = (v4u*)H; pp.ct = ct;
  k_pre<<<8 + PRE_KV + PRE_FILL, 256, 0, stream>>>(pp);
  MP mp;
  mp.Wq = B_Wq; mp.Wk = B_Wk; mp.Wv = B_Wv; mp.Wo = B_Wo; mp.x0 = A_q; mp.kvT = kvT; mp.ssm_k = A_ssmk; mp.ssm_D = A_ssmD; mp.mem0 = B_mem0;
  mp.out = (u16*)d_out; mp.win = win; mp.ct = ct; mp.Y = Y; mp.H = H; mp.OT = OT;
  const int smem_bytes = L_TOTAL;
  (void)hipFuncSetAttribute((const void*)k_main, hipFuncAttributeMaxDynamicSharedMemorySize, smem_bytes);
  k_main<<<256, 256, smem_bytes, stream>>>(mp);
}
```
